```python
import torch

def _softmax(x, axis=-1):
    return torch.softmax(x, dim=axis)

def _gelu(x):
    return torch.nn.functional.gelu(x, approximate='tanh')

def _silu(x):
    return torch.nn.functional.silu(x)

def _sigmoid(x):
    return torch.sigmoid(x)

def _layernorm(x, axis=-1, eps=1e-05):
    m = x.mean(dim=axis, keepdim=True)
    v = x.var(dim=axis, keepdim=True, unbiased=False)
    return (x - m) / torch.sqrt(v + eps)

def _scatter_add(tgt, idx, src, axis=0):
    return tgt.clone().index_add_(axis, idx, src)

def _segment_sum(src, seg, n):
    out = torch.zeros((n,) + src.shape[1:], dtype=src.dtype)
    return out.index_add_(0, seg, src)

def _pad(x, pw):
    flat = tuple((v for pair in reversed(pw) for v in pair))
    return torch.nn.functional.pad(x, flat)
kernel_name = 'adaLN_cond__gqa_block_cached__mixer_gmlp__prompt'

def setup_inputs():

    _w = lambda a: (lambda t: t.to(torch.bfloat16) if t.is_floating_point() else t)(torch.as_tensor(a))

    def _A_setup():
        B, Sq, D = (1, 2048, 576)
        Hq, Hkv, Sk, Dh = (32, 16, 2048, 128)
        assert Hq % Hkv == 0, (Hq, Hkv)
        return {'x': _w((lambda *s: torch.randn(*s, dtype=torch.bfloat16) * 0.05)(B, Sq, D)), 'k_cache': _w((lambda *s: torch.randn(*s, dtype=torch.bfloat16) * 3.2)(B, Hkv, Sk, Dh)), 'v_cache': _w((lambda *s: torch.randn(*s, dtype=torch.bfloat16) * 0.4)(B, Hkv, Sk, Dh)), 'wq': _w((lambda *s: torch.randn(*s, dtype=torch.bfloat16) * 3.2)(D, Hq * Dh) * D ** (-0.5)), 'wo': _w((lambda *s: torch.randn(*s, dtype=torch.bfloat16) * 0.4)(Hq * Dh, D) * (Hq * Dh) ** (-0.5))}
    _A_in = _A_setup()

    def _B_setup():
        B, S, D, F = (1, 2048, 576, 4608)
        Fh = F // 2
        return {'x': _w((lambda *s: torch.randn(*s, dtype=torch.bfloat16) * 0.05)(B, S, D)), 'w_in': _w((lambda *s: torch.randn(*s, dtype=torch.bfloat16) * 1.6)(D, 2 * Fh)), 'w_sp': _w(0.01 * (lambda *s: torch.randn(*s, dtype=torch.bfloat16) * 1.6)(S, S)), 'b_sp': _w(1.0 + 0.01 * (lambda *s: torch.randn(*s, dtype=torch.bfloat16) * 0.05)(S)), 'w_out': _w((lambda *s: torch.randn(*s, dtype=torch.bfloat16) * 0.05)(Fh, D))}
    _B_in = _B_setup()
    return {'A_x': _A_in['x'], 'A_k_cache': _A_in['k_cache'], 'A_v_cache': _A_in['v_cache'], 'A_wq': _A_in['wq'], 'A_wo': _A_in['wo'], 'B_x': _B_in['x'], 'B_w_in': _B_in['w_in'], 'B_w_sp': _B_in['w_sp'], 'B_b_sp': _B_in['b_sp'], 'B_w_out': _B_in['w_out'], 'c': _w((lambda *s: torch.randn(*s, dtype=torch.bfloat16) * 0.2)(1, 576) * 8.0), 'w_mod': _w((lambda *s: torch.randn(*s, dtype=torch.bfloat16) * 0.2)(576, 6 * 576) * 576 ** (-0.5) * 8.0)}

def reference(A_x, A_k_cache, A_v_cache, A_wq, A_wo, B_x, B_w_in, B_w_sp, B_b_sp, B_w_out, c, w_mod):

    def _A_ref(x, k_cache, v_cache, wq, wo):
        B, Sq, D = x.shape
        Hq, Dh = (32, 128)
        Hkv = k_cache.shape[1]
        rep = Hq // Hkv
        q = (lambda x, *d: x.permute(*d))((x @ wq).reshape(B, Sq, Hq, Dh), 0, 2, 1, 3)
        k = (lambda x, n, axis: torch.repeat_interleave(x, n, dim=axis))(k_cache, rep, axis=1)
        v = (lambda x, n, axis: torch.repeat_interleave(x, n, dim=axis))(v_cache, rep, axis=1)
        scores = torch.einsum('bhqd,bhkd->bhqk', q, k) * Dh ** (-0.5)
        p = _softmax(scores, axis=-1)
        o = torch.einsum('bhqk,bhkd->bhqd', p, v)
        return (lambda x, *d: x.permute(*d))(o, 0, 2, 1, 3).reshape(B, Sq, Hq * Dh) @ wo

    def _B_ref(x, w_in, w_sp, b_sp, w_out):
        B, S, D = x.shape
        F = w_in.shape[-1]
        Fh = F // 2
        z = _gelu(torch.einsum('bsd,df->bsf', x, w_in))
        z1 = z[..., :Fh].clone()
        z2 = _layernorm(z[..., Fh:], axis=-1)
        g = torch.einsum('ts,bsf->btf', w_sp, z2) + b_sp[None, :, None]
        return torch.einsum('bsf,fd->bsd', z1 * g, w_out)
    x = A_x
    mod = (_silu(c) @ w_mod).reshape(c.shape[0], 6, 1, -1)
    sa, ca, ga, sb, cb, gb = (mod[:, 0].clone(), mod[:, 1].clone(), mod[:, 2].clone(), mod[:, 3].clone(), mod[:, 4].clone(), mod[:, 5].clone())
    h = _layernorm(x, axis=-1) * (1 + ca) + sa
    x = x + ga * _A_ref(h, A_k_cache, A_v_cache, A_wq, A_wo)
    h = _layernorm(x, axis=-1) * (1 + cb) + sb
    x = x + gb * _B_ref(h, B_w_in, B_w_sp, B_b_sp, B_w_out)
    return x
```

```cpp
#include <hip/hip_runtime.h>
#include <hip/hip_bf16.h>

typedef unsigned short bf16_t;
typedef short bf16x8 __attribute__((ext_vector_type(8)));
typedef short s16x4 __attribute__((ext_vector_type(4)));
typedef float f32x4 __attribute__((ext_vector_type(4)));
typedef float f32x16 __attribute__((ext_vector_type(16)));
typedef unsigned u32x4 __attribute__((ext_vector_type(4)));
typedef unsigned u32x2 __attribute__((ext_vector_type(2)));
#define DI __device__ __forceinline__
#define LAS __attribute__((address_space(3)))

constexpr int S_ = 2048, D_ = 576, HQ = 32, HKV = 16, DH = 128, QD = HQ * DH, F_ = 4608, FH = 2304;

DI float bf2f(bf16_t v) { return __uint_as_float((unsigned)v << 16); }
DI float bflo(unsigned v) { return __uint_as_float(v << 16); }
DI float bfhi(unsigned v) { return __uint_as_float(v & 0xffff0000u); }
DI unsigned cvtpk(float lo, float hi) { unsigned r; asm("v_cvt_pk_bf16_f32 %0, %1, %2" : "=v"(r) : "v"(lo), "v"(hi)); return r; }

__global__ __launch_bounds__(256) void k_mod(const bf16_t* __restrict__ c, const bf16_t* __restrict__ w, float* __restrict__ mod) {
  __shared__ float red[32][65];
  const int t = threadIdx.x, c8 = t & 7, rg = t >> 3, n0 = blockIdx.x * 64 + c8 * 8;
  float acc[8] = {0, 0, 0, 0, 0, 0, 0, 0};
#pragma unroll
  for (int i = 0; i < 18; ++i) {
    const int k = rg + 32 * i;
    const u32x4 v = *(const u32x4*)(w + (size_t)k * 3456 + n0);
    const float cv = bf2f(c[k]); const float s = cv / (1.f + __expf(-cv));
#pragma unroll
    for (int j = 0; j < 4; ++j) { acc[2 * j] += s * bflo(v[j]); acc[2 * j + 1] += s * bfhi(v[j]); }
  }
#pragma unroll
  for (int j = 0; j < 8; ++j) red[rg][c8 * 8 + j] = acc[j];
  __syncthreads();
  if (t < 64) { float s = 0; for (int r = 0; r < 32; ++r) s += red[r][t]; mod[blockIdx.x * 64 + t] = s; }
}

template <int NC, bool MODULATE>
__global__ __launch_bounds__(256) void k_ln(const bf16_t* __restrict__ x, const float* __restrict__ shift, const float* __restrict__ scale, bf16_t* __restrict__ h, int ldx, int ldh) {
  constexpr int NCH = NC / 64;
  const int t = threadIdx.x, l8 = t & 7, row = blockIdx.x * 32 + (t >> 3);
  u32x4 v[NCH]; float s = 0;
#pragma unroll
  for (int i = 0; i < NCH; ++i) { v[i] = *(const u32x4*)(x + (size_t)row * ldx + (i * 8 + l8) * 8);
#pragma unroll
    for (int j = 0; j < 4; ++j) s += bflo(v[i][j]) + bfhi(v[i][j]); }
  s += __shfl_xor(s, 1); s += __shfl_xor(s, 2); s += __shfl_xor(s, 4);
  const float m = s * (1.f / NC); float q = 0;
#pragma unroll
  for (int i = 0; i < NCH; ++i)
#pragma unroll
    for (int j = 0; j < 4; ++j) { const float a = bflo(v[i][j]) - m, b = bfhi(v[i][j]) - m; q += a * a + b * b; }
  q += __shfl_xor(q, 1); q += __shfl_xor(q, 2); q += __shfl_xor(q, 4);
  const float r = 1.f / sqrtf(q * (1.f / NC) + 1e-5f);
#pragma unroll
  for (int i = 0; i < NCH; ++i) { u32x4 o; const int c0 = (i * 8 + l8) * 8;
#pragma unroll
    for (int j = 0; j < 4; ++j) { float a = (bflo(v[i][j]) - m) * r, b = (bfhi(v[i][j]) - m) * r;
      if (MODULATE) { a = a * (1.f + scale[c0 + 2 * j]) + shift[c0 + 2 * j]; b = b * (1.f + scale[c0 + 2 * j + 1]) + shift[c0 + 2 * j + 1]; }
      o[j] = cvtpk(a, b); }
    *(u32x4*)(h + (size_t)row * ldh + c0) = o; }
}

struct EpiArgs { bf16_t* out; const bf16_t* res; const float* gate; const bf16_t* bias; const bf16_t* mul; int ldo; int ldr; float scale; int pad; };
enum { EPI_SCALE = 0, EPI_RESGATE = 1, EPI_GELU = 2, EPI_SPATIAL = 3 };
DI float gelu_t(float x) { const float c1 = -2.f * 0.7978845608028654f * 1.4426950408889634f, c2 = c1 * 0.044715f;
  const float e = __builtin_amdgcn_exp2f(x * (c1 + c2 * x * x)); return x * __builtin_amdgcn_rcpf(1.f + e); }

template <int BM, int BN, int EPI>
__global__ __launch_bounds__(256) void k_gemm(const bf16_t* __restrict__ A, const bf16_t* __restrict__ B, EpiArgs ep, int lda, int ldb, int K, int zero) {
  constexpr int BK = 64, WM = BM / 2, WN = BN / 2, MI = WM / 16, NI = WN / 16;
  constexpr int BSTR = BN * 2 + 32;
  constexpr int ABYTES = BM * 128, BBYTES = 64 * BSTR;
  constexpr int ACH = BM * 8 / 256, BCH = 64 * (BN / 8) / 256;
  __shared__ __attribute__((aligned(16))) char lds[2 * (ABYTES + BBYTES)];
  const int t = threadIdx.x, lane = t & 63, wid = t >> 6, wr = wid >> 1, wc = wid & 1;
  const int m0 = blockIdx.y * BM, n0 = blockIdx.x * BN;
  u32x4 ra[ACH], rb[BCH];
  auto gload = [&](int kt) {
#pragma unroll
    for (int i = 0; i < ACH; ++i) { const int id = t + 256 * i, r = id >> 3, c = id & 7; ra[i] = *(const u32x4*)(A + (size_t)(m0 + r) * lda + kt * BK + c * 8); }
#pragma unroll
    for (int i = 0; i < BCH; ++i) { const int id = t + 256 * i, k = id / (BN / 8), c = id % (BN / 8); rb[i] = *(const u32x4*)(B + (size_t)(kt * BK + k) * ldb + n0 + c * 8); }
  };
  auto swrite = [&](int buf) {
    char* sa = lds + buf * (ABYTES + BBYTES); char* sb = sa + ABYTES;
#pragma unroll
    for (int i = 0; i < ACH; ++i) { const int id = t + 256 * i, r = id >> 3, c = id & 7; *(u32x4*)(sa + r * 128 + ((c ^ ((r >> 1) & 7)) << 4)) = ra[i]; }
#pragma unroll
    for (int i = 0; i < BCH; ++i) { const int id = t + 256 * i, k = id / (BN / 8), c = id % (BN / 8);
      const int rho = (k & ~0xC) | ((k & 4) << 1) | ((k & 8) >> 1); *(u32x4*)(sb + rho * BSTR + c * 16) = rb[i]; }
  };
  f32x4 acc[MI][NI];
#pragma unroll
  for (int i = 0; i < MI; ++i)
#pragma unroll
    for (int j = 0; j < NI; ++j) acc[i][j] = (f32x4){0.f, 0.f, 0.f, 0.f};
  const int nt = K / BK;
  gload(0); swrite(0); __syncthreads();
  const int fr = lane & 15, fq = lane >> 4, q4 = (lane & 15) >> 2, p4 = lane & 3;
  for (int kt = 0; kt < nt; ++kt) {
    if (kt + 1 < nt) gload(kt + 1);
    const char* sa = lds + (kt & 1) * (ABYTES + BBYTES); const char* sb = sa + ABYTES;
#pragma unroll
    for (int ks = 0; ks < 2; ++ks) {
      bf16x8 af[MI], bfr[NI];
#pragma unroll
      for (int i = 0; i < MI; ++i) { const int r = wr * WM + i * 16 + fr, c = ks * 4 + fq; af[i] = *(const bf16x8*)(sa + r * 128 + ((c ^ ((r >> 1) & 7)) << 4)); }
#pragma unroll
      for (int j = 0; j < NI; ++j) {
        const int rho0 = ks * 32 + 16 * (fq >> 1) + 4 * (fq & 1) + q4; const int col = wc * WN + j * 16 + p4 * 4;
        const s16x4 lo = __builtin_amdgcn_ds_read_tr16_b64_v4i16((LAS s16x4*)(sb + rho0 * BSTR + col * 2));
        const s16x4 hi = __builtin_amdgcn_ds_read_tr16_b64_v4i16((LAS s16x4*)(sb + (rho0 + 8) * BSTR + col * 2));
        bfr[j] = __builtin_shufflevector(lo, hi, 0, 1, 2, 3, 4, 5, 6, 7);
      }
#pragma unroll
      for (int i = 0; i < MI; ++i)
#pragma unroll
        for (int j = 0; j < NI; ++j) acc[i][j] = __builtin_amdgcn_mfma_f32_16x16x32_bf16(bfr[j], af[i], acc[i][j], 0, 0, 0);
    }
    if (kt + 1 < nt) swrite((kt + 1) & 1);
    __syncthreads();
  }
#pragma unroll
  for (int i = 0; i < MI; ++i)
#pragma unroll
    for (int j = 0; j < NI; ++j) {
      const int m = m0 + wr * WM + i * 16 + fr, n = n0 + wc * WN + j * 16 + fq * 4;
      f32x4 v = acc[i][j];
      if (EPI == EPI_SCALE) { v = v * ep.scale; }
      else if (EPI == EPI_RESGATE) { const u32x2 r = *(const u32x2*)(ep.res + (size_t)m * ep.ldr + n); const f32x4 g = *(const f32x4*)(ep.gate + n);
        v[0] = bflo(r[0]) + g[0] * v[0]; v[1] = bfhi(r[0]) + g[1] * v[1]; v[2] = bflo(r[1]) + g[2] * v[2]; v[3] = bfhi(r[1]) + g[3] * v[3]; }
      else if (EPI == EPI_GELU) { v[0] = gelu_t(v[0]); v[1] = gelu_t(v[1]); v[2] = gelu_t(v[2]); v[3] = gelu_t(v[3]); }
      else if (EPI == EPI_SPATIAL) { const float b = bf2f(ep.bias[m]); const u32x2 r = *(const u32x2*)(ep.mul + (size_t)m * ep.ldr + n);
        v[0] = bflo(r[0]) * (v[0] + b); v[1] = bfhi(r[0]) * (v[1] + b); v[2] = bflo(r[1]) * (v[2] + b); v[3] = bfhi(r[1]) * (v[3] + b); }
      u32x2 o; o[0] = cvtpk(v[0], v[1]); o[1] = cvtpk(v[2], v[3]);
      *(u32x2*)(ep.out + (size_t)m * ep.ldo + n) = o;
    }
}

constexpr int NW = 8, QBLK = 32, KVBLK = 64;
constexpr float THR = 11.f;
constexpr size_t SHM_V = KVBLK * DH * 2, SHM_K = KVBLK * DH * 2, SHM_ATTN = 2 * SHM_V + 2 * SHM_K + NW * 64 * 4;
#define KSWZ(row, colB) ((row) * 256 + ((colB) ^ (((row) & 7) << 4)))
#define SBAR() __builtin_amdgcn_sched_barrier(0)
DI int crow(int r, int hi) { return (r & 3) + 8 * (r >> 2) + 4 * hi; }
DI void partialSM(f32x16& p0, f32x16& p1, float& m_reg, float& mn, float& alpha) {
  float pmax = p0[0]; for (int r = 1; r < 16; ++r) pmax = fmaxf(pmax, p0[r]); for (int r = 0; r < 16; ++r) pmax = fmaxf(pmax, p1[r]);
  { auto rr = __builtin_amdgcn_permlane32_swap(__float_as_uint(pmax), __float_as_uint(pmax), false, false);
    pmax = fmaxf(__uint_as_float(rr[0]), __uint_as_float(rr[1])); }
  if (__builtin_expect(__all(pmax - m_reg <= THR), 1)) { mn = m_reg; alpha = 1.f; }
  else { mn = fmaxf(m_reg, pmax); alpha = __builtin_amdgcn_exp2f(m_reg - mn); m_reg = mn; }
  for (int r = 0; r < 16; ++r) p0[r] = p0[r] - mn; for (int r = 0; r < 16; ++r) p1[r] = p1[r] - mn;
  for (int r = 0; r < 16; ++r) p0[r] = __builtin_amdgcn_exp2f(p0[r]);
}
DI void finishSM(f32x16& p0, f32x16& p1, float alpha, float& l_reg, bf16x8& pa0, bf16x8& pa1, bf16x8& pa2, bf16x8& pa3) {
  for (int r = 0; r < 16; ++r) p1[r] = __builtin_amdgcn_exp2f(p1[r]);
  float ps = 0; for (int r = 0; r < 16; ++r) ps += p0[r]; for (int r = 0; r < 16; ++r) ps += p1[r];
  { auto rr = __builtin_amdgcn_permlane32_swap(__float_as_uint(ps), __float_as_uint(ps), false, false);
    ps = __uint_as_float(rr[0]) + __uint_as_float(rr[1]); }
  l_reg = l_reg * alpha + ps;
#define PK4(P, BASE, OUT) do { unsigned a0 = cvtpk(P[BASE + 0], P[BASE + 1]), a1 = cvtpk(P[BASE + 2], P[BASE + 3]);   \
    unsigned b0 = cvtpk(P[BASE + 4], P[BASE + 5]), b1 = cvtpk(P[BASE + 6], P[BASE + 7]);                              \
    auto r0 = __builtin_amdgcn_permlane32_swap(a0, b0, false, false); auto r1 = __builtin_amdgcn_permlane32_swap(a1, b1, false, false); \
    u32x4 w = {r0[0], r1[0], r0[1], r1[1]}; OUT = *reinterpret_cast<bf16x8*>(&w); } while (0)
  PK4(p0, 0, pa0); PK4(p0, 8, pa1); PK4(p1, 0, pa2); PK4(p1, 8, pa3);
#undef PK4
}
DI void qkt(f32x16& p0, f32x16& p1, const bf16_t* Ks, const bf16x8* qr, int r32, int hi) {
  p0 = f32x16{}; p1 = f32x16{};
  for (int d0 = 0; d0 < 8; ++d0) { int cb = (d0 * 16 + hi * 8) * 2;
    bf16x8 b0 = *reinterpret_cast<const bf16x8*>((const char*)Ks + KSWZ(r32, cb));
    bf16x8 b1 = *reinterpret_cast<const bf16x8*>((const char*)Ks + KSWZ(32 + r32, cb));
    p0 = __builtin_amdgcn_mfma_f32_32x32x16_bf16(b0, qr[d0], p0, 0, 0, 0);
    p1 = __builtin_amdgcn_mfma_f32_32x32x16_bf16(b1, qr[d0], p1, 0, 0, 0); }
}
DI int v_st(int k, int c) { const int kk = (k & ~0xC) | ((k & 4) << 1) | ((k & 8) >> 1); return ((kk >> 3) * 4 + (c >> 5)) * 512 + ((kk & 7) * 32 + (c & 31)) * 2; }
DI int v_rd_base(int lane) { return ((lane & 3) << 3) | (((lane >> 2) & 3) << 6) | (((lane >> 4) & 1) << 5) | (((lane >> 5) & 1) << 8); }
constexpr int v_rd_off(int d0, int ks, int half) { return d0 * 512 + ks * 4096 + half * 2048; }
template <int OFF> DI s16x4 tr_read(int vb) {
  s16x4 r; asm volatile("ds_read_b64_tr_b16 %0, %1 offset:%2" : "=&v"(r) : "v"(vb), "i"(OFF) : "memory"); return r;
}
template <int D0> DI void pv_one(f32x16& od, int vb, bf16x8 pa0, bf16x8 pa1, bf16x8 pa2, bf16x8 pa3) {
  const s16x4 l0 = tr_read<v_rd_off(D0, 0, 0)>(vb), h0 = tr_read<v_rd_off(D0, 0, 1)>(vb), l1 = tr_read<v_rd_off(D0, 1, 0)>(vb), h1 = tr_read<v_rd_off(D0, 1, 1)>(vb);
  const s16x4 l2 = tr_read<v_rd_off(D0, 2, 0)>(vb), h2 = tr_read<v_rd_off(D0, 2, 1)>(vb), l3 = tr_read<v_rd_off(D0, 3, 0)>(vb), h3 = tr_read<v_rd_off(D0, 3, 1)>(vb);
  asm volatile("s_waitcnt lgkmcnt(0)" ::: "memory"); SBAR();
#define PK(L, H) (bf16x8){L[0], L[1], L[2], L[3], H[0], H[1], H[2], H[3]}
  od = __builtin_amdgcn_mfma_f32_32x32x16_bf16(pa0, PK(l0, h0), od, 0, 0, 0);
  od = __builtin_amdgcn_mfma_f32_32x32x16_bf16(pa1, PK(l1, h1), od, 0, 0, 0);
  od = __builtin_amdgcn_mfma_f32_32x32x16_bf16(pa2, PK(l2, h2), od, 0, 0, 0);
  od = __builtin_amdgcn_mfma_f32_32x32x16_bf16(pa3, PK(l3, h3), od, 0, 0, 0);
#undef PK
}
DI void pv_d0(f32x16* o, int vb, bf16x8 pa0, bf16x8 pa1, bf16x8 pa2, bf16x8 pa3) {
  pv_one<0>(o[0], vb, pa0, pa1, pa2, pa3); pv_one<1>(o[1], vb, pa0, pa1, pa2, pa3); pv_one<2>(o[2], vb, pa0, pa1, pa2, pa3); pv_one<3>(o[3], vb, pa0, pa1, pa2, pa3);
}
__global__ __launch_bounds__(512, 1) void k_attn(const bf16_t* __restrict__ Q, const bf16_t* __restrict__ K, const bf16_t* __restrict__ V, bf16_t* __restrict__ O) {
  extern __shared__ __attribute__((aligned(16))) char lds[];
  constexpr int LDQ = QD, LDK = DH, LDO = QD;
  const int h = blockIdx.y, kvh = h >> 1, i0 = blockIdx.x * 256;
  const bf16_t* Qb = Q + (size_t)i0 * LDQ + h * DH; const bf16_t* Kh = K + (size_t)kvh * S_ * DH; const bf16_t* Vh = V + (size_t)kvh * S_ * DH;
  bf16_t* Ob = O + (size_t)i0 * LDO + h * DH;
  const int seq = S_;
  const int tid = threadIdx.x, wid = tid >> 6, lane = tid & 63, r32 = lane & 31, hi = lane >> 5;
  bf16_t* V_lds = (bf16_t*)lds; bf16_t* K_lds = (bf16_t*)(lds + 2 * SHM_V);
  float* ws = (float*)(lds + 2 * SHM_V + 2 * SHM_K) + wid * 64; float* li_l = ws; float* al_l = ws + 32;
  float m_reg = -1e30f, l_reg = 0; f32x16 o[4] = {}; bf16x8 qr[8];
  const bf16_t* Qw = Qb + (long)(wid * QBLK + r32) * LDQ + hi * 8;
#pragma unroll
  for (int d0 = 0; d0 < 8; ++d0) qr[d0] = *(const bf16x8*)(Qw + d0 * 16);
  const int sr = tid >> 4, sc = (tid & 15) * 8, vst0 = v_st(sr, sc), vst1 = v_st(32 + sr, sc);
  const int vb0 = (int)(uintptr_t)V_lds + v_rd_base(lane);
  struct { bf16x8 vs0, vs1, ks0, ks1; } sr_[2];
#define LD8(p) (*(const bf16x8*)(p))
#define SLOAD(i, k0) do { sr_[i].vs0 = LD8(&Vh[(long)((k0) + sr) * LDK + sc]); sr_[i].vs1 = LD8(&Vh[(long)((k0) + 32 + sr) * LDK + sc]); \
    sr_[i].ks0 = LD8(&Kh[(long)((k0) + sr) * LDK + sc]); sr_[i].ks1 = LD8(&Kh[(long)((k0) + 32 + sr) * LDK + sc]); } while (0)
#define SWRITE(b, i) do { *(bf16x8*)((char*)V_lds + (b) * SHM_V + vst0) = sr_[i].vs0;          \
    *(bf16x8*)((char*)V_lds + (b) * SHM_V + vst1) = sr_[i].vs1; int kc = sc * 2;               \
    *(bf16x8*)((char*)K_lds + (b) * SHM_K + KSWZ(sr, kc)) = sr_[i].ks0;                       \
    *(bf16x8*)((char*)K_lds + (b) * SHM_K + KSWZ(32 + sr, kc)) = sr_[i].ks1; } while (0)
#define SWAIT() asm volatile("s_waitcnt vmcnt(4)" ::: "memory")
#define RESC(a) do { if (__any((a) < 1.f)) { if (hi == 0) al_l[r32] = (a); asm volatile("s_waitcnt lgkmcnt(0)" ::: "memory"); \
    for (int d = 0; d < 4; ++d) for (int r = 0; r < 16; ++r) o[d][r] *= al_l[crow(r, hi)]; } } while (0)
  f32x16 pA0, pA1, pB0, pB1; float mnA, mnB, alA, alB; bf16x8 pa0, pa1, pa2, pa3; const int NT = seq / KVBLK;
  constexpr int SE = 0, SO = 1, SDEPTH = 2;
  SLOAD(SE, 0); asm volatile("s_waitcnt vmcnt(0)" ::: "memory"); SWRITE(0, SE); __syncthreads();
  qkt(pA0, pA1, K_lds, qr, r32, hi); partialSM(pA0, pA1, m_reg, mnA, alA);
  SLOAD(SO, KVBLK); SLOAD(SE, 2 * KVBLK);
  SWAIT(); SWRITE(1, SO); __syncthreads();
  for (int j = 1; j + 1 < NT; j += 2) {
    SBAR(); qkt(pB0, pB1, (bf16_t*)((char*)K_lds + SHM_K), qr, r32, hi);
    finishSM(pA0, pA1, alA, l_reg, pa0, pa1, pa2, pa3); SBAR();
    SLOAD(SO, (j + SDEPTH) * KVBLK); SBAR();
    pv_d0(o, vb0, pa0, pa1, pa2, pa3); partialSM(pB0, pB1, m_reg, mnB, alB);
    __syncthreads(); SWAIT(); SWRITE(0, SE);
    RESC(alB); __syncthreads();
    SBAR(); qkt(pA0, pA1, K_lds, qr, r32, hi);
    finishSM(pB0, pB1, alB, l_reg, pa0, pa1, pa2, pa3); SBAR();
    if (j + 3 < NT) SLOAD(SE, (j + 1 + SDEPTH) * KVBLK); SBAR();
    pv_d0(o, vb0 + (int)SHM_V, pa0, pa1, pa2, pa3); partialSM(pA0, pA1, m_reg, mnA, alA);
    __syncthreads(); SWAIT(); SWRITE(1, SO);
    RESC(alA); __syncthreads();
  }
  SBAR(); qkt(pB0, pB1, (bf16_t*)((char*)K_lds + SHM_K), qr, r32, hi);
  finishSM(pA0, pA1, alA, l_reg, pa0, pa1, pa2, pa3); SBAR();
  pv_d0(o, vb0, pa0, pa1, pa2, pa3); partialSM(pB0, pB1, m_reg, mnB, alB);
  __syncthreads(); RESC(alB);
  finishSM(pB0, pB1, alB, l_reg, pa0, pa1, pa2, pa3); SBAR();
  pv_d0(o, vb0 + (int)SHM_V, pa0, pa1, pa2, pa3);
  if (hi == 0) li_l[r32] = l_reg; asm volatile("s_waitcnt lgkmcnt(0)" ::: "memory");
  float rli[16];
#pragma unroll
  for (int r = 0; r < 16; ++r) rli[r] = __builtin_amdgcn_rcpf(li_l[crow(r, hi)]);
  bf16_t* Ow = Ob + (long)(wid * QBLK) * LDO;
#pragma unroll
  for (int r = 0; r < 16; ++r) { int orow = crow(r, hi);
    for (int d0 = 0; d0 < 4; ++d0) Ow[(long)orow * LDO + d0 * 32 + r32] = (bf16_t)(cvtpk(o[d0][r] * rli[r], 0.f) & 0xffff); }
}

constexpr size_t WS_MOD = 0;
constexpr size_t WS_H = 16384;
constexpr size_t WS_Q = WS_H + (size_t)S_ * D_ * 2;
constexpr size_t WS_O = WS_Q + (size_t)S_ * QD * 2;
constexpr size_t WS_X2 = WS_O + (size_t)S_ * QD * 2;
constexpr size_t WS_H2 = WS_X2 + (size_t)S_ * D_ * 2;
constexpr size_t WS_Z = WS_H2 + (size_t)S_ * D_ * 2;
constexpr size_t WS_Z2N = WS_Z + (size_t)S_ * F_ * 2;
constexpr size_t WS_U = WS_Z2N + (size_t)S_ * FH * 2;
constexpr size_t WS_END = WS_U + (size_t)S_ * FH * 2;

extern "C" void kernel_launch(void* const* d_in, const int* in_sizes, int n_in, void* d_out, int out_size, void* d_ws, size_t ws_size, hipStream_t stream) {
  const bf16_t* x = (const bf16_t*)d_in[0]; const bf16_t* kc = (const bf16_t*)d_in[1]; const bf16_t* vc = (const bf16_t*)d_in[2];
  const bf16_t* wq = (const bf16_t*)d_in[3]; const bf16_t* wo = (const bf16_t*)d_in[4];
  const bf16_t* w_in = (const bf16_t*)d_in[6]; const bf16_t* w_sp = (const bf16_t*)d_in[7]; const bf16_t* b_sp = (const bf16_t*)d_in[8]; const bf16_t* w_out = (const bf16_t*)d_in[9];
  const bf16_t* c = (const bf16_t*)d_in[10]; const bf16_t* w_mod = (const bf16_t*)d_in[11];
  char* ws = (char*)d_ws;
  float* mod = (float*)(ws + WS_MOD); bf16_t* h = (bf16_t*)(ws + WS_H); bf16_t* q = (bf16_t*)(ws + WS_Q); bf16_t* o = (bf16_t*)(ws + WS_O);
  bf16_t* x2 = (bf16_t*)(ws + WS_X2); bf16_t* h2 = (bf16_t*)(ws + WS_H2); bf16_t* z = (bf16_t*)(ws + WS_Z); bf16_t* z2n = (bf16_t*)(ws + WS_Z2N); bf16_t* u = (bf16_t*)(ws + WS_U);
  (void)hipFuncSetAttribute((const void*)k_attn, hipFuncAttributeMaxDynamicSharedMemorySize, (int)SHM_ATTN);
  k_mod<<<54, 256, 0, stream>>>(c, w_mod, mod);
  k_ln<576, true><<<64, 256, 0, stream>>>(x, mod + 0 * D_, mod + 1 * D_, h, D_, D_);
  { EpiArgs e{q, nullptr, nullptr, nullptr, nullptr, QD, 0, 0.088388347648318440f * 1.4426950408889634f, 0};
    k_gemm<128, 128, EPI_SCALE><<<dim3(QD / 128, S_ / 128), 256, 0, stream>>>(h, wq, e, D_, QD, D_, 0); }
  k_attn<<<dim3(8, 32), 512, SHM_ATTN, stream>>>(q, kc, vc, o);
  { EpiArgs e{x2, x, mod + 2 * D_, nullptr, nullptr, D_, D_, 0.f, 0};
    k_gemm<64, 64, EPI_RESGATE><<<dim3(D_ / 64, S_ / 64), 256, 0, stream>>>(o, wo, e, QD, D_, QD, 0); }
  k_ln<576, true><<<64, 256, 0, stream>>>(x2, mod + 3 * D_, mod + 4 * D_, h2, D_, D_);
  { EpiArgs e{z, nullptr, nullptr, nullptr, nullptr, F_, 0, 0.f, 0};
    k_gemm<128, 128, EPI_GELU><<<dim3(F_ / 128, S_ / 128), 256, 0, stream>>>(h2, w_in, e, D_, F_, D_, 0); }
  k_ln<2304, false><<<64, 256, 0, stream>>>(z + FH, nullptr, nullptr, z2n, F_, FH);
  { EpiArgs e{u, nullptr, nullptr, b_sp, z, FH, F_, 0.f, 0};
    k_gemm<128, 128, EPI_SPATIAL><<<dim3(FH / 128, S_ / 128), 256, 0, stream>>>(w_sp, z2n, e, S_, FH, S_, 0); }
  { EpiArgs e{(bf16_t*)d_out, x2, mod + 5 * D_, nullptr, nullptr, D_, D_, 0.f, 0};
    k_gemm<64, 64, EPI_RESGATE><<<dim3(D_ / 64, S_ / 64), 256, 0, stream>>>(u, w_out, e, FH, D_, FH, 0); }
}
```

```cpp
#include <hip/hip_runtime.h>
#include <hip/hip_bf16.h>

typedef unsigned short bf16_t;
typedef short bf16x8 __attribute__((ext_vector_type(8)));
typedef short s16x4 __attribute__((ext_vector_type(4)));
typedef float f32x4 __attribute__((ext_vector_type(4)));
typedef float f32x16 __attribute__((ext_vector_type(16)));
typedef unsigned u32x4 __attribute__((ext_vector_type(4)));
typedef unsigned u32x2 __attribute__((ext_vector_type(2)));
#define DI __device__ __forceinline__
#define LAS __attribute__((address_space(3)))

#define STAMP(k, i) do {} while (0)
constexpr int S_ = 2048, D_ = 576, HQ = 32, HKV = 16, DH = 128, QD = HQ * DH, F_ = 4608, FH = 2304;

DI float bf2f(bf16_t v) { return __uint_as_float((unsigned)v << 16); }
DI float bflo(unsigned v) { return __uint_as_float(v << 16); }
DI float bfhi(unsigned v) { return __uint_as_float(v & 0xffff0000u); }
DI unsigned cvtpk(float lo, float hi) { unsigned r; asm("v_cvt_pk_bf16_f32 %0, %1, %2" : "=v"(r) : "v"(lo), "v"(hi)); return r; }

template <int MB, int NKB> DI size_t frag_chunk(int s, int k8) {
  const int st = s / (MB * 16), sr = s % (MB * 16), sl = sr & 31, mb = 2 * (sr >> 5) + ((sl >> 2) & 1), i = 4 * (sl >> 3) + (sl & 3);
  return (((size_t)(st * NKB + (k8 >> 2)) * MB + mb) * 64) + (k8 & 3) * 16 + i;
}
template <int MB, int NKB> DI size_t frag_elem(int s, int k) { return frag_chunk<MB, NKB>(s, k >> 3) * 8 + (k & 7); }
DI int rowperm(int beta, int i) { return 8 * (i >> 2) + 4 * beta + (i & 3); }
template <int NB> DI int colperm(int nb, int i) { return ((NB & 1) && nb == NB - 1) ? 16 * nb + i : 32 * (nb >> 1) + rowperm(nb & 1, i); }

__global__ __launch_bounds__(256) void k_mod(const bf16_t* __restrict__ c, const bf16_t* __restrict__ w, float* __restrict__ mod) {
  __shared__ float red[32][65];
  const int t = threadIdx.x, c8 = t & 7, rg = t >> 3, n0 = blockIdx.x * 64 + c8 * 8;
  float acc[8] = {0, 0, 0, 0, 0, 0, 0, 0};
#pragma unroll
  for (int i = 0; i < 18; ++i) {
    const int k = rg + 32 * i;
    const u32x4 v = *(const u32x4*)(w + (size_t)k * 3456 + n0);
    const float cv = bf2f(c[k]); const float s = cv / (1.f + __expf(-cv));
#pragma unroll
    for (int j = 0; j < 4; ++j) { acc[2 * j] += s * bflo(v[j]); acc[2 * j + 1] += s * bfhi(v[j]); }
  }
#pragma unroll
  for (int j = 0; j < 8; ++j) red[rg][c8 * 8 + j] = acc[j];
  __syncthreads();
  if (t < 64) { float s = 0; for (int r = 0; r < 32; ++r) s += red[r][t]; mod[blockIdx.x * 64 + t] = s; }
}

template <int NC, bool MODULATE>
__global__ __launch_bounds__(256) void k_ln(const bf16_t* __restrict__ x, const float* __restrict__ shift, const float* __restrict__ scale, bf16_t* __restrict__ h, int ldx, int ldh) {
  constexpr int NCH = NC / 64;
  const int t = threadIdx.x, l8 = t & 7, row = blockIdx.x * 32 + (t >> 3);
  u32x4 v[NCH]; float s = 0;
#pragma unroll
  for (int i = 0; i < NCH; ++i) { v[i] = *(const u32x4*)(x + (size_t)row * ldx + (i * 8 + l8) * 8);
#pragma unroll
    for (int j = 0; j < 4; ++j) s += bflo(v[i][j]) + bfhi(v[i][j]); }
  s += __shfl_xor(s, 1); s += __shfl_xor(s, 2); s += __shfl_xor(s, 4);
  const float m = s * (1.f / NC); float q = 0;
#pragma unroll
  for (int i = 0; i < NCH; ++i)
#pragma unroll
    for (int j = 0; j < 4; ++j) { const float a = bflo(v[i][j]) - m, b = bfhi(v[i][j]) - m; q += a * a + b * b; }
  q += __shfl_xor(q, 1); q += __shfl_xor(q, 2); q += __shfl_xor(q, 4);
  const float r = 1.f / sqrtf(q * (1.f / NC) + 1e-5f);
#pragma unroll
  for (int i = 0; i < NCH; ++i) { u32x4 o; const int c0 = (i * 8 + l8) * 8;
#pragma unroll
    for (int j = 0; j < 4; ++j) { float a = (bflo(v[i][j]) - m) * r, b = (bfhi(v[i][j]) - m) * r;
      if (MODULATE) { a = a * (1.f + scale[c0 + 2 * j]) + shift[c0 + 2 * j]; b = b * (1.f + scale[c0 + 2 * j + 1]) + shift[c0 + 2 * j + 1]; }
      o[j] = cvtpk(a, b); }
    *(u32x4*)(h + (size_t)row * ldh + c0) = o; }
}

struct EpiArgs { bf16_t* out; const bf16_t* res; const float* gate; const bf16_t* bias; const bf16_t* mul; int ldo; int ldr; float scale; int pad; };
enum { EPI_SCALE = 0, EPI_RESGATE = 1, EPI_GELU = 2, EPI_SPATIAL = 3 };
DI float gelu_t(float x) { const float c1 = -2.f * 0.7978845608028654f * 1.4426950408889634f, c2 = c1 * 0.044715f;
  const float e = __builtin_amdgcn_exp2f(x * (c1 + c2 * x * x)); return x * __builtin_amdgcn_rcpf(1.f + e); }

template <int BM, int BN, int EPI>
__global__ __launch_bounds__(256) void k_gemm(const bf16_t* __restrict__ A, const bf16_t* __restrict__ B, EpiArgs ep, int lda, int ldb, int K, int zero) {
  constexpr int BK = 64, WM = BM / 2, WN = BN / 2, MI = WM / 16, NI = WN / 16;
  constexpr int BSTR = BN * 2 + 32;
  constexpr int ABYTES = BM * 128, BBYTES = 64 * BSTR;
  constexpr int ACH = BM * 8 / 256, BCH = 64 * (BN / 8) / 256;
  __shared__ __attribute__((aligned(16))) char lds[2 * (ABYTES + BBYTES)];
  const int t = threadIdx.x, lane = t & 63, wid = t >> 6, wr = wid >> 1, wc = wid & 1;
  const int m0 = blockIdx.y * BM, n0 = blockIdx.x * BN;
  u32x4 ra[ACH], rb[BCH];
  auto gload = [&](int kt) {
#pragma unroll
    for (int i = 0; i < ACH; ++i) { const int id = t + 256 * i, r = id >> 3, c = id & 7; ra[i] = *(const u32x4*)(A + (size_t)(m0 + r) * lda + kt * BK + c * 8); }
#pragma unroll
    for (int i = 0; i < BCH; ++i) { const int id = t + 256 * i, k = id / (BN / 8), c = id % (BN / 8); rb[i] = *(const u32x4*)(B + (size_t)(kt * BK + k) * ldb + n0 + c * 8); }
  };
  auto swrite = [&](int buf) {
    char* sa = lds + buf * (ABYTES + BBYTES); char* sb = sa + ABYTES;
#pragma unroll
    for (int i = 0; i < ACH; ++i) { const int id = t + 256 * i, r = id >> 3, c = id & 7; *(u32x4*)(sa + r * 128 + ((c ^ ((r >> 1) & 7)) << 4)) = ra[i]; }
#pragma unroll
    for (int i = 0; i < BCH; ++i) { const int id = t + 256 * i, k = id / (BN / 8), c = id % (BN / 8);
      const int rho = (k & ~0xC) | ((k & 4) << 1) | ((k & 8) >> 1); *(u32x4*)(sb + rho * BSTR + c * 16) = rb[i]; }
  };
  f32x4 acc[MI][NI];
#pragma unroll
  for (int i = 0; i < MI; ++i)
#pragma unroll
    for (int j = 0; j < NI; ++j) acc[i][j] = (f32x4){0.f, 0.f, 0.f, 0.f};
  const int nt = K / BK;
  gload(0); swrite(0); __syncthreads();
  const int fr = lane & 15, fq = lane >> 4, q4 = (lane & 15) >> 2, p4 = lane & 3;
  for (int kt = 0; kt < nt; ++kt) {
    if (kt + 1 < nt) gload(kt + 1);
    const char* sa = lds + (kt & 1) * (ABYTES + BBYTES); const char* sb = sa + ABYTES;
#pragma unroll
    for (int ks = 0; ks < 2; ++ks) {
      bf16x8 af[MI], bfr[NI];
#pragma unroll
      for (int i = 0; i < MI; ++i) { const int r = wr * WM + i * 16 + fr, c = ks * 4 + fq; af[i] = *(const bf16x8*)(sa + r * 128 + ((c ^ ((r >> 1) & 7)) << 4)); }
#pragma unroll
      for (int j = 0; j < NI; ++j) {
        const int rho0 = ks * 32 + 16 * (fq >> 1) + 4 * (fq & 1) + q4; const int col = wc * WN + j * 16 + p4 * 4;
        const s16x4 lo = __builtin_amdgcn_ds_read_tr16_b64_v4i16((LAS s16x4*)(sb + rho0 * BSTR + col * 2));
        const s16x4 hi = __builtin_amdgcn_ds_read_tr16_b64_v4i16((LAS s16x4*)(sb + (rho0 + 8) * BSTR + col * 2));
        bfr[j] = __builtin_shufflevector(lo, hi, 0, 1, 2, 3, 4, 5, 6, 7);
      }
#pragma unroll
      for (int i = 0; i < MI; ++i)
#pragma unroll
        for (int j = 0; j < NI; ++j) acc[i][j] = __builtin_amdgcn_mfma_f32_16x16x32_bf16(bfr[j], af[i], acc[i][j], 0, 0, 0);
    }
    if (kt + 1 < nt) swrite((kt + 1) & 1);
    __syncthreads();
  }
#pragma unroll
  for (int i = 0; i < MI; ++i)
#pragma unroll
    for (int j = 0; j < NI; ++j) {
      const int m = m0 + wr * WM + i * 16 + fr, n = n0 + wc * WN + j * 16 + fq * 4;
      f32x4 v = acc[i][j];
      if (EPI == EPI_SCALE) { v = v * ep.scale; }
      else if (EPI == EPI_RESGATE) { const u32x2 r = *(const u32x2*)(ep.res + (size_t)m * ep.ldr + n); const f32x4 g = *(const f32x4*)(ep.gate + n);
        v[0] = bflo(r[0]) + g[0] * v[0]; v[1] = bfhi(r[0]) + g[1] * v[1]; v[2] = bflo(r[1]) + g[2] * v[2]; v[3] = bfhi(r[1]) + g[3] * v[3]; }
      else if (EPI == EPI_GELU) { v[0] = gelu_t(v[0]); v[1] = gelu_t(v[1]); v[2] = gelu_t(v[2]); v[3] = gelu_t(v[3]); }
      else if (EPI == EPI_SPATIAL) { const float b = bf2f(ep.bias[m]); const u32x2 r = *(const u32x2*)(ep.mul + (size_t)m * ep.ldr + n);
        v[0] = bflo(r[0]) * (v[0] + b); v[1] = bfhi(r[0]) * (v[1] + b); v[2] = bflo(r[1]) * (v[2] + b); v[3] = bfhi(r[1]) * (v[3] + b); }
      u32x2 o; o[0] = cvtpk(v[0], v[1]); o[1] = cvtpk(v[2], v[3]);
      *(u32x2*)(ep.out + (size_t)m * ep.ldo + n) = o;
    }
}

constexpr int NW = 8, QBLK = 32, KVBLK = 64;
constexpr float THR = 11.f;
constexpr size_t SHM_V = KVBLK * DH * 2, SHM_K = KVBLK * DH * 2, SHM_ATTN = 2 * SHM_V + 2 * SHM_K + NW * 64 * 4;
#define KSWZ(row, colB) ((row) * 256 + ((colB) ^ (((row) & 7) << 4)))
#define SBAR() __builtin_amdgcn_sched_barrier(0)
DI int crow(int r, int hi) { return (r & 3) + 8 * (r >> 2) + 4 * hi; }
DI void partialSM(f32x16& p0, f32x16& p1, float& m_reg, float& mn, float& alpha) {
  float pmax = p0[0]; for (int r = 1; r < 16; ++r) pmax = fmaxf(pmax, p0[r]); for (int r = 0; r < 16; ++r) pmax = fmaxf(pmax, p1[r]);
  { auto rr = __builtin_amdgcn_permlane32_swap(__float_as_uint(pmax), __float_as_uint(pmax), false, false);
    pmax = fmaxf(__uint_as_float(rr[0]), __uint_as_float(rr[1])); }
  if (__builtin_expect(__all(pmax - m_reg <= THR), 1)) { mn = m_reg; alpha = 1.f; }
  else { mn = fmaxf(m_reg, pmax); alpha = __builtin_amdgcn_exp2f(m_reg - mn); m_reg = mn; }
  for (int r = 0; r < 16; ++r) p0[r] = p0[r] - mn; for (int r = 0; r < 16; ++r) p1[r] = p1[r] - mn;
  for (int r = 0; r < 16; ++r) p0[r] = __builtin_amdgcn_exp2f(p0[r]);
}
DI void finishSM(f32x16& p0, f32x16& p1, float alpha, float& l_reg, bf16x8& pa0, bf16x8& pa1, bf16x8& pa2, bf16x8& pa3) {
  for (int r = 0; r < 16; ++r) p1[r] = __builtin_amdgcn_exp2f(p1[r]);
  float ps = 0; for (int r = 0; r < 16; ++r) ps += p0[r]; for (int r = 0; r < 16; ++r) ps += p1[r];
  { auto rr = __builtin_amdgcn_permlane32_swap(__float_as_uint(ps), __float_as_uint(ps), false, false);
    ps = __uint_as_float(rr[0]) + __uint_as_float(rr[1]); }
  l_reg = l_reg * alpha + ps;
#define PK4(P, BASE, OUT) do { unsigned a0 = cvtpk(P[BASE + 0], P[BASE + 1]), a1 = cvtpk(P[BASE + 2], P[BASE + 3]);   \
    unsigned b0 = cvtpk(P[BASE + 4], P[BASE + 5]), b1 = cvtpk(P[BASE + 6], P[BASE + 7]);                              \
    auto r0 = __builtin_amdgcn_permlane32_swap(a0, b0, false, false); auto r1 = __builtin_amdgcn_permlane32_swap(a1, b1, false, false); \
    u32x4 w = {r0[0], r1[0], r0[1], r1[1]}; OUT = *reinterpret_cast<bf16x8*>(&w); } while (0)
  PK4(p0, 0, pa0); PK4(p0, 8, pa1); PK4(p1, 0, pa2); PK4(p1, 8, pa3);
#undef PK4
}
DI void qkt(f32x16& p0, f32x16& p1, const bf16_t* Ks, const bf16x8* qr, int r32, int hi) {
  p0 = f32x16{}; p1 = f32x16{};
  for (int d0 = 0; d0 < 8; ++d0) { int cb = (d0 * 16 + hi * 8) * 2;
    bf16x8 b0 = *reinterpret_cast<const bf16x8*>((const char*)Ks + KSWZ(r32, cb));
    bf16x8 b1 = *reinterpret_cast<const bf16x8*>((const char*)Ks + KSWZ(32 + r32, cb));
    p0 = __builtin_amdgcn_mfma_f32_32x32x16_bf16(b0, qr[d0], p0, 0, 0, 0);
    p1 = __builtin_amdgcn_mfma_f32_32x32x16_bf16(b1, qr[d0], p1, 0, 0, 0); }
}
DI int v_st(int k, int c) { const int kk = (k & ~0xC) | ((k & 4) << 1) | ((k & 8) >> 1); return ((kk >> 3) * 4 + (c >> 5)) * 512 + ((kk & 7) * 32 + (c & 31)) * 2; }
DI int v_rd_base(int lane) { return ((lane & 3) << 3) | (((lane >> 2) & 3) << 6) | (((lane >> 4) & 1) << 5) | (((lane >> 5) & 1) << 8); }
constexpr int v_rd_off(int d0, int ks, int half) { return d0 * 512 + ks * 4096 + half * 2048; }
template <int OFF> DI s16x4 tr_read(int vb) {
  s16x4 r; asm volatile("ds_read_b64_tr_b16 %0, %1 offset:%2" : "=&v"(r) : "v"(vb), "i"(OFF) : "memory"); return r;
}
template <int D0> DI void pv_one(f32x16& od, int vb, bf16x8 pa0, bf16x8 pa1, bf16x8 pa2, bf16x8 pa3) {
  const s16x4 l0 = tr_read<v_rd_off(D0, 0, 0)>(vb), h0 = tr_read<v_rd_off(D0, 0, 1)>(vb), l1 = tr_read<v_rd_off(D0, 1, 0)>(vb), h1 = tr_read<v_rd_off(D0, 1, 1)>(vb);
  const s16x4 l2 = tr_read<v_rd_off(D0, 2, 0)>(vb), h2 = tr_read<v_rd_off(D0, 2, 1)>(vb), l3 = tr_read<v_rd_off(D0, 3, 0)>(vb), h3 = tr_read<v_rd_off(D0, 3, 1)>(vb);
  asm volatile("s_waitcnt lgkmcnt(0)" ::: "memory"); SBAR();
#define PK(L, H) (bf16x8){L[0], L[1], L[2], L[3], H[0], H[1], H[2], H[3]}
  od = __builtin_amdgcn_mfma_f32_32x32x16_bf16(pa0, PK(l0, h0), od, 0, 0, 0);
  od = __builtin_amdgcn_mfma_f32_32x32x16_bf16(pa1, PK(l1, h1), od, 0, 0, 0);
  od = __builtin_amdgcn_mfma_f32_32x32x16_bf16(pa2, PK(l2, h2), od, 0, 0, 0);
  od = __builtin_amdgcn_mfma_f32_32x32x16_bf16(pa3, PK(l3, h3), od, 0, 0, 0);
#undef PK
}
DI void pv_d0(f32x16* o, int vb, bf16x8 pa0, bf16x8 pa1, bf16x8 pa2, bf16x8 pa3) {
  pv_one<0>(o[0], vb, pa0, pa1, pa2, pa3); pv_one<1>(o[1], vb, pa0, pa1, pa2, pa3); pv_one<2>(o[2], vb, pa0, pa1, pa2, pa3); pv_one<3>(o[3], vb, pa0, pa1, pa2, pa3);
}
__global__ __launch_bounds__(512, 1) void k_attn(const bf16_t* __restrict__ Q, const bf16_t* __restrict__ K, const bf16_t* __restrict__ V, bf16_t* __restrict__ O) {
  extern __shared__ __attribute__((aligned(16))) char lds[];
  constexpr int LDQ = QD, LDK = DH, LDO = QD;
  const int h = blockIdx.y, kvh = h >> 1, i0 = blockIdx.x * 256;
  const bf16_t* Qb = Q + (size_t)i0 * LDQ + h * DH; const bf16_t* Kh = K + (size_t)kvh * S_ * DH; const bf16_t* Vh = V + (size_t)kvh * S_ * DH;
  bf16_t* Ob = O + (size_t)i0 * LDO + h * DH;
  const int seq = S_;
  const int tid = threadIdx.x, wid = tid >> 6, lane = tid & 63, r32 = lane & 31, hi = lane >> 5;
  bf16_t* V_lds = (bf16_t*)lds; bf16_t* K_lds = (bf16_t*)(lds + 2 * SHM_V);
  float* ws = (float*)(lds + 2 * SHM_V + 2 * SHM_K) + wid * 64; float* li_l = ws; float* al_l = ws + 32;
  float m_reg = -1e30f, l_reg = 0; f32x16 o[4] = {}; bf16x8 qr[8];
  const bf16_t* Qw = Qb + (long)(wid * QBLK + r32) * LDQ + hi * 8;
#pragma unroll
  for (int d0 = 0; d0 < 8; ++d0) qr[d0] = *(const bf16x8*)(Qw + d0 * 16);
  const int sr = tid >> 4, sc = (tid & 15) * 8, vst0 = v_st(sr, sc), vst1 = v_st(32 + sr, sc);
  const int vb0 = (int)(uintptr_t)V_lds + v_rd_base(lane);
  struct { bf16x8 vs0, vs1, ks0, ks1; } sr_[2];
#define LD8(p) (*(const bf16x8*)(p))
#define SLOAD(i, k0) do { sr_[i].vs0 = LD8(&Vh[(long)((k0) + sr) * LDK + sc]); sr_[i].vs1 = LD8(&Vh[(long)((k0) + 32 + sr) * LDK + sc]); \
    sr_[i].ks0 = LD8(&Kh[(long)((k0) + sr) * LDK + sc]); sr_[i].ks1 = LD8(&Kh[(long)((k0) + 32 + sr) * LDK + sc]); } while (0)
#define SWRITE(b, i) do { *(bf16x8*)((char*)V_lds + (b) * SHM_V + vst0) = sr_[i].vs0;          \
    *(bf16x8*)((char*)V_lds + (b) * SHM_V + vst1) = sr_[i].vs1; int kc = sc * 2;               \
    *(bf16x8*)((char*)K_lds + (b) * SHM_K + KSWZ(sr, kc)) = sr_[i].ks0;                       \
    *(bf16x8*)((char*)K_lds + (b) * SHM_K + KSWZ(32 + sr, kc)) = sr_[i].ks1; } while (0)
#define SWAIT() asm volatile("s_waitcnt vmcnt(4)" ::: "memory")
#define RESC(a) do { if (__any((a) < 1.f)) { if (hi == 0) al_l[r32] = (a); asm volatile("s_waitcnt lgkmcnt(0)" ::: "memory"); \
    for (int d = 0; d < 4; ++d) for (int r = 0; r < 16; ++r) o[d][r] *= al_l[crow(r, hi)]; } } while (0)
  f32x16 pA0, pA1, pB0, pB1; float mnA, mnB, alA, alB; bf16x8 pa0, pa1, pa2, pa3; const int NT = seq / KVBLK;
  constexpr int SE = 0, SO = 1, SDEPTH = 2;
  SLOAD(SE, 0); asm volatile("s_waitcnt vmcnt(0)" ::: "memory"); SWRITE(0, SE); __syncthreads();
  qkt(pA0, pA1, K_lds, qr, r32, hi); partialSM(pA0, pA1, m_reg, mnA, alA);
  SLOAD(SO, KVBLK); SLOAD(SE, 2 * KVBLK);
  SWAIT(); SWRITE(1, SO); __syncthreads();
  for (int j = 1; j + 1 < NT; j += 2) {
    SBAR(); qkt(pB0, pB1, (bf16_t*)((char*)K_lds + SHM_K), qr, r32, hi);
    finishSM(pA0, pA1, alA, l_reg, pa0, pa1, pa2, pa3); SBAR();
    SLOAD(SO, (j + SDEPTH) * KVBLK); SBAR();
    pv_d0(o, vb0, pa0, pa1, pa2, pa3); partialSM(pB0, pB1, m_reg, mnB, alB);
    __syncthreads(); SWAIT(); SWRITE(0, SE);
    RESC(alB); __syncthreads();
    SBAR(); qkt(pA0, pA1, K_lds, qr, r32, hi);
    finishSM(pB0, pB1, alB, l_reg, pa0, pa1, pa2, pa3); SBAR();
    if (j + 3 < NT) SLOAD(SE, (j + 1 + SDEPTH) * KVBLK); SBAR();
    pv_d0(o, vb0 + (int)SHM_V, pa0, pa1, pa2, pa3); partialSM(pA0, pA1, m_reg, mnA, alA);
    __syncthreads(); SWAIT(); SWRITE(1, SO);
    RESC(alA); __syncthreads();
  }
  SBAR(); qkt(pB0, pB1, (bf16_t*)((char*)K_lds + SHM_K), qr, r32, hi);
  finishSM(pA0, pA1, alA, l_reg, pa0, pa1, pa2, pa3); SBAR();
  pv_d0(o, vb0, pa0, pa1, pa2, pa3); partialSM(pB0, pB1, m_reg, mnB, alB);
  __syncthreads(); RESC(alB);
  finishSM(pB0, pB1, alB, l_reg, pa0, pa1, pa2, pa3); SBAR();
  pv_d0(o, vb0 + (int)SHM_V, pa0, pa1, pa2, pa3);
  if (hi == 0) li_l[r32] = l_reg; asm volatile("s_waitcnt lgkmcnt(0)" ::: "memory");
  float rli[16];
#pragma unroll
  for (int r = 0; r < 16; ++r) rli[r] = __builtin_amdgcn_rcpf(li_l[crow(r, hi)]);
#pragma unroll
  for (int r = 0; r < 16; ++r) { const int s = i0 + wid * QBLK + crow(r, hi);
    for (int d0 = 0; d0 < 4; ++d0) O[frag_elem<4, 128>(s, h * DH + d0 * 32 + r32)] = (bf16_t)(cvtpk(o[d0][r] * rli[r], 0.f) & 0xffff); }
}

DI int dt_nb(int t) { return 4 + (t & 1); }
DI int dt_b0(int t) { return 9 * (t >> 1) + 4 * (t & 1); }
__global__ __launch_bounds__(256) void k_pack(const bf16_t* __restrict__ wo, const bf16_t* __restrict__ w_in, const bf16_t* __restrict__ w_sp, const bf16_t* __restrict__ w_out,
                                              bf16_t* __restrict__ pwo, bf16_t* __restrict__ pwin, bf16_t* __restrict__ pwsp, bf16_t* __restrict__ pwout) {
  constexpr int N_WO = 36 * 128 * 64, N_WIN = 288 * 18 * 64, N_WSP = 128 * 64 * 64, N_WOUT = 36 * 72 * 64;
  int id = blockIdx.x * 256 + threadIdx.x;
  const int lane = id & 63, i = lane & 15, c = lane >> 4;
  unsigned short v[8];
  if (id < N_WO || (id >= N_WO + N_WIN + N_WSP && id < N_WO + N_WIN + N_WSP + N_WOUT)) {
    const bool isO = id < N_WO; const int NKB = isO ? 128 : 72; const int f = (isO ? id : id - (N_WO + N_WIN + N_WSP)) >> 6;
    int t = 0; for (int tt = 1; tt < 8; ++tt) if (f >= dt_b0(tt) * NKB) t = tt;
    const int r = f - dt_b0(t) * NKB, NB = dt_nb(t), kb = r / NB, nb = r % NB;
    const int col = dt_b0(t) * 16 + (NB == 5 ? colperm<5>(nb, i) : colperm<4>(nb, i));
    const bf16_t* w = isO ? wo : w_out;
    for (int e = 0; e < 8; ++e) v[e] = w[(size_t)(kb * 32 + c * 8 + e) * D_ + col];
    bf16_t* dst = (isO ? pwo : pwout) + ((size_t)f * 64 + lane) * 8;
    for (int e = 0; e < 8; ++e) dst[e] = v[e];
  } else if (id < N_WO + N_WIN) {
    const int f = (id - N_WO) >> 6;
    const int ct = f / (18 * 9), kb = (f / 9) % 18, nb = f % 9;
    const int col = ct * 144 + colperm<9>(nb, i);
    for (int e = 0; e < 8; ++e) v[e] = w_in[(size_t)(kb * 32 + c * 8 + e) * F_ + col];
    bf16_t* dst = pwin + ((size_t)f * 64 + lane) * 8;
    for (int e = 0; e < 8; ++e) dst[e] = v[e];
  } else if (id < N_WO + N_WIN + N_WSP) {
    const int f = (id - N_WO - N_WIN) >> 6;
    const int tt = f >> 9, kb = (f >> 3) & 63, mb = f & 7;
    const int row = tt * 128 + (mb >> 1) * 32 + rowperm(mb & 1, i);
    *(u32x4*)(pwsp + ((size_t)f * 64 + lane) * 8) = *(const u32x4*)(w_sp + (size_t)row * S_ + kb * 32 + c * 8);
  }
}

template <int MB, int NB, int KBW, int DEPTH>
DI void stream_mma(f32x4 (&acc)[MB][NB], const bf16_t* __restrict__ xp, const bf16_t* __restrict__ yp) {
  bf16x8 xa[DEPTH][NB], yb[DEPTH][MB];
#pragma unroll
  for (int m = 0; m < MB; ++m)
#pragma unroll
    for (int n = 0; n < NB; ++n) acc[m][n] = (f32x4){0.f, 0.f, 0.f, 0.f};
#pragma unroll
  for (int s = 0; s < KBW + DEPTH - 1; ++s) {
    if (s < KBW) {
#pragma unroll
      for (int m = 0; m < MB; ++m) yb[s % DEPTH][m] = *(const bf16x8*)(yp + (size_t)(s * MB + m) * 512);
#pragma unroll
      for (int n = 0; n < NB; ++n) xa[s % DEPTH][n] = *(const bf16x8*)(xp + (size_t)(s * NB + n) * 512);
    }
    SBAR();
    if (s >= DEPTH - 1) {
      const int u = (s - (DEPTH - 1)) % DEPTH;
#pragma unroll
      for (int m = 0; m < MB; ++m)
#pragma unroll
        for (int n = 0; n < NB; ++n) acc[m][n] = __builtin_amdgcn_mfma_f32_16x16x32_bf16(xa[u][n], yb[u][m], acc[m][n], 0, 0, 0);
    }
    SBAR();
  }
}

template <int NB, int NKB, int DEPTH, bool FINAL>
DI void g576_body(const bf16_t* __restrict__ Y, const bf16_t* __restrict__ W, const bf16_t* __restrict__ res, const float* __restrict__ gate, bf16_t* __restrict__ out, float2* __restrict__ st,
                  int mt, int t, char* lds) {
  constexpr int KBW = NKB / 4;
  const int lane = threadIdx.x & 63, wid = __builtin_amdgcn_readfirstlane(threadIdx.x >> 6);
  f32x4 acc[4][NB];
  STAMP(FINAL ? 5 : 2, 0);
  stream_mma<4, NB, KBW, DEPTH>(acc, W + ((size_t)dt_b0(t) * NKB + (size_t)wid * KBW * NB) * 512 + lane * 8, Y + ((size_t)(mt * NKB + wid * KBW) * 4) * 512 + lane * 8);
  STAMP(FINAL ? 5 : 2, 1);
  f32x4* red = (f32x4*)lds;
#pragma unroll
  for (int m = 0; m < 4; ++m)
    if (m != wid) {
#pragma unroll
      for (int n = 0; n < NB; ++n) red[((wid * 4 + m) * NB + n) * 64 + lane] = acc[m][n];
    }
  __syncthreads();
  f32x4 v[NB];
#pragma unroll
  for (int n = 0; n < NB; ++n) { v[n] = wid == 0 ? acc[0][n] : wid == 1 ? acc[1][n] : wid == 2 ? acc[2][n] : acc[3][n]; }
#pragma unroll
  for (int w = 0; w < 4; ++w)
    if (w != wid) {
#pragma unroll
      for (int n = 0; n < NB; ++n) v[n] += red[((w * 4 + wid) * NB + n) * 64 + lane];
    }
  const int j = lane & 15, g = lane >> 4;
  const int s = mt * 64 + (wid >> 1) * 32 + rowperm(wid & 1, j), d0 = dt_b0(t) * 16;
  float o[NB * 4];
#pragma unroll
  for (int p = 0; p < NB / 2; ++p) {
    const int d = d0 + 32 * p + 8 * g;
    const u32x4 r = FINAL ? *(const u32x4*)(res + frag_chunk<2, 18>(s, d >> 3) * 8) : *(const u32x4*)(res + (size_t)s * D_ + d);
    const f32x4 g0 = *(const f32x4*)(gate + d), g1 = *(const f32x4*)(gate + d + 4);
    float* q = o + 8 * p;
    q[0] = bflo(r[0]) + g0[0] * v[2 * p][0]; q[1] = bfhi(r[0]) + g0[1] * v[2 * p][1]; q[2] = bflo(r[1]) + g0[2] * v[2 * p][2]; q[3] = bfhi(r[1]) + g0[3] * v[2 * p][3];
    q[4] = bflo(r[2]) + g1[0] * v[2 * p + 1][0]; q[5] = bfhi(r[2]) + g1[1] * v[2 * p + 1][1]; q[6] = bflo(r[3]) + g1[2] * v[2 * p + 1][2]; q[7] = bfhi(r[3]) + g1[3] * v[2 * p + 1][3];
    u32x4 w; w[0] = cvtpk(q[0], q[1]); w[1] = cvtpk(q[2], q[3]); w[2] = cvtpk(q[4], q[5]); w[3] = cvtpk(q[6], q[7]);
    if (FINAL) *(u32x4*)(out + (size_t)s * D_ + d) = w;
    else { *(u32x4*)(out + frag_chunk<2, 18>(s, d >> 3) * 8) = w;
#pragma unroll
      for (int e = 0; e < 4; ++e) { q[2 * e] = bflo(w[e]); q[2 * e + 1] = bfhi(w[e]); } }
  }
  if (NB & 1) {
    const int d = d0 + 16 * (NB - 1) + 4 * g;
    const u32x2 r = FINAL ? *(const u32x2*)(res + frag_elem<2, 18>(s, d)) : *(const u32x2*)(res + (size_t)s * D_ + d);
    const f32x4 g0 = *(const f32x4*)(gate + d);
    float* q = o + 4 * (NB - 1);
    q[0] = bflo(r[0]) + g0[0] * v[NB - 1][0]; q[1] = bfhi(r[0]) + g0[1] * v[NB - 1][1]; q[2] = bflo(r[1]) + g0[2] * v[NB - 1][2]; q[3] = bfhi(r[1]) + g0[3] * v[NB - 1][3];
    u32x2 w; w[0] = cvtpk(q[0], q[1]); w[1] = cvtpk(q[2], q[3]);
    if (FINAL) *(u32x2*)(out + (size_t)s * D_ + d) = w;
    else { *(u32x2*)(out + frag_elem<2, 18>(s, d)) = w; q[0] = bflo(w[0]); q[1] = bfhi(w[0]); q[2] = bflo(w[1]); q[3] = bfhi(w[1]); }
  }
  if (!FINAL) {
    float sum = 0;
#pragma unroll
    for (int e = 0; e < NB * 4; ++e) sum += o[e];
    sum += __shfl_xor(sum, 16); sum += __shfl_xor(sum, 32);
    const float mean = sum * (1.f / (NB * 16)); float m2 = 0;
#pragma unroll
    for (int e = 0; e < NB * 4; ++e) { const float dd = o[e] - mean; m2 += dd * dd; }
    m2 += __shfl_xor(m2, 16); m2 += __shfl_xor(m2, 32);
    if (g == 0) st[s * 8 + t] = make_float2(mean, m2);
  }
  STAMP(FINAL ? 5 : 2, 2);
  asm volatile("s_waitcnt vmcnt(0)" ::: "memory");
  STAMP(FINAL ? 5 : 2, 3);
}
template <int NKB, int DEPTH, bool FINAL>
__global__ __launch_bounds__(256, 1) void k_g576(const bf16_t* __restrict__ Y, const bf16_t* __restrict__ W, const bf16_t* __restrict__ res, const float* __restrict__ gate, bf16_t* __restrict__ out, float2* __restrict__ st) {
  extern __shared__ __attribute__((aligned(16))) char lds[];
  const int xcd = blockIdx.x & 7, j = blockIdx.x >> 3;
  const int mt = xcd * 4 + (j & 3), t = j >> 2;
  if (t & 1) g576_body<5, NKB, DEPTH, FINAL>(Y, W, res, gate, out, st, mt, t, lds);
  else g576_body<4, NKB, DEPTH, FINAL>(Y, W, res, gate, out, st, mt, t, lds);
}

DI void glds16(const void* g, unsigned lds_base) {
  unsigned sv; asm volatile("s_mov_b32 %0, m0\n\ts_mov_b32 m0, %2\n\ts_nop 0\n\tglobal_load_lds_dwordx4 %1, off\n\ts_mov_b32 m0, %0" : "=&s"(sv) : "v"(g), "s"(lds_base) : "memory"); }
#define RFL(x) __builtin_amdgcn_readfirstlane(x)
typedef __amdgpu_buffer_rsrc_t srd_t;
DI srd_t make_srd(const void* p, unsigned bytes) { return __builtin_amdgcn_make_buffer_rsrc((void*)p, (short)0, (int)bytes, 0x00020000); }
DI void dma16(srd_t srd, unsigned voff, unsigned soff, unsigned lds_dst) {
  asm volatile("s_mov_b32 m0, %0\n\ts_nop 3\n\tbuffer_load_dwordx4 %1, %2, %3 offen lds" :: "s"(RFL(lds_dst)), "v"(voff), "s"(srd), "s"(RFL(soff)) : "memory"); }
#define WAITBAR(n3, n2) do { if (wid < 2) asm volatile("s_waitcnt vmcnt(" #n3 ") lgkmcnt(0)\n\ts_barrier" ::: "memory"); else asm volatile("s_waitcnt vmcnt(" #n2 ") lgkmcnt(0)\n\ts_barrier" ::: "memory"); } while (0)
#define MFMA16(a, b, c) __builtin_amdgcn_mfma_f32_16x16x32_bf16(a, b, c, 0, 0, 0)

__global__ __launch_bounds__(512, 2) void k_g4(const bf16_t* __restrict__ Z2, const bf16_t* __restrict__ Wsp, const bf16_t* __restrict__ Z1, const bf16_t* __restrict__ bsp, bf16_t* __restrict__ U) {
  extern __shared__ __attribute__((aligned(16))) char lds[];
  constexpr int STB = 34 * 1024, NSTEP = 32;
  STAMP(4, 0);
  const int xcd = blockIdx.x & 7, jj = blockIdx.x >> 3;
  const int tt = (xcd >> 1) * 4 + (jj & 3), ct = (xcd & 1) * 8 + (jj >> 2);
  const int lane = threadIdx.x & 63, wid = RFL(threadIdx.x >> 6), wr = wid & 3, kh = wid >> 2;
  const unsigned ldsb = RFL((unsigned)(size_t)lds);
  const int p0 = wid < 2 ? 3 * wid : 2 + 2 * wid;
  const srd_t sx = make_srd(Z2, S_ * FH * 2), sy = make_srd(Wsp, S_ * S_ * 2);
  const unsigned xsrc = (ct * 64 * 9 + p0) * 1024, ysrc = (tt * 64 * 8 + kh * 8 + wr * 2) * 1024;
  const unsigned l16 = lane * 16;
  const unsigned xdst = ldsb + p0 * 1024, ydst = ldsb + 18 * 1024 + wid * 2048;
  auto piece = [&](int st, int slot, int i) {
    if (i < 2) dma16(sy, l16, ysrc + st * 16384 + i * 1024, ydst + slot * STB + i * 1024);
    else if (i < 4 || wid < 2) dma16(sx, l16, xsrc + st * 18432 + (i - 2) * 1024, xdst + slot * STB + (i - 2) * 1024);
  };
#pragma unroll
  for (int st = 0; st < 4; ++st)
#pragma unroll
    for (int i = 0; i < 5; ++i) piece(st, st, i);
  f32x4 acc[2][9];
#pragma unroll
  for (int b = 0; b < 2; ++b)
#pragma unroll
    for (int n = 0; n < 9; ++n) acc[b][n] = (f32x4){0.f, 0.f, 0.f, 0.f};
  bf16x8 fr[2][11];
  const LAS char* xrd = (const LAS char*)lds + kh * 9216 + lane * 16;
  const LAS char* yrd = (const LAS char*)lds + 18 * 1024 + wid * 2048 + lane * 16;
  auto rd = [&](int buf, int slot, int i) { fr[buf][i] = i < 2 ? *(const LAS bf16x8*)(yrd + slot * STB + i * 1024) : *(const LAS bf16x8*)(xrd + slot * STB + (i - 2) * 1024); };
  WAITBAR(15, 12);
  STAMP(4, 1);
#pragma unroll
  for (int i = 0; i < 11; ++i) rd(0, 0, i);
  auto step = [&](int st, int u, bool do_rd, bool do_dma) {
#pragma unroll
    for (int i = 0; i < 18; ++i) {
      acc[i & 1][i >> 1] = MFMA16(fr[u & 1][2 + (i >> 1)], fr[u & 1][i & 1], acc[i & 1][i >> 1]);
      if (do_rd && i < 11) rd((u + 1) & 1, (u + 1) & 3, i);
      if (do_dma && i >= 11 && i < 16) piece(st + 4, u & 3, i - 11);
      SBAR();
    }
  };
  for (int s0 = 0; s0 < NSTEP - 4; s0 += 4) {
#pragma unroll
    for (int u = 0; u < 4; ++u) { WAITBAR(10, 8); SBAR(); step(s0 + u, u, true, true); }
  }
  WAITBAR(10, 8); SBAR(); step(28, 0, true, false);
  WAITBAR(5, 4); SBAR(); step(29, 1, true, false);
  WAITBAR(0, 0); SBAR(); step(30, 2, true, false);
  SBAR(); step(31, 3, false, false);
  STAMP(4, 2);
  asm volatile("s_waitcnt lgkmcnt(0)\n\ts_barrier" ::: "memory");
  f32x4* red = (f32x4*)lds;
  f32x4 v[9];
#pragma unroll
  for (int n = 0; n < 9; ++n) { red[(wid * 9 + n) * 64 + lane] = kh ? acc[0][n] : acc[1][n]; v[n] = kh ? acc[1][n] : acc[0][n]; }
  const int j = lane & 15, g = lane >> 4, tg = tt * 4 + wr;
  const int t = tg * 32 + rowperm(kh, j); const float bias = bf2f(bsp[t]);
  const char* z1p = (const char*)Z1 + (size_t)((ct * 64 + tg) * 2 + kh) * 4608;
  u32x4 zr[4];
#pragma unroll
  for (int p = 0; p < 4; ++p) zr[p] = *(const u32x4*)(z1p + p * 1024 + lane * 16);
  const u32x2 zr8 = *(const u32x2*)(z1p + 4096 + lane * 8);
  __syncthreads();
#pragma unroll
  for (int n = 0; n < 9; ++n) v[n] += red[((wid ^ 4) * 9 + n) * 64 + lane] + bias;
#pragma unroll
  for (int p = 0; p < 4; ++p) {
    const u32x4 r = zr[p]; const f32x4 a = v[2 * p], c = v[2 * p + 1];
    u32x4 w; w[0] = cvtpk(bflo(r[0]) * a[0], bfhi(r[0]) * a[1]); w[1] = cvtpk(bflo(r[1]) * a[2], bfhi(r[1]) * a[3]);
    w[2] = cvtpk(bflo(r[2]) * c[0], bfhi(r[2]) * c[1]); w[3] = cvtpk(bflo(r[3]) * c[2], bfhi(r[3]) * c[3]);
    *(u32x4*)(U + frag_chunk<4, 72>(t, ct * 18 + 4 * p + g) * 8) = w;
  }
  { const f32x4 a = v[8];
    u32x2 w; w[0] = cvtpk(bflo(zr8[0]) * a[0], bfhi(zr8[0]) * a[1]); w[1] = cvtpk(bflo(zr8[1]) * a[2], bfhi(zr8[1]) * a[3]);
    *(u32x2*)(U + frag_elem<4, 72>(t, ct * 144 + 128 + 4 * g)) = w; }
  STAMP(4, 3);
  asm volatile("s_waitcnt vmcnt(0)" ::: "memory");
  STAMP(4, 4);
}

DI float row16_sum(float v) {
  v += __shfl_xor(v, 1); v += __shfl_xor(v, 2); v += __shfl_xor(v, 4); v += __shfl_xor(v, 8); return v; }
constexpr int G3_RING = 4 * 18 * 1024, G3_RED = G3_RING, G3_MOD = G3_RED + 80 * 1024, G3_LDS = G3_MOD + 4608;
__global__ __launch_bounds__(512, 2) void k_g3(const bf16_t* __restrict__ X2, const bf16_t* __restrict__ Win, const float2* __restrict__ st2, const float* __restrict__ shift, const float* __restrict__ scale,
                                               bf16_t* __restrict__ Z1, bf16_t* __restrict__ Z2, float2* __restrict__ st3) {
  extern __shared__ __attribute__((aligned(16))) char lds[];
  constexpr int STB = 18 * 1024;
  STAMP(3, 0);
  const int xcd = blockIdx.x & 7, jj = blockIdx.x >> 3;
  const int mt = (xcd >> 1) * 4 + (jj & 3), ct = (xcd & 1) * 8 + (jj >> 2);
  const int lane = threadIdx.x & 63, wid = RFL(threadIdx.x >> 6), wr = wid & 3, kh = wid >> 2;
  const int j = lane & 15, g = lane >> 4, sg = mt * 4 + wr;
  const unsigned ldsb = RFL((unsigned)(size_t)lds);
  f32x4 mv = (f32x4){0.f, 0.f, 0.f, 0.f};
  if (threadIdx.x < 288) mv = threadIdx.x < 144 ? *(const f32x4*)(scale + threadIdx.x * 4) + 1.f : *(const f32x4*)(shift + (threadIdx.x - 144) * 4);
  f32x4 stp[2][4];
#pragma unroll
  for (int b = 0; b < 2; ++b)
#pragma unroll
    for (int t = 0; t < 4; ++t) stp[b][t] = *(const f32x4*)(st2 + (sg * 32 + rowperm(b, j)) * 8 + 2 * t);
  u32x4 hf[9][2];
#pragma unroll
  for (int k = 0; k < 9; ++k)
#pragma unroll
    for (int b = 0; b < 2; ++b) hf[k][b] = *(const u32x4*)(X2 + ((size_t)((sg * 18 + 2 * k + kh) * 2 + b) * 64 + lane) * 8);
  SBAR();
  const int p0 = wid < 2 ? 3 * wid : 2 + 2 * wid;
  const srd_t sx = make_srd(Win, D_ * F_ * 2); const unsigned xsrc = p0 * 1024, l16 = lane * 16;
  const unsigned xdst = ldsb + p0 * 1024;
  auto piece = [&](int st, int i) {
    const int strip = st < 9 ? ct + 16 : ct, sk = st < 9 ? st : st - 9;
    if (i < 2 || wid < 2) dma16(sx, l16, xsrc + (strip * 162 + sk * 18 + i) * 1024, xdst + (st & 3) * STB + i * 1024);
  };
#pragma unroll
  for (int st = 0; st < 4; ++st)
#pragma unroll
    for (int i = 0; i < 3; ++i) piece(st, i);
  SBAR();
  if (threadIdx.x < 288) *(f32x4*)(lds + G3_MOD + (threadIdx.x < 144 ? threadIdx.x * 32 : (threadIdx.x - 144) * 32 + 16)) = mv;
  float rr[2], mr[2];
#pragma unroll
  for (int b = 0; b < 2; ++b) {
    float mean = 0;
#pragma unroll
    for (int t = 0; t < 4; ++t) mean += stp[b][t][0] * 64.f + stp[b][t][2] * 80.f;
    mean *= (1.f / D_); float m2 = 0;
#pragma unroll
    for (int t = 0; t < 4; ++t) { const float d0 = stp[b][t][0] - mean, d1 = stp[b][t][2] - mean; m2 += stp[b][t][1] + 64.f * d0 * d0 + stp[b][t][3] + 80.f * d1 * d1; }
    rr[b] = 1.f / sqrtf(m2 * (1.f / D_) + 1e-5f); mr[b] = -mean * rr[b];
  }
  asm volatile("s_waitcnt lgkmcnt(0)\n\ts_barrier" ::: "memory");
  STAMP(3, 1);
  auto normalize = [&](int k) {
    const LAS char* mp = (const LAS char*)lds + G3_MOD + ((2 * k + kh) * 8 + g * 2) * 32;
    const f32x4 a0 = *(const LAS f32x4*)(mp), s0 = *(const LAS f32x4*)(mp + 16), a1 = *(const LAS f32x4*)(mp + 32), s1 = *(const LAS f32x4*)(mp + 48);
#pragma unroll
    for (int b = 0; b < 2; ++b) {
      const u32x4 r = hf[k][b]; u32x4 w;
      w[0] = cvtpk((bflo(r[0]) * rr[b] + mr[b]) * a0[0] + s0[0], (bfhi(r[0]) * rr[b] + mr[b]) * a0[1] + s0[1]);
      w[1] = cvtpk((bflo(r[1]) * rr[b] + mr[b]) * a0[2] + s0[2], (bfhi(r[1]) * rr[b] + mr[b]) * a0[3] + s0[3]);
      w[2] = cvtpk((bflo(r[2]) * rr[b] + mr[b]) * a1[0] + s1[0], (bfhi(r[2]) * rr[b] + mr[b]) * a1[1] + s1[1]);
      w[3] = cvtpk((bflo(r[3]) * rr[b] + mr[b]) * a1[2] + s1[2], (bfhi(r[3]) * rr[b] + mr[b]) * a1[3] + s1[3]);
      hf[k][b] = w;
    }
  };
#pragma unroll
  for (int k = 0; k < 9; ++k) normalize(k);
  SBAR();
  bf16x8 wf[2][9];
  const LAS char* xrd = (const LAS char*)lds + kh * 9216 + lane * 16;
  STAMP(3, 2);
  WAITBAR(9, 6);
#pragma unroll
  for (int n = 0; n < 9; ++n) wf[0][n] = *(const LAS bf16x8*)(xrd + n * 1024);
  f32x4 acc[2][9];
  f32x4* red = (f32x4*)(lds + G3_RED);
#pragma unroll
  for (int half = 0; half < 2; ++half) {
#pragma unroll
    for (int b = 0; b < 2; ++b)
#pragma unroll
      for (int n = 0; n < 9; ++n) acc[b][n] = (f32x4){0.f, 0.f, 0.f, 0.f};
#pragma unroll
    for (int sk = 0; sk < 9; ++sk) {
      const int st = half * 9 + sk;
      if (st <= 14) WAITBAR(6, 4); else if (st == 15) WAITBAR(3, 2); else if (st == 16) WAITBAR(0, 0);
      SBAR();
#pragma unroll
      for (int i = 0; i < 18; ++i) {
        const int b = i & 1, n = i >> 1; const bf16x8 hh = __builtin_bit_cast(bf16x8, hf[sk][b]);
        acc[b][n] = half == 0 ? MFMA16(hh, wf[st & 1][n], acc[b][n]) : MFMA16(wf[st & 1][n], hh, acc[b][n]);
        if (st < 17 && i < 9) wf[(st + 1) & 1][i] = *(const LAS bf16x8*)(xrd + ((st + 1) & 3) * STB + i * 1024);
        if (st + 4 < 18 && i >= 10 && i < 13) piece(st + 4, i - 10);
        SBAR();
      }
    }
    STAMP(3, 3 + 2 * half);
    if (half == 0) {
#pragma unroll
      for (int q = 0; q < 5; ++q)
#pragma unroll
        for (int b = 0; b < 2; ++b) { if (kh) red[(wid * 10 + q * 2 + b) * 64 + lane] = acc[b][q]; else if (q < 4) red[(wid * 10 + q * 2 + b) * 64 + lane] = acc[b][5 + q]; }
      asm volatile("s_waitcnt lgkmcnt(0)\n\ts_barrier" ::: "memory");
      float sum[8] = {0, 0, 0, 0, 0, 0, 0, 0}; u32x4 zw[5];
#pragma unroll
      for (int q = 0; q < 5; ++q) {
        if (q < 4 || !kh) {
          u32x4 w;
#pragma unroll
          for (int b = 0; b < 2; ++b) { const f32x4 a = (kh ? acc[b][(5 + q) % 9] : acc[b][q]) + red[((wid ^ 4) * 10 + q * 2 + b) * 64 + lane];
            w[2 * b] = cvtpk(gelu_t(a[0]), gelu_t(a[1])); w[2 * b + 1] = cvtpk(gelu_t(a[2]), gelu_t(a[3])); }
          *(u32x4*)(Z2 + ((size_t)((ct * 64 + sg) * 9 + kh * 5 + q) * 64 + lane) * 8) = w;
#pragma unroll
          for (int e = 0; e < 4; ++e) { sum[2 * e] += bflo(w[e]); sum[2 * e + 1] += bfhi(w[e]); }
          zw[q] = w;
        } else zw[q] = (u32x4){0u, 0u, 0u, 0u};
      }
#pragma unroll
      for (int e = 0; e < 8; ++e) {
        const float mean = row16_sum(sum[e]) * (kh ? 1.f / 64.f : 1.f / 80.f); float m2 = 0;
#pragma unroll
        for (int q = 0; q < 5; ++q) { const float dd = ((e & 1) ? bfhi(zw[q][e >> 1]) : bflo(zw[q][e >> 1])) - mean; if (q < 4 || !kh) m2 += dd * dd; }
        m2 = row16_sum(m2);
        if (j == 0) st3[(sg * 32 + 8 * g + e) * 32 + ct * 2 + kh] = make_float2(mean, m2);
      }
    } else {
#pragma unroll
      for (int n = 0; n < 9; ++n) red[(wid * 10 + n) * 64 + lane] = kh ? acc[0][n] : acc[1][n];
      asm volatile("s_waitcnt lgkmcnt(0)\n\ts_barrier" ::: "memory");
      f32x4 v[9];
#pragma unroll
      for (int n = 0; n < 9; ++n) v[n] = (kh ? acc[1][n] : acc[0][n]) + red[((wid ^ 4) * 10 + n) * 64 + lane];
      char* z1p = (char*)Z1 + (size_t)((ct * 64 + sg) * 2 + kh) * 4608;
#pragma unroll
      for (int p = 0; p < 4; ++p) {
        u32x4 w; w[0] = cvtpk(gelu_t(v[2 * p][0]), gelu_t(v[2 * p][1])); w[1] = cvtpk(gelu_t(v[2 * p][2]), gelu_t(v[2 * p][3]));
        w[2] = cvtpk(gelu_t(v[2 * p + 1][0]), gelu_t(v[2 * p + 1][1])); w[3] = cvtpk(gelu_t(v[2 * p + 1][2]), gelu_t(v[2 * p + 1][3]));
        *(u32x4*)(z1p + p * 1024 + lane * 16) = w;
      }
      u32x2 w; w[0] = cvtpk(gelu_t(v[8][0]), gelu_t(v[8][1])); w[1] = cvtpk(gelu_t(v[8][2]), gelu_t(v[8][3]));
      *(u32x2*)(z1p + 4096 + lane * 8) = w;
    }
    STAMP(3, 4 + 2 * half);
  }
  asm volatile("s_waitcnt vmcnt(0)" ::: "memory");
  STAMP(3, 7);
}

__global__ __launch_bounds__(256) void k_normz2(bf16_t* __restrict__ Z2, const float2* __restrict__ st3) {
  __shared__ float2 rm[32];
  const int kb = blockIdx.x >> 2, cq = blockIdx.x & 3, t = threadIdx.x;
  if (t < 32) { const int s = kb * 32 + t; float mean = 0; float2 p[32];
    for (int i = 0; i < 32; ++i) { p[i] = st3[s * 32 + i]; mean += p[i].x * ((i & 1) ? 64.f : 80.f); }
    mean *= (1.f / FH); float m2 = 0; for (int i = 0; i < 32; ++i) { const float dd = p[i].x - mean; m2 += p[i].y + ((i & 1) ? 64.f : 80.f) * dd * dd; }
    const float r = 1.f / sqrtf(m2 * (1.f / FH) + 1e-5f); rm[t] = make_float2(r, -mean * r); }
  __syncthreads();
  const int lane = t & 63, w = t >> 6, g = lane >> 4, ct = cq * 4 + w;
#pragma unroll
  for (int n = 0; n < 9; ++n) {
    u32x4* p = (u32x4*)(Z2 + ((size_t)((ct * 64 + kb) * 9 + n) * 64 + lane) * 8); u32x4 v = *p, o;
#pragma unroll
    for (int e = 0; e < 4; ++e) { const float2 a = rm[8 * g + 2 * e], b = rm[8 * g + 2 * e + 1]; o[e] = cvtpk(bflo(v[e]) * a.x + a.y, bfhi(v[e]) * b.x + b.y); }
    *p = o;
  }
}

constexpr size_t WS_MOD = 0;
constexpr size_t WS_ST2 = 16384;
constexpr size_t WS_ST3 = WS_ST2 + 2048 * 8 * 8;
constexpr size_t WS_H = WS_ST3 + 2048 * 32 * 8;
constexpr size_t WS_Q = WS_H + (size_t)S_ * D_ * 2;
constexpr size_t WS_O = WS_Q + (size_t)S_ * QD * 2;
constexpr size_t WS_X2 = WS_O + (size_t)S_ * QD * 2;
constexpr size_t WS_Z1 = WS_X2 + (size_t)S_ * D_ * 2;
constexpr size_t WS_Z2 = WS_Z1 + (size_t)S_ * FH * 2;
constexpr size_t WS_U = WS_Z2 + (size_t)S_ * FH * 2;
constexpr size_t WS_PWO = WS_U + (size_t)S_ * FH * 2;
constexpr size_t WS_PWIN = WS_PWO + (size_t)QD * D_ * 2;
constexpr size_t WS_PWSP = WS_PWIN + (size_t)D_ * F_ * 2;
constexpr size_t WS_PWOUT = WS_PWSP + (size_t)S_ * S_ * 2;
constexpr size_t WS_END = WS_PWOUT + (size_t)FH * D_ * 2;

#define G2_DEPTH 6
#define G5_DEPTH 6
#define G4_DEPTH 2
extern "C" void kernel_launch(void* const* d_in, const int* in_sizes, int n_in, void* d_out, int out_size, void* d_ws, size_t ws_size, hipStream_t stream) {
  const bf16_t* x = (const bf16_t*)d_in[0]; const bf16_t* kc = (const bf16_t*)d_in[1]; const bf16_t* vc = (const bf16_t*)d_in[2];
  const bf16_t* wq = (const bf16_t*)d_in[3]; const bf16_t* wo = (const bf16_t*)d_in[4];
  const bf16_t* w_in = (const bf16_t*)d_in[6]; const bf16_t* w_sp = (const bf16_t*)d_in[7]; const bf16_t* b_sp = (const bf16_t*)d_in[8]; const bf16_t* w_out = (const bf16_t*)d_in[9];
  const bf16_t* c = (const bf16_t*)d_in[10]; const bf16_t* w_mod = (const bf16_t*)d_in[11];
  char* ws = (char*)d_ws;
  float* mod = (float*)(ws + WS_MOD); bf16_t* h = (bf16_t*)(ws + WS_H); bf16_t* q = (bf16_t*)(ws + WS_Q); bf16_t* o = (bf16_t*)(ws + WS_O);
  bf16_t* x2 = (bf16_t*)(ws + WS_X2); bf16_t* z1 = (bf16_t*)(ws + WS_Z1); bf16_t* z2 = (bf16_t*)(ws + WS_Z2); bf16_t* u = (bf16_t*)(ws + WS_U);
  float2* st2 = (float2*)(ws + WS_ST2); float2* st3 = (float2*)(ws + WS_ST3);
  bf16_t* pwo = (bf16_t*)(ws + WS_PWO); bf16_t* pwin = (bf16_t*)(ws + WS_PWIN); bf16_t* pwsp = (bf16_t*)(ws + WS_PWSP); bf16_t* pwout = (bf16_t*)(ws + WS_PWOUT);
  (void)hipFuncSetAttribute((const void*)k_attn, hipFuncAttributeMaxDynamicSharedMemorySize, (int)SHM_ATTN);
  (void)hipFuncSetAttribute((const void*)k_g576<128, G2_DEPTH, false>, hipFuncAttributeMaxDynamicSharedMemorySize, 81920);
  (void)hipFuncSetAttribute((const void*)k_g576<72, G5_DEPTH, true>, hipFuncAttributeMaxDynamicSharedMemorySize, 81920);
  (void)hipFuncSetAttribute((const void*)k_g4, hipFuncAttributeMaxDynamicSharedMemorySize, (4 * 34 * 1024));
  (void)hipFuncSetAttribute((const void*)k_g3, hipFuncAttributeMaxDynamicSharedMemorySize, G3_LDS);
  k_mod<<<54, 256, 0, stream>>>(c, w_mod, mod);
  k_ln<576, true><<<64, 256, 0, stream>>>(x, mod + 0 * D_, mod + 1 * D_, h, D_, D_);
  { EpiArgs e{q, nullptr, nullptr, nullptr, nullptr, QD, 0, 0.088388347648318440f * 1.4426950408889634f, 0};
    k_gemm<128, 128, EPI_SCALE><<<dim3(QD / 128, S_ / 128), 256, 0, stream>>>(h, wq, e, D_, QD, D_, 0); }
  k_pack<<<(36 * 128 + 288 * 18 + 128 * 64 + 36 * 72) / 4, 256, 0, stream>>>(wo, w_in, w_sp, w_out, pwo, pwin, pwsp, pwout);
  k_attn<<<dim3(8, 32), 512, SHM_ATTN, stream>>>(q, kc, vc, o);
  k_g576<128, G2_DEPTH, false><<<256, 256, 81920, stream>>>(o, pwo, x, mod + 2 * D_, x2, st2);
  k_g3<<<256, 512, G3_LDS, stream>>>(x2, pwin, st2, mod + 3 * D_, mod + 4 * D_, z1, z2, st3);
  k_normz2<<<256, 256, 0, stream>>>(z2, st3);
  k_g4<<<256, 512, (4 * 34 * 1024), stream>>>(z2, pwsp, z1, b_sp, u);
  k_g576<72, G5_DEPTH, true><<<256, 256, 81920, stream>>>(u, pwout, x2, mod + 5 * D_, (bf16_t*)d_out, nullptr);
}
```

```cpp
#include <hip/hip_runtime.h>
#include <hip/hip_bf16.h>

typedef unsigned short bf16_t;
typedef short bf16x8 __attribute__((ext_vector_type(8)));
typedef short s16x4 __attribute__((ext_vector_type(4)));
typedef float f32x4 __attribute__((ext_vector_type(4)));
typedef float f32x16 __attribute__((ext_vector_type(16)));
typedef unsigned u32x4 __attribute__((ext_vector_type(4)));
typedef unsigned u32x2 __attribute__((ext_vector_type(2)));
typedef unsigned u32x8 __attribute__((ext_vector_type(8)));
typedef unsigned u32x32 __attribute__((ext_vector_type(32)));
#define DI __device__ __forceinline__

constexpr int S_ = 2048, D_ = 576, HQ = 32, HKV = 16, DH = 128, QD = HQ * DH, F_ = 4608, FH = 2304;

DI float bf2f(bf16_t v) { return __uint_as_float((unsigned)v << 16); }
DI float bflo(unsigned v) { return __uint_as_float(v << 16); }
DI float bfhi(unsigned v) { return __uint_as_float(v & 0xffff0000u); }
DI unsigned cvtpk(float lo, float hi) { unsigned r; asm("v_cvt_pk_bf16_f32 %0, %1, %2" : "=v"(r) : "v"(lo), "v"(hi)); return r; }

template <int MB, int NKB> DI size_t frag_chunk(int s, int k8) {
  const int st = s / (MB * 16), sr = s % (MB * 16), sl = sr & 31, mb = 2 * (sr >> 5) + ((sl >> 2) & 1), i = 4 * (sl >> 3) + (sl & 3);
  return (((size_t)(st * NKB + (k8 >> 2)) * MB + mb) * 64) + (k8 & 3) * 16 + i;
}
template <int MB, int NKB> DI size_t frag_elem(int s, int k) { return frag_chunk<MB, NKB>(s, k >> 3) * 8 + (k & 7); }
DI int rowperm(int beta, int i) { return 8 * (i >> 2) + 4 * beta + (i & 3); }
template <int NB> DI int colperm(int nb, int i) { return ((NB & 1) && nb == NB - 1) ? 16 * nb + i : 32 * (nb >> 1) + rowperm(nb & 1, i); }

#define CPV 1
#define CODE_OFF (CPV == 1 ? (bid >> 3) * 4096 + (tid >> 3) * 128 : CPV == 2 ? ((bid >> 3) & 15) * 8192 + (tid >> 2) * 128 : ((bid >> 3) & 7) * 32768 + tid * 128)
#define SBAR() __builtin_amdgcn_sched_barrier(0)
#define RFL(x) __builtin_amdgcn_readfirstlane(x)
typedef __amdgpu_buffer_rsrc_t srd_t;
DI srd_t make_srd(const void* p, unsigned bytes) { return __builtin_amdgcn_make_buffer_rsrc((void*)p, (short)0, (int)bytes, 0x00020000); }
DI unsigned cvtpk_h(float lo, float hi) { unsigned r; asm("v_cvt_pk_f16_f32 %0, %1, %2" : "=v"(r) : "v"(lo), "v"(hi)); return r; }
DI float gelu_t(float x) { const float c1 = -2.f * 0.7978845608028654f * 1.4426950408889634f, c2 = c1 * 0.044715f;
  const float e = __builtin_amdgcn_exp2f(x * (c1 + c2 * x * x)); return x * __builtin_amdgcn_rcpf(1.f + e); }

DI unsigned win_base(unsigned st, unsigned n) { return (st & 15) * 324 + n + (st >= 16 ? (n <= 2 ? 0 : n <= 5 ? 51 : 102) : (n <= 3 ? 162 : n <= 5 ? 230 : 264)); }
DI unsigned win_stride(unsigned st, unsigned n) { return st >= 16 ? (n <= 2 ? 3 : n <= 5 ? 3 : 3) : (n <= 3 ? 4 : n <= 5 ? 2 : 3); }
constexpr unsigned PK_WIN = QD * D_ * 2, PK_WOUT = PK_WIN + D_ * F_ * 2, PK_END = PK_WOUT + FH * D_ * 2, MB = 1 << 20;
constexpr unsigned WS_MODP = 0, WS_MOD = 4 * 13824;
constexpr unsigned WS_EX = 81920;
constexpr unsigned WS_FB = WS_EX + 262144, WS_FZ = WS_FB + 16384, WS_FA = WS_FZ + 16384;
constexpr unsigned WS_XN = WS_EX + 2048 * 32 * 8;
constexpr unsigned WS_O = 4 * MB, WS_Z2 = 20 * MB, WS_PK = 29 * MB, WS_SP = 44 * MB, WS_CAL = WS_EX - 1024;
constexpr unsigned WS_Y = 76 * MB, WS_E2R = WS_Y + 9 * MB, WS_E2 = WS_Y + 10 * MB, WS_U = WS_Y + 16 * MB, WS_UR = WS_Y + 24 * MB + MB / 2, WS_AS = WS_UR + MB, WS_END = WS_AS + F_ * 8;
static_assert(WS_XN + S_ * D_ * 2 + 262144 + 294912 <= WS_O && WS_PK + PK_END <= WS_SP && 5 * 13824 + 4608 <= WS_CAL, "");
#define nr(o, hp) RFL((o) | (((hp ^ ((o) >> 21)) & 1) << 13))
DI unsigned yoff(unsigned kb) { return kb < 16 ? (kb >> 2) * 524288 + (kb & 3) * 2048 : 24 * MB + (kb & 1) * 2048; }
#define XL 1
#define ZATT 2
#define NS5 0
#define HSTEP 7
#define ZD 1
constexpr unsigned XN_DESC = S_ * D_ * 2, WS_PAR = WS_XN + XN_DESC + 262144;
#define KQX 0
#define DUMP 0x10000
DI u32x4 srd4(const void* p, unsigned bytes) { const unsigned long long a = (unsigned long long)p; return (u32x4){(unsigned)a, (unsigned)(a >> 32) & 0xffff, bytes, 0x20000u}; }
typedef float f32x2 __attribute__((ext_vector_type(2)));
template <class P> DI void st16(P p, unsigned o, u32x4 v) { asm volatile("global_store_dwordx4 %0, %1, %2 sc1\n\ts_nop 1" :: "v"(o), "v"(v), "s"(p) : "memory"); }
template <class P> DI void st4(P p, unsigned o, unsigned v) { asm volatile("global_store_dword %0, %1, %2 sc1" :: "v"(o), "v"(v), "s"(p) : "memory"); }
DI f32x2 unpk(unsigned v) { return (f32x2){bflo(v), bfhi(v)}; }
template <int C> DI float ror_add(float v) { return v + __builtin_bit_cast(float, __builtin_amdgcn_update_dpp(0, __builtin_bit_cast(int, v), 0x120 + C, 0xf, 0xf, false)); }
DI float row16_sum(float v) { return ror_add<1>(ror_add<2>(ror_add<4>(ror_add<8>(v)))); }
#define SELFN 72
#define ZOLD 0
#define CALR 2
#define ARGS(...) asm volatile("" : __VA_ARGS__ : "s"(c), "s"(w), "s"(x), "s"(ws), "s"(bsp), "s"(wo), "s"(w_in), "s"(w_out))
extern "C" __global__ __launch_bounds__(256) void k_pre(const char* __restrict__ c, const char* __restrict__ w, const char* __restrict__ x, char* __restrict__ ws, const char* __restrict__ bsp, const char* wo, const char* w_in, const char* w_out,
    const char* K, const char* V, const char* w_sp, const char* out) {
  __shared__ float red[41][65];
  const int t = threadIdx.x; const unsigned bx = blockIdx.x - 16;
  unsigned pf0 = t * 128;
  if (t < SELFN) asm volatile("s_getpc_b64 s[98:99]\n\tglobal_load_dword %0, %0, s[98:99]" : "+v"(pf0) :: "s98", "s99");
  SBAR();
  if (blockIdx.x < 16) {
    unsigned q = blockIdx.x;
    ARGS("+s"(q));
    if (t < 64) {
      const unsigned die = __builtin_amdgcn_s_getreg(20 | (2 << 6)), pg = q < 6 ? 2 + q + (q >> 1) : q < 8 ? 22 + 6 * (q & 1) : 30 + q + (q >> 1) - 4;
      unsigned off = pg * 2 * MB + (((die ^ pg) & 1) << 13) + q * 0x4500 % 0x1f00 / 256 * 256 + (q & 7) * 16384 + (t & 15) * 4;
      if (q < 4 && t < 2) st4(ws, WS_CAL + (16 + q + 4 * t) * 4, (die ^ t) << 31);
      unsigned b0 = ~0u, b1 = ~0u;
#pragma unroll 1
      for (int r = 0; r < CALR; ++r) {
        unsigned long long t0, t1, t2;
        asm volatile("s_memtime %0\n\ts_waitcnt lgkmcnt(0)\n\tglobal_store_dword %3, %3, %5 sc1\n\ts_waitcnt vmcnt(0)\n\ts_memtime %1\n\ts_waitcnt lgkmcnt(0)\n\tglobal_store_dword %4, %4, %5 sc1\n\ts_waitcnt vmcnt(0)\n\ts_memtime %2\n\ts_waitcnt lgkmcnt(0)"
                     : "=&s"(t0), "=&s"(t1), "=&s"(t2) : "v"(off), "v"(off ^ 0x2000), "s"(ws) : "memory");
        b0 = min(b0, (unsigned)(t1 - t0)); b1 = min(b1, (unsigned)(t2 - t1));
      }
      if (t == 0) st4(ws, WS_CAL + q * 4, b1 - b0);
    }
    if (t >> 6 == 1) {
#define make_srd srd4
#undef RFL
#define RFL(x) (x)
    typedef u32x4 srd_t;
    const unsigned tid = (t & 3) * 64, bid = q * 16 + ((t >> 2) & 15), hp = 0, half = 0; unsigned* tp = (unsigned*)red;
  const unsigned lane = tid & 63, w = RFL(tid >> 6), b = w & 1, hf = w >> 1;
  const unsigned xcd = bid & 7, jj = bid >> 3, kq = (xcd ^ KQX) & 3, rb = half * 4 + (jj >> 3), mt = 2 * rb + (jj & 1), nt = (jj >> 1) & 3, sHl = (jj & 7);
  const unsigned ldsb = 0, OOB = 0x7f000000u, vCp = CODE_OFF;
  const srd_t sWS = make_srd(ws, WS_END);
  const unsigned sLx = (ldsb + w * 9216), s2Xo = (WS_PK + (nt * 128 + kq * 32 + w) * 9216), sFBr = nr(WS_FB, hp) + (((2 * mt + hf) & 15) * 4 + kq) * 128;
  const unsigned r64 = ((sFBr) >> 9) & 15, gw4 = ((bid * 4 + w) * 4) & 4095;
  const unsigned vY2 = nr(WS_O + kq * 4 * MB + r64 * 262144, hp) + b * 2048 + lane * 16, sHl15 = sHl << 15;
  const unsigned ct = kq * 4 + nt, j = lane & 15, g = lane >> 4, sg = mt * 4 + w;
  const unsigned vL16 = (lane * 16), vL8 = lane * 8;
  const unsigned vXr0 = ldsb + vL16, vXr1 = vXr0 + 36864, vXr2 = vXr0 + 2 * 36864, vXr3 = vXr0 + 3 * 36864;
  const unsigned bLo = ((unsigned)(size_t)bsp), bHi = ((unsigned)((size_t)bsp >> 32)), oLo = ((unsigned)(size_t)out), oHi = ((unsigned)((size_t)out >> 32)), sPo3 = WS_PAR + 131072 + gw4 * 32, sPo4 = WS_PAR + 262144 + gw4 * 8;
  const srd_t sXin = make_srd(x, S_ * D_ * 2), sB = make_srd((void*)(bLo | (size_t)bHi << 32), S_ * 2), sOut = make_srd((void*)(oLo | (size_t)oHi << 32), S_ * D_ * 2);
  const unsigned vU1 = vXr2 + (b * 9 + hf * 4) * 1024, vV1 = vXr2 + (b * 9 + 8) * 1024, vX2 = ldsb + 4 * 36864 + 1024 + tid * 16;
  const unsigned s = half * 1024 + (r64 >> 1) * 128 + kq * 32 + rowperm(b, j), d0 = nt * 144 + 64 * hf + 8 * g, d8 = nt * 144 + 128 + 4 * g, m8 = (hf - 1) & OOB;
  const unsigned vR = (s * D_ + d0) * 2, vR8 = (s * D_ + d8) * 2 | m8;
  const unsigned vG = WS_MOD + (2 * D_ + d0) * 4, vG8 = WS_MOD + (2 * D_ + d8) * 4, vG58 = vG8 + 3 * D_ * 4, vRc = vL16 + hf * 2048;
  const unsigned vFC = WS_FB + (tid >> 1) * 128 + (tid & 1) * 16;
  const unsigned sOwn = w ^ kq, tl = ((mt & 7) * 4 + nt) * 4, sx = (kq - w - 1) & 3, sSnd = nr(WS_E2 + sx * 2 * MB + (tl + w) * 16384, hp), sRcv = nr(WS_E2 + (tl + kq) * 16384, hp) + b * 4096;
  const unsigned er = nr(WS_E2R, hp) + tl * 8192, sSnd2 = er + hf * 16384 + b * 4096 + sx * 1024, sRcv2 = (er + (kq >> 1) * 16384 + (kq & 1) * 4096 + b * 512), sRcv5 = sRcv2 - MB;
  const unsigned fc = bid, strip = fc / 9, fn = fc - strip * 9, fc2 = 256 + (fc & 31), strip2 = fc2 / 9, fn2 = fc2 - strip2 * 9, m2 = fc < 32 ? 0 : OOB;
  const unsigned sJob = RFL(win_stride(strip, fn) * 1024), sJob2 = RFL(fc < 32 ? win_stride(strip2, fn2) * 1024 : 0), wb = WS_PK + (unsigned)PK_WIN + win_base(strip, fn) * 1024, wb2 = WS_PK + (unsigned)PK_WIN + win_base(strip2, fn2) * 1024;
  const unsigned vW = wb + vL16, vW2 = wb2 + vL16, vT = WS_MOD + 13824 + (lane & 3) * 1152 + g * 16;
  const unsigned pk = ((sOwn - 1) & 3) * 8 + (lane >> 3), mp = pk < 18 ? 0 : OOB, vWp = (wb + pk * sJob + (lane & 7) * 128) | mp, vWp2 = (wb2 + pk * sJob2 + (lane & 7) * 128) | mp | m2;
  const unsigned vPk = pk, vPl = (lane & 7) * 128 | mp, sWb = wb, sWb2 = wb2 | m2;
  const unsigned mj = (j | sOwn) == 0 ? 0 : OOB;
  const unsigned yb = nr(WS_Y, hp) + ((mt & 7) * 4 + kq) * 16384, ybb = yb + b * 1024, k0 = (nt * 144 + 64 * hf) >> 5, dg = ((nt & 1) * 2 + g), yl = (j * 16 + (dg & 3) * 256), hAS = (g * 32 | mj), hSt = (rowperm(0, j) * 64 | (g == 0 ? 0 : OOB)), hF8 = (j * 16 + (g >> 1) * 256 + (g & 1) * 8);
  const unsigned aS1 = (WS_AS + fc * 128), aS2 = ((WS_AS + fc2 * 128) | m2), ySb = (yb + 24 * MB + 4096 + b * 256 + (2 * nt + hf) * 8);
  const unsigned Y0 = (ybb + yoff(k0)), Y1 = (ybb + yoff(k0 + 1)), Y2 = (ybb + yoff(k0 + 2)), Y8 = ((ybb + yoff((nt * 144 + 128) >> 5) + (nt & 1) * 512) | m8);
  unsigned yAS = aS1 | hAS, yAS2 = aS2 | hAS, ySt = ySb + hSt;
  unsigned yF = yl + (dg < 4 ? Y0 : Y1), yF2 = yl + (dg < 4 ? Y1 : Y2), yF8 = Y8 + hF8, yU1 = vU1, yV1 = vV1, yX2 = vX2, yRc = vRc, yL8 = vL8;
  const unsigned ck8 = 18 * (j >> 2) + 4 * (j & 2) + 4; unsigned yCan = yoff(ck8 >> 2) + (j & 1) * 1024 + (ck8 & 3) * 256;
  const unsigned long long sMsk = hf * 0x3f800000u; const unsigned sInv = RFL(__float_as_uint(hf ? 1.f / 80 : 1.f / 64));
  const unsigned tbl = ldsb + 4 * 36864 + w * 256, vOwn = vXr1 + w * 9216;
  const unsigned s3Yo = (nr(WS_Y, hp) + (sg & 31) * 16384), vS = s3Yo + 24 * MB + 4096 + (lane & 31) * 64, ctb = (WS_AS + ct * 1152), vA2 = ctb + 16 * 1152 + j * 8, vA1 = ctb + g * 32;
  const unsigned vTw = tbl + (lane & 31) * 4, vTr = tbl + g * 32, vT1 = tbl + rowperm(0, j) * 4;
  const unsigned ex = (nr(WS_EX, hp) + (mt & 7) * 32768 + w * 256), hEw = ((ct >> 3) * 16384 + (ct & 7) * 1024 + g * 64 + (j >> 2) * 16), vEw4 = ex + hEw, hEr = ((lane >> 5) * 16384 + (lane & 31) * 8), vEr = ex + hEr;
  const unsigned sMt16 = ((mt & 7) << 16), fz = (nr(WS_FZ, hp) + (ct * 4 + w) * 128), vFl = fz + (j & 7) * 4, vFs = lane == 0 ? fz + (sMt16 >> 14) : OOB;
  const unsigned vB = half * 2048 + (sMt16 >> 8) + w * 64 + 2 * rowperm(0, j), y4b = (nr(WS_SP, hp) + (mt & 7) * 4 * MB + w * 2048), vY4 = y4b + vL16;
  const unsigned s3Xo = WS_PK + (unsigned)PK_WIN + ct * 324 * 1024 + w * 9216, sXo4 = nr(WS_Z2 + (ct * 32 + w) * 16384, hp), sXr4 = nr(WS_Z2 + 8 * MB, hp) + (ct >> 1) * 16384 + (ct & 1) * 4096 + w * 1024;
  const unsigned sUo = nr(WS_U + ((sg & 31) * 4 + kq) * 65536, hp), sUr = (nr(WS_UR, hp) + ((sg & 31) * 2 + (kq >> 1)) * 16384 + (kq & 1) * 4096), sNt = nt, vU8 = sUr + (nt >> 1) * 2048 + (nt & 1) * 512 + hF8;
  const unsigned vG5 = vG + 3 * D_ * 4;
  const unsigned hw = w < 3 ? 5120 * w : 13312, sLh = ldsb + hw, x5 = WS_PK + (unsigned)PK_WOUT + (nt * 72 + kq * 18) * 9216, s5X4 = x5 + 16 * 9216 + hw;
  const unsigned s5X0 = x5 + (4 * nt + w) * 9216, s5X1 = x5 + (4 * ((nt + 1 + hf) & 3) + b) * 9216, s5X2 = x5 + (4 * ((nt + 3 - 2 * hf) & 3) + w) * 9216, s5X3 = x5 + (4 * ((nt + 2 + hf) & 3) + 2 + b) * 9216;
  tp[(t & 63) * 41 + 0] = bLo;
  tp[(t & 63) * 41 + 1] = bHi;
  tp[(t & 63) * 41 + 2] = oLo;
  tp[(t & 63) * 41 + 3] = oHi;
  tp[(t & 63) * 41 + 4] = s3Xo;
  tp[(t & 63) * 41 + 5] = s5X0;
  tp[(t & 63) * 41 + 6] = s5X1;
  tp[(t & 63) * 41 + 7] = s5X2;
  tp[(t & 63) * 41 + 8] = s5X3;
  tp[(t & 63) * 41 + 9] = s5X4;
  tp[(t & 63) * 41 + 10] = sMt16;
  tp[(t & 63) * 41 + 11] = sLh;
  tp[(t & 63) * 41 + 12] = aS1;
  tp[(t & 63) * 41 + 13] = aS2;
  tp[(t & 63) * 41 + 14] = ctb;
  tp[(t & 63) * 41 + 15] = s3Yo;
  tp[(t & 63) * 41 + 16] = sRcv;
  tp[(t & 63) * 41 + 17] = sRcv2;
  tp[(t & 63) * 41 + 18] = sSnd;
  tp[(t & 63) * 41 + 19] = sSnd2;
  tp[(t & 63) * 41 + 20] = sUo;
  tp[(t & 63) * 41 + 21] = sUr;
  tp[(t & 63) * 41 + 22] = sXo4;
  tp[(t & 63) * 41 + 23] = sXr4;
  tp[(t & 63) * 41 + 24] = Y0;
  tp[(t & 63) * 41 + 25] = Y1;
  tp[(t & 63) * 41 + 26] = Y2;
  tp[(t & 63) * 41 + 27] = Y8;
  tp[(t & 63) * 41 + 28] = ySb;
  tp[(t & 63) * 41 + 29] = ex;
  tp[(t & 63) * 41 + 30] = fz;
  tp[(t & 63) * 41 + 31] = y4b;
  tp[(t & 63) * 41 + 32] = sWb;
  tp[(t & 63) * 41 + 33] = sWb2;
  tp[(t & 63) * 41 + 34] = sJob;
  tp[(t & 63) * 41 + 35] = sJob2;
  for (int i = 0; i < 2; ++i) { const unsigned* e = tp + (i * 32 + ((t & 63) >> 1)) * 41 + 32; st16(ws, WS_PAR + 262144 + q * 2048 + i * 1024 + (t & 63) * 16, (u32x4){e[0], e[1], e[2], e[3]}); }
  for (int i = 0; i < 8; ++i) { const unsigned* e = tp + (i * 8 + ((t & 63) >> 3)) * 41 + (t & 7) * 4; st16(ws, WS_PAR + 131072 + q * 8192 + i * 1024 + (t & 63) * 16, (u32x4){e[0], e[1], e[2], e[3]}); }
#undef make_srd
#undef RFL
#define RFL(x) __builtin_amdgcn_readfirstlane(x)
    }
    asm volatile("" :: "v"(pf0));
    return;
  }
  if (bx >= 344) {
    unsigned pf = 0; const unsigned pid = ((bx - 344) >> 3) * 256 + t;
#define CPB 32
    if (bx < 344 + CPB)
      asm volatile("s_getpc_b64 s[98:99]\n\ts_add_u32 s98, s98, k_main@rel32@lo+4\n\ts_addc_u32 s99, s99, k_main@rel32@hi+12\n\tv_min_u32 %0, code_end_lbl-k_main-4, %1\n\tglobal_load_dword %0, %0, s[98:99]"
                   : "=&v"(pf) : "v"(pid * 128) : "s98", "s99", "scc");
    unsigned zo = WS_EX + ((bx - 376) * 256 + t) * 16;
    ARGS("+v"(zo));
    if (ZD && bx - 376 < 73) st16(ws, zo, (u32x4){0u, 0u, 0u, 0u});
    SBAR();
  unsigned vDesc;
  { const unsigned id = ((t & 63) >> 3) * 1024 + (bx - 344) * 4 + (t >> 6), un = id / 5, part = id - un * 5, field = t & 7;
    const bool isO = un < 512, isI = !isO && un < 1088, valid = id < 6880;
    const unsigned v = isO ? un : isI ? un - 512 : un - 1088;
    const unsigned kb = isI ? v >> 5 : v >> 2, st = isI ? v & 31 : v & 3, pitch = isI ? F_ * 2 : D_ * 2, nkb = isO ? 128 : 72;
    const unsigned kq5 = kb / 18, r5 = kb - kq5 * 18; const bool rem = !isO && !isI && r5 >= 16;
    const unsigned row0 = (isO || isI) ? kb * 32 : kq5 * 576 + (rem ? (r5 - 16) * 288 + 128 : (r5 >> 2) * 144 + (r5 & 3) * 32);
    const unsigned soff = row0 * pitch + (st * 144 + 32 * part) * 2, tot = isO ? QD * D_ * 2 : isI ? D_ * F_ * 2 : FH * D_ * 2;
    const unsigned long long src = (unsigned long long)(isO ? wo : isI ? w_in : w_out) + (valid ? soff : 0);
    const unsigned fr = isI ? win_base(st, 2 * part) + kb * win_stride(st, 2 * part) : (st * nkb + kb) * 9 + 2 * part;
    const unsigned fr1 = isI ? win_base(st, 2 * part + 1) + kb * win_stride(st, 2 * part + 1) : fr + 1, pb = WS_PK + (isO ? 0u : isI ? (unsigned)PK_WIN : (unsigned)PK_WOUT);
    const unsigned d0 = valid ? pb + fr * 1024 : 0x7f000000u, d1 = (part == 4 || !valid) ? 0x7f000000u : pb + fr1 * 1024;
    vDesc = field == 0 ? (unsigned)src : field == 1 ? (unsigned)(src >> 32) & 0xffff : field == 2 ? (valid ? tot - soff : 0) : field == 3 ? 0x20000u : field == 4 ? d0 : field == 5 ? d1 : field == 6 ? pitch + (((part == 4) + 2 * isO) << 24) : (rem ? 144 : isO ? 8 : 16) * pitch - 1024;
  }
    st4(ws, WS_XN + XN_DESC + ((bx - 344) * 256 + t) * 4, vDesc);
    {
#define make_srd srd4
    typedef u32x4 srd_t;
    const unsigned tid = RFL(t), bid = bx - 344, hp = 0, half = 0; const char* wq = ws; unsigned pv = 0;
  const unsigned lane = tid & 63, w = RFL(tid >> 6), b = w & 1, hf = w >> 1;
  const unsigned xcd = bid & 7, jj = bid >> 3, kq = (xcd ^ KQX) & 3, rb = half * 4 + (jj >> 3), mt = 2 * rb + (jj & 1), nt = (jj >> 1) & 3, sHl = (jj & 7);
  const unsigned ldsb = 0, OOB = 0x7f000000u, vCp = CODE_OFF;
  const srd_t sWS = make_srd(ws, WS_END);
  const unsigned sLx = (ldsb + w * 9216), s2Xo = (WS_PK + (nt * 128 + kq * 32 + w) * 9216), sFBr = nr(WS_FB, hp) + (((2 * mt + hf) & 15) * 4 + kq) * 128;
  const unsigned r64 = ((sFBr) >> 9) & 15, gw4 = ((bid * 4 + w) * 4) & 4095;
  const unsigned vY2 = nr(WS_O + kq * 4 * MB + r64 * 262144, hp) + b * 2048 + lane * 16, sHl15 = sHl << 15;
  const unsigned hi = lane >> 5, i32 = lane & 31, h = kq * 8 + sHl, kvh = h >> 1;
  const unsigned p4 = lane & 3, q4 = (lane >> 2) & 3, sub = (lane >> 4) & 1, r8 = (lane >> 2) & 7, sL = ldsb;
  const unsigned vKrd = ldsb + 2048 * ((lane >> 3) & 1) + 64 * (lane & 7) + 16 * ((lane >> 4) ^ ((lane >> 1) & 2));
  const unsigned vVr = ldsb + 0x10000 + 2048 * hi + 64 * (4 * sub + q4) + 32 * sub + 8 * p4;
  const unsigned vTr = ldsb + 0x10000 + 2048 * (lane >> 4) + 64 * q4 + 16 * p4;
  const unsigned vVs0 = 256 * r8 + 64 * hi + 16 * (p4 ^ (2 * sub)) + 4096, vVs1 = vVs0 + 128 - 1024;
  const unsigned vWs = 8192 * r8 + 64 * hi + 16 * p4;
  const unsigned vX = lane * 16;
  const unsigned vAS = ldsb + 0x24000 + 64 * (lane >> 4);
  const unsigned gi = 36 * w + (lane < 36 ? lane : 35);
  const unsigned vMod = 16 * gi, vTw = ldsb + 0x24000 + (gi >> 1) * 64 + 16 * (gi & 1);
  const unsigned ci = 32 * w + i32, ts = ci >> 4, vTw2 = ldsb + 0x24000 + 16 * ci + 8 * hi;
  const unsigned vMod2 = (ts < 7 ? ts : min(HSTEP + (h >> 1), 17u)) * 128 + ((ci >> 2) & 3) * 32 + (ci & 1) * 16 + ((ci & 2) ? 0 : 2304) + hi * 27648;
  const unsigned vPre = lane < 12 ? 4096 + ((((h & 1) * 4 + (jj >> 3)) * 4 + w) * 12 + lane) * 128 : OOB, l56 = lane < 56 ? 0 : OOB;
  const unsigned vXp = (16384 + (lane * 8 + sHl) * 128) | l56, wl = lane * 16 + (jj >> 3) * 4 + w, vWt = (4096 + (128 + (wl >> 1)) * 8192 + h * 256 + (wl & 1) * 128) | l56;
  const srd_t sK = make_srd((const char*)K + (size_t)kvh * S_ * 256 - 4096, S_ * 256 + 4096), sV = make_srd((const char*)V + (size_t)kvh * S_ * 256 - 4096, S_ * 256 + 4096);
  const srd_t sW = make_srd((const char*)wq - 4096, D_ * QD * 2 + 4096);
  const unsigned sLw = ldsb + 4096 * w, sKV = 4096 * w, sWo = w * 16 * 8192 + h * 256, sXo = WS_XN + (rb * 4 + w) * 36 * 2048, sOo = nr(WS_O + kq * 4 * MB + ((rb * 4 + w) & 15) * 262144, hp) + sHl15;
  const unsigned sDesc = WS_XN + XN_DESC + bid * 1024 + w * 256, sHsh = 16 + xcd;
  const unsigned gw = bid * 4 + w, spkb = (gw & 3) + 4 * ((gw >> 5) & 15), spmb = (xcd & 3) + 4 * (jj >> 4);
  const unsigned mi = gw * 64 + lane, vMs = mi * 4, vMd = mi < 3456 ? vMs + 4 * 13824 : 0x7f000000u;
  const bool isS = mi >= 3 * D_ && mi < 4 * D_, isC = mi >= 4 * D_ && mi < 5 * D_;
  const unsigned vTab = (mi * 2 + (isC ? 5 * 13824 - 8 * D_ : 5 * 13824 + 2304 - 6 * D_)) | (isC || isS ? 0 : OOB); const float vOne = isC ? 1.f : 0.f;
  const unsigned vSp = (8 * ((lane & 15) >> 2) + (lane & 3)) * 4096 + (lane >> 4) * 16;
  const unsigned sSpo = (((spmb >> 1) * 32 + 4 * (spmb & 1)) * 4096 + spkb * 64), sSpd = (nr(WS_SP, hp) + spkb * 16384 + spmb * 1024);
  const unsigned sZe = ZOLD ? WS_Y + gw * 1024 : nr(WS_Y, hp) + ((xcd & 3) * 16 + (jj >> 1)) * 16384 + (jj & 1) * 4096 + w * 1024, sFAw = WS_FA + gw * 4, sFAr = WS_FA + w * 1024, sFBw = nr(WS_FB, hp) + (((rb * 4 + w) & 15) * 4 + kq) * 128 + sHl * 4;
  const unsigned sOl = ldsb + (((hf ^ jj) & 1) ? DUMP : 0x20000 + b * 16384), sYl = ldsb + 0x20000 + hf * 16384 + b * 2048;
  const srd_t sSp = make_srd(w_sp, S_ * S_ * 2); const char* sOp = (const char*)out + gw * 2304;
  const unsigned sTch = s2Xo + ((jj >> 3) * 2 + (jj & 1)) * 36864, sPo = WS_PAR + gw * 128;
  asm("v_writelane_b32 %0, %1, 0" : "+v"(pv) : "s"(sK[0]));
  asm("v_writelane_b32 %0, %1, 1" : "+v"(pv) : "s"(sK[1]));
  asm("v_writelane_b32 %0, %1, 2" : "+v"(pv) : "s"(sK[2]));
  asm("v_writelane_b32 %0, %1, 3" : "+v"(pv) : "s"(sK[3]));
  asm("v_writelane_b32 %0, %1, 4" : "+v"(pv) : "s"(sV[0]));
  asm("v_writelane_b32 %0, %1, 5" : "+v"(pv) : "s"(sV[1]));
  asm("v_writelane_b32 %0, %1, 6" : "+v"(pv) : "s"(sV[2]));
  asm("v_writelane_b32 %0, %1, 7" : "+v"(pv) : "s"(sV[3]));
  asm("v_writelane_b32 %0, %1, 8" : "+v"(pv) : "s"(sSp[0]));
  asm("v_writelane_b32 %0, %1, 9" : "+v"(pv) : "s"(sSp[1]));
  asm("v_writelane_b32 %0, %1, 10" : "+v"(pv) : "s"(sSp[2]));
  asm("v_writelane_b32 %0, %1, 11" : "+v"(pv) : "s"(sSp[3]));
  asm("v_writelane_b32 %0, %1, 12" : "+v"(pv) : "s"((unsigned)(unsigned long long)sOp));
  asm("v_writelane_b32 %0, %1, 13" : "+v"(pv) : "s"((unsigned)((unsigned long long)sOp >> 32)));
  asm("v_writelane_b32 %0, %1, 14" : "+v"(pv) : "s"(sDesc));
  asm("v_writelane_b32 %0, %1, 15" : "+v"(pv) : "s"(sFAw));
  asm("v_writelane_b32 %0, %1, 16" : "+v"(pv) : "s"(sWo));
  asm("v_writelane_b32 %0, %1, 17" : "+v"(pv) : "s"(sXo));
  asm("v_writelane_b32 %0, %1, 18" : "+v"(pv) : "s"(sSpo));
  asm("v_writelane_b32 %0, %1, 19" : "+v"(pv) : "s"(sSpd));
  asm("v_writelane_b32 %0, %1, 20" : "+v"(pv) : "s"(sZe));
  asm("v_writelane_b32 %0, %1, 21" : "+v"(pv) : "s"(sFAr));
  asm("v_writelane_b32 %0, %1, 22" : "+v"(pv) : "s"(sOl));
  asm("v_writelane_b32 %0, %1, 23" : "+v"(pv) : "s"(sHl));
  asm("v_writelane_b32 %0, %1, 24" : "+v"(pv) : "s"(s2Xo));
  asm("v_writelane_b32 %0, %1, 25" : "+v"(pv) : "s"(sTch));
  asm("v_writelane_b32 %0, %1, 26" : "+v"(pv) : "s"(sLx));
  asm("v_writelane_b32 %0, %1, 27" : "+v"(pv) : "s"(sOo));
  asm("v_writelane_b32 %0, %1, 28" : "+v"(pv) : "s"(sYl));
  asm("v_writelane_b32 %0, %1, 29" : "+v"(pv) : "s"(sFBw));
  asm("v_writelane_b32 %0, %1, 30" : "+v"(pv) : "s"(sFBr));
  asm("v_writelane_b32 %0, %1, 31" : "+v"(pv) : "s"(sHsh));
  if ((t & 63) < 32) st4(ws, sPo + (t & 63) * 4, pv);
#undef make_srd
    }
    asm volatile("s_waitcnt vmcnt(2)" :: "v"(pf), "v"(pf0));
    return;
  }
  if (bx >= 128) {
    const int bb = bx - 128, cg = XL ? (bb >> 3) * 2 + (bb & 1) : bb % 54, kq = XL ? (bb & 7) >> 1 : bb / 54, c8 = t & 7, rg = t >> 3, n0 = cg * 64 + c8 * 8;
    u32x4 v[5]; bf16_t cv[5]; unsigned mo[5], co = (kq * 144 + rg) * 2, c4 = (kq * 144 + 128 + (rg & 15)) * 2;
#pragma unroll
    for (int i = 0; i < 5; ++i) mo[i] = ((kq * 144 + (i < 4 ? rg + 32 * i : 128 + (rg & 15))) * 3456 + n0) * 2;
    ARGS("+v"(co), "+v"(c4), "+v"(mo[0]), "+v"(mo[1]), "+v"(mo[2]), "+v"(mo[3]), "+v"(mo[4]));
#pragma unroll
    for (int i = 0; i < 5; ++i) cv[i] = *(const bf16_t*)(c + (i < 4 ? co : c4) + (i < 4 ? 64 * i : 0));
#pragma unroll
    for (int i = 0; i < 5; ++i) v[i] = *(const u32x4*)(w + mo[i]);
    if (!ZD && bb < 73) st16(ws, WS_EX + (bb * 256 + t) * 16, (u32x4){0u, 0u, 0u, 0u});
    if (bb == 215) { const unsigned z = *(const unsigned*)(bsp + t * 16); asm volatile("" :: "v"(z)); }
    f32x2 acc[4] = {{0, 0}, {0, 0}, {0, 0}, {0, 0}}; float sl[5];
#pragma unroll
    for (int i = 0; i < 5; ++i) { const float cf = bf2f(cv[i]); sl[i] = cf * __builtin_amdgcn_rcpf(1.f + __builtin_amdgcn_exp2f(cf * -1.4426950408889634f)); }
#pragma unroll
    for (int i = 0; i < 5; ++i)
      if (i < 4 || rg < 16) {
#pragma unroll
        for (int j = 0; j < 4; ++j) acc[j] += sl[i] * unpk(v[i][j]);
      }
#pragma unroll
    for (int j = 0; j < 8; ++j) { const float a = ror_add<8>(acc[j >> 1][j & 1]); if (!(t & 8)) red[t >> 4][c8 * 8 + j] = a; }
    __syncthreads();
    if (t < 64) { float s = 0;
#pragma unroll
      for (int r = 0; r < 16; ++r) s += red[r][t];
      st4(ws, WS_MODP + (kq * 3456 + cg * 64 + t) * 4, __float_as_uint(s)); }
  } else {
    const int l = t & 15, row = (XL ? (bx & 7) * 16 + (bx >> 3) : bx) * 16 + (t >> 4); const bool on = l < 12;
    u32x4 v[6];
    unsigned xo = (row * D_ + (on ? l : 0) * 16) * 2;
    ARGS("+v"(xo));
#pragma unroll
    for (int i = 0; i < 6; ++i) v[i] = *(const u32x4*)(x + xo + ((i >> 1) * 384 + (i & 1) * 16));
    f32x2 f[24], s2 = {0, 0};
#pragma unroll
    for (int i = 0; i < 24; ++i) { f[i] = unpk(v[i >> 2][i & 3]); s2 += f[i]; }
    const float m = row16_sum(on ? s2[0] + s2[1] : 0.f) * (1.f / D_); f32x2 q2 = {0, 0};
#pragma unroll
    for (int i = 0; i < 24; ++i) { f[i] -= m; q2 += f[i] * f[i]; }
    const float r = __builtin_amdgcn_rsqf(row16_sum(on ? q2[0] + q2[1] : 0.f) * (1.f / D_) + 1e-5f);
    const unsigned base = WS_XN + (row >> 6) * 36 * 2048 + ((row >> 5) & 1) * 2048 + ((row >> 2) & 1) * 1024 + (((row >> 3) & 3) * 4 + (row & 3)) * 16 + (l >> 1) * 4096 + (l & 1) * 512;
    if (on) {
#pragma unroll
      for (int i = 0; i < 6; ++i) { u32x4 o;
#pragma unroll
        for (int e = 0; e < 4; ++e) { const f32x2 z = f[4 * i + e] * r; o[e] = cvtpk_h(z[0], z[1]); }
        st16(ws, base + (i >> 1) * 24576 + (i & 1) * 256, o); }
    }
  }
  asm volatile("" :: "v"(pf0));
}
constexpr int MAIN_LDS = 160 * 1024;
extern "C" __global__ __launch_bounds__(256, 1) __attribute__((aligned(4096))) void k_main(char* __restrict__ ws, const bf16_t* __restrict__ wq, const bf16_t* __restrict__ K, const bf16_t* __restrict__ V, const bf16_t* __restrict__ w_sp,
    const bf16_t* __restrict__ x, const bf16_t* __restrict__ bsp, bf16_t* __restrict__ out) {
  SBAR();
  unsigned tid = threadIdx.x, bid = blockIdx.x;
  asm volatile("s_mov_b64 exec, 0\ns_cmp_eq_u32 %0, 0\ns_cbranch_scc1 LPa_0\ns_cmp_eq_u32 %0, 1\ns_cbranch_scc1 LPa_1\ns_cmp_eq_u32 %0, 2\ns_cbranch_scc1 LPa_2\ns_branch LPa_3\nLRa:\ns_mov_b64 exec, -1" :: "s"(RFL(tid >> 6)) : "scc");
  SBAR();
  unsigned r_s16;
  unsigned r_s17;
  unsigned r_s18;
  unsigned r_s19;
  unsigned r_s20;
  unsigned r_s21;
  unsigned r_s22;
  unsigned r_s23;
  unsigned r_s24;
  unsigned r_s25;
  unsigned r_s26;
  unsigned r_s27;
  unsigned r_s28;
  unsigned r_s29;
  unsigned r_s30;
  unsigned r_s31;
  unsigned r_s32;
  unsigned r_s33;
  unsigned r_s34;
  unsigned r_s35;
  unsigned r_s36;
  unsigned r_s37;
  unsigned r_s38;
  unsigned r_s39;
  unsigned r_s40;
  unsigned r_s41;
  unsigned r_s42;
  unsigned r_s43;
  unsigned r_s44;
  unsigned r_s45;
  unsigned r_s46;
  unsigned r_s47;
  unsigned r_s57;
  unsigned r_s60;
  unsigned r_s69;
  unsigned r_s71;
  unsigned r_s73;
  unsigned r_s76;
  u32x32 r_v32;
  u32x8 r_v40;
  u32x8 r_v48;
  u32x8 r_v56;
  u32x32 r_v64;
  u32x32 r_v96;
  u32x32 r_v128;
  u32x32 r_v160;
  u32x32 r_v192;
  u32x32 r_v224;
  unsigned sLw_;
  unsigned sPo_;
  unsigned sWo_;
  unsigned sXo_;
  unsigned vMod2_;
  unsigned vWs_;
  unsigned vX_;
  srd_t sWS_, sW_;
  {
  const unsigned hp = 0, half = 0;
  const unsigned lane = tid & 63, w = RFL(tid >> 6), b = w & 1, hf = w >> 1;
  const unsigned xcd = bid & 7, jj = bid >> 3, kq = (xcd ^ KQX) & 3, rb = half * 4 + (jj >> 3), mt = 2 * rb + (jj & 1), nt = (jj >> 1) & 3, sHl = (jj & 7);
  const unsigned ldsb = 0, OOB = 0x7f000000u, vCp = CODE_OFF;
  const srd_t sWS = make_srd(ws, WS_END);
  const unsigned sLx = (ldsb + w * 9216), s2Xo = (WS_PK + (nt * 128 + kq * 32 + w) * 9216), sFBr = nr(WS_FB, hp) + (((2 * mt + hf) & 15) * 4 + kq) * 128;
  const unsigned r64 = ((sFBr) >> 9) & 15, gw4 = ((bid * 4 + w) * 4) & 4095;
  const unsigned vY2 = nr(WS_O + kq * 4 * MB + r64 * 262144, hp) + b * 2048 + lane * 16, sHl15 = sHl << 15;
  const unsigned hi = lane >> 5, i32 = lane & 31, h = kq * 8 + sHl, kvh = h >> 1;
  const unsigned p4 = lane & 3, q4 = (lane >> 2) & 3, sub = (lane >> 4) & 1, r8 = (lane >> 2) & 7, sL = ldsb;
  const unsigned vKrd = ldsb + 2048 * ((lane >> 3) & 1) + 64 * (lane & 7) + 16 * ((lane >> 4) ^ ((lane >> 1) & 2));
  const unsigned vVr = ldsb + 0x10000 + 2048 * hi + 64 * (4 * sub + q4) + 32 * sub + 8 * p4;
  const unsigned vTr = ldsb + 0x10000 + 2048 * (lane >> 4) + 64 * q4 + 16 * p4;
  const unsigned vVs0 = 256 * r8 + 64 * hi + 16 * (p4 ^ (2 * sub)) + 4096, vVs1 = vVs0 + 128 - 1024;
  const unsigned vWs = 8192 * r8 + 64 * hi + 16 * p4;
  const unsigned vX = lane * 16;
  const unsigned vAS = ldsb + 0x24000 + 64 * (lane >> 4);
  const unsigned gi = 36 * w + (lane < 36 ? lane : 35);
  const unsigned vMod = 16 * gi, vTw = ldsb + 0x24000 + (gi >> 1) * 64 + 16 * (gi & 1);
  const unsigned ci = 32 * w + i32, ts = ci >> 4, vTw2 = ldsb + 0x24000 + 16 * ci + 8 * hi;
  const unsigned vMod2 = (ts < 7 ? ts : min(HSTEP + (h >> 1), 17u)) * 128 + ((ci >> 2) & 3) * 32 + (ci & 1) * 16 + ((ci & 2) ? 0 : 2304) + hi * 27648;
  const unsigned vPre = lane < 12 ? 4096 + ((((h & 1) * 4 + (jj >> 3)) * 4 + w) * 12 + lane) * 128 : OOB, l56 = lane < 56 ? 0 : OOB;
  const unsigned vXp = (16384 + (lane * 8 + sHl) * 128) | l56, wl = lane * 16 + (jj >> 3) * 4 + w, vWt = (4096 + (128 + (wl >> 1)) * 8192 + h * 256 + (wl & 1) * 128) | l56;
  const srd_t sK = make_srd((const char*)K + (size_t)kvh * S_ * 256 - 4096, S_ * 256 + 4096), sV = make_srd((const char*)V + (size_t)kvh * S_ * 256 - 4096, S_ * 256 + 4096);
  const srd_t sW = make_srd((const char*)wq - 4096, D_ * QD * 2 + 4096);
  const unsigned sLw = ldsb + 4096 * w, sKV = 4096 * w, sWo = w * 16 * 8192 + h * 256, sXo = WS_XN + (rb * 4 + w) * 36 * 2048, sOo = nr(WS_O + kq * 4 * MB + ((rb * 4 + w) & 15) * 262144, hp) + sHl15;
  const unsigned sDesc = WS_XN + XN_DESC + bid * 1024 + w * 256, sHsh = 16 + xcd;
  const unsigned gw = bid * 4 + w, spkb = (gw & 3) + 4 * ((gw >> 5) & 15), spmb = (xcd & 3) + 4 * (jj >> 4);
  const unsigned mi = gw * 64 + lane, vMs = mi * 4, vMd = mi < 3456 ? vMs + 4 * 13824 : 0x7f000000u;
  const bool isS = mi >= 3 * D_ && mi < 4 * D_, isC = mi >= 4 * D_ && mi < 5 * D_;
  const unsigned vTab = (mi * 2 + (isC ? 5 * 13824 - 8 * D_ : 5 * 13824 + 2304 - 6 * D_)) | (isC || isS ? 0 : OOB); const float vOne = isC ? 1.f : 0.f;
  const unsigned vSp = (8 * ((lane & 15) >> 2) + (lane & 3)) * 4096 + (lane >> 4) * 16;
  const unsigned sSpo = (((spmb >> 1) * 32 + 4 * (spmb & 1)) * 4096 + spkb * 64), sSpd = (nr(WS_SP, hp) + spkb * 16384 + spmb * 1024);
  const unsigned sZe = ZOLD ? WS_Y + gw * 1024 : nr(WS_Y, hp) + ((xcd & 3) * 16 + (jj >> 1)) * 16384 + (jj & 1) * 4096 + w * 1024, sFAw = WS_FA + gw * 4, sFAr = WS_FA + w * 1024, sFBw = nr(WS_FB, hp) + (((rb * 4 + w) & 15) * 4 + kq) * 128 + sHl * 4;
  const unsigned sOl = ldsb + (((hf ^ jj) & 1) ? DUMP : 0x20000 + b * 16384), sYl = ldsb + 0x20000 + hf * 16384 + b * 2048;
  const srd_t sSp = make_srd(w_sp, S_ * S_ * 2); const char* sOp = (const char*)out + gw * 2304;
  const unsigned sTch = s2Xo + ((jj >> 3) * 2 + (jj & 1)) * 36864, sPo = WS_PAR + gw * 128;
  sLw_ = sLw;
  sPo_ = sPo;
  sWo_ = sWo;
  sXo_ = sXo;
  vMod2_ = vMod2;
  vWs_ = vWs;
  vX_ = vX;
  asm volatile("" : "+s"(sLw_), "+s"(sPo_), "+s"(sWo_), "+s"(sXo_), "+v"(vMod2_), "+v"(vWs_), "+v"(vX_));
  SBAR();
  char* ws_ = ws; const char* wq_ = (const char*)wq;
  asm volatile("" : "+s"(ws_), "+s"(wq_));
  sWS_ = make_srd(ws_, WS_END); sW_ = make_srd(wq_ - 4096, D_ * QD * 2 + 4096);
  asm volatile(
R"(L_pb0:
s_nop 4
.p2align 3
s_getreg_b32 s76, hwreg(20, 2, 1)
s_buffer_load_dwordx16 s[16:31], %[sWS], %[sPo] offset:0
s_buffer_load_dwordx16 s[32:47], %[sWS], %[sPo] offset:64
s_add_u32 m0, %[sLw], 0
s_add_u32 s60, %[sWo], 4096
buffer_load_dwordx4 %[vWs], %[sW], s60 offen offset:0 lds
s_add_u32 s60, %[sWo], 3200
buffer_load_dwordx4 %[vWs], %[sW], s60 offen offset:1024 lds
s_add_u32 s60, %[sWo], 67584
buffer_load_dwordx4 %[vWs], %[sW], s60 offen offset:2048 lds
s_add_u32 s60, %[sWo], 66688
buffer_load_dwordx4 %[vWs], %[sW], s60 offen offset:3072 lds
s_mul_i32 s57, s76, 1179648
s_mov_b32 s60, 80896
s_add_u32 s57, s57, %[sXo]
v_bfe_u32 v68, %[vX], 2, 7
buffer_load_dword v68, v68, %[sWS], s60 offen offset:0
s_mov_b32 s60, 13824
buffer_load_dwordx4 v[32:35], %[vMod2], %[sWS], 0 offen offset:0
buffer_load_dwordx4 v[36:39], %[vMod2], %[sWS], s60 offen offset:0
buffer_load_dwordx4 v[160:163], %[vX], %[sWS], s57 offen offset:0
buffer_load_dwordx4 v[164:167], %[vX], %[sWS], s57 offen offset:1024
buffer_load_dwordx4 v[168:171], %[vX], %[sWS], s57 offen offset:2048
buffer_load_dwordx4 v[172:175], %[vX], %[sWS], s57 offen offset:3072
L_pe0:
)"
    : "=&{s16}"(r_s16), "=&{s17}"(r_s17), "=&{s18}"(r_s18), "=&{s19}"(r_s19), "=&{s20}"(r_s20), "=&{s21}"(r_s21), "=&{s22}"(r_s22), "=&{s23}"(r_s23), "=&{s24}"(r_s24), "=&{s25}"(r_s25), "=&{s26}"(r_s26), "=&{s27}"(r_s27), "=&{s28}"(r_s28), "=&{s29}"(r_s29), "=&{s30}"(r_s30), "=&{s31}"(r_s31), "=&{s32}"(r_s32), "=&{s33}"(r_s33), "=&{s34}"(r_s34), "=&{s35}"(r_s35), "=&{s36}"(r_s36), "=&{s37}"(r_s37), "=&{s38}"(r_s38), "=&{s39}"(r_s39), "=&{s40}"(r_s40), "=&{s41}"(r_s41), "=&{s42}"(r_s42), "=&{s43}"(r_s43), "=&{s44}"(r_s44), "=&{s45}"(r_s45), "=&{s46}"(r_s46), "=&{s47}"(r_s47), "=&{s57}"(r_s57), "=&{s60}"(r_s60), "=&{s76}"(r_s76), "=&{v[32:63]}"(r_v32), "=&{v[64:95]}"(r_v64), "=&{v[160:191]}"(r_v160), "+v"(tid), "+s"(bid)
    : [sLw] "s"(sLw_), [sPo] "s"(sPo_), [sW] "s"(sW_), [sWS] "s"(sWS_), [sWo] "s"(sWo_), [sXo] "s"(sXo_), [vMod2] "v"(vMod2_), [vWs] "v"(vWs_), [vX] "v"(vX_)
    : "memory", "vcc", "scc", "m0","a0","a1","a2","a3","a4","a5","a6","a7","a8","a9","a10","a11","a12","a13","a14","a15","a16","a17","a18","a19","a20","a21","a22","a23","a24","a25","a26","a27","a28","a29","a30","a31","a32","a33","a34","a35","a36","a37","a38","a39","a40","a41","a42","a43","a44","a45","a46","a47","a48","a49","a50","a51","a52","a53","a54","a55","a56","a57","a58","a59","a60","a61","a62","a63","a64","a65","a66","a67","a68","a69","a70","a71","a72","a73","a74","a75","a76","a77","a78","a79","a80","a81","a82","a83","a84","a85","a86","a87","a88","a89","a90","a91","a92","a93","a94","a95","a96","a97","a98","a99","a100","a101","a102","a103","a104","a105","a106","a107","a108","a109","a110","a111","a112","a113","a114","a115","a116","a117","a118","a119","a120","a121","a122","a123","a124","a125","a126","a127","a128","a129","a130","a131","a132","a133","a134","a135","a136","a137","a138","a139","a140","a141","a142","a143","a144","a145","a146","a147","a148","a149","a150","a151","a152","a153","a154","a155","a156","a157","a158","a159","a160","a161","a162","a163","a164","a165","a166","a167","a168","a169","a170","a171","a172","a173","a174","a175","a176","a177","a178","a179","a180","a181","a182","a183","a184","a185","a186","a187","a188","a189","a190","a191","a192","a193","a194","a195","a196","a197","a198","a199","a200","a201","a202","a203","a204","a205","a206","a207","a208","a209","a210","a211","a212","a213","a214","a215","a216","a217","a218","a219","a220","a221","a222","a223","a224","a225","a226","a227","a228","a229","a230","a231","a232","a233","a234","a235","a236","a237","a238","a239","a240","a241","a242","a243","a244","a245","a246","a247","a248","a249","a250","a251","a252","a253","a254","a255");
  }
  bid = RFL(bid);
  unsigned zvKrd;
  unsigned zvSp;
  unsigned zvVr;
  unsigned zvVs0;
  unsigned zvVs1;
  {
  const unsigned hp = 0, half = 0;
  const unsigned lane = tid & 63, w = RFL(tid >> 6), b = w & 1, hf = w >> 1;
  const unsigned xcd = bid & 7, jj = bid >> 3, kq = (xcd ^ KQX) & 3, rb = half * 4 + (jj >> 3), mt = 2 * rb + (jj & 1), nt = (jj >> 1) & 3, sHl = (jj & 7);
  const unsigned ldsb = 0, OOB = 0x7f000000u, vCp = CODE_OFF;
  const srd_t sWS = make_srd(ws, WS_END);
  const unsigned sLx = (ldsb + w * 9216), s2Xo = (WS_PK + (nt * 128 + kq * 32 + w) * 9216), sFBr = nr(WS_FB, hp) + (((2 * mt + hf) & 15) * 4 + kq) * 128;
  const unsigned r64 = ((sFBr) >> 9) & 15, gw4 = ((bid * 4 + w) * 4) & 4095;
  const unsigned vY2 = nr(WS_O + kq * 4 * MB + r64 * 262144, hp) + b * 2048 + lane * 16, sHl15 = sHl << 15;
  const unsigned hi = lane >> 5, i32 = lane & 31, h = kq * 8 + sHl, kvh = h >> 1;
  const unsigned p4 = lane & 3, q4 = (lane >> 2) & 3, sub = (lane >> 4) & 1, r8 = (lane >> 2) & 7, sL = ldsb;
  const unsigned vKrd = ldsb + 2048 * ((lane >> 3) & 1) + 64 * (lane & 7) + 16 * ((lane >> 4) ^ ((lane >> 1) & 2));
  const unsigned vVr = ldsb + 0x10000 + 2048 * hi + 64 * (4 * sub + q4) + 32 * sub + 8 * p4;
  const unsigned vTr = ldsb + 0x10000 + 2048 * (lane >> 4) + 64 * q4 + 16 * p4;
  const unsigned vVs0 = 256 * r8 + 64 * hi + 16 * (p4 ^ (2 * sub)) + 4096, vVs1 = vVs0 + 128 - 1024;
  const unsigned vWs = 8192 * r8 + 64 * hi + 16 * p4;
  const unsigned vX = lane * 16;
  const unsigned vAS = ldsb + 0x24000 + 64 * (lane >> 4);
  const unsigned gi = 36 * w + (lane < 36 ? lane : 35);
  const unsigned vMod = 16 * gi, vTw = ldsb + 0x24000 + (gi >> 1) * 64 + 16 * (gi & 1);
  const unsigned ci = 32 * w + i32, ts = ci >> 4, vTw2 = ldsb + 0x24000 + 16 * ci + 8 * hi;
  const unsigned vMod2 = (ts < 7 ? ts : min(HSTEP + (h >> 1), 17u)) * 128 + ((ci >> 2) & 3) * 32 + (ci & 1) * 16 + ((ci & 2) ? 0 : 2304) + hi * 27648;
  const unsigned vPre = lane < 12 ? 4096 + ((((h & 1) * 4 + (jj >> 3)) * 4 + w) * 12 + lane) * 128 : OOB, l56 = lane < 56 ? 0 : OOB;
  const unsigned vXp = (16384 + (lane * 8 + sHl) * 128) | l56, wl = lane * 16 + (jj >> 3) * 4 + w, vWt = (4096 + (128 + (wl >> 1)) * 8192 + h * 256 + (wl & 1) * 128) | l56;
  const srd_t sK = make_srd((const char*)K + (size_t)kvh * S_ * 256 - 4096, S_ * 256 + 4096), sV = make_srd((const char*)V + (size_t)kvh * S_ * 256 - 4096, S_ * 256 + 4096);
  const srd_t sW = make_srd((const char*)wq - 4096, D_ * QD * 2 + 4096);
  const unsigned sLw = ldsb + 4096 * w, sKV = 4096 * w, sWo = w * 16 * 8192 + h * 256, sXo = WS_XN + (rb * 4 + w) * 36 * 2048, sOo = nr(WS_O + kq * 4 * MB + ((rb * 4 + w) & 15) * 262144, hp) + sHl15;
  const unsigned sDesc = WS_XN + XN_DESC + bid * 1024 + w * 256, sHsh = 16 + xcd;
  const unsigned gw = bid * 4 + w, spkb = (gw & 3) + 4 * ((gw >> 5) & 15), spmb = (xcd & 3) + 4 * (jj >> 4);
  const unsigned mi = gw * 64 + lane, vMs = mi * 4, vMd = mi < 3456 ? vMs + 4 * 13824 : 0x7f000000u;
  const bool isS = mi >= 3 * D_ && mi < 4 * D_, isC = mi >= 4 * D_ && mi < 5 * D_;
  const unsigned vTab = (mi * 2 + (isC ? 5 * 13824 - 8 * D_ : 5 * 13824 + 2304 - 6 * D_)) | (isC || isS ? 0 : OOB); const float vOne = isC ? 1.f : 0.f;
  const unsigned vSp = (8 * ((lane & 15) >> 2) + (lane & 3)) * 4096 + (lane >> 4) * 16;
  const unsigned sSpo = (((spmb >> 1) * 32 + 4 * (spmb & 1)) * 4096 + spkb * 64), sSpd = (nr(WS_SP, hp) + spkb * 16384 + spmb * 1024);
  const unsigned sZe = ZOLD ? WS_Y + gw * 1024 : nr(WS_Y, hp) + ((xcd & 3) * 16 + (jj >> 1)) * 16384 + (jj & 1) * 4096 + w * 1024, sFAw = WS_FA + gw * 4, sFAr = WS_FA + w * 1024, sFBw = nr(WS_FB, hp) + (((rb * 4 + w) & 15) * 4 + kq) * 128 + sHl * 4;
  const unsigned sOl = ldsb + (((hf ^ jj) & 1) ? DUMP : 0x20000 + b * 16384), sYl = ldsb + 0x20000 + hf * 16384 + b * 2048;
  const srd_t sSp = make_srd(w_sp, S_ * S_ * 2); const char* sOp = (const char*)out + gw * 2304;
  const unsigned sTch = s2Xo + ((jj >> 3) * 2 + (jj & 1)) * 36864, sPo = WS_PAR + gw * 128;
  asm volatile(
R"(L_pb1:
s_nop 4
.p2align 3
s_waitcnt lgkmcnt(0)
s_add_u32 s57, s57, 4096
buffer_load_dwordx4 v[176:179], %[vX], %[sWS], s57 offen offset:0
buffer_load_dwordx4 v[180:183], %[vX], %[sWS], s57 offen offset:1024
buffer_load_dwordx4 v[184:187], %[vX], %[sWS], s57 offen offset:2048
buffer_load_dwordx4 v[188:191], %[vX], %[sWS], s57 offen offset:3072
s_bfe_u32 s70, s32, 327688
s_lshl_b32 s96, s70, 11
s_add_u32 s96, s96, s57
s_add_u32 s96, s96, 24576
s_cmp_lt_u32 s70, 22
s_cselect_b32 s96, s96, 0x7f000000
buffer_load_dwordx4 v[72:75], %[vX], %[sWS], s96 offen offset:0
buffer_load_dwordx4 v[76:79], %[vX], %[sWS], s96 offen offset:1024
s_add_u32 m0, %[sLw], 16384
s_add_u32 s60, s32, 528384
buffer_load_dwordx4 %[vWs], %[sW], s60 offen offset:0 lds
s_add_u32 s60, s32, 527488
buffer_load_dwordx4 %[vWs], %[sW], s60 offen offset:1024 lds
s_add_u32 s60, s32, 591872
buffer_load_dwordx4 %[vWs], %[sW], s60 offen offset:2048 lds
s_add_u32 s60, s32, 590976
buffer_load_dwordx4 %[vWs], %[sW], s60 offen offset:3072 lds
v_subrev_u32 v28, 65536, %[vTr]
v_add_u32 v29, 65536, %[vTr]
s_mov_b32 s54, 0x7d800000
s_mov_b32 s55, 0x42d00000
s_mov_b32 s59, 0
s_mov_b32 s69, 1048576
s_waitcnt vmcnt(14)
s_mov_b32 s61, 0x3e0293ee
v_cmp_gt_i32 vcc, 0, v68
v_add_f32 v32, v32, v36
v_add_f32 v33, v33, v37
v_add_f32 v34, v34, v38
v_add_f32_e64 v35, v35, v39
v_bfe_i32 v36, %[vX], 5, 1
v_bfi_b32 v36, v36, 0, s61
v_permlane32_swap_b32 v32, v34
v_permlane32_swap_b32 v33, v35
s_nop 0
v_add_f32 v32, v32, v34
v_add_f32_e64 v33, v33, v35
v_fma_f32 v32, v32, s61, v36
v_fma_f32 v33, v33, s61, v36
ds_write_b64 %[vTw2], v[32:33] offset:0
s_lshr_b32 s72, vcc_lo, s47
s_and_b32 s72, s72, 1
s_cmp_lg_u32 s72, s76
s_cbranch_scc1 L_half_%=
L_halfok_%=:
s_waitcnt lgkmcnt(0)
s_barrier
ds_read_b128 v[80:83], %[vAS] offset:1792
ds_read_b128 v[84:87], %[vAS] offset:1808
ds_read_b128 v[88:91], %[vAS] offset:1824
ds_read_b128 v[92:95], %[vAS] offset:1840
ds_read_b128 v[240:243], %[vAS] offset:0
ds_read_b128 v[248:251], %[vAS] offset:32
ds_read_b128 v[244:247], %[vAS] offset:16
ds_read_b128 v[252:255], %[vAS] offset:48
ds_read_b64_tr_b16 a[128:129], v28 offset:0
ds_read_b64_tr_b16 a[130:131], v28 offset:256
ds_read_b64_tr_b16 a[132:133], v28 offset:8
ds_read_b64_tr_b16 a[134:135], v28 offset:264
ds_read_b64_tr_b16 a[136:137], v28 offset:512
ds_read_b64_tr_b16 a[138:139], v28 offset:768
ds_read_b64_tr_b16 a[140:141], v28 offset:520
ds_read_b64_tr_b16 a[142:143], v28 offset:776
ds_read_b64_tr_b16 a[144:145], v28 offset:1024
ds_read_b64_tr_b16 a[146:147], v28 offset:1280
ds_read_b64_tr_b16 a[148:149], v28 offset:1032
ds_read_b64_tr_b16 a[150:151], v28 offset:1288
ds_read_b64_tr_b16 a[152:153], v28 offset:1536
ds_read_b64_tr_b16 a[154:155], v28 offset:1792
ds_read_b64_tr_b16 a[156:157], v28 offset:1544
ds_read_b64_tr_b16 a[158:159], v28 offset:1800
s_add_u32 s57, s57, 4096
buffer_load_dwordx4 v[192:195], %[vX], %[sWS], s57 offen offset:0
buffer_load_dwordx4 v[196:199], %[vX], %[sWS], s57 offen offset:1024
buffer_load_dwordx4 v[200:203], %[vX], %[sWS], s57 offen offset:2048
buffer_load_dwordx4 v[204:207], %[vX], %[sWS], s57 offen offset:3072
s_waitcnt vmcnt(8) lgkmcnt(15)
v_fma_mix_f32 v96, v72, v80, v88 op_sel_hi:[1,0,0]
v_fma_mix_f32 v97, v72, v81, v89 op_sel:[1,0,0] op_sel_hi:[1,0,0]
v_fma_mix_f32 v98, v73, v82, v90 op_sel_hi:[1,0,0]
v_fma_mix_f32 v99, v73, v83, v91 op_sel:[1,0,0] op_sel_hi:[1,0,0]
v_fma_mix_f32 v100, v74, v84, v92 op_sel_hi:[1,0,0]
v_fma_mix_f32 v101, v74, v85, v93 op_sel:[1,0,0] op_sel_hi:[1,0,0]
v_fma_mix_f32 v102, v75, v86, v94 op_sel_hi:[1,0,0]
v_fma_mix_f32 v103, v75, v87, v95 op_sel:[1,0,0] op_sel_hi:[1,0,0]
v_cvt_pk_bf16_f32 v72, v96, v97
v_cvt_pk_bf16_f32 v73, v98, v99
v_cvt_pk_bf16_f32 v74, v100, v101
v_cvt_pk_bf16_f32 v75, v102, v103
buffer_store_dwordx4 v[72:75], %[vX], %[sWS], s96 offen offset:0 sc1
v_fma_mix_f32 v96, v76, v80, v88 op_sel_hi:[1,0,0]
v_fma_mix_f32 v97, v76, v81, v89 op_sel:[1,0,0] op_sel_hi:[1,0,0]
v_fma_mix_f32 v98, v77, v82, v90 op_sel_hi:[1,0,0]
v_fma_mix_f32 v99, v77, v83, v91 op_sel:[1,0,0] op_sel_hi:[1,0,0]
v_fma_mix_f32 v100, v78, v84, v92 op_sel_hi:[1,0,0]
v_fma_mix_f32 v101, v78, v85, v93 op_sel:[1,0,0] op_sel_hi:[1,0,0]
v_fma_mix_f32 v102, v79, v86, v94 op_sel_hi:[1,0,0]
v_fma_mix_f32 v103, v79, v87, v95 op_sel:[1,0,0] op_sel_hi:[1,0,0]
v_cvt_pk_bf16_f32 v76, v96, v97
v_cvt_pk_bf16_f32 v77, v98, v99
v_cvt_pk_bf16_f32 v78, v100, v101
v_cvt_pk_bf16_f32 v79, v102, v103
buffer_store_dwordx4 v[76:79], %[vX], %[sWS], s96 offen offset:1024 sc1
v_fma_mix_f32 v24, v160, v240, v248 op_sel_hi:[1,0,0]
v_fma_mix_f32 v25, v160, v241, v249 op_sel:[1,0,0] op_sel_hi:[1,0,0]
v_cvt_pk_bf16_f32 v160, v24, v25
v_fma_mix_f32 v26, v164, v240, v248 op_sel_hi:[1,0,0]
v_fma_mix_f32 v27, v164, v241, v249 op_sel:[1,0,0] op_sel_hi:[1,0,0]
v_cvt_pk_bf16_f32 v164, v26, v27
v_fma_mix_f32 v24, v168, v240, v248 op_sel_hi:[1,0,0]
v_fma_mix_f32 v25, v168, v241, v249 op_sel:[1,0,0] op_sel_hi:[1,0,0]
v_cvt_pk_bf16_f32 v168, v24, v25
v_fma_mix_f32 v26, v172, v240, v248 op_sel_hi:[1,0,0]
v_fma_mix_f32 v27, v172, v241, v249 op_sel:[1,0,0] op_sel_hi:[1,0,0]
v_cvt_pk_bf16_f32 v172, v26, v27
v_fma_mix_f32 v24, v161, v242, v250 op_sel_hi:[1,0,0]
v_fma_mix_f32 v25, v161, v243, v251 op_sel:[1,0,0] op_sel_hi:[1,0,0]
v_cvt_pk_bf16_f32 v161, v24, v25
v_fma_mix_f32 v26, v165, v242, v250 op_sel_hi:[1,0,0]
v_fma_mix_f32 v27, v165, v243, v251 op_sel:[1,0,0] op_sel_hi:[1,0,0]
v_cvt_pk_bf16_f32 v165, v26, v27
v_fma_mix_f32 v24, v169, v242, v250 op_sel_hi:[1,0,0]
v_fma_mix_f32 v25, v169, v243, v251 op_sel:[1,0,0] op_sel_hi:[1,0,0]
v_cvt_pk_bf16_f32 v169, v24, v25
v_fma_mix_f32 v26, v173, v242, v250 op_sel_hi:[1,0,0]
v_fma_mix_f32 v27, v173, v243, v251 op_sel:[1,0,0] op_sel_hi:[1,0,0]
v_cvt_pk_bf16_f32 v173, v26, v27
ds_read_b128 v[240:243], %[vAS] offset:256
ds_read_b128 v[248:251], %[vAS] offset:288
v_fma_mix_f32 v24, v162, v244, v252 op_sel_hi:[1,0,0]
v_fma_mix_f32 v25, v162, v245, v253 op_sel:[1,0,0] op_sel_hi:[1,0,0]
v_cvt_pk_bf16_f32 v162, v24, v25
v_fma_mix_f32 v26, v166, v244, v252 op_sel_hi:[1,0,0]
v_fma_mix_f32 v27, v166, v245, v253 op_sel:[1,0,0] op_sel_hi:[1,0,0]
v_cvt_pk_bf16_f32 v166, v26, v27
v_fma_mix_f32 v24, v170, v244, v252 op_sel_hi:[1,0,0]
v_fma_mix_f32 v25, v170, v245, v253 op_sel:[1,0,0] op_sel_hi:[1,0,0]
v_cvt_pk_bf16_f32 v170, v24, v25
v_fma_mix_f32 v26, v174, v244, v252 op_sel_hi:[1,0,0]
v_fma_mix_f32 v27, v174, v245, v253 op_sel:[1,0,0] op_sel_hi:[1,0,0]
v_cvt_pk_bf16_f32 v174, v26, v27
v_fma_mix_f32 v24, v163, v246, v254 op_sel_hi:[1,0,0]
v_fma_mix_f32 v25, v163, v247, v255 op_sel:[1,0,0] op_sel_hi:[1,0,0]
v_cvt_pk_bf16_f32 v163, v24, v25
v_fma_mix_f32 v26, v167, v246, v254 op_sel_hi:[1,0,0]
v_fma_mix_f32 v27, v167, v247, v255 op_sel:[1,0,0] op_sel_hi:[1,0,0]
v_cvt_pk_bf16_f32 v167, v26, v27
v_fma_mix_f32 v24, v171, v246, v254 op_sel_hi:[1,0,0]
v_fma_mix_f32 v25, v171, v247, v255 op_sel:[1,0,0] op_sel_hi:[1,0,0]
v_cvt_pk_bf16_f32 v171, v24, v25
v_fma_mix_f32 v26, v175, v246, v254 op_sel_hi:[1,0,0]
v_fma_mix_f32 v27, v175, v247, v255 op_sel:[1,0,0] op_sel_hi:[1,0,0]
v_cvt_pk_bf16_f32 v175, v26, v27
s_waitcnt lgkmcnt(2)
v_mfma_f32_16x16x32_bf16 v[32:35], a[128:131], v[160:163], 0
s_waitcnt lgkmcnt(0)
v_fma_mix_f32 v24, v176, v240, v248 op_sel_hi:[1,0,0]
v_fma_mix_f32 v25, v176, v241, v249 op_sel:[1,0,0] op_sel_hi:[1,0,0]
v_mfma_f32_16x16x32_bf16 v[40:43], a[128:131], v[164:167], 0
ds_read_b128 v[244:247], %[vAS] offset:272
v_cvt_pk_bf16_f32 v176, v24, v25
v_mfma_f32_16x16x32_bf16 v[48:51], a[128:131], v[168:171], 0
ds_read_b128 v[252:255], %[vAS] offset:304
v_fma_mix_f32 v26, v180, v240, v248 op_sel_hi:[1,0,0]
v_fma_mix_f32 v27, v180, v241, v249 op_sel:[1,0,0] op_sel_hi:[1,0,0]
v_mfma_f32_16x16x32_bf16 v[56:59], a[128:131], v[172:175], 0
s_add_u32 s57, s57, 4096
buffer_load_dwordx4 v[208:211], %[vX], %[sWS], s57 offen offset:0
v_cvt_pk_bf16_f32 v180, v26, v27
v_mfma_f32_16x16x32_bf16 v[36:39], a[132:135], v[160:163], 0
ds_read_b64_tr_b16 a[160:161], v28 offset:8192
v_fma_mix_f32 v24, v184, v240, v248 op_sel_hi:[1,0,0]
v_fma_mix_f32 v25, v184, v241, v249 op_sel:[1,0,0] op_sel_hi:[1,0,0]
v_mfma_f32_16x16x32_bf16 v[44:47], a[132:135], v[164:167], 0
ds_read_b64_tr_b16 a[162:163], v28 offset:8448
v_cvt_pk_bf16_f32 v184, v24, v25
v_mfma_f32_16x16x32_bf16 v[52:55], a[132:135], v[168:171], 0
ds_read_b64_tr_b16 a[164:165], v28 offset:8200
v_fma_mix_f32 v26, v188, v240, v248 op_sel_hi:[1,0,0]
v_fma_mix_f32 v27, v188, v241, v249 op_sel:[1,0,0] op_sel_hi:[1,0,0]
v_mfma_f32_16x16x32_bf16 v[60:63], a[132:135], v[172:175], 0
buffer_load_dwordx4 v[212:215], %[vX], %[sWS], s57 offen offset:1024
v_cvt_pk_bf16_f32 v188, v26, v27
v_mfma_f32_16x16x32_bf16 v[64:67], a[136:139], v[160:163], 0
ds_read_b64_tr_b16 a[166:167], v28 offset:8456
v_fma_mix_f32 v24, v177, v242, v250 op_sel_hi:[1,0,0]
v_fma_mix_f32 v25, v177, v243, v251 op_sel:[1,0,0] op_sel_hi:[1,0,0]
v_mfma_f32_16x16x32_bf16 v[72:75], a[136:139], v[164:167], 0
ds_read_b64_tr_b16 a[168:169], v28 offset:8704
v_cvt_pk_bf16_f32 v177, v24, v25
v_mfma_f32_16x16x32_bf16 v[80:83], a[136:139], v[168:171], 0
)"
R"(ds_read_b64_tr_b16 a[170:171], v28 offset:8960
v_fma_mix_f32 v26, v181, v242, v250 op_sel_hi:[1,0,0]
v_fma_mix_f32 v27, v181, v243, v251 op_sel:[1,0,0] op_sel_hi:[1,0,0]
v_mfma_f32_16x16x32_bf16 v[88:91], a[136:139], v[172:175], 0
buffer_load_dwordx4 v[216:219], %[vX], %[sWS], s57 offen offset:2048
v_cvt_pk_bf16_f32 v181, v26, v27
v_mfma_f32_16x16x32_bf16 v[68:71], a[140:143], v[160:163], 0
s_and_b32 s72, vcc_lo, 0x7fff
ds_read_b64_tr_b16 a[172:173], v28 offset:8712
v_fma_mix_f32 v24, v185, v242, v250 op_sel_hi:[1,0,0]
v_fma_mix_f32 v25, v185, v243, v251 op_sel:[1,0,0] op_sel_hi:[1,0,0]
v_mfma_f32_16x16x32_bf16 v[76:79], a[140:143], v[164:167], 0
ds_read_b64_tr_b16 a[174:175], v28 offset:8968
v_cvt_pk_bf16_f32 v185, v24, v25
v_mfma_f32_16x16x32_bf16 v[84:87], a[140:143], v[168:171], 0
s_bcnt1_i32_b32 s72, s72
ds_read_b64_tr_b16 a[176:177], v28 offset:9216
v_fma_mix_f32 v26, v189, v242, v250 op_sel_hi:[1,0,0]
v_fma_mix_f32 v27, v189, v243, v251 op_sel:[1,0,0] op_sel_hi:[1,0,0]
v_mfma_f32_16x16x32_bf16 v[92:95], a[140:143], v[172:175], 0
buffer_load_dwordx4 v[220:223], %[vX], %[sWS], s57 offen offset:3072
v_cvt_pk_bf16_f32 v189, v26, v27
v_mfma_f32_16x16x32_bf16 v[96:99], a[144:147], v[160:163], 0
s_lshl_b32 s73, s76, 13
s_cmp_gt_u32 s72, 7
s_cselect_b32 s71, 0x2000, 0
s_waitcnt lgkmcnt(9)
v_fma_mix_f32 v24, v178, v244, v252 op_sel_hi:[1,0,0]
v_fma_mix_f32 v25, v178, v245, v253 op_sel:[1,0,0] op_sel_hi:[1,0,0]
v_mfma_f32_16x16x32_bf16 v[104:107], a[144:147], v[164:167], 0
ds_read_b64_tr_b16 a[178:179], v28 offset:9472
v_cvt_pk_bf16_f32 v178, v24, v25
v_mfma_f32_16x16x32_bf16 v[112:115], a[144:147], v[168:171], 0
s_xor_b32 s71, s71, s73
ds_read_b64_tr_b16 a[180:181], v28 offset:9224
v_fma_mix_f32 v26, v182, v244, v252 op_sel_hi:[1,0,0]
v_fma_mix_f32 v27, v182, v245, v253 op_sel:[1,0,0] op_sel_hi:[1,0,0]
v_mfma_f32_16x16x32_bf16 v[120:123], a[144:147], v[172:175], 0
s_add_u32 s57, s57, 4096
buffer_load_dwordx4 v[224:227], %[vX], %[sWS], s57 offen offset:0
v_cvt_pk_bf16_f32 v182, v26, v27
v_mfma_f32_16x16x32_bf16 v[100:103], a[148:151], v[160:163], 0
ds_read_b64_tr_b16 a[182:183], v28 offset:9480
v_fma_mix_f32 v24, v186, v244, v252 op_sel_hi:[1,0,0]
v_fma_mix_f32 v25, v186, v245, v253 op_sel:[1,0,0] op_sel_hi:[1,0,0]
v_mfma_f32_16x16x32_bf16 v[108:111], a[148:151], v[164:167], 0
ds_read_b64_tr_b16 a[184:185], v28 offset:9728
v_cvt_pk_bf16_f32 v186, v24, v25
v_mfma_f32_16x16x32_bf16 v[116:119], a[148:151], v[168:171], 0
ds_read_b64_tr_b16 a[186:187], v28 offset:9984
v_fma_mix_f32 v26, v190, v244, v252 op_sel_hi:[1,0,0]
v_fma_mix_f32 v27, v190, v245, v253 op_sel:[1,0,0] op_sel_hi:[1,0,0]
v_mfma_f32_16x16x32_bf16 v[124:127], a[148:151], v[172:175], 0
buffer_load_dwordx4 v[228:231], %[vX], %[sWS], s57 offen offset:1024
v_cvt_pk_bf16_f32 v190, v26, v27
v_mfma_f32_16x16x32_bf16 v[128:131], a[152:155], v[160:163], 0
ds_read_b64_tr_b16 a[188:189], v28 offset:9736
v_fma_mix_f32 v24, v179, v246, v254 op_sel_hi:[1,0,0]
v_fma_mix_f32 v25, v179, v247, v255 op_sel:[1,0,0] op_sel_hi:[1,0,0]
v_mfma_f32_16x16x32_bf16 v[136:139], a[152:155], v[164:167], 0
ds_read_b64_tr_b16 a[190:191], v28 offset:9992
v_cvt_pk_bf16_f32 v179, v24, v25
v_mfma_f32_16x16x32_bf16 v[144:147], a[152:155], v[168:171], 0
ds_read_b128 v[240:243], %[vAS] offset:512
v_fma_mix_f32 v26, v183, v246, v254 op_sel_hi:[1,0,0]
v_fma_mix_f32 v27, v183, v247, v255 op_sel:[1,0,0] op_sel_hi:[1,0,0]
v_mfma_f32_16x16x32_bf16 v[152:155], a[152:155], v[172:175], 0
buffer_load_dwordx4 v[232:235], %[vX], %[sWS], s57 offen offset:2048
v_cvt_pk_bf16_f32 v183, v26, v27
v_mfma_f32_16x16x32_bf16 v[132:135], a[156:159], v[160:163], 0
ds_read_b128 v[248:251], %[vAS] offset:544
v_fma_mix_f32 v24, v187, v246, v254 op_sel_hi:[1,0,0]
v_fma_mix_f32 v25, v187, v247, v255 op_sel:[1,0,0] op_sel_hi:[1,0,0]
v_mfma_f32_16x16x32_bf16 v[140:143], a[156:159], v[164:167], 0
v_cvt_pk_bf16_f32 v187, v24, v25
v_mfma_f32_16x16x32_bf16 v[148:151], a[156:159], v[168:171], 0
v_fma_mix_f32 v26, v191, v246, v254 op_sel_hi:[1,0,0]
v_fma_mix_f32 v27, v191, v247, v255 op_sel:[1,0,0] op_sel_hi:[1,0,0]
v_mfma_f32_16x16x32_bf16 v[156:159], a[156:159], v[172:175], 0
buffer_load_dwordx4 v[236:239], %[vX], %[sWS], s57 offen offset:3072
v_cvt_pk_bf16_f32 v191, v26, v27
s_waitcnt vmcnt(14) lgkmcnt(2)
s_barrier
v_mfma_f32_16x16x32_bf16 v[32:35], a[160:163], v[176:179], v[32:35]
s_waitcnt vmcnt(10) lgkmcnt(0)
v_fma_mix_f32 v24, v192, v240, v248 op_sel_hi:[1,0,0]
v_fma_mix_f32 v25, v192, v241, v249 op_sel:[1,0,0] op_sel_hi:[1,0,0]
v_mfma_f32_16x16x32_bf16 v[40:43], a[160:163], v[180:183], v[40:43]
ds_read_b128 v[244:247], %[vAS] offset:528
v_cvt_pk_bf16_f32 v192, v24, v25
v_mfma_f32_16x16x32_bf16 v[48:51], a[160:163], v[184:187], v[48:51]
ds_read_b128 v[252:255], %[vAS] offset:560
v_fma_mix_f32 v26, v196, v240, v248 op_sel_hi:[1,0,0]
v_fma_mix_f32 v27, v196, v241, v249 op_sel:[1,0,0] op_sel_hi:[1,0,0]
v_mfma_f32_16x16x32_bf16 v[56:59], a[160:163], v[188:191], v[56:59]
s_add_u32 s57, s57, 4096
buffer_load_dwordx4 v[160:163], %[vX], %[sWS], s57 offen offset:0
v_cvt_pk_bf16_f32 v196, v26, v27
v_mfma_f32_16x16x32_bf16 v[36:39], a[164:167], v[176:179], v[36:39]
ds_read_b64_tr_b16 a[128:129], v28 offset:16384
v_fma_mix_f32 v24, v200, v240, v248 op_sel_hi:[1,0,0]
v_fma_mix_f32 v25, v200, v241, v249 op_sel:[1,0,0] op_sel_hi:[1,0,0]
v_mfma_f32_16x16x32_bf16 v[44:47], a[164:167], v[180:183], v[44:47]
ds_read_b64_tr_b16 a[130:131], v28 offset:16640
v_cvt_pk_bf16_f32 v200, v24, v25
v_mfma_f32_16x16x32_bf16 v[52:55], a[164:167], v[184:187], v[52:55]
ds_read_b64_tr_b16 a[132:133], v28 offset:16392
v_fma_mix_f32 v26, v204, v240, v248 op_sel_hi:[1,0,0]
v_fma_mix_f32 v27, v204, v241, v249 op_sel:[1,0,0] op_sel_hi:[1,0,0]
v_mfma_f32_16x16x32_bf16 v[60:63], a[164:167], v[188:191], v[60:63]
buffer_load_dwordx4 v[164:167], %[vX], %[sWS], s57 offen offset:1024
v_cvt_pk_bf16_f32 v204, v26, v27
v_mfma_f32_16x16x32_bf16 v[64:67], a[168:171], v[176:179], v[64:67]
ds_read_b64_tr_b16 a[134:135], v28 offset:16648
v_fma_mix_f32 v24, v193, v242, v250 op_sel_hi:[1,0,0]
v_fma_mix_f32 v25, v193, v243, v251 op_sel:[1,0,0] op_sel_hi:[1,0,0]
v_mfma_f32_16x16x32_bf16 v[72:75], a[168:171], v[180:183], v[72:75]
ds_read_b64_tr_b16 a[136:137], v28 offset:16896
v_cvt_pk_bf16_f32 v193, v24, v25
v_mfma_f32_16x16x32_bf16 v[80:83], a[168:171], v[184:187], v[80:83]
ds_read_b64_tr_b16 a[138:139], v28 offset:17152
v_fma_mix_f32 v26, v197, v242, v250 op_sel_hi:[1,0,0]
v_fma_mix_f32 v27, v197, v243, v251 op_sel:[1,0,0] op_sel_hi:[1,0,0]
v_mfma_f32_16x16x32_bf16 v[88:91], a[168:171], v[188:191], v[88:91]
buffer_load_dwordx4 v[168:171], %[vX], %[sWS], s57 offen offset:2048
v_cvt_pk_bf16_f32 v197, v26, v27
v_mfma_f32_16x16x32_bf16 v[68:71], a[172:175], v[176:179], v[68:71]
ds_read_b64_tr_b16 a[140:141], v28 offset:16904
v_fma_mix_f32 v24, v201, v242, v250 op_sel_hi:[1,0,0]
v_fma_mix_f32 v25, v201, v243, v251 op_sel:[1,0,0] op_sel_hi:[1,0,0]
v_mfma_f32_16x16x32_bf16 v[76:79], a[172:175], v[180:183], v[76:79]
ds_read_b64_tr_b16 a[142:143], v28 offset:17160
v_cvt_pk_bf16_f32 v201, v24, v25
v_mfma_f32_16x16x32_bf16 v[84:87], a[172:175], v[184:187], v[84:87]
ds_read_b64_tr_b16 a[144:145], v28 offset:17408
v_fma_mix_f32 v26, v205, v242, v250 op_sel_hi:[1,0,0]
v_fma_mix_f32 v27, v205, v243, v251 op_sel:[1,0,0] op_sel_hi:[1,0,0]
v_mfma_f32_16x16x32_bf16 v[92:95], a[172:175], v[188:191], v[92:95]
buffer_load_dwordx4 v[172:175], %[vX], %[sWS], s57 offen offset:3072
v_cvt_pk_bf16_f32 v205, v26, v27
v_mfma_f32_16x16x32_bf16 v[96:99], a[176:179], v[176:179], v[96:99]
s_waitcnt lgkmcnt(9)
v_fma_mix_f32 v24, v194, v244, v252 op_sel_hi:[1,0,0]
v_fma_mix_f32 v25, v194, v245, v253 op_sel:[1,0,0] op_sel_hi:[1,0,0]
v_mfma_f32_16x16x32_bf16 v[104:107], a[176:179], v[180:183], v[104:107]
s_xor_b32 s70, s45, s71
s_waitcnt vmcnt(12)
buffer_store_dword %[vTr], off, %[sWS], s70 offset:32 sc1
ds_read_b64_tr_b16 a[146:147], v28 offset:17664
v_cvt_pk_bf16_f32 v194, v24, v25
v_mfma_f32_16x16x32_bf16 v[112:115], a[176:179], v[184:187], v[112:115]
s_and_b32 s70, s70, 4294966784
ds_read_b64_tr_b16 a[148:149], v28 offset:17416
v_fma_mix_f32 v26, v198, v244, v252 op_sel_hi:[1,0,0]
v_fma_mix_f32 v27, v198, v245, v253 op_sel:[1,0,0] op_sel_hi:[1,0,0]
v_mfma_f32_16x16x32_bf16 v[120:123], a[176:179], v[188:191], v[120:123]
s_add_u32 m0, %[sLw], 32768
s_add_u32 s60, s32, 1052672
buffer_load_dwordx4 %[vWs], %[sW], s60 offen offset:0 lds
v_cvt_pk_bf16_f32 v198, v26, v27
v_mfma_f32_16x16x32_bf16 v[100:103], a[180:183], v[176:179], v[100:103]
ds_read_b64_tr_b16 a[150:151], v28 offset:17672
v_fma_mix_f32 v24, v202, v244, v252 op_sel_hi:[1,0,0]
v_fma_mix_f32 v25, v202, v245, v253 op_sel:[1,0,0] op_sel_hi:[1,0,0]
v_mfma_f32_16x16x32_bf16 v[108:111], a[180:183], v[180:183], v[108:111]
ds_read_b64_tr_b16 a[152:153], v28 offset:17920
v_cvt_pk_bf16_f32 v202, v24, v25
v_mfma_f32_16x16x32_bf16 v[116:119], a[180:183], v[184:187], v[116:119]
ds_read_b64_tr_b16 a[154:155], v28 offset:18176
v_fma_mix_f32 v26, v206, v244, v252 op_sel_hi:[1,0,0]
v_fma_mix_f32 v27, v206, v245, v253 op_sel:[1,0,0] op_sel_hi:[1,0,0]
v_mfma_f32_16x16x32_bf16 v[124:127], a[180:183], v[188:191], v[124:127]
s_add_u32 s60, s32, 1051776
buffer_load_dwordx4 %[vWs], %[sW], s60 offen offset:1024 lds
v_cvt_pk_bf16_f32 v206, v26, v27
v_mfma_f32_16x16x32_bf16 v[128:131], a[184:187], v[176:179], v[128:131]
ds_read_b64_tr_b16 a[156:157], v28 offset:17928
v_fma_mix_f32 v24, v195, v246, v254 op_sel_hi:[1,0,0]
v_fma_mix_f32 v25, v195, v247, v255 op_sel:[1,0,0] op_sel_hi:[1,0,0]
v_mfma_f32_16x16x32_bf16 v[136:139], a[184:187], v[180:183], v[136:139]
ds_read_b64_tr_b16 a[158:159], v28 offset:18184
v_cvt_pk_bf16_f32 v195, v24, v25
v_mfma_f32_16x16x32_bf16 v[144:147], a[184:187], v[184:187], v[144:147]
ds_read_b128 v[240:243], %[vAS] offset:768
v_fma_mix_f32 v26, v199, v246, v254 op_sel_hi:[1,0,0]
v_fma_mix_f32 v27, v199, v247, v255 op_sel:[1,0,0] op_sel_hi:[1,0,0]
v_mfma_f32_16x16x32_bf16 v[152:155], a[184:187], v[188:191], v[152:155]
s_add_u32 s60, s32, 1116160
buffer_load_dwordx4 %[vWs], %[sW], s60 offen offset:2048 lds
v_cvt_pk_bf16_f32 v199, v26, v27
v_mfma_f32_16x16x32_bf16 v[132:135], a[188:191], v[176:179], v[132:135]
ds_read_b128 v[248:251], %[vAS] offset:800
v_fma_mix_f32 v24, v203, v246, v254 op_sel_hi:[1,0,0]
v_fma_mix_f32 v25, v203, v247, v255 op_sel:[1,0,0] op_sel_hi:[1,0,0]
v_mfma_f32_16x16x32_bf16 v[140:143], a[188:191], v[180:183], v[140:143]
v_cvt_pk_bf16_f32 v203, v24, v25
v_mfma_f32_16x16x32_bf16 v[148:151], a[188:191], v[184:187], v[148:151]
v_fma_mix_f32 v26, v207, v246, v254 op_sel_hi:[1,0,0]
v_fma_mix_f32 v27, v207, v247, v255 op_sel:[1,0,0] op_sel_hi:[1,0,0]
v_mfma_f32_16x16x32_bf16 v[156:159], a[188:191], v[188:191], v[156:159]
)"
R"(s_add_u32 s60, s32, 1115264
buffer_load_dwordx4 %[vWs], %[sW], s60 offen offset:3072 lds
v_cvt_pk_bf16_f32 v207, v26, v27
s_waitcnt lgkmcnt(2)
v_mfma_f32_16x16x32_bf16 v[32:35], a[128:131], v[192:195], v[32:35]
s_waitcnt vmcnt(13) lgkmcnt(0)
v_fma_mix_f32 v24, v208, v240, v248 op_sel_hi:[1,0,0]
v_fma_mix_f32 v25, v208, v241, v249 op_sel:[1,0,0] op_sel_hi:[1,0,0]
v_mfma_f32_16x16x32_bf16 v[40:43], a[128:131], v[196:199], v[40:43]
ds_read_b128 v[244:247], %[vAS] offset:784
v_cvt_pk_bf16_f32 v208, v24, v25
v_mfma_f32_16x16x32_bf16 v[48:51], a[128:131], v[200:203], v[48:51]
ds_read_b128 v[252:255], %[vAS] offset:816
v_fma_mix_f32 v26, v212, v240, v248 op_sel_hi:[1,0,0]
v_fma_mix_f32 v27, v212, v241, v249 op_sel:[1,0,0] op_sel_hi:[1,0,0]
v_mfma_f32_16x16x32_bf16 v[56:59], a[128:131], v[204:207], v[56:59]
s_add_u32 s57, s57, 4096
buffer_load_dwordx4 v[176:179], %[vX], %[sWS], s57 offen offset:0
v_cvt_pk_bf16_f32 v212, v26, v27
v_mfma_f32_16x16x32_bf16 v[36:39], a[132:135], v[192:195], v[36:39]
ds_read_b64_tr_b16 a[160:161], v28 offset:24576
v_fma_mix_f32 v24, v216, v240, v248 op_sel_hi:[1,0,0]
v_fma_mix_f32 v25, v216, v241, v249 op_sel:[1,0,0] op_sel_hi:[1,0,0]
v_mfma_f32_16x16x32_bf16 v[44:47], a[132:135], v[196:199], v[44:47]
ds_read_b64_tr_b16 a[162:163], v28 offset:24832
v_cvt_pk_bf16_f32 v216, v24, v25
v_mfma_f32_16x16x32_bf16 v[52:55], a[132:135], v[200:203], v[52:55]
ds_read_b64_tr_b16 a[164:165], v28 offset:24584
v_fma_mix_f32 v26, v220, v240, v248 op_sel_hi:[1,0,0]
v_fma_mix_f32 v27, v220, v241, v249 op_sel:[1,0,0] op_sel_hi:[1,0,0]
v_mfma_f32_16x16x32_bf16 v[60:63], a[132:135], v[204:207], v[60:63]
buffer_load_dwordx4 v[180:183], %[vX], %[sWS], s57 offen offset:1024
v_cvt_pk_bf16_f32 v220, v26, v27
v_mfma_f32_16x16x32_bf16 v[64:67], a[136:139], v[192:195], v[64:67]
ds_read_b64_tr_b16 a[166:167], v28 offset:24840
v_fma_mix_f32 v24, v209, v242, v250 op_sel_hi:[1,0,0]
v_fma_mix_f32 v25, v209, v243, v251 op_sel:[1,0,0] op_sel_hi:[1,0,0]
v_mfma_f32_16x16x32_bf16 v[72:75], a[136:139], v[196:199], v[72:75]
ds_read_b64_tr_b16 a[168:169], v28 offset:25088
v_cvt_pk_bf16_f32 v209, v24, v25
v_mfma_f32_16x16x32_bf16 v[80:83], a[136:139], v[200:203], v[80:83]
ds_read_b64_tr_b16 a[170:171], v28 offset:25344
v_fma_mix_f32 v26, v213, v242, v250 op_sel_hi:[1,0,0]
v_fma_mix_f32 v27, v213, v243, v251 op_sel:[1,0,0] op_sel_hi:[1,0,0]
v_mfma_f32_16x16x32_bf16 v[88:91], a[136:139], v[204:207], v[88:91]
buffer_load_dwordx4 v[184:187], %[vX], %[sWS], s57 offen offset:2048
v_cvt_pk_bf16_f32 v213, v26, v27
v_mfma_f32_16x16x32_bf16 v[68:71], a[140:143], v[192:195], v[68:71]
ds_read_b64_tr_b16 a[172:173], v28 offset:25096
v_fma_mix_f32 v24, v217, v242, v250 op_sel_hi:[1,0,0]
v_fma_mix_f32 v25, v217, v243, v251 op_sel:[1,0,0] op_sel_hi:[1,0,0]
v_mfma_f32_16x16x32_bf16 v[76:79], a[140:143], v[196:199], v[76:79]
LPa_0:
s_cbranch_execz LPa_4
ds_read_b64_tr_b16 a[174:175], v28 offset:25352
v_cvt_pk_bf16_f32 v217, v24, v25
v_mfma_f32_16x16x32_bf16 v[84:87], a[140:143], v[200:203], v[84:87]
ds_read_b64_tr_b16 a[176:177], v28 offset:25600
v_fma_mix_f32 v26, v221, v242, v250 op_sel_hi:[1,0,0]
v_fma_mix_f32 v27, v221, v243, v251 op_sel:[1,0,0] op_sel_hi:[1,0,0]
v_mfma_f32_16x16x32_bf16 v[92:95], a[140:143], v[204:207], v[92:95]
buffer_load_dwordx4 v[188:191], %[vX], %[sWS], s57 offen offset:3072
v_cvt_pk_bf16_f32 v221, v26, v27
v_mfma_f32_16x16x32_bf16 v[96:99], a[144:147], v[192:195], v[96:99]
s_waitcnt lgkmcnt(9)
v_fma_mix_f32 v24, v210, v244, v252 op_sel_hi:[1,0,0]
v_fma_mix_f32 v25, v210, v245, v253 op_sel:[1,0,0] op_sel_hi:[1,0,0]
v_mfma_f32_16x16x32_bf16 v[104:107], a[144:147], v[196:199], v[104:107]
ds_read_b64_tr_b16 a[178:179], v28 offset:25856
v_cvt_pk_bf16_f32 v210, v24, v25
v_mfma_f32_16x16x32_bf16 v[112:115], a[144:147], v[200:203], v[112:115]
ds_read_b64_tr_b16 a[180:181], v28 offset:25608
v_fma_mix_f32 v26, v214, v244, v252 op_sel_hi:[1,0,0]
v_fma_mix_f32 v27, v214, v245, v253 op_sel:[1,0,0] op_sel_hi:[1,0,0]
v_mfma_f32_16x16x32_bf16 v[120:123], a[144:147], v[204:207], v[120:123]
s_add_u32 m0, %[sLw], 65536
s_add_u32 s60, s32, 1576960
buffer_load_dwordx4 %[vWs], %[sW], s60 offen offset:0 lds
v_cvt_pk_bf16_f32 v214, v26, v27
v_mfma_f32_16x16x32_bf16 v[100:103], a[148:151], v[192:195], v[100:103]
ds_read_b64_tr_b16 a[182:183], v28 offset:25864
v_fma_mix_f32 v24, v218, v244, v252 op_sel_hi:[1,0,0]
v_fma_mix_f32 v25, v218, v245, v253 op_sel:[1,0,0] op_sel_hi:[1,0,0]
v_mfma_f32_16x16x32_bf16 v[108:111], a[148:151], v[196:199], v[108:111]
ds_read_b64_tr_b16 a[184:185], v28 offset:26112
v_cvt_pk_bf16_f32 v218, v24, v25
v_mfma_f32_16x16x32_bf16 v[116:119], a[148:151], v[200:203], v[116:119]
ds_read_b64_tr_b16 a[186:187], v28 offset:26368
v_fma_mix_f32 v26, v222, v244, v252 op_sel_hi:[1,0,0]
v_fma_mix_f32 v27, v222, v245, v253 op_sel:[1,0,0] op_sel_hi:[1,0,0]
v_mfma_f32_16x16x32_bf16 v[124:127], a[148:151], v[204:207], v[124:127]
s_add_u32 s60, s32, 1576064
buffer_load_dwordx4 %[vWs], %[sW], s60 offen offset:1024 lds
v_cvt_pk_bf16_f32 v222, v26, v27
v_mfma_f32_16x16x32_bf16 v[128:131], a[152:155], v[192:195], v[128:131]
ds_read_b64_tr_b16 a[188:189], v28 offset:26120
v_fma_mix_f32 v24, v211, v246, v254 op_sel_hi:[1,0,0]
v_fma_mix_f32 v25, v211, v247, v255 op_sel:[1,0,0] op_sel_hi:[1,0,0]
v_mfma_f32_16x16x32_bf16 v[136:139], a[152:155], v[196:199], v[136:139]
ds_read_b64_tr_b16 a[190:191], v28 offset:26376
v_cvt_pk_bf16_f32 v211, v24, v25
v_mfma_f32_16x16x32_bf16 v[144:147], a[152:155], v[200:203], v[144:147]
ds_read_b128 v[240:243], %[vAS] offset:1024
v_fma_mix_f32 v26, v215, v246, v254 op_sel_hi:[1,0,0]
v_fma_mix_f32 v27, v215, v247, v255 op_sel:[1,0,0] op_sel_hi:[1,0,0]
v_mfma_f32_16x16x32_bf16 v[152:155], a[152:155], v[204:207], v[152:155]
s_add_u32 s60, s32, 1640448
buffer_load_dwordx4 %[vWs], %[sW], s60 offen offset:2048 lds
v_cvt_pk_bf16_f32 v215, v26, v27
v_mfma_f32_16x16x32_bf16 v[132:135], a[156:159], v[192:195], v[132:135]
ds_read_b128 v[248:251], %[vAS] offset:1056
v_fma_mix_f32 v24, v219, v246, v254 op_sel_hi:[1,0,0]
v_fma_mix_f32 v25, v219, v247, v255 op_sel:[1,0,0] op_sel_hi:[1,0,0]
v_mfma_f32_16x16x32_bf16 v[140:143], a[156:159], v[196:199], v[140:143]
v_cvt_pk_bf16_f32 v219, v24, v25
v_mfma_f32_16x16x32_bf16 v[148:151], a[156:159], v[200:203], v[148:151]
v_fma_mix_f32 v26, v223, v246, v254 op_sel_hi:[1,0,0]
v_fma_mix_f32 v27, v223, v247, v255 op_sel:[1,0,0] op_sel_hi:[1,0,0]
v_mfma_f32_16x16x32_bf16 v[156:159], a[156:159], v[204:207], v[156:159]
s_add_u32 s60, s32, 1639552
buffer_load_dwordx4 %[vWs], %[sW], s60 offen offset:3072 lds
v_cvt_pk_bf16_f32 v223, v26, v27
v_and_b32 v30, 384, %[vX]
v_bfe_u32 v24, %[vX], 2, 5
v_or_b32 v30, v30, v24
buffer_load_dword v30, v30, %[sWS], s70 offen offset:32 sc1
s_waitcnt vmcnt(9) lgkmcnt(2)
s_barrier
v_mfma_f32_16x16x32_bf16 v[32:35], a[160:163], v[208:211], v[32:35]
s_waitcnt lgkmcnt(0)
v_fma_mix_f32 v24, v224, v240, v248 op_sel_hi:[1,0,0]
v_fma_mix_f32 v25, v224, v241, v249 op_sel:[1,0,0] op_sel_hi:[1,0,0]
v_mfma_f32_16x16x32_bf16 v[40:43], a[160:163], v[212:215], v[40:43]
ds_read_b128 v[244:247], %[vAS] offset:1040
v_cvt_pk_bf16_f32 v224, v24, v25
v_mfma_f32_16x16x32_bf16 v[48:51], a[160:163], v[216:219], v[48:51]
ds_read_b128 v[252:255], %[vAS] offset:1072
v_fma_mix_f32 v26, v228, v240, v248 op_sel_hi:[1,0,0]
v_fma_mix_f32 v27, v228, v241, v249 op_sel:[1,0,0] op_sel_hi:[1,0,0]
v_mfma_f32_16x16x32_bf16 v[56:59], a[160:163], v[220:223], v[56:59]
ds_read_b64_tr_b16 a[128:129], v28 offset:32768
v_cvt_pk_bf16_f32 v228, v26, v27
v_mfma_f32_16x16x32_bf16 v[36:39], a[164:167], v[208:211], v[36:39]
ds_read_b64_tr_b16 a[130:131], v28 offset:33024
v_fma_mix_f32 v24, v232, v240, v248 op_sel_hi:[1,0,0]
v_fma_mix_f32 v25, v232, v241, v249 op_sel:[1,0,0] op_sel_hi:[1,0,0]
v_mfma_f32_16x16x32_bf16 v[44:47], a[164:167], v[212:215], v[44:47]
ds_read_b64_tr_b16 a[132:133], v28 offset:32776
v_cvt_pk_bf16_f32 v232, v24, v25
v_mfma_f32_16x16x32_bf16 v[52:55], a[164:167], v[216:219], v[52:55]
ds_read_b64_tr_b16 a[134:135], v28 offset:33032
v_fma_mix_f32 v26, v236, v240, v248 op_sel_hi:[1,0,0]
v_fma_mix_f32 v27, v236, v241, v249 op_sel:[1,0,0] op_sel_hi:[1,0,0]
v_mfma_f32_16x16x32_bf16 v[60:63], a[164:167], v[220:223], v[60:63]
ds_read_b64_tr_b16 a[136:137], v28 offset:33280
v_cvt_pk_bf16_f32 v236, v26, v27
v_mfma_f32_16x16x32_bf16 v[64:67], a[168:171], v[208:211], v[64:67]
ds_read_b64_tr_b16 a[138:139], v28 offset:33536
v_fma_mix_f32 v24, v225, v242, v250 op_sel_hi:[1,0,0]
v_fma_mix_f32 v25, v225, v243, v251 op_sel:[1,0,0] op_sel_hi:[1,0,0]
v_mfma_f32_16x16x32_bf16 v[72:75], a[168:171], v[212:215], v[72:75]
ds_read_b64_tr_b16 a[140:141], v28 offset:33288
v_cvt_pk_bf16_f32 v225, v24, v25
v_mfma_f32_16x16x32_bf16 v[80:83], a[168:171], v[216:219], v[80:83]
ds_read_b64_tr_b16 a[142:143], v28 offset:33544
v_fma_mix_f32 v26, v229, v242, v250 op_sel_hi:[1,0,0]
v_fma_mix_f32 v27, v229, v243, v251 op_sel:[1,0,0] op_sel_hi:[1,0,0]
v_mfma_f32_16x16x32_bf16 v[88:91], a[168:171], v[220:223], v[88:91]
ds_read_b64_tr_b16 a[144:145], v28 offset:33792
v_cvt_pk_bf16_f32 v229, v26, v27
v_mfma_f32_16x16x32_bf16 v[68:71], a[172:175], v[208:211], v[68:71]
ds_read_b64_tr_b16 a[146:147], v28 offset:34048
v_fma_mix_f32 v24, v233, v242, v250 op_sel_hi:[1,0,0]
v_fma_mix_f32 v25, v233, v243, v251 op_sel:[1,0,0] op_sel_hi:[1,0,0]
v_mfma_f32_16x16x32_bf16 v[76:79], a[172:175], v[212:215], v[76:79]
ds_read_b64_tr_b16 a[148:149], v28 offset:33800
v_cvt_pk_bf16_f32 v233, v24, v25
v_mfma_f32_16x16x32_bf16 v[84:87], a[172:175], v[216:219], v[84:87]
ds_read_b64_tr_b16 a[150:151], v28 offset:34056
v_fma_mix_f32 v26, v237, v242, v250 op_sel_hi:[1,0,0]
v_fma_mix_f32 v27, v237, v243, v251 op_sel:[1,0,0] op_sel_hi:[1,0,0]
v_mfma_f32_16x16x32_bf16 v[92:95], a[172:175], v[220:223], v[92:95]
ds_read_b64_tr_b16 a[152:153], v28 offset:34304
v_cvt_pk_bf16_f32 v237, v26, v27
v_mfma_f32_16x16x32_bf16 v[96:99], a[176:179], v[208:211], v[96:99]
s_waitcnt lgkmcnt(13)
v_fma_mix_f32 v24, v226, v244, v252 op_sel_hi:[1,0,0]
v_fma_mix_f32 v25, v226, v245, v253 op_sel:[1,0,0] op_sel_hi:[1,0,0]
v_mfma_f32_16x16x32_bf16 v[104:107], a[176:179], v[212:215], v[104:107]
ds_read_b64_tr_b16 a[154:155], v28 offset:34560
v_cvt_pk_bf16_f32 v226, v24, v25
v_mfma_f32_16x16x32_bf16 v[112:115], a[176:179], v[216:219], v[112:115]
ds_read_b64_tr_b16 a[156:157], v28 offset:34312
v_fma_mix_f32 v26, v230, v244, v252 op_sel_hi:[1,0,0]
v_fma_mix_f32 v27, v230, v245, v253 op_sel:[1,0,0] op_sel_hi:[1,0,0]
v_mfma_f32_16x16x32_bf16 v[120:123], a[176:179], v[220:223], v[120:123]
s_add_u32 m0, %[sLw], 81920
s_add_u32 s60, s32, 2101248
buffer_load_dwordx4 %[vWs], %[sW], s60 offen offset:0 lds
v_cvt_pk_bf16_f32 v230, v26, v27
v_mfma_f32_16x16x32_bf16 v[100:103], a[180:183], v[208:211], v[100:103]
)"
R"(ds_read_b64_tr_b16 a[158:159], v28 offset:34568
v_fma_mix_f32 v24, v234, v244, v252 op_sel_hi:[1,0,0]
v_fma_mix_f32 v25, v234, v245, v253 op_sel:[1,0,0] op_sel_hi:[1,0,0]
v_mfma_f32_16x16x32_bf16 v[108:111], a[180:183], v[212:215], v[108:111]
ds_read_b128 v[240:243], %[vAS] offset:1280
v_cvt_pk_bf16_f32 v234, v24, v25
v_mfma_f32_16x16x32_bf16 v[116:119], a[180:183], v[216:219], v[116:119]
ds_read_b128 v[248:251], %[vAS] offset:1312
v_fma_mix_f32 v26, v238, v244, v252 op_sel_hi:[1,0,0]
v_fma_mix_f32 v27, v238, v245, v253 op_sel:[1,0,0] op_sel_hi:[1,0,0]
v_mfma_f32_16x16x32_bf16 v[124:127], a[180:183], v[220:223], v[124:127]
s_add_u32 s60, s32, 2100352
buffer_load_dwordx4 %[vWs], %[sW], s60 offen offset:1024 lds
v_cvt_pk_bf16_f32 v238, v26, v27
v_mfma_f32_16x16x32_bf16 v[128:131], a[184:187], v[208:211], v[128:131]
v_fma_mix_f32 v24, v227, v246, v254 op_sel_hi:[1,0,0]
v_fma_mix_f32 v25, v227, v247, v255 op_sel:[1,0,0] op_sel_hi:[1,0,0]
v_mfma_f32_16x16x32_bf16 v[136:139], a[184:187], v[212:215], v[136:139]
v_cvt_pk_bf16_f32 v227, v24, v25
v_mfma_f32_16x16x32_bf16 v[144:147], a[184:187], v[216:219], v[144:147]
v_fma_mix_f32 v26, v231, v246, v254 op_sel_hi:[1,0,0]
v_fma_mix_f32 v27, v231, v247, v255 op_sel:[1,0,0] op_sel_hi:[1,0,0]
v_mfma_f32_16x16x32_bf16 v[152:155], a[184:187], v[220:223], v[152:155]
s_add_u32 s60, s32, 2164736
buffer_load_dwordx4 %[vWs], %[sW], s60 offen offset:2048 lds
v_cvt_pk_bf16_f32 v231, v26, v27
v_mfma_f32_16x16x32_bf16 v[132:135], a[188:191], v[208:211], v[132:135]
v_fma_mix_f32 v24, v235, v246, v254 op_sel_hi:[1,0,0]
v_fma_mix_f32 v25, v235, v247, v255 op_sel:[1,0,0] op_sel_hi:[1,0,0]
v_mfma_f32_16x16x32_bf16 v[140:143], a[188:191], v[212:215], v[140:143]
v_cvt_pk_bf16_f32 v235, v24, v25
v_mfma_f32_16x16x32_bf16 v[148:151], a[188:191], v[216:219], v[148:151]
v_fma_mix_f32 v26, v239, v246, v254 op_sel_hi:[1,0,0]
v_fma_mix_f32 v27, v239, v247, v255 op_sel:[1,0,0] op_sel_hi:[1,0,0]
v_mfma_f32_16x16x32_bf16 v[156:159], a[188:191], v[220:223], v[156:159]
s_add_u32 s60, s32, 2163840
buffer_load_dwordx4 %[vWs], %[sW], s60 offen offset:3072 lds
v_cvt_pk_bf16_f32 v239, v26, v27
s_waitcnt lgkmcnt(2)
v_mfma_f32_16x16x32_bf16 v[32:35], a[128:131], v[224:227], v[32:35]
s_waitcnt lgkmcnt(0)
s_waitcnt vmcnt(4)
v_cmp_eq_u32 vcc, 0, v30
s_cbranch_vccnz L_hs_%=
L_hsok_%=:
v_fma_mix_f32 v24, v160, v240, v248 op_sel_hi:[1,0,0]
v_fma_mix_f32 v25, v160, v241, v249 op_sel:[1,0,0] op_sel_hi:[1,0,0]
v_mfma_f32_16x16x32_bf16 v[40:43], a[128:131], v[228:231], v[40:43]
ds_read_b128 v[244:247], %[vAS] offset:1296
v_cvt_pk_bf16_f32 v160, v24, v25
v_mfma_f32_16x16x32_bf16 v[48:51], a[128:131], v[232:235], v[48:51]
ds_read_b128 v[252:255], %[vAS] offset:1328
v_fma_mix_f32 v26, v164, v240, v248 op_sel_hi:[1,0,0]
v_fma_mix_f32 v27, v164, v241, v249 op_sel:[1,0,0] op_sel_hi:[1,0,0]
v_mfma_f32_16x16x32_bf16 v[56:59], a[128:131], v[236:239], v[56:59]
s_add_u32 s57, s57, 4096
buffer_load_dwordx4 v[192:195], %[vX], %[sWS], s57 offen offset:0 sc1
v_cvt_pk_bf16_f32 v164, v26, v27
v_mfma_f32_16x16x32_bf16 v[36:39], a[132:135], v[224:227], v[36:39]
ds_read_b64_tr_b16 a[160:161], v28 offset:40960
v_fma_mix_f32 v24, v168, v240, v248 op_sel_hi:[1,0,0]
v_fma_mix_f32 v25, v168, v241, v249 op_sel:[1,0,0] op_sel_hi:[1,0,0]
v_mfma_f32_16x16x32_bf16 v[44:47], a[132:135], v[228:231], v[44:47]
ds_read_b64_tr_b16 a[162:163], v28 offset:41216
v_cvt_pk_bf16_f32 v168, v24, v25
v_mfma_f32_16x16x32_bf16 v[52:55], a[132:135], v[232:235], v[52:55]
ds_read_b64_tr_b16 a[164:165], v28 offset:40968
v_fma_mix_f32 v26, v172, v240, v248 op_sel_hi:[1,0,0]
v_fma_mix_f32 v27, v172, v241, v249 op_sel:[1,0,0] op_sel_hi:[1,0,0]
v_mfma_f32_16x16x32_bf16 v[60:63], a[132:135], v[236:239], v[60:63]
buffer_load_dwordx4 v[196:199], %[vX], %[sWS], s57 offen offset:1024 sc1
v_cvt_pk_bf16_f32 v172, v26, v27
v_mfma_f32_16x16x32_bf16 v[64:67], a[136:139], v[224:227], v[64:67]
ds_read_b64_tr_b16 a[166:167], v28 offset:41224
v_fma_mix_f32 v24, v161, v242, v250 op_sel_hi:[1,0,0]
v_fma_mix_f32 v25, v161, v243, v251 op_sel:[1,0,0] op_sel_hi:[1,0,0]
v_mfma_f32_16x16x32_bf16 v[72:75], a[136:139], v[228:231], v[72:75]
ds_read_b64_tr_b16 a[168:169], v28 offset:41472
v_cvt_pk_bf16_f32 v161, v24, v25
v_mfma_f32_16x16x32_bf16 v[80:83], a[136:139], v[232:235], v[80:83]
ds_read_b64_tr_b16 a[170:171], v28 offset:41728
v_fma_mix_f32 v26, v165, v242, v250 op_sel_hi:[1,0,0]
v_fma_mix_f32 v27, v165, v243, v251 op_sel:[1,0,0] op_sel_hi:[1,0,0]
v_mfma_f32_16x16x32_bf16 v[88:91], a[136:139], v[236:239], v[88:91]
buffer_load_dwordx4 v[200:203], %[vX], %[sWS], s57 offen offset:2048 sc1
v_cvt_pk_bf16_f32 v165, v26, v27
v_mfma_f32_16x16x32_bf16 v[68:71], a[140:143], v[224:227], v[68:71]
ds_read_b64_tr_b16 a[172:173], v28 offset:41480
v_fma_mix_f32 v24, v169, v242, v250 op_sel_hi:[1,0,0]
v_fma_mix_f32 v25, v169, v243, v251 op_sel:[1,0,0] op_sel_hi:[1,0,0]
v_mfma_f32_16x16x32_bf16 v[76:79], a[140:143], v[228:231], v[76:79]
ds_read_b64_tr_b16 a[174:175], v28 offset:41736
v_cvt_pk_bf16_f32 v169, v24, v25
v_mfma_f32_16x16x32_bf16 v[84:87], a[140:143], v[232:235], v[84:87]
ds_read_b64_tr_b16 a[176:177], v28 offset:41984
v_fma_mix_f32 v26, v173, v242, v250 op_sel_hi:[1,0,0]
v_fma_mix_f32 v27, v173, v243, v251 op_sel:[1,0,0] op_sel_hi:[1,0,0]
v_mfma_f32_16x16x32_bf16 v[92:95], a[140:143], v[236:239], v[92:95]
buffer_load_dwordx4 v[204:207], %[vX], %[sWS], s57 offen offset:3072 sc1
v_cvt_pk_bf16_f32 v173, v26, v27
v_mfma_f32_16x16x32_bf16 v[96:99], a[144:147], v[224:227], v[96:99]
s_waitcnt lgkmcnt(9)
v_fma_mix_f32 v24, v162, v244, v252 op_sel_hi:[1,0,0]
v_fma_mix_f32 v25, v162, v245, v253 op_sel:[1,0,0] op_sel_hi:[1,0,0]
v_mfma_f32_16x16x32_bf16 v[104:107], a[144:147], v[228:231], v[104:107]
ds_read_b64_tr_b16 a[178:179], v28 offset:42240
v_cvt_pk_bf16_f32 v162, v24, v25
v_mfma_f32_16x16x32_bf16 v[112:115], a[144:147], v[232:235], v[112:115]
ds_read_b64_tr_b16 a[180:181], v28 offset:41992
v_fma_mix_f32 v26, v166, v244, v252 op_sel_hi:[1,0,0]
v_fma_mix_f32 v27, v166, v245, v253 op_sel:[1,0,0] op_sel_hi:[1,0,0]
v_mfma_f32_16x16x32_bf16 v[120:123], a[144:147], v[236:239], v[120:123]
s_add_u32 s57, s57, 4096
buffer_load_dwordx4 v[208:211], %[vX], %[sWS], s57 offen offset:0 sc1
v_cvt_pk_bf16_f32 v166, v26, v27
v_mfma_f32_16x16x32_bf16 v[100:103], a[148:151], v[224:227], v[100:103]
ds_read_b64_tr_b16 a[182:183], v28 offset:42248
v_fma_mix_f32 v24, v170, v244, v252 op_sel_hi:[1,0,0]
v_fma_mix_f32 v25, v170, v245, v253 op_sel:[1,0,0] op_sel_hi:[1,0,0]
v_mfma_f32_16x16x32_bf16 v[108:111], a[148:151], v[228:231], v[108:111]
ds_read_b64_tr_b16 a[184:185], v28 offset:42496
v_cvt_pk_bf16_f32 v170, v24, v25
v_mfma_f32_16x16x32_bf16 v[116:119], a[148:151], v[232:235], v[116:119]
ds_read_b64_tr_b16 a[186:187], v28 offset:42752
v_fma_mix_f32 v26, v174, v244, v252 op_sel_hi:[1,0,0]
v_fma_mix_f32 v27, v174, v245, v253 op_sel:[1,0,0] op_sel_hi:[1,0,0]
v_mfma_f32_16x16x32_bf16 v[124:127], a[148:151], v[236:239], v[124:127]
buffer_load_dwordx4 v[212:215], %[vX], %[sWS], s57 offen offset:1024 sc1
v_cvt_pk_bf16_f32 v174, v26, v27
v_mfma_f32_16x16x32_bf16 v[128:131], a[152:155], v[224:227], v[128:131]
ds_read_b64_tr_b16 a[188:189], v28 offset:42504
v_fma_mix_f32 v24, v163, v246, v254 op_sel_hi:[1,0,0]
v_fma_mix_f32 v25, v163, v247, v255 op_sel:[1,0,0] op_sel_hi:[1,0,0]
v_mfma_f32_16x16x32_bf16 v[136:139], a[152:155], v[228:231], v[136:139]
ds_read_b64_tr_b16 a[190:191], v28 offset:42760
v_cvt_pk_bf16_f32 v163, v24, v25
v_mfma_f32_16x16x32_bf16 v[144:147], a[152:155], v[232:235], v[144:147]
ds_read_b128 v[240:243], %[vAS] offset:1536
v_fma_mix_f32 v26, v167, v246, v254 op_sel_hi:[1,0,0]
v_fma_mix_f32 v27, v167, v247, v255 op_sel:[1,0,0] op_sel_hi:[1,0,0]
v_mfma_f32_16x16x32_bf16 v[152:155], a[152:155], v[236:239], v[152:155]
s_add_u32 m0, %[sLw], 98304
s_add_u32 s60, s32, 2625536
buffer_load_dwordx4 %[vWs], %[sW], s60 offen offset:0 lds
v_cvt_pk_bf16_f32 v167, v26, v27
v_mfma_f32_16x16x32_bf16 v[132:135], a[156:159], v[224:227], v[132:135]
ds_read_b128 v[248:251], %[vAS] offset:1568
v_fma_mix_f32 v24, v171, v246, v254 op_sel_hi:[1,0,0]
v_fma_mix_f32 v25, v171, v247, v255 op_sel:[1,0,0] op_sel_hi:[1,0,0]
v_mfma_f32_16x16x32_bf16 v[140:143], a[156:159], v[228:231], v[140:143]
v_cvt_pk_bf16_f32 v171, v24, v25
v_mfma_f32_16x16x32_bf16 v[148:151], a[156:159], v[232:235], v[148:151]
v_fma_mix_f32 v26, v175, v246, v254 op_sel_hi:[1,0,0]
v_fma_mix_f32 v27, v175, v247, v255 op_sel:[1,0,0] op_sel_hi:[1,0,0]
v_mfma_f32_16x16x32_bf16 v[156:159], a[156:159], v[236:239], v[156:159]
s_add_u32 s60, s32, 2624640
buffer_load_dwordx4 %[vWs], %[sW], s60 offen offset:1024 lds
v_cvt_pk_bf16_f32 v175, v26, v27
s_waitcnt lgkmcnt(2)
s_barrier
v_mfma_f32_16x16x32_bf16 v[32:35], a[160:163], v[160:163], v[32:35]
s_waitcnt lgkmcnt(0)
v_fma_mix_f32 v24, v176, v240, v248 op_sel_hi:[1,0,0]
v_fma_mix_f32 v25, v176, v241, v249 op_sel:[1,0,0] op_sel_hi:[1,0,0]
v_mfma_f32_16x16x32_bf16 v[40:43], a[160:163], v[164:167], v[40:43]
ds_read_b128 v[244:247], %[vAS] offset:1552
v_cvt_pk_bf16_f32 v176, v24, v25
v_mfma_f32_16x16x32_bf16 v[48:51], a[160:163], v[168:171], v[48:51]
ds_read_b128 v[252:255], %[vAS] offset:1584
v_fma_mix_f32 v26, v180, v240, v248 op_sel_hi:[1,0,0]
v_fma_mix_f32 v27, v180, v241, v249 op_sel:[1,0,0] op_sel_hi:[1,0,0]
v_mfma_f32_16x16x32_bf16 v[56:59], a[160:163], v[172:175], v[56:59]
buffer_load_dwordx4 v[216:219], %[vX], %[sWS], s57 offen offset:2048 sc1
v_cvt_pk_bf16_f32 v180, v26, v27
v_mfma_f32_16x16x32_bf16 v[36:39], a[164:167], v[160:163], v[36:39]
ds_read_b64_tr_b16 a[128:129], %[vTr] offset:0
v_fma_mix_f32 v24, v184, v240, v248 op_sel_hi:[1,0,0]
v_fma_mix_f32 v25, v184, v241, v249 op_sel:[1,0,0] op_sel_hi:[1,0,0]
v_mfma_f32_16x16x32_bf16 v[44:47], a[164:167], v[164:167], v[44:47]
ds_read_b64_tr_b16 a[130:131], %[vTr] offset:256
v_cvt_pk_bf16_f32 v184, v24, v25
v_mfma_f32_16x16x32_bf16 v[52:55], a[164:167], v[168:171], v[52:55]
ds_read_b64_tr_b16 a[132:133], %[vTr] offset:8
v_fma_mix_f32 v26, v188, v240, v248 op_sel_hi:[1,0,0]
v_fma_mix_f32 v27, v188, v241, v249 op_sel:[1,0,0] op_sel_hi:[1,0,0]
v_mfma_f32_16x16x32_bf16 v[60:63], a[164:167], v[172:175], v[60:63]
buffer_load_dwordx4 v[220:223], %[vX], %[sWS], s57 offen offset:3072 sc1
v_cvt_pk_bf16_f32 v188, v26, v27
v_mfma_f32_16x16x32_bf16 v[64:67], a[168:171], v[160:163], v[64:67]
ds_read_b64_tr_b16 a[134:135], %[vTr] offset:264
v_fma_mix_f32 v24, v177, v242, v250 op_sel_hi:[1,0,0]
v_fma_mix_f32 v25, v177, v243, v251 op_sel:[1,0,0] op_sel_hi:[1,0,0]
v_mfma_f32_16x16x32_bf16 v[72:75], a[168:171], v[164:167], v[72:75]
ds_read_b64_tr_b16 a[136:137], %[vTr] offset:512
v_cvt_pk_bf16_f32 v177, v24, v25
v_mfma_f32_16x16x32_bf16 v[80:83], a[168:171], v[168:171], v[80:83]
ds_read_b64_tr_b16 a[138:139], %[vTr] offset:768
v_fma_mix_f32 v26, v181, v242, v250 op_sel_hi:[1,0,0]
v_fma_mix_f32 v27, v181, v243, v251 op_sel:[1,0,0] op_sel_hi:[1,0,0]
v_mfma_f32_16x16x32_bf16 v[88:91], a[168:171], v[172:175], v[88:91]
)"
R"(s_add_u32 s57, s57, 4096
buffer_load_dwordx4 v[224:227], %[vX], %[sWS], s57 offen offset:0 sc1
v_cvt_pk_bf16_f32 v181, v26, v27
v_mfma_f32_16x16x32_bf16 v[68:71], a[172:175], v[160:163], v[68:71]
ds_read_b64_tr_b16 a[140:141], %[vTr] offset:520
v_fma_mix_f32 v24, v185, v242, v250 op_sel_hi:[1,0,0]
v_fma_mix_f32 v25, v185, v243, v251 op_sel:[1,0,0] op_sel_hi:[1,0,0]
v_mfma_f32_16x16x32_bf16 v[76:79], a[172:175], v[164:167], v[76:79]
ds_read_b64_tr_b16 a[142:143], %[vTr] offset:776
v_cvt_pk_bf16_f32 v185, v24, v25
v_mfma_f32_16x16x32_bf16 v[84:87], a[172:175], v[168:171], v[84:87]
ds_read_b64_tr_b16 a[144:145], %[vTr] offset:1024
v_fma_mix_f32 v26, v189, v242, v250 op_sel_hi:[1,0,0]
v_fma_mix_f32 v27, v189, v243, v251 op_sel:[1,0,0] op_sel_hi:[1,0,0]
v_mfma_f32_16x16x32_bf16 v[92:95], a[172:175], v[172:175], v[92:95]
buffer_load_dwordx4 v[228:231], %[vX], %[sWS], s57 offen offset:1024 sc1
v_cvt_pk_bf16_f32 v189, v26, v27
v_mfma_f32_16x16x32_bf16 v[96:99], a[176:179], v[160:163], v[96:99]
s_waitcnt lgkmcnt(9)
v_fma_mix_f32 v24, v178, v244, v252 op_sel_hi:[1,0,0]
v_fma_mix_f32 v25, v178, v245, v253 op_sel:[1,0,0] op_sel_hi:[1,0,0]
v_mfma_f32_16x16x32_bf16 v[104:107], a[176:179], v[164:167], v[104:107]
ds_read_b64_tr_b16 a[146:147], %[vTr] offset:1280
v_cvt_pk_bf16_f32 v178, v24, v25
v_mfma_f32_16x16x32_bf16 v[112:115], a[176:179], v[168:171], v[112:115]
ds_read_b64_tr_b16 a[148:149], %[vTr] offset:1032
v_fma_mix_f32 v26, v182, v244, v252 op_sel_hi:[1,0,0]
v_fma_mix_f32 v27, v182, v245, v253 op_sel:[1,0,0] op_sel_hi:[1,0,0]
v_mfma_f32_16x16x32_bf16 v[120:123], a[176:179], v[172:175], v[120:123]
buffer_load_dwordx4 v[232:235], %[vX], %[sWS], s57 offen offset:2048 sc1
v_cvt_pk_bf16_f32 v182, v26, v27
v_mfma_f32_16x16x32_bf16 v[100:103], a[180:183], v[160:163], v[100:103]
ds_read_b64_tr_b16 a[150:151], %[vTr] offset:1288
v_fma_mix_f32 v24, v186, v244, v252 op_sel_hi:[1,0,0]
v_fma_mix_f32 v25, v186, v245, v253 op_sel:[1,0,0] op_sel_hi:[1,0,0]
v_mfma_f32_16x16x32_bf16 v[108:111], a[180:183], v[164:167], v[108:111]
ds_read_b64_tr_b16 a[152:153], %[vTr] offset:1536
v_cvt_pk_bf16_f32 v186, v24, v25
v_mfma_f32_16x16x32_bf16 v[116:119], a[180:183], v[168:171], v[116:119]
ds_read_b64_tr_b16 a[154:155], %[vTr] offset:1792
v_fma_mix_f32 v26, v190, v244, v252 op_sel_hi:[1,0,0]
v_fma_mix_f32 v27, v190, v245, v253 op_sel:[1,0,0] op_sel_hi:[1,0,0]
v_mfma_f32_16x16x32_bf16 v[124:127], a[180:183], v[172:175], v[124:127]
buffer_load_dwordx4 v[236:239], %[vX], %[sWS], s57 offen offset:3072 sc1
v_cvt_pk_bf16_f32 v190, v26, v27
v_mfma_f32_16x16x32_bf16 v[128:131], a[184:187], v[160:163], v[128:131]
ds_read_b64_tr_b16 a[156:157], %[vTr] offset:1544
v_fma_mix_f32 v24, v179, v246, v254 op_sel_hi:[1,0,0]
v_fma_mix_f32 v25, v179, v247, v255 op_sel:[1,0,0] op_sel_hi:[1,0,0]
v_mfma_f32_16x16x32_bf16 v[136:139], a[184:187], v[164:167], v[136:139]
ds_read_b64_tr_b16 a[158:159], %[vTr] offset:1800
v_cvt_pk_bf16_f32 v179, v24, v25
v_mfma_f32_16x16x32_bf16 v[144:147], a[184:187], v[168:171], v[144:147]
v_fma_mix_f32 v26, v183, v246, v254 op_sel_hi:[1,0,0]
v_fma_mix_f32 v27, v183, v247, v255 op_sel:[1,0,0] op_sel_hi:[1,0,0]
v_mfma_f32_16x16x32_bf16 v[152:155], a[184:187], v[172:175], v[152:155]
s_add_u32 s60, s32, 2689024
buffer_load_dwordx4 %[vWs], %[sW], s60 offen offset:2048 lds
v_cvt_pk_bf16_f32 v183, v26, v27
v_mfma_f32_16x16x32_bf16 v[132:135], a[188:191], v[160:163], v[132:135]
v_fma_mix_f32 v24, v187, v246, v254 op_sel_hi:[1,0,0]
v_fma_mix_f32 v25, v187, v247, v255 op_sel:[1,0,0] op_sel_hi:[1,0,0]
v_mfma_f32_16x16x32_bf16 v[140:143], a[188:191], v[164:167], v[140:143]
v_cvt_pk_bf16_f32 v187, v24, v25
v_mfma_f32_16x16x32_bf16 v[148:151], a[188:191], v[168:171], v[148:151]
v_fma_mix_f32 v26, v191, v246, v254 op_sel_hi:[1,0,0]
v_fma_mix_f32 v27, v191, v247, v255 op_sel:[1,0,0] op_sel_hi:[1,0,0]
v_mfma_f32_16x16x32_bf16 v[156:159], a[188:191], v[172:175], v[156:159]
s_add_u32 s60, s32, 2688128
buffer_load_dwordx4 %[vWs], %[sW], s60 offen offset:3072 lds
v_cvt_pk_bf16_f32 v191, v26, v27
s_waitcnt lgkmcnt(0)
v_mfma_f32_16x16x32_bf16 v[32:35], a[128:131], v[176:179], v[32:35]
v_lshrrev_b32_e64 v254, 2, %[vX]
v_mfma_f32_16x16x32_bf16 v[40:43], a[128:131], v[180:183], v[40:43]
ds_read_b64_tr_b16 a[160:161], %[vTr] offset:8192
v_lshrrev_b32_e64 v255, 3, %[vX]
v_mfma_f32_16x16x32_bf16 v[48:51], a[128:131], v[184:187], v[48:51]
ds_read_b64_tr_b16 a[162:163], %[vTr] offset:8448
v_lshrrev_b32_e64 v246, 4, %[vX]
v_mfma_f32_16x16x32_bf16 v[56:59], a[128:131], v[188:191], v[56:59]
ds_read_b64_tr_b16 a[164:165], %[vTr] offset:8200
v_and_b32_e64 v246, 48, v246
v_mfma_f32_16x16x32_bf16 v[36:39], a[132:135], v[176:179], v[36:39]
ds_read_b64_tr_b16 a[166:167], %[vTr] offset:8456
v_and_b32 v244, 96, v255
v_mfma_f32_16x16x32_bf16 v[44:47], a[132:135], v[180:183], v[44:47]
ds_read_b64_tr_b16 a[168:169], %[vTr] offset:8704
v_and_b32_e64 v245, 48, %[vX]
v_mfma_f32_16x16x32_bf16 v[52:55], a[132:135], v[184:187], v[52:55]
ds_read_b64_tr_b16 a[170:171], %[vTr] offset:8960
v_lshl_or_b32 %[vSp], v245, 8, v246
v_mfma_f32_16x16x32_bf16 v[60:63], a[132:135], v[188:191], v[60:63]
ds_read_b64_tr_b16 a[172:173], %[vTr] offset:8712
v_xor_b32_e64 v244, v244, v245
v_mfma_f32_16x16x32_bf16 v[64:67], a[136:139], v[176:179], v[64:67]
ds_read_b64_tr_b16 a[174:175], %[vTr] offset:8968
v_and_b32 v245, 448, %[vX]
v_mfma_f32_16x16x32_bf16 v[72:75], a[136:139], v[180:183], v[72:75]
ds_read_b64_tr_b16 a[176:177], %[vTr] offset:9216
v_lshl_or_b32 v244, v245, 2, v244
v_mfma_f32_16x16x32_bf16 v[80:83], a[136:139], v[184:187], v[80:83]
ds_read_b64_tr_b16 a[178:179], %[vTr] offset:9472
v_add_u32 %[vVs0], 4096, v244
v_mfma_f32_16x16x32_bf16 v[88:91], a[136:139], v[188:191], v[88:91]
s_add_u32 s57, s57, 4096
buffer_load_dwordx4 v[160:163], %[vX], %[sWS], s57 offen offset:0 sc1
v_mfma_f32_16x16x32_bf16 v[68:71], a[140:143], v[176:179], v[68:71]
ds_read_b64_tr_b16 a[180:181], %[vTr] offset:9224
v_add_u32 %[vVs1], 3200, v244
v_mfma_f32_16x16x32_bf16 v[76:79], a[140:143], v[180:183], v[76:79]
ds_read_b64_tr_b16 a[182:183], %[vTr] offset:9480
v_and_b32 v244, 512, %[vX]
v_mfma_f32_16x16x32_bf16 v[84:87], a[140:143], v[184:187], v[84:87]
ds_read_b64_tr_b16 a[184:185], %[vTr] offset:9728
v_lshl_or_b32 v245, v244, 2, v245
v_mfma_f32_16x16x32_bf16 v[92:95], a[140:143], v[188:191], v[92:95]
buffer_load_dwordx4 v[164:167], %[vX], %[sWS], s57 offen offset:1024 sc1
v_mfma_f32_16x16x32_bf16 v[96:99], a[144:147], v[176:179], v[96:99]
v_and_or_b32 v245, v255, 32, v245
v_mfma_f32_16x16x32_bf16 v[104:107], a[144:147], v[180:183], v[104:107]
ds_read_b64_tr_b16 a[186:187], %[vTr] offset:9984
v_lshrrev_b32_e64 v244, 1, %[vX]
v_mfma_f32_16x16x32_bf16 v[112:115], a[144:147], v[184:187], v[112:115]
ds_read_b64_tr_b16 a[188:189], %[vTr] offset:9736
v_and_or_b32 v245, v244, 24, v245
v_mfma_f32_16x16x32_bf16 v[120:123], a[144:147], v[188:191], v[120:123]
buffer_load_dwordx4 v[168:171], %[vX], %[sWS], s57 offen offset:2048 sc1
v_mfma_f32_16x16x32_bf16 v[100:103], a[148:151], v[176:179], v[100:103]
ds_read_b64_tr_b16 a[190:191], %[vTr] offset:9992
v_add_u32 %[vVr], 65536, v245
v_mfma_f32_16x16x32_bf16 v[108:111], a[148:151], v[180:183], v[108:111]
v_and_b32_e64 v244, 32, v244
v_mfma_f32_16x16x32_bf16 v[116:119], a[148:151], v[184:187], v[116:119]
v_xor_b32_e64 v246, v246, v244
v_mfma_f32_16x16x32_bf16 v[124:127], a[148:151], v[188:191], v[124:127]
buffer_load_dwordx4 v[172:175], %[vX], %[sWS], s57 offen offset:3072 sc1
v_mfma_f32_16x16x32_bf16 v[128:131], a[152:155], v[176:179], v[128:131]
v_and_b32 v244, 112, %[vX]
v_mfma_f32_16x16x32_bf16 v[136:139], a[152:155], v[180:183], v[136:139]
v_lshl_or_b32 v246, v244, 2, v246
v_mfma_f32_16x16x32_bf16 v[144:147], a[152:155], v[184:187], v[144:147]
v_and_b32 v244, 128, %[vX]
v_mfma_f32_16x16x32_bf16 v[152:155], a[152:155], v[188:191], v[152:155]
s_add_u32 m0, %[sLw], 49152
s_add_u32 s60, s32, 3149824
buffer_load_dwordx4 %[vWs], %[sW], s60 offen offset:0 lds
v_mfma_f32_16x16x32_bf16 v[132:135], a[156:159], v[176:179], v[132:135]
v_lshl_or_b32 %[vKrd], v244, 4, v246
v_mfma_f32_16x16x32_bf16 v[140:143], a[156:159], v[180:183], v[140:143]
v_and_b32 v244, 192, %[vX]
v_mfma_f32_16x16x32_bf16 v[148:151], a[156:159], v[184:187], v[148:151]
v_lshl_or_b32 %[vSp], v244, 9, %[vSp]
v_mfma_f32_16x16x32_bf16 v[156:159], a[156:159], v[188:191], v[156:159]
s_add_u32 s60, s32, 3148928
buffer_load_dwordx4 %[vWs], %[sW], s60 offen offset:1024 lds
s_waitcnt vmcnt(18) lgkmcnt(0)
s_barrier
v_mfma_f32_16x16x32_bf16 v[32:35], a[160:163], v[192:195], v[32:35]
s_bfe_u32 s93, s31, 327687
v_mfma_f32_16x16x32_bf16 v[40:43], a[160:163], v[196:199], v[40:43]
ds_read_b64_tr_b16 a[128:129], %[vTr] offset:16384
s_lshl_b32 s93, s93, 12
v_mfma_f32_16x16x32_bf16 v[48:51], a[160:163], v[200:203], v[48:51]
ds_read_b64_tr_b16 a[130:131], %[vTr] offset:16640
s_lshr_b32 s94, %[sKV], 2
v_mfma_f32_16x16x32_bf16 v[56:59], a[160:163], v[204:207], v[56:59]
ds_read_b64_tr_b16 a[132:133], %[vTr] offset:16392
s_add_u32 s93, s93, s94
v_mfma_f32_16x16x32_bf16 v[36:39], a[164:167], v[192:195], v[36:39]
ds_read_b64_tr_b16 a[134:135], %[vTr] offset:16648
v_and_b32 v253, 896, %[vX]
v_mfma_f32_16x16x32_bf16 v[44:47], a[164:167], v[196:199], v[44:47]
ds_read_b64_tr_b16 a[136:137], %[vTr] offset:16896
v_add_u32 v253, s93, v253
v_mfma_f32_16x16x32_bf16 v[52:55], a[164:167], v[200:203], v[52:55]
LPa_1:
s_cbranch_execz LPa_5
ds_read_b64_tr_b16 a[138:139], %[vTr] offset:17152
v_mfma_f32_16x16x32_bf16 v[60:63], a[164:167], v[204:207], v[60:63]
ds_read_b64_tr_b16 a[140:141], %[vTr] offset:16904
v_mfma_f32_16x16x32_bf16 v[64:67], a[168:171], v[192:195], v[64:67]
ds_read_b64_tr_b16 a[142:143], %[vTr] offset:17160
v_mfma_f32_16x16x32_bf16 v[72:75], a[168:171], v[196:199], v[72:75]
ds_read_b64_tr_b16 a[144:145], %[vTr] offset:17408
v_mfma_f32_16x16x32_bf16 v[80:83], a[168:171], v[200:203], v[80:83]
ds_read_b64_tr_b16 a[146:147], %[vTr] offset:17664
v_mfma_f32_16x16x32_bf16 v[88:91], a[168:171], v[204:207], v[88:91]
s_add_u32 s57, s57, 4096
buffer_load_dwordx4 v[176:179], %[vX], %[sWS], s57 offen offset:0 sc1
v_mfma_f32_16x16x32_bf16 v[68:71], a[172:175], v[192:195], v[68:71]
ds_read_b64_tr_b16 a[148:149], %[vTr] offset:17416
v_mfma_f32_16x16x32_bf16 v[76:79], a[172:175], v[196:199], v[76:79]
ds_read_b64_tr_b16 a[150:151], %[vTr] offset:17672
v_mfma_f32_16x16x32_bf16 v[84:87], a[172:175], v[200:203], v[84:87]
ds_read_b64_tr_b16 a[152:153], %[vTr] offset:17920
v_mfma_f32_16x16x32_bf16 v[92:95], a[172:175], v[204:207], v[92:95]
buffer_load_dwordx4 v[180:183], %[vX], %[sWS], s57 offen offset:1024 sc1
v_mfma_f32_16x16x32_bf16 v[96:99], a[176:179], v[192:195], v[96:99]
v_mfma_f32_16x16x32_bf16 v[104:107], a[176:179], v[196:199], v[104:107]
ds_read_b64_tr_b16 a[154:155], %[vTr] offset:18176
)"
R"(v_mfma_f32_16x16x32_bf16 v[112:115], a[176:179], v[200:203], v[112:115]
ds_read_b64_tr_b16 a[156:157], %[vTr] offset:17928
v_mfma_f32_16x16x32_bf16 v[120:123], a[176:179], v[204:207], v[120:123]
buffer_load_dwordx4 v[184:187], %[vX], %[sWS], s57 offen offset:2048 sc1
v_mfma_f32_16x16x32_bf16 v[100:103], a[180:183], v[192:195], v[100:103]
ds_read_b64_tr_b16 a[158:159], %[vTr] offset:18184
v_mfma_f32_16x16x32_bf16 v[108:111], a[180:183], v[196:199], v[108:111]
v_mfma_f32_16x16x32_bf16 v[116:119], a[180:183], v[200:203], v[116:119]
v_mfma_f32_16x16x32_bf16 v[124:127], a[180:183], v[204:207], v[124:127]
buffer_load_dwordx4 v[188:191], %[vX], %[sWS], s57 offen offset:3072 sc1
v_mfma_f32_16x16x32_bf16 v[128:131], a[184:187], v[192:195], v[128:131]
v_mfma_f32_16x16x32_bf16 v[136:139], a[184:187], v[196:199], v[136:139]
v_mfma_f32_16x16x32_bf16 v[144:147], a[184:187], v[200:203], v[144:147]
v_mfma_f32_16x16x32_bf16 v[152:155], a[184:187], v[204:207], v[152:155]
s_add_u32 s60, s32, 3213312
buffer_load_dwordx4 %[vWs], %[sW], s60 offen offset:2048 lds
v_mfma_f32_16x16x32_bf16 v[132:135], a[188:191], v[192:195], v[132:135]
v_mfma_f32_16x16x32_bf16 v[140:143], a[188:191], v[196:199], v[140:143]
v_mfma_f32_16x16x32_bf16 v[148:151], a[188:191], v[200:203], v[148:151]
v_mfma_f32_16x16x32_bf16 v[156:159], a[188:191], v[204:207], v[156:159]
s_add_u32 s60, s32, 3212416
buffer_load_dwordx4 %[vWs], %[sW], s60 offen offset:3072 lds
s_waitcnt vmcnt(18) lgkmcnt(0)
v_mfma_f32_16x16x32_bf16 v[32:35], a[128:131], v[208:211], v[32:35]
v_mfma_f32_16x16x32_bf16 v[40:43], a[128:131], v[212:215], v[40:43]
ds_read_b64_tr_b16 a[160:161], %[vTr] offset:24576
v_mfma_f32_16x16x32_bf16 v[48:51], a[128:131], v[216:219], v[48:51]
ds_read_b64_tr_b16 a[162:163], %[vTr] offset:24832
v_mfma_f32_16x16x32_bf16 v[56:59], a[128:131], v[220:223], v[56:59]
s_add_u32 s57, s57, 4096
buffer_load_dwordx4 v[192:195], %[vX], %[sWS], s57 offen offset:0 sc1
v_mfma_f32_16x16x32_bf16 v[36:39], a[132:135], v[208:211], v[36:39]
ds_read_b64_tr_b16 a[164:165], %[vTr] offset:24584
v_mfma_f32_16x16x32_bf16 v[44:47], a[132:135], v[212:215], v[44:47]
ds_read_b64_tr_b16 a[166:167], %[vTr] offset:24840
v_mfma_f32_16x16x32_bf16 v[52:55], a[132:135], v[216:219], v[52:55]
ds_read_b64_tr_b16 a[168:169], %[vTr] offset:25088
v_mfma_f32_16x16x32_bf16 v[60:63], a[132:135], v[220:223], v[60:63]
buffer_load_dwordx4 v[196:199], %[vX], %[sWS], s57 offen offset:1024 sc1
v_mfma_f32_16x16x32_bf16 v[64:67], a[136:139], v[208:211], v[64:67]
ds_read_b64_tr_b16 a[170:171], %[vTr] offset:25344
v_mfma_f32_16x16x32_bf16 v[72:75], a[136:139], v[212:215], v[72:75]
ds_read_b64_tr_b16 a[172:173], %[vTr] offset:25096
v_mfma_f32_16x16x32_bf16 v[80:83], a[136:139], v[216:219], v[80:83]
ds_read_b64_tr_b16 a[174:175], %[vTr] offset:25352
v_mfma_f32_16x16x32_bf16 v[88:91], a[136:139], v[220:223], v[88:91]
buffer_load_dwordx4 v[200:203], %[vX], %[sWS], s57 offen offset:2048 sc1
v_mfma_f32_16x16x32_bf16 v[68:71], a[140:143], v[208:211], v[68:71]
ds_read_b64_tr_b16 a[176:177], %[vTr] offset:25600
v_mfma_f32_16x16x32_bf16 v[76:79], a[140:143], v[212:215], v[76:79]
ds_read_b64_tr_b16 a[178:179], %[vTr] offset:25856
v_mfma_f32_16x16x32_bf16 v[84:87], a[140:143], v[216:219], v[84:87]
ds_read_b64_tr_b16 a[180:181], %[vTr] offset:25608
v_mfma_f32_16x16x32_bf16 v[92:95], a[140:143], v[220:223], v[92:95]
buffer_load_dwordx4 v[204:207], %[vX], %[sWS], s57 offen offset:3072 sc1
v_mfma_f32_16x16x32_bf16 v[96:99], a[144:147], v[208:211], v[96:99]
v_mfma_f32_16x16x32_bf16 v[104:107], a[144:147], v[212:215], v[104:107]
ds_read_b64_tr_b16 a[182:183], %[vTr] offset:25864
v_mfma_f32_16x16x32_bf16 v[112:115], a[144:147], v[216:219], v[112:115]
ds_read_b64_tr_b16 a[184:185], %[vTr] offset:26112
v_mfma_f32_16x16x32_bf16 v[120:123], a[144:147], v[220:223], v[120:123]
s_add_u32 m0, %[sLw], 114688
s_add_u32 s60, s32, 3674112
buffer_load_dwordx4 %[vWs], %[sW], s60 offen offset:0 lds
v_mfma_f32_16x16x32_bf16 v[100:103], a[148:151], v[208:211], v[100:103]
ds_read_b64_tr_b16 a[186:187], %[vTr] offset:26368
v_mfma_f32_16x16x32_bf16 v[108:111], a[148:151], v[212:215], v[108:111]
ds_read_b64_tr_b16 a[188:189], %[vTr] offset:26120
v_mfma_f32_16x16x32_bf16 v[116:119], a[148:151], v[216:219], v[116:119]
ds_read_b64_tr_b16 a[190:191], %[vTr] offset:26376
v_mfma_f32_16x16x32_bf16 v[124:127], a[148:151], v[220:223], v[124:127]
s_add_u32 s60, s32, 3673216
buffer_load_dwordx4 %[vWs], %[sW], s60 offen offset:1024 lds
v_mfma_f32_16x16x32_bf16 v[128:131], a[152:155], v[208:211], v[128:131]
v_mfma_f32_16x16x32_bf16 v[136:139], a[152:155], v[212:215], v[136:139]
v_mfma_f32_16x16x32_bf16 v[144:147], a[152:155], v[216:219], v[144:147]
s_and_b32 s61, s31, 4095
s_lshl_b32 s61, s61, 6
v_mfma_f32_16x16x32_bf16 v[152:155], a[152:155], v[220:223], v[152:155]
buffer_load_dword v24, v254, %[sWS], s61 offen offset:0
v_mfma_f32_16x16x32_bf16 v[132:135], a[156:159], v[208:211], v[132:135]
v_mfma_f32_16x16x32_bf16 v[140:143], a[156:159], v[212:215], v[140:143]
v_mfma_f32_16x16x32_bf16 v[148:151], a[156:159], v[216:219], v[148:151]
s_add_u32 s61, s61, 13824
v_mfma_f32_16x16x32_bf16 v[156:159], a[156:159], v[220:223], v[156:159]
buffer_load_dword v25, v254, %[sWS], s61 offen offset:0
s_waitcnt vmcnt(20) lgkmcnt(0)
s_barrier
v_mfma_f32_16x16x32_bf16 v[32:35], a[160:163], v[224:227], v[32:35]
v_lshrrev_b32 v240, 6, %[vX]
v_mfma_f32_16x16x32_bf16 v[40:43], a[160:163], v[228:231], v[40:43]
ds_read_b64_tr_b16 a[128:129], %[vTr] offset:32768
v_and_b32_e64 v241, 48, %[vX]
v_mfma_f32_16x16x32_bf16 v[48:51], a[160:163], v[232:235], v[48:51]
ds_read_b64_tr_b16 a[130:131], %[vTr] offset:33024
v_and_b32 v242, 768, %[vX]
v_mfma_f32_16x16x32_bf16 v[56:59], a[160:163], v[236:239], v[56:59]
s_add_u32 s57, s57, 4096
buffer_load_dwordx4 v[208:211], %[vX], %[sWS], s57 offen offset:0 sc1
v_mfma_f32_16x16x32_bf16 v[36:39], a[164:167], v[224:227], v[36:39]
ds_read_b64_tr_b16 a[132:133], %[vTr] offset:32776
v_bfe_u32 v248, %[vX], 8, 1
v_mfma_f32_16x16x32_bf16 v[44:47], a[164:167], v[228:231], v[44:47]
ds_read_b64_tr_b16 a[134:135], %[vTr] offset:33032
v_mul_u32_u24 v248, 13824, v248
v_mfma_f32_16x16x32_bf16 v[52:55], a[164:167], v[232:235], v[52:55]
ds_read_b64_tr_b16 a[136:137], %[vTr] offset:33280
v_lshrrev_b32_e64 v243, 9, %[vX]
v_mfma_f32_16x16x32_bf16 v[60:63], a[164:167], v[236:239], v[60:63]
buffer_load_dwordx4 v[212:215], %[vX], %[sWS], s57 offen offset:1024 sc1
v_mfma_f32_16x16x32_bf16 v[64:67], a[168:171], v[224:227], v[64:67]
ds_read_b64_tr_b16 a[138:139], %[vTr] offset:33536
v_mul_u32_u24 v243, 4608, v243
v_mfma_f32_16x16x32_bf16 v[72:75], a[168:171], v[228:231], v[72:75]
ds_read_b64_tr_b16 a[140:141], %[vTr] offset:33288
v_sub_u32_e64 v248, v248, v243
v_mfma_f32_16x16x32_bf16 v[80:83], a[168:171], v[232:235], v[80:83]
ds_read_b64_tr_b16 a[142:143], %[vTr] offset:33544
v_add_u32_e64 v242, v242, %[vX]
v_mfma_f32_16x16x32_bf16 v[88:91], a[168:171], v[236:239], v[88:91]
buffer_load_dwordx4 v[216:219], %[vX], %[sWS], s57 offen offset:2048 sc1
v_mfma_f32_16x16x32_bf16 v[68:71], a[172:175], v[224:227], v[68:71]
ds_read_b64_tr_b16 a[144:145], %[vTr] offset:33792
s_lshr_b32 s93, %[sKV], 1
v_mfma_f32_16x16x32_bf16 v[76:79], a[172:175], v[228:231], v[76:79]
ds_read_b64_tr_b16 a[146:147], %[vTr] offset:34048
s_add_u32 s93, s93, %[sL]
v_mfma_f32_16x16x32_bf16 v[84:87], a[172:175], v[232:235], v[84:87]
ds_read_b64_tr_b16 a[148:149], %[vTr] offset:33800
s_add_u32 s93, s93, 131072
v_mfma_f32_16x16x32_bf16 v[92:95], a[172:175], v[236:239], v[92:95]
buffer_load_dwordx4 v[220:223], %[vX], %[sWS], s57 offen offset:3072 sc1
v_mfma_f32_16x16x32_bf16 v[96:99], a[176:179], v[224:227], v[96:99]
v_add_u32_e64 v242, s93, v242
v_mfma_f32_16x16x32_bf16 v[104:107], a[176:179], v[228:231], v[104:107]
ds_read_b64_tr_b16 a[150:151], %[vTr] offset:34056
v_lshrrev_b32_e64 v243, 1, v241
v_mfma_f32_16x16x32_bf16 v[112:115], a[176:179], v[232:235], v[112:115]
ds_read_b64_tr_b16 a[152:153], %[vTr] offset:34304
s_add_u32 s93, %[sL], 155648
v_mfma_f32_16x16x32_bf16 v[120:123], a[176:179], v[236:239], v[120:123]
s_add_u32 s60, s32, 3737600
buffer_load_dwordx4 %[vWs], %[sW], s60 offen offset:2048 lds
v_mfma_f32_16x16x32_bf16 v[100:103], a[180:183], v[224:227], v[100:103]
ds_read_b64_tr_b16 a[154:155], %[vTr] offset:34560
v_lshrrev_b32_e64 v249, 2, v241
v_mfma_f32_16x16x32_bf16 v[108:111], a[180:183], v[228:231], v[108:111]
ds_read_b64_tr_b16 a[156:157], %[vTr] offset:34312
v_add_u32 v249, s93, v249
v_mfma_f32_16x16x32_bf16 v[116:119], a[180:183], v[232:235], v[116:119]
ds_read_b64_tr_b16 a[158:159], %[vTr] offset:34568
s_lshr_b32 s94, %[sKV], 10
v_mfma_f32_16x16x32_bf16 v[124:127], a[180:183], v[236:239], v[124:127]
s_add_u32 s60, s32, 3736704
buffer_load_dwordx4 %[vWs], %[sW], s60 offen offset:3072 lds
v_mfma_f32_16x16x32_bf16 v[128:131], a[184:187], v[224:227], v[128:131]
s_add_u32 s93, s93, s94
v_mfma_f32_16x16x32_bf16 v[136:139], a[184:187], v[228:231], v[136:139]
v_mov_b32 v250, s93
v_mfma_f32_16x16x32_bf16 v[144:147], a[184:187], v[232:235], v[144:147]
s_add_u32 s61, s61, 13824
v_mov_b32_e64 v252, 0
v_mfma_f32_16x16x32_bf16 v[152:155], a[184:187], v[236:239], v[152:155]
buffer_load_dword v26, v254, %[sWS], s61 offen offset:0
v_mfma_f32_16x16x32_bf16 v[132:135], a[188:191], v[224:227], v[132:135]
ds_write_b32 v250, v252 offset:0
v_mfma_f32_16x16x32_bf16 v[140:143], a[188:191], v[228:231], v[140:143]
v_lshrrev_b32_e64 v251, 4, %[vX]
v_mfma_f32_16x16x32_bf16 v[148:151], a[188:191], v[232:235], v[148:151]
s_add_u32 s61, s61, 13824
v_xor_b32 v251, v251, %[vX]
v_mfma_f32_16x16x32_bf16 v[156:159], a[188:191], v[236:239], v[156:159]
buffer_load_dword v27, v254, %[sWS], s61 offen offset:0
s_waitcnt vmcnt(24) lgkmcnt(0)
v_mfma_f32_16x16x32_bf16 v[32:35], a[128:131], v[160:163], v[32:35]
v_bfe_u32 v251, v251, 5, 1
v_mfma_f32_16x16x32_bf16 v[40:43], a[128:131], v[164:167], v[40:43]
ds_read_b64_tr_b16 a[160:161], %[vTr] offset:40960
v_add_u32_e64 v251, -1, v251
v_mfma_f32_16x16x32_bf16 v[48:51], a[128:131], v[168:171], v[48:51]
ds_read_b64_tr_b16 a[162:163], %[vTr] offset:41216
v_and_b32 v251, 1065369472, v251
v_mfma_f32_16x16x32_bf16 v[56:59], a[128:131], v[172:175], v[56:59]
ds_read_b64_tr_b16 a[164:165], %[vTr] offset:40968
v_bfe_i32 v252, %[vX], 4, 1
v_mfma_f32_16x16x32_bf16 v[36:39], a[132:135], v[160:163], v[36:39]
ds_read_b64_tr_b16 a[166:167], %[vTr] offset:41224
v_and_b32 v252, 43690, v252
v_mfma_f32_16x16x32_bf16 v[44:47], a[132:135], v[164:167], v[44:47]
ds_read_b64_tr_b16 a[168:169], %[vTr] offset:41472
v_or_b32 v252, 17476, v252
v_mfma_f32_16x16x32_bf16 v[52:55], a[132:135], v[168:171], v[52:55]
ds_read_b64_tr_b16 a[170:171], %[vTr] offset:41728
s_getpc_b64 s[98:99]
s_add_u32 s98, s98, L_tail@rel32@lo+4
s_addc_u32 s99, s99, L_tail@rel32@hi+12
v_mfma_f32_16x16x32_bf16 v[60:63], a[132:135], v[172:175], v[60:63]
s_add_u32 s57, s57, 4096
buffer_load_dwordx4 v[224:227], %[vX], %[sWS], s57 offen offset:0 sc1
v_mfma_f32_16x16x32_bf16 v[64:67], a[136:139], v[160:163], v[64:67]
)"
R"(ds_read_b64_tr_b16 a[172:173], %[vTr] offset:41480
v_min_u32 v253, code_end_lbl-L_tail-4, v253
v_mfma_f32_16x16x32_bf16 v[72:75], a[136:139], v[164:167], v[72:75]
ds_read_b64_tr_b16 a[174:175], %[vTr] offset:41736
s_mov_b32 s92, s30
v_mfma_f32_16x16x32_bf16 v[80:83], a[136:139], v[168:171], v[80:83]
ds_read_b64_tr_b16 a[176:177], %[vTr] offset:41984
s_mov_b32 s68, s31
v_mfma_f32_16x16x32_bf16 v[88:91], a[136:139], v[172:175], v[88:91]
buffer_load_dwordx4 v[228:231], %[vX], %[sWS], s57 offen offset:1024 sc1
v_mfma_f32_16x16x32_bf16 v[68:71], a[140:143], v[160:163], v[68:71]
ds_read_b64_tr_b16 a[178:179], %[vTr] offset:42240
v_mfma_f32_16x16x32_bf16 v[76:79], a[140:143], v[164:167], v[76:79]
ds_read_b64_tr_b16 a[180:181], %[vTr] offset:41992
v_mfma_f32_16x16x32_bf16 v[84:87], a[140:143], v[168:171], v[84:87]
ds_read_b64_tr_b16 a[182:183], %[vTr] offset:42248
v_mfma_f32_16x16x32_bf16 v[92:95], a[140:143], v[172:175], v[92:95]
buffer_load_dwordx4 v[232:235], %[vX], %[sWS], s57 offen offset:2048 sc1
v_mfma_f32_16x16x32_bf16 v[96:99], a[144:147], v[160:163], v[96:99]
v_mfma_f32_16x16x32_bf16 v[104:107], a[144:147], v[164:167], v[104:107]
ds_read_b64_tr_b16 a[184:185], %[vTr] offset:42496
v_mfma_f32_16x16x32_bf16 v[112:115], a[144:147], v[168:171], v[112:115]
ds_read_b64_tr_b16 a[186:187], %[vTr] offset:42752
v_mfma_f32_16x16x32_bf16 v[120:123], a[144:147], v[172:175], v[120:123]
buffer_load_dwordx4 v[236:239], %[vX], %[sWS], s57 offen offset:3072 sc1
v_mfma_f32_16x16x32_bf16 v[100:103], a[148:151], v[160:163], v[100:103]
ds_read_b64_tr_b16 a[188:189], %[vTr] offset:42504
v_mfma_f32_16x16x32_bf16 v[108:111], a[148:151], v[164:167], v[108:111]
ds_read_b64_tr_b16 a[190:191], %[vTr] offset:42760
v_mfma_f32_16x16x32_bf16 v[116:119], a[148:151], v[168:171], v[116:119]
v_mfma_f32_16x16x32_bf16 v[124:127], a[148:151], v[172:175], v[124:127]
buffer_load_dword v30, %[vX], %[sWS], s30 offen offset:0
v_mfma_f32_16x16x32_bf16 v[128:131], a[152:155], v[160:163], v[128:131]
v_mfma_f32_16x16x32_bf16 v[136:139], a[152:155], v[164:167], v[136:139]
v_mfma_f32_16x16x32_bf16 v[144:147], a[152:155], v[168:171], v[144:147]
v_mfma_f32_16x16x32_bf16 v[152:155], a[152:155], v[172:175], v[152:155]
s_add_u32 m0, %[sLw], 131072
s_add_u32 s60, s32, 4198400
buffer_load_dwordx4 %[vWs], %[sW], s60 offen offset:0 lds
v_mfma_f32_16x16x32_bf16 v[132:135], a[156:159], v[160:163], v[132:135]
v_mfma_f32_16x16x32_bf16 v[140:143], a[156:159], v[164:167], v[140:143]
v_mfma_f32_16x16x32_bf16 v[148:151], a[156:159], v[168:171], v[148:151]
v_mfma_f32_16x16x32_bf16 v[156:159], a[156:159], v[172:175], v[156:159]
s_add_u32 s60, s32, 4197504
buffer_load_dwordx4 %[vWs], %[sW], s60 offen offset:1024 lds
s_waitcnt vmcnt(23) lgkmcnt(0)
s_barrier
v_mfma_f32_16x16x32_bf16 v[32:35], a[160:163], v[176:179], v[32:35]
v_mfma_f32_16x16x32_bf16 v[40:43], a[160:163], v[180:183], v[40:43]
ds_read_b64_tr_b16 a[128:129], v28 offset:49152
v_mfma_f32_16x16x32_bf16 v[48:51], a[160:163], v[184:187], v[48:51]
ds_read_b64_tr_b16 a[130:131], v28 offset:49408
v_mfma_f32_16x16x32_bf16 v[56:59], a[160:163], v[188:191], v[56:59]
ds_read_b64_tr_b16 a[132:133], v28 offset:49160
v_mfma_f32_16x16x32_bf16 v[36:39], a[164:167], v[176:179], v[36:39]
ds_read_b64_tr_b16 a[134:135], v28 offset:49416
v_mfma_f32_16x16x32_bf16 v[44:47], a[164:167], v[180:183], v[44:47]
ds_read_b64_tr_b16 a[136:137], v28 offset:49664
v_mfma_f32_16x16x32_bf16 v[52:55], a[164:167], v[184:187], v[52:55]
ds_read_b64_tr_b16 a[138:139], v28 offset:49920
v_mfma_f32_16x16x32_bf16 v[60:63], a[164:167], v[188:191], v[60:63]
ds_read_b64_tr_b16 a[140:141], v28 offset:49672
v_mfma_f32_16x16x32_bf16 v[64:67], a[168:171], v[176:179], v[64:67]
ds_read_b64_tr_b16 a[142:143], v28 offset:49928
v_mfma_f32_16x16x32_bf16 v[72:75], a[168:171], v[180:183], v[72:75]
ds_read_b64_tr_b16 a[144:145], v28 offset:50176
v_mfma_f32_16x16x32_bf16 v[80:83], a[168:171], v[184:187], v[80:83]
ds_read_b64_tr_b16 a[146:147], v28 offset:50432
v_mfma_f32_16x16x32_bf16 v[88:91], a[168:171], v[188:191], v[88:91]
s_add_u32 s57, s57, 4096
buffer_load_dwordx4 v[160:163], %[vX], %[sWS], s57 offen offset:0 sc1
v_mfma_f32_16x16x32_bf16 v[68:71], a[172:175], v[176:179], v[68:71]
ds_read_b64_tr_b16 a[148:149], v28 offset:50184
v_mfma_f32_16x16x32_bf16 v[76:79], a[172:175], v[180:183], v[76:79]
ds_read_b64_tr_b16 a[150:151], v28 offset:50440
v_mfma_f32_16x16x32_bf16 v[84:87], a[172:175], v[184:187], v[84:87]
ds_read_b64_tr_b16 a[152:153], v28 offset:50688
v_mfma_f32_16x16x32_bf16 v[92:95], a[172:175], v[188:191], v[92:95]
buffer_load_dwordx4 v[164:167], %[vX], %[sWS], s57 offen offset:1024 sc1
v_mfma_f32_16x16x32_bf16 v[96:99], a[176:179], v[176:179], v[96:99]
v_mfma_f32_16x16x32_bf16 v[104:107], a[176:179], v[180:183], v[104:107]
ds_read_b64_tr_b16 a[154:155], v28 offset:50944
v_mfma_f32_16x16x32_bf16 v[112:115], a[176:179], v[184:187], v[112:115]
ds_read_b64_tr_b16 a[156:157], v28 offset:50696
v_mfma_f32_16x16x32_bf16 v[120:123], a[176:179], v[188:191], v[120:123]
buffer_load_dwordx4 v[168:171], %[vX], %[sWS], s57 offen offset:2048 sc1
v_mfma_f32_16x16x32_bf16 v[100:103], a[180:183], v[176:179], v[100:103]
ds_read_b64_tr_b16 a[158:159], v28 offset:50952
v_mfma_f32_16x16x32_bf16 v[108:111], a[180:183], v[180:183], v[108:111]
v_mfma_f32_16x16x32_bf16 v[116:119], a[180:183], v[184:187], v[116:119]
v_mfma_f32_16x16x32_bf16 v[124:127], a[180:183], v[188:191], v[124:127]
buffer_load_dwordx4 v[172:175], %[vX], %[sWS], s57 offen offset:3072 sc1
v_mfma_f32_16x16x32_bf16 v[128:131], a[184:187], v[176:179], v[128:131]
v_mfma_f32_16x16x32_bf16 v[136:139], a[184:187], v[180:183], v[136:139]
v_mfma_f32_16x16x32_bf16 v[144:147], a[184:187], v[184:187], v[144:147]
v_mfma_f32_16x16x32_bf16 v[152:155], a[184:187], v[188:191], v[152:155]
s_add_u32 s60, s32, 4261888
buffer_load_dwordx4 %[vWs], %[sW], s60 offen offset:2048 lds
v_mfma_f32_16x16x32_bf16 v[132:135], a[188:191], v[176:179], v[132:135]
v_mfma_f32_16x16x32_bf16 v[140:143], a[188:191], v[180:183], v[140:143]
v_mfma_f32_16x16x32_bf16 v[148:151], a[188:191], v[184:187], v[148:151]
v_mfma_f32_16x16x32_bf16 v[156:159], a[188:191], v[188:191], v[156:159]
s_add_u32 s60, s32, 4260992
buffer_load_dwordx4 %[vWs], %[sW], s60 offen offset:3072 lds
s_waitcnt vmcnt(25) lgkmcnt(0)
v_mfma_f32_16x16x32_bf16 v[32:35], a[128:131], v[192:195], v[32:35]
v_mfma_f32_16x16x32_bf16 v[40:43], a[128:131], v[196:199], v[40:43]
ds_read_b64_tr_b16 a[160:161], v28 offset:57344
v_mfma_f32_16x16x32_bf16 v[48:51], a[128:131], v[200:203], v[48:51]
ds_read_b64_tr_b16 a[162:163], v28 offset:57600
v_mfma_f32_16x16x32_bf16 v[56:59], a[128:131], v[204:207], v[56:59]
s_add_u32 s57, s57, 4096
buffer_load_dwordx4 v[176:179], %[vX], %[sWS], s57 offen offset:0 sc1
v_mfma_f32_16x16x32_bf16 v[36:39], a[132:135], v[192:195], v[36:39]
ds_read_b64_tr_b16 a[164:165], v28 offset:57352
v_mfma_f32_16x16x32_bf16 v[44:47], a[132:135], v[196:199], v[44:47]
ds_read_b64_tr_b16 a[166:167], v28 offset:57608
v_mfma_f32_16x16x32_bf16 v[52:55], a[132:135], v[200:203], v[52:55]
ds_read_b64_tr_b16 a[168:169], v28 offset:57856
v_mfma_f32_16x16x32_bf16 v[60:63], a[132:135], v[204:207], v[60:63]
buffer_load_dwordx4 v[180:183], %[vX], %[sWS], s57 offen offset:1024 sc1
v_mfma_f32_16x16x32_bf16 v[64:67], a[136:139], v[192:195], v[64:67]
ds_read_b64_tr_b16 a[170:171], v28 offset:58112
v_mfma_f32_16x16x32_bf16 v[72:75], a[136:139], v[196:199], v[72:75]
ds_read_b64_tr_b16 a[172:173], v28 offset:57864
v_mfma_f32_16x16x32_bf16 v[80:83], a[136:139], v[200:203], v[80:83]
ds_read_b64_tr_b16 a[174:175], v28 offset:58120
v_mfma_f32_16x16x32_bf16 v[88:91], a[136:139], v[204:207], v[88:91]
buffer_load_dwordx4 v[184:187], %[vX], %[sWS], s57 offen offset:2048 sc1
v_mfma_f32_16x16x32_bf16 v[68:71], a[140:143], v[192:195], v[68:71]
ds_read_b64_tr_b16 a[176:177], v28 offset:58368
v_mfma_f32_16x16x32_bf16 v[76:79], a[140:143], v[196:199], v[76:79]
ds_read_b64_tr_b16 a[178:179], v28 offset:58624
v_mfma_f32_16x16x32_bf16 v[84:87], a[140:143], v[200:203], v[84:87]
ds_read_b64_tr_b16 a[180:181], v28 offset:58376
v_mfma_f32_16x16x32_bf16 v[92:95], a[140:143], v[204:207], v[92:95]
buffer_load_dwordx4 v[188:191], %[vX], %[sWS], s57 offen offset:3072 sc1
v_mfma_f32_16x16x32_bf16 v[96:99], a[144:147], v[192:195], v[96:99]
v_mfma_f32_16x16x32_bf16 v[104:107], a[144:147], v[196:199], v[104:107]
ds_read_b64_tr_b16 a[182:183], v28 offset:58632
v_mfma_f32_16x16x32_bf16 v[112:115], a[144:147], v[200:203], v[112:115]
s_add_u32 s56, %[sKV], 0
s_add_u32 m0, %[sLw], 0
ds_read_b64_tr_b16 a[184:185], v28 offset:58880
v_mfma_f32_16x16x32_bf16 v[120:123], a[144:147], v[204:207], v[120:123]
buffer_load_dwordx4 %[vVs0], s[16:19], s56 offen offset:0 lds
v_mfma_f32_16x16x32_bf16 v[100:103], a[148:151], v[192:195], v[100:103]
ds_read_b64_tr_b16 a[186:187], v28 offset:59136
v_mfma_f32_16x16x32_bf16 v[108:111], a[148:151], v[196:199], v[108:111]
ds_read_b64_tr_b16 a[188:189], v28 offset:58888
v_mfma_f32_16x16x32_bf16 v[116:119], a[148:151], v[200:203], v[116:119]
ds_read_b64_tr_b16 a[190:191], v28 offset:59144
v_mfma_f32_16x16x32_bf16 v[124:127], a[148:151], v[204:207], v[124:127]
buffer_load_dwordx4 %[vVs1], s[16:19], s56 offen offset:1024 lds
v_mfma_f32_16x16x32_bf16 v[128:131], a[152:155], v[192:195], v[128:131]
v_mfma_f32_16x16x32_bf16 v[136:139], a[152:155], v[196:199], v[136:139]
v_mfma_f32_16x16x32_bf16 v[144:147], a[152:155], v[200:203], v[144:147]
v_mfma_f32_16x16x32_bf16 v[152:155], a[152:155], v[204:207], v[152:155]
buffer_load_dwordx4 %[vVs0], s[16:19], s56 offen offset:2048 lds
v_mfma_f32_16x16x32_bf16 v[132:135], a[156:159], v[192:195], v[132:135]
v_mfma_f32_16x16x32_bf16 v[140:143], a[156:159], v[196:199], v[140:143]
v_mfma_f32_16x16x32_bf16 v[148:151], a[156:159], v[200:203], v[148:151]
v_mfma_f32_16x16x32_bf16 v[156:159], a[156:159], v[204:207], v[156:159]
buffer_load_dwordx4 %[vVs1], s[16:19], s56 offen offset:3072 lds
s_waitcnt vmcnt(23) lgkmcnt(0)
s_barrier
v_mfma_f32_16x16x32_bf16 v[32:35], a[160:163], v[208:211], v[32:35]
v_mfma_f32_16x16x32_bf16 v[40:43], a[160:163], v[212:215], v[40:43]
ds_read_b64_tr_b16 a[128:129], %[vTr] offset:49152
v_mfma_f32_16x16x32_bf16 v[48:51], a[160:163], v[216:219], v[48:51]
ds_read_b64_tr_b16 a[130:131], %[vTr] offset:49408
v_mfma_f32_16x16x32_bf16 v[56:59], a[160:163], v[220:223], v[56:59]
s_add_u32 s57, s57, 4096
buffer_load_dwordx4 v[192:195], %[vX], %[sWS], s57 offen offset:0 sc1
v_mfma_f32_16x16x32_bf16 v[36:39], a[164:167], v[208:211], v[36:39]
ds_read_b64_tr_b16 a[132:133], %[vTr] offset:49160
v_mfma_f32_16x16x32_bf16 v[44:47], a[164:167], v[212:215], v[44:47]
ds_read_b64_tr_b16 a[134:135], %[vTr] offset:49416
v_mfma_f32_16x16x32_bf16 v[52:55], a[164:167], v[216:219], v[52:55]
ds_read_b64_tr_b16 a[136:137], %[vTr] offset:49664
v_mfma_f32_16x16x32_bf16 v[60:63], a[164:167], v[220:223], v[60:63]
buffer_load_dwordx4 v[196:199], %[vX], %[sWS], s57 offen offset:1024 sc1
v_mfma_f32_16x16x32_bf16 v[64:67], a[168:171], v[208:211], v[64:67]
ds_read_b64_tr_b16 a[138:139], %[vTr] offset:49920
v_mfma_f32_16x16x32_bf16 v[72:75], a[168:171], v[212:215], v[72:75]
ds_read_b64_tr_b16 a[140:141], %[vTr] offset:49672
v_mfma_f32_16x16x32_bf16 v[80:83], a[168:171], v[216:219], v[80:83]
ds_read_b64_tr_b16 a[142:143], %[vTr] offset:49928
v_mfma_f32_16x16x32_bf16 v[88:91], a[168:171], v[220:223], v[88:91]
buffer_load_dwordx4 v[200:203], %[vX], %[sWS], s57 offen offset:2048 sc1
v_mfma_f32_16x16x32_bf16 v[68:71], a[172:175], v[208:211], v[68:71]
ds_read_b64_tr_b16 a[144:145], %[vTr] offset:50176
v_mfma_f32_16x16x32_bf16 v[76:79], a[172:175], v[212:215], v[76:79]
ds_read_b64_tr_b16 a[146:147], %[vTr] offset:50432
v_mfma_f32_16x16x32_bf16 v[84:87], a[172:175], v[216:219], v[84:87]
ds_read_b64_tr_b16 a[148:149], %[vTr] offset:50184
v_mfma_f32_16x16x32_bf16 v[92:95], a[172:175], v[220:223], v[92:95]
buffer_load_dwordx4 v[204:207], %[vX], %[sWS], s57 offen offset:3072 sc1
v_mfma_f32_16x16x32_bf16 v[96:99], a[176:179], v[208:211], v[96:99]
v_mfma_f32_16x16x32_bf16 v[104:107], a[176:179], v[212:215], v[104:107]
)"
R"(ds_read_b64_tr_b16 a[150:151], %[vTr] offset:50440
v_mfma_f32_16x16x32_bf16 v[112:115], a[176:179], v[216:219], v[112:115]
s_add_u32 m0, %[sLw], 65536
ds_read_b64_tr_b16 a[152:153], %[vTr] offset:50688
v_mfma_f32_16x16x32_bf16 v[120:123], a[176:179], v[220:223], v[120:123]
buffer_load_dwordx4 %[vVs0], s[20:23], s56 offen offset:0 lds
v_mfma_f32_16x16x32_bf16 v[100:103], a[180:183], v[208:211], v[100:103]
ds_read_b64_tr_b16 a[154:155], %[vTr] offset:50944
v_mfma_f32_16x16x32_bf16 v[108:111], a[180:183], v[212:215], v[108:111]
ds_read_b64_tr_b16 a[156:157], %[vTr] offset:50696
v_mfma_f32_16x16x32_bf16 v[116:119], a[180:183], v[216:219], v[116:119]
ds_read_b64_tr_b16 a[158:159], %[vTr] offset:50952
v_mfma_f32_16x16x32_bf16 v[124:127], a[180:183], v[220:223], v[124:127]
buffer_load_dwordx4 %[vVs1], s[20:23], s56 offen offset:1024 lds
v_mfma_f32_16x16x32_bf16 v[128:131], a[184:187], v[208:211], v[128:131]
v_mfma_f32_16x16x32_bf16 v[136:139], a[184:187], v[212:215], v[136:139]
v_mfma_f32_16x16x32_bf16 v[144:147], a[184:187], v[216:219], v[144:147]
v_mfma_f32_16x16x32_bf16 v[152:155], a[184:187], v[220:223], v[152:155]
buffer_load_dwordx4 %[vVs0], s[20:23], s56 offen offset:2048 lds
v_mfma_f32_16x16x32_bf16 v[132:135], a[188:191], v[208:211], v[132:135]
v_mfma_f32_16x16x32_bf16 v[140:143], a[188:191], v[212:215], v[140:143]
v_mfma_f32_16x16x32_bf16 v[148:151], a[188:191], v[216:219], v[148:151]
v_mfma_f32_16x16x32_bf16 v[156:159], a[188:191], v[220:223], v[156:159]
buffer_load_dwordx4 %[vVs1], s[20:23], s56 offen offset:3072 lds
s_waitcnt vmcnt(25) lgkmcnt(0)
v_mfma_f32_16x16x32_bf16 v[32:35], a[128:131], v[224:227], v[32:35]
v_add_f32 v24, v24, v25
v_mfma_f32_16x16x32_bf16 v[40:43], a[128:131], v[228:231], v[40:43]
ds_read_b64_tr_b16 a[160:161], %[vTr] offset:57344
v_add_f32_e64 v26, v26, v27
v_mfma_f32_16x16x32_bf16 v[48:51], a[128:131], v[232:235], v[48:51]
ds_read_b64_tr_b16 a[162:163], %[vTr] offset:57600
v_add_f32 v24, v24, v26
v_mfma_f32_16x16x32_bf16 v[56:59], a[128:131], v[236:239], v[56:59]
ds_read_b64_tr_b16 a[164:165], %[vTr] offset:57352
s_bfe_u32 s93, s31, 0xa0002
s_sub_u32 s94, s93, 36
s_cmp_lt_u32 s94, 9
s_cselect_b32 s94, 1.0, 0
s_cselect_b32 s61, 0, 3456
s_add_u32 s61, s61, 64512
v_mfma_f32_16x16x32_bf16 v[36:39], a[132:135], v[224:227], v[36:39]
ds_read_b64_tr_b16 a[166:167], %[vTr] offset:57608
v_add_f32_e64 v25, s94, v24
v_mfma_f32_16x16x32_bf16 v[44:47], a[132:135], v[228:231], v[44:47]
ds_read_b64_tr_b16 a[168:169], %[vTr] offset:57856
s_lshl_b32 s94, s93, 7
s_add_u32 s61, s61, s94
v_mfma_f32_16x16x32_bf16 v[52:55], a[132:135], v[232:235], v[52:55]
ds_read_b64_tr_b16 a[170:171], %[vTr] offset:58112
s_sub_u32 s94, s93, 27
s_cmp_lt_u32 s94, 18
s_cselect_b32 s94, s61, 0x7f000000
v_mfma_f32_16x16x32_bf16 v[60:63], a[132:135], v[236:239], v[60:63]
ds_read_b64_tr_b16 a[172:173], %[vTr] offset:57864
s_lshl_b32 s61, s93, 8
s_add_u32 s61, s61, 55296
v_mfma_f32_16x16x32_bf16 v[64:67], a[136:139], v[224:227], v[64:67]
ds_read_b64_tr_b16 a[174:175], %[vTr] offset:58120
s_cmp_lt_u32 s93, 54
s_cselect_b32 s93, s61, 0x7f000000
v_mfma_f32_16x16x32_bf16 v[72:75], a[136:139], v[228:231], v[72:75]
ds_read_b64_tr_b16 a[176:177], %[vTr] offset:58368
v_cvt_pk_bf16_f32 v26, v25, v25
v_mfma_f32_16x16x32_bf16 v[80:83], a[136:139], v[232:235], v[80:83]
ds_read_b64_tr_b16 a[178:179], %[vTr] offset:58624
v_and_b32 v27, 4294901760, v26
v_mfma_f32_16x16x32_bf16 v[88:91], a[136:139], v[236:239], v[88:91]
ds_read_b64_tr_b16 a[180:181], %[vTr] offset:58376
v_sub_f32_e64 v25, v25, v27
v_mfma_f32_16x16x32_bf16 v[68:71], a[140:143], v[224:227], v[68:71]
ds_read_b64_tr_b16 a[182:183], %[vTr] offset:58632
v_cvt_pk_bf16_f32 v27, v25, v25
v_mfma_f32_16x16x32_bf16 v[76:79], a[140:143], v[228:231], v[76:79]
ds_read_b64_tr_b16 a[184:185], %[vTr] offset:58880
v_mfma_f32_16x16x32_bf16 v[84:87], a[140:143], v[232:235], v[84:87]
ds_read_b64_tr_b16 a[186:187], %[vTr] offset:59136
v_mfma_f32_16x16x32_bf16 v[92:95], a[140:143], v[236:239], v[92:95]
ds_read_b64_tr_b16 a[188:189], %[vTr] offset:58888
v_mfma_f32_16x16x32_bf16 v[96:99], a[144:147], v[224:227], v[96:99]
v_mfma_f32_16x16x32_bf16 v[104:107], a[144:147], v[228:231], v[104:107]
ds_read_b64_tr_b16 a[190:191], %[vTr] offset:59144
v_mfma_f32_16x16x32_bf16 v[112:115], a[144:147], v[232:235], v[112:115]
s_add_u32 s56, %[sKV], 16384
s_add_u32 m0, %[sLw], 16384
v_mfma_f32_16x16x32_bf16 v[120:123], a[144:147], v[236:239], v[120:123]
buffer_load_dwordx4 %[vVs0], s[16:19], s56 offen offset:0 lds
v_mfma_f32_16x16x32_bf16 v[100:103], a[148:151], v[224:227], v[100:103]
v_mfma_f32_16x16x32_bf16 v[108:111], a[148:151], v[228:231], v[108:111]
v_mfma_f32_16x16x32_bf16 v[116:119], a[148:151], v[232:235], v[116:119]
v_mfma_f32_16x16x32_bf16 v[124:127], a[148:151], v[236:239], v[124:127]
buffer_load_dwordx4 %[vVs1], s[16:19], s56 offen offset:1024 lds
v_mfma_f32_16x16x32_bf16 v[128:131], a[152:155], v[224:227], v[128:131]
v_mfma_f32_16x16x32_bf16 v[136:139], a[152:155], v[228:231], v[136:139]
v_mfma_f32_16x16x32_bf16 v[144:147], a[152:155], v[232:235], v[144:147]
v_mfma_f32_16x16x32_bf16 v[152:155], a[152:155], v[236:239], v[152:155]
buffer_load_dwordx4 %[vVs0], s[16:19], s56 offen offset:2048 lds
v_mfma_f32_16x16x32_bf16 v[132:135], a[156:159], v[224:227], v[132:135]
v_mfma_f32_16x16x32_bf16 v[140:143], a[156:159], v[228:231], v[140:143]
v_mfma_f32_16x16x32_bf16 v[148:151], a[156:159], v[232:235], v[148:151]
v_mfma_f32_16x16x32_bf16 v[156:159], a[156:159], v[236:239], v[156:159]
buffer_load_dwordx4 %[vVs1], s[16:19], s56 offen offset:3072 lds
s_waitcnt vmcnt(20) lgkmcnt(0)
s_barrier
v_mfma_f32_16x16x32_bf16 v[32:35], a[160:163], v[160:163], v[32:35]
v_mfma_f32_16x16x32_bf16 v[40:43], a[160:163], v[164:167], v[40:43]
ds_read_b64_tr_b16 a[128:129], v29 offset:0
v_mfma_f32_16x16x32_bf16 v[48:51], a[160:163], v[168:171], v[48:51]
LPa_2:
s_cbranch_execz LRa
ds_read_b64_tr_b16 a[130:131], v29 offset:256
v_mfma_f32_16x16x32_bf16 v[56:59], a[160:163], v[172:175], v[56:59]
ds_read_b64_tr_b16 a[132:133], v29 offset:8
v_mfma_f32_16x16x32_bf16 v[36:39], a[164:167], v[160:163], v[36:39]
ds_read_b64_tr_b16 a[134:135], v29 offset:264
v_mfma_f32_16x16x32_bf16 v[44:47], a[164:167], v[164:167], v[44:47]
ds_read_b64_tr_b16 a[136:137], v29 offset:512
v_mfma_f32_16x16x32_bf16 v[52:55], a[164:167], v[168:171], v[52:55]
s_add_u32 m0, %[sLw], 81920
ds_read_b64_tr_b16 a[138:139], v29 offset:768
v_mfma_f32_16x16x32_bf16 v[60:63], a[164:167], v[172:175], v[60:63]
buffer_load_dwordx4 %[vVs0], s[20:23], s56 offen offset:0 lds
v_mfma_f32_16x16x32_bf16 v[64:67], a[168:171], v[160:163], v[64:67]
ds_read_b64_tr_b16 a[140:141], v29 offset:520
v_mfma_f32_16x16x32_bf16 v[72:75], a[168:171], v[164:167], v[72:75]
ds_read_b64_tr_b16 a[142:143], v29 offset:776
v_mfma_f32_16x16x32_bf16 v[80:83], a[168:171], v[168:171], v[80:83]
ds_read_b64_tr_b16 a[144:145], v29 offset:1024
v_mfma_f32_16x16x32_bf16 v[88:91], a[168:171], v[172:175], v[88:91]
buffer_load_dwordx4 %[vVs1], s[20:23], s56 offen offset:1024 lds
v_mfma_f32_16x16x32_bf16 v[68:71], a[172:175], v[160:163], v[68:71]
ds_read_b64_tr_b16 a[146:147], v29 offset:1280
v_mfma_f32_16x16x32_bf16 v[76:79], a[172:175], v[164:167], v[76:79]
ds_read_b64_tr_b16 a[148:149], v29 offset:1032
v_mfma_f32_16x16x32_bf16 v[84:87], a[172:175], v[168:171], v[84:87]
ds_read_b64_tr_b16 a[150:151], v29 offset:1288
v_mfma_f32_16x16x32_bf16 v[92:95], a[172:175], v[172:175], v[92:95]
buffer_load_dwordx4 %[vVs0], s[20:23], s56 offen offset:2048 lds
v_mfma_f32_16x16x32_bf16 v[96:99], a[176:179], v[160:163], v[96:99]
v_mfma_f32_16x16x32_bf16 v[104:107], a[176:179], v[164:167], v[104:107]
ds_read_b64_tr_b16 a[152:153], v29 offset:1536
v_mfma_f32_16x16x32_bf16 v[112:115], a[176:179], v[168:171], v[112:115]
ds_read_b64_tr_b16 a[154:155], v29 offset:1792
v_mfma_f32_16x16x32_bf16 v[120:123], a[176:179], v[172:175], v[120:123]
buffer_load_dwordx4 %[vVs1], s[20:23], s56 offen offset:3072 lds
v_mfma_f32_16x16x32_bf16 v[100:103], a[180:183], v[160:163], v[100:103]
ds_read_b64_tr_b16 a[156:157], v29 offset:1544
v_mfma_f32_16x16x32_bf16 v[108:111], a[180:183], v[164:167], v[108:111]
ds_read_b64_tr_b16 a[158:159], v29 offset:1800
v_mfma_f32_16x16x32_bf16 v[116:119], a[180:183], v[168:171], v[116:119]
v_mfma_f32_16x16x32_bf16 v[124:127], a[180:183], v[172:175], v[124:127]
buffer_store_dword v24, v254, %[sWS], s93 offen offset:0 sc1
v_mfma_f32_16x16x32_bf16 v[128:131], a[184:187], v[160:163], v[128:131]
v_mfma_f32_16x16x32_bf16 v[136:139], a[184:187], v[164:167], v[136:139]
v_mfma_f32_16x16x32_bf16 v[144:147], a[184:187], v[168:171], v[144:147]
v_mfma_f32_16x16x32_bf16 v[152:155], a[184:187], v[172:175], v[152:155]
buffer_store_short v26, v255, %[sWS], s94 offen offset:0 sc1
v_mfma_f32_16x16x32_bf16 v[132:135], a[188:191], v[160:163], v[132:135]
v_mfma_f32_16x16x32_bf16 v[140:143], a[188:191], v[164:167], v[140:143]
v_mfma_f32_16x16x32_bf16 v[148:151], a[188:191], v[168:171], v[148:151]
v_mfma_f32_16x16x32_bf16 v[156:159], a[188:191], v[172:175], v[156:159]
buffer_store_short v27, v255, %[sWS], s94 offen offset:1152 sc1
s_waitcnt vmcnt(23) lgkmcnt(0)
v_mfma_f32_16x16x32_bf16 v[32:35], a[128:131], v[176:179], v[32:35]
v_mfma_f32_16x16x32_bf16 v[40:43], a[128:131], v[180:183], v[40:43]
ds_read_b64_tr_b16 a[160:161], v29 offset:8192
v_mfma_f32_16x16x32_bf16 v[48:51], a[128:131], v[184:187], v[48:51]
ds_read_b64_tr_b16 a[162:163], v29 offset:8448
v_mfma_f32_16x16x32_bf16 v[56:59], a[128:131], v[188:191], v[56:59]
ds_read_b64_tr_b16 a[164:165], v29 offset:8200
v_mfma_f32_16x16x32_bf16 v[36:39], a[132:135], v[176:179], v[36:39]
ds_read_b64_tr_b16 a[166:167], v29 offset:8456
v_mfma_f32_16x16x32_bf16 v[44:47], a[132:135], v[180:183], v[44:47]
ds_read_b64_tr_b16 a[168:169], v29 offset:8704
v_mfma_f32_16x16x32_bf16 v[52:55], a[132:135], v[184:187], v[52:55]
ds_read_b64_tr_b16 a[170:171], v29 offset:8960
v_mfma_f32_16x16x32_bf16 v[60:63], a[132:135], v[188:191], v[60:63]
ds_read_b64_tr_b16 a[172:173], v29 offset:8712
v_mfma_f32_16x16x32_bf16 v[64:67], a[136:139], v[176:179], v[64:67]
ds_read_b64_tr_b16 a[174:175], v29 offset:8968
v_mfma_f32_16x16x32_bf16 v[72:75], a[136:139], v[180:183], v[72:75]
ds_read_b64_tr_b16 a[176:177], v29 offset:9216
v_mfma_f32_16x16x32_bf16 v[80:83], a[136:139], v[184:187], v[80:83]
ds_read_b64_tr_b16 a[178:179], v29 offset:9472
v_mfma_f32_16x16x32_bf16 v[88:91], a[136:139], v[188:191], v[88:91]
ds_read_b64_tr_b16 a[180:181], v29 offset:9224
v_mfma_f32_16x16x32_bf16 v[68:71], a[140:143], v[176:179], v[68:71]
ds_read_b64_tr_b16 a[182:183], v29 offset:9480
v_mfma_f32_16x16x32_bf16 v[76:79], a[140:143], v[180:183], v[76:79]
ds_read_b64_tr_b16 a[184:185], v29 offset:9728
v_mfma_f32_16x16x32_bf16 v[84:87], a[140:143], v[184:187], v[84:87]
ds_read_b64_tr_b16 a[186:187], v29 offset:9984
v_mfma_f32_16x16x32_bf16 v[92:95], a[140:143], v[188:191], v[92:95]
ds_read_b64_tr_b16 a[188:189], v29 offset:9736
v_mfma_f32_16x16x32_bf16 v[96:99], a[144:147], v[176:179], v[96:99]
v_mfma_f32_16x16x32_bf16 v[104:107], a[144:147], v[180:183], v[104:107]
ds_read_b64_tr_b16 a[190:191], v29 offset:9992
v_mfma_f32_16x16x32_bf16 v[112:115], a[144:147], v[184:187], v[112:115]
s_add_u32 s56, %[sKV], 32768
s_add_u32 m0, %[sLw], 32768
v_mfma_f32_16x16x32_bf16 v[120:123], a[144:147], v[188:191], v[120:123]
)"
R"(buffer_load_dwordx4 %[vVs0], s[16:19], s56 offen offset:0 lds
v_mfma_f32_16x16x32_bf16 v[100:103], a[148:151], v[176:179], v[100:103]
v_mfma_f32_16x16x32_bf16 v[108:111], a[148:151], v[180:183], v[108:111]
v_mfma_f32_16x16x32_bf16 v[116:119], a[148:151], v[184:187], v[116:119]
v_mfma_f32_16x16x32_bf16 v[124:127], a[148:151], v[188:191], v[124:127]
buffer_load_dwordx4 %[vVs1], s[16:19], s56 offen offset:1024 lds
v_mfma_f32_16x16x32_bf16 v[128:131], a[152:155], v[176:179], v[128:131]
s_lshl_b32 s52, s76, 22
v_mfma_f32_16x16x32_bf16 v[136:139], a[152:155], v[180:183], v[136:139]
v_mfma_f32_16x16x32_bf16 v[144:147], a[152:155], v[184:187], v[144:147]
s_add_u32 s52, s52, s34
v_mfma_f32_16x16x32_bf16 v[152:155], a[152:155], v[188:191], v[152:155]
buffer_load_dwordx4 %[vVs0], s[16:19], s56 offen offset:2048 lds
v_mfma_f32_16x16x32_bf16 v[132:135], a[156:159], v[176:179], v[132:135]
s_xor_b32 s53, s35, s71
v_mfma_f32_16x16x32_bf16 v[140:143], a[156:159], v[180:183], v[140:143]
v_mfma_f32_16x16x32_bf16 v[148:151], a[156:159], v[184:187], v[148:151]
s_xor_b32 s97, s36, s71
v_mfma_f32_16x16x32_bf16 v[156:159], a[156:159], v[188:191], v[156:159]
buffer_load_dwordx4 %[vVs1], s[16:19], s56 offen offset:3072 lds
s_waitcnt vmcnt(15) lgkmcnt(0)
s_barrier
v_mfma_f32_16x16x32_bf16 v[32:35], a[160:163], v[192:195], v[32:35]
ds_read_b128 a[192:195], %[vKrd] offset:0
v_mov_b32 v224, v240
v_mov_b32 v225, v241
v_mfma_f32_16x16x32_bf16 v[40:43], a[160:163], v[196:199], v[40:43]
ds_read_b128 a[208:211], %[vKrd] offset:4096
v_mov_b32 v226, v242
v_mov_b32 v227, v243
v_mfma_f32_16x16x32_bf16 v[36:39], a[164:167], v[192:195], v[36:39]
ds_read_b128 a[196:199], %[vKrd] offset:512
v_mov_b32 v230, v248
v_mov_b32 v209, v249
v_mfma_f32_16x16x32_bf16 v[44:47], a[164:167], v[196:199], v[44:47]
ds_read_b128 a[212:215], %[vKrd] offset:4608
v_mov_b32 v208, v250
v_mov_b32 v212, v251
v_mfma_f32_16x16x32_bf16 v[64:67], a[168:171], v[192:195], v[64:67]
ds_read_b128 a[200:203], %[vKrd] offset:1024
v_mov_b32 v231, v252
v_mov_b32 v244, 0
v_mfma_f32_16x16x32_bf16 v[72:75], a[168:171], v[196:199], v[72:75]
ds_read_b128 a[216:219], %[vKrd] offset:5120
v_mov_b32 v245, 0
v_mov_b32 v246, 0
v_mfma_f32_16x16x32_bf16 v[68:71], a[172:175], v[192:195], v[68:71]
ds_read_b128 a[204:207], %[vKrd] offset:1536
v_mov_b32_e64 v247, 0
v_mfma_f32_16x16x32_bf16 v[76:79], a[172:175], v[196:199], v[76:79]
ds_read_b128 a[220:223], %[vKrd] offset:5632
v_mov_b32_e64 v210, 1
v_mfma_f32_16x16x32_bf16 v[96:99], a[176:179], v[192:195], v[96:99]
s_add_u32 m0, %[sLw], 98304
v_mov_b32_e64 v213, v212
v_mfma_f32_16x16x32_bf16 v[104:107], a[176:179], v[196:199], v[104:107]
buffer_load_dwordx4 %[vVs0], s[20:23], s56 offen offset:0 lds
v_mfma_f32_16x16x32_bf16 v[100:103], a[180:183], v[192:195], v[100:103]
v_mov_b32 v214, v212
v_mov_b32 v215, v212
v_mfma_f32_16x16x32_bf16 v[108:111], a[180:183], v[196:199], v[108:111]
buffer_load_dwordx4 %[vVs1], s[20:23], s56 offen offset:1024 lds
v_mfma_f32_16x16x32_bf16 v[128:131], a[184:187], v[192:195], v[128:131]
v_mov_b32 v216, 0
v_mov_b32 v217, 0
v_mfma_f32_16x16x32_bf16 v[136:139], a[184:187], v[196:199], v[136:139]
buffer_load_dwordx4 %[vVs0], s[20:23], s56 offen offset:2048 lds
v_mfma_f32_16x16x32_bf16 v[132:135], a[188:191], v[192:195], v[132:135]
v_mov_b32 v218, 0
v_mov_b32 v219, 0
v_mfma_f32_16x16x32_bf16 v[140:143], a[188:191], v[196:199], v[140:143]
buffer_load_dwordx4 %[vVs1], s[20:23], s56 offen offset:3072 lds
v_mfma_f32_16x16x32_bf16 v[48:51], a[160:163], v[200:203], v[48:51]
v_mov_b32 v220, 0
v_mov_b32 v221, 0
v_mfma_f32_16x16x32_bf16 v[56:59], a[160:163], v[204:207], v[56:59]
v_mov_b32 v222, 0
v_mov_b32 v223, 0
v_mfma_f32_16x16x32_bf16 v[52:55], a[164:167], v[200:203], v[52:55]
v_cvt_pk_bf16_f32 v32, v32, v33
v_cvt_pk_bf16_f32 v33, v34, v35
v_mfma_f32_16x16x32_bf16 v[60:63], a[164:167], v[204:207], v[60:63]
v_cvt_pk_bf16_f32 v34, v36, v37
v_cvt_pk_bf16_f32 v35, v38, v39
v_mfma_f32_16x16x32_bf16 v[80:83], a[168:171], v[200:203], v[80:83]
v_cvt_pk_bf16_f32 v36, v40, v41
v_cvt_pk_bf16_f32 v37, v42, v43
v_mfma_f32_16x16x32_bf16 v[88:91], a[168:171], v[204:207], v[88:91]
v_cvt_pk_bf16_f32 v38, v44, v45
v_cvt_pk_bf16_f32 v39, v46, v47
v_mfma_f32_16x16x32_bf16 v[84:87], a[172:175], v[200:203], v[84:87]
v_cvt_pk_bf16_f32 v64, v64, v65
v_cvt_pk_bf16_f32 v65, v66, v67
v_mfma_f32_16x16x32_bf16 v[92:95], a[172:175], v[204:207], v[92:95]
v_cvt_pk_bf16_f32 v66, v68, v69
v_cvt_pk_bf16_f32 v67, v70, v71
v_mfma_f32_16x16x32_bf16 v[112:115], a[176:179], v[200:203], v[112:115]
v_cvt_pk_bf16_f32 v68, v72, v73
v_cvt_pk_bf16_f32 v69, v74, v75
v_mfma_f32_16x16x32_bf16 v[120:123], a[176:179], v[204:207], v[120:123]
v_cvt_pk_bf16_f32 v70, v76, v77
v_cvt_pk_bf16_f32 v71, v78, v79
v_mfma_f32_16x16x32_bf16 v[116:119], a[180:183], v[200:203], v[116:119]
v_cvt_pk_bf16_f32 v96, v96, v97
v_cvt_pk_bf16_f32 v97, v98, v99
v_mfma_f32_16x16x32_bf16 v[124:127], a[180:183], v[204:207], v[124:127]
v_cvt_pk_bf16_f32 v98, v100, v101
v_cvt_pk_bf16_f32 v99, v102, v103
v_mfma_f32_16x16x32_bf16 v[144:147], a[184:187], v[200:203], v[144:147]
v_cvt_pk_bf16_f32 v100, v104, v105
v_cvt_pk_bf16_f32 v101, v106, v107
v_mfma_f32_16x16x32_bf16 v[152:155], a[184:187], v[204:207], v[152:155]
v_cvt_pk_bf16_f32 v102, v108, v109
v_cvt_pk_bf16_f32 v103, v110, v111
v_mfma_f32_16x16x32_bf16 v[148:151], a[188:191], v[200:203], v[148:151]
v_cvt_pk_bf16_f32 v128, v128, v129
v_cvt_pk_bf16_f32 v129, v130, v131
v_mfma_f32_16x16x32_bf16 v[156:159], a[188:191], v[204:207], v[156:159]
v_cvt_pk_bf16_f32 v130, v132, v133
v_cvt_pk_bf16_f32 v131, v134, v135
s_add_u32 s56, %[sKV], 49152
v_cvt_pk_bf16_f32 v132, v136, v137
v_cvt_pk_bf16_f32 v133, v138, v139
v_cvt_pk_bf16_f32 v134, v140, v141
v_cvt_pk_bf16_f32 v135, v142, v143
v_xor_b32 v140, 32, %[vVr]
v_mov_b32 v104, v253
v_mov_b32 v106, 0
s_waitcnt lgkmcnt(0)
v_mfma_f32_16x16x32_bf16 v[160:163], a[192:195], v[32:35], 0
ds_read_b128 a[224:227], %[vKrd] offset:8192
v_cvt_pk_bf16_f32 v48, v48, v49
v_cvt_pk_bf16_f32 v49, v50, v51
v_mfma_f32_16x16x32_bf16 v[164:167], a[208:211], v[32:35], 0
s_add_u32 m0, %[sLw], 49152
ds_read_b64_tr_b16 a[128:129], %[vVr] offset:0
v_cvt_pk_bf16_f32 v50, v52, v53
v_cvt_pk_bf16_f32 v51, v54, v55
v_mfma_f32_16x16x32_bf16 v[160:163], a[196:199], v[64:67], v[160:163]
ds_read_b128 a[240:243], %[vKrd] offset:12288
v_cvt_pk_bf16_f32 v52, v56, v57
v_cvt_pk_bf16_f32 v53, v58, v59
v_mfma_f32_16x16x32_bf16 v[164:167], a[212:215], v[64:67], v[164:167]
ds_read_b64_tr_b16 a[130:131], %[vVr] offset:4096
buffer_load_dwordx4 %[vVs0], s[16:19], s56 offen offset:0 lds
v_cvt_pk_bf16_f32 v54, v60, v61
v_cvt_pk_bf16_f32 v55, v62, v63
v_mfma_f32_16x16x32_bf16 v[160:163], a[200:203], v[96:99], v[160:163]
ds_read_b128 a[228:231], %[vKrd] offset:8704
v_cvt_pk_bf16_f32 v80, v80, v81
v_cvt_pk_bf16_f32 v81, v82, v83
v_mfma_f32_16x16x32_bf16 v[164:167], a[216:219], v[96:99], v[164:167]
ds_read_b64_tr_b16 a[132:133], v140 offset:0
v_cvt_pk_bf16_f32 v82, v84, v85
v_cvt_pk_bf16_f32 v83, v86, v87
v_mfma_f32_16x16x32_bf16 v[160:163], a[204:207], v[128:131], v[160:163]
ds_read_b128 a[244:247], %[vKrd] offset:12800
v_cvt_pk_bf16_f32 v84, v88, v89
v_cvt_pk_bf16_f32 v85, v90, v91
v_mfma_f32_16x16x32_bf16 v[164:167], a[220:223], v[128:131], v[164:167]
ds_read_b64_tr_b16 a[134:135], v140 offset:4096
buffer_load_dwordx4 %[vVs1], s[16:19], s56 offen offset:1024 lds
v_cvt_pk_bf16_f32 v86, v92, v93
v_cvt_pk_bf16_f32 v87, v94, v95
v_mfma_f32_16x16x32_bf16 v[168:171], a[192:195], v[36:39], 0
ds_read_b128 a[232:235], %[vKrd] offset:9216
v_cvt_pk_bf16_f32 v112, v112, v113
v_cvt_pk_bf16_f32 v113, v114, v115
v_mfma_f32_16x16x32_bf16 v[172:175], a[208:211], v[36:39], 0
ds_read_b64_tr_b16 a[136:137], %[vVr] offset:512
v_cvt_pk_bf16_f32 v114, v116, v117
v_cvt_pk_bf16_f32 v115, v118, v119
v_mfma_f32_16x16x32_bf16 v[168:171], a[196:199], v[68:71], v[168:171]
ds_read_b128 a[248:251], %[vKrd] offset:13312
v_cvt_pk_bf16_f32 v116, v120, v121
v_cvt_pk_bf16_f32 v117, v122, v123
v_mfma_f32_16x16x32_bf16 v[172:175], a[212:215], v[68:71], v[172:175]
ds_read_b64_tr_b16 a[138:139], %[vVr] offset:4608
buffer_load_dwordx4 %[vVs0], s[16:19], s56 offen offset:2048 lds
v_cvt_pk_bf16_f32 v118, v124, v125
v_cvt_pk_bf16_f32 v119, v126, v127
v_mfma_f32_16x16x32_bf16 v[168:171], a[200:203], v[100:103], v[168:171]
ds_read_b128 a[236:239], %[vKrd] offset:9728
v_cvt_pk_bf16_f32 v144, v144, v145
v_cvt_pk_bf16_f32 v145, v146, v147
v_mfma_f32_16x16x32_bf16 v[172:175], a[216:219], v[100:103], v[172:175]
ds_read_b64_tr_b16 a[140:141], v140 offset:512
v_cvt_pk_bf16_f32 v146, v148, v149
v_cvt_pk_bf16_f32 v147, v150, v151
v_mfma_f32_16x16x32_bf16 v[168:171], a[204:207], v[132:135], v[168:171]
ds_read_b128 a[252:255], %[vKrd] offset:13824
v_cvt_pk_bf16_f32 v148, v152, v153
v_cvt_pk_bf16_f32 v149, v154, v155
v_mfma_f32_16x16x32_bf16 v[172:175], a[220:223], v[132:135], v[172:175]
ds_read_b64_tr_b16 a[142:143], v140 offset:4608
buffer_load_dwordx4 %[vVs1], s[16:19], s56 offen offset:3072 lds
v_cvt_pk_bf16_f32 v150, v156, v157
v_cvt_pk_bf16_f32 v151, v158, v159
L_slot1_%=:
ds_read_b64_tr_b16 a[144:145], %[vVr] offset:1024
)"
R"(ds_read_b64_tr_b16 a[146:147], %[vVr] offset:5120
ds_read_b64_tr_b16 a[148:149], v140 offset:1024
ds_read_b64_tr_b16 a[150:151], v140 offset:5120
ds_read_b64_tr_b16 a[152:153], %[vVr] offset:1536
ds_read_b64_tr_b16 a[154:155], %[vVr] offset:5632
ds_read_b64_tr_b16 a[156:157], v140 offset:1536
ds_read_b64_tr_b16 a[158:159], v140 offset:5632
v_mfma_f32_16x16x32_bf16 v[176:179], a[192:195], v[48:51], 0
v_max3_f32 v88, v160, v161, v162
v_max3_f32 v90, v163, v164, v165
v_max3_f32 v89, v168, v169, v170
v_mfma_f32_16x16x32_bf16 v[180:183], a[208:211], v[48:51], 0
v_max3_f32 v91, v171, v172, v173
v_max3_f32 v88, v88, v166, v167
v_max3_f32 v89, v89, v174, v175
v_mfma_f32_16x16x32_bf16 v[176:179], a[196:199], v[80:83], v[176:179]
v_max_f32 v88, v88, v90
v_max_f32 v89, v89, v91
v_mov_b32_e64 v90, v88
v_mfma_f32_16x16x32_bf16 v[180:183], a[212:215], v[80:83], v[180:183]
v_mov_b32 v91, v89
s_nop 0
v_permlane32_swap_b32_e64 v88, v90
v_mfma_f32_16x16x32_bf16 v[176:179], a[200:203], v[112:115], v[176:179]
v_permlane32_swap_b32 v89, v91
s_nop 0
v_max_f32_e64 v88, v88, v90
v_mfma_f32_16x16x32_bf16 v[180:183], a[216:219], v[112:115], v[180:183]
v_max_f32 v89, v89, v91
v_mov_b32 v90, v88
v_mov_b32_e64 v91, v89
v_mfma_f32_16x16x32_bf16 v[176:179], a[204:207], v[144:147], v[176:179]
s_nop 0
v_permlane16_swap_b32 v88, v90
v_permlane16_swap_b32_e64 v89, v91
v_mfma_f32_16x16x32_bf16 v[180:183], a[220:223], v[144:147], v[180:183]
s_nop 0
v_max_f32 v88, v88, v90
v_max_f32_e64 v89, v89, v91
v_mfma_f32_16x16x32_bf16 v[184:187], a[192:195], v[52:55], 0
v_ceil_f32 v88, v88
v_ceil_f32 v89, v89
v_add_f32_e64 v88, s55, v88
v_mfma_f32_16x16x32_bf16 v[188:191], a[208:211], v[52:55], 0
v_add_f32 v89, s55, v89
v_sub_f32 v160, v160, v88
v_sub_f32_e64 v161, v161, v88
v_mfma_f32_16x16x32_bf16 v[184:187], a[196:199], v[84:87], v[184:187]
v_sub_f32 v162, v162, v88
v_sub_f32 v163, v163, v88
v_sub_f32_e64 v164, v164, v88
v_mfma_f32_16x16x32_bf16 v[188:191], a[212:215], v[84:87], v[188:191]
v_sub_f32 v165, v165, v88
v_sub_f32 v166, v166, v88
v_sub_f32_e64 v167, v167, v88
v_mfma_f32_16x16x32_bf16 v[184:187], a[200:203], v[116:119], v[184:187]
v_sub_f32 v168, v168, v89
v_sub_f32 v169, v169, v89
v_sub_f32_e64 v170, v170, v89
v_mfma_f32_16x16x32_bf16 v[188:191], a[216:219], v[116:119], v[188:191]
v_sub_f32 v171, v171, v89
v_sub_f32 v172, v172, v89
v_sub_f32_e64 v173, v173, v89
v_mfma_f32_16x16x32_bf16 v[184:187], a[204:207], v[148:151], v[184:187]
v_sub_f32 v174, v174, v89
v_sub_f32 v175, v175, v89
v_sub_f32_e64 v192, 0, v88
v_mfma_f32_16x16x32_bf16 v[188:191], a[220:223], v[148:151], v[188:191]
v_sub_f32 v193, 0, v88
v_sub_f32 v194, 0, v88
v_sub_f32 v195, 0, v88
v_sub_f32 v196, 0, v89
v_sub_f32 v197, 0, v89
v_sub_f32 v198, 0, v89
v_sub_f32 v199, 0, v89
v_exp_f32 v72, v160
v_exp_f32 v73, v161
v_exp_f32 v74, v162
v_cvt_pk_bf16_f32 v40, v72, v73
v_exp_f32 v75, v163
v_exp_f32 v72, v164
v_cvt_pk_bf16_f32 v41, v74, v75
v_exp_f32 v73, v165
v_exp_f32 v74, v166
v_cvt_pk_bf16_f32 v42, v72, v73
v_exp_f32 v75, v167
v_exp_f32 v72, v168
v_cvt_pk_bf16_f32 v43, v74, v75
v_exp_f32 v73, v169
v_exp_f32 v74, v170
v_cvt_pk_bf16_f32 v44, v72, v73
v_exp_f32 v75, v171
v_exp_f32 v72, v172
v_cvt_pk_bf16_f32 v45, v74, v75
v_exp_f32 v73, v173
v_exp_f32 v74, v174
v_cvt_pk_bf16_f32 v46, v72, v73
v_exp_f32_e64 v75, v175
s_nop 0
v_cvt_pk_bf16_f32 v47, v74, v75
s_waitcnt lgkmcnt(0)
s_buffer_load_dwordx8 s[80:87], %[sWS], s30
ds_read_b64_tr_b16 a[160:161], %[vVr] offset:8192
ds_read_b64_tr_b16 a[162:163], %[vVr] offset:12288
ds_read_b64_tr_b16 a[164:165], v140 offset:8192
ds_read_b64_tr_b16 a[166:167], v140 offset:12288
ds_read_b64_tr_b16 a[168:169], %[vVr] offset:8704
ds_read_b64_tr_b16 a[170:171], %[vVr] offset:12800
ds_read_b64_tr_b16 a[172:173], v140 offset:8704
ds_read_b64_tr_b16 a[174:175], v140 offset:12800
v_smfmac_f32_16x16x64_bf16 v[216:219], v[212:215], v[40:47], v231
v_max3_f32 v88, v176, v177, v178
v_max3_f32 v90, v179, v180, v181
v_max3_f32 v89, v184, v185, v186
v_mfma_f32_16x16x32_bf16 a[0:3], a[128:131], v[40:43], 0
v_max3_f32 v91, v187, v188, v189
v_max3_f32 v88, v88, v182, v183
v_max3_f32 v89, v89, v190, v191
v_mfma_f32_16x16x32_bf16 a[32:35], a[128:131], v[44:47], 0
v_max_f32 v88, v88, v90
v_max_f32 v89, v89, v91
v_mov_b32_e64 v90, v88
v_mfma_f32_16x16x32_bf16 a[4:7], a[132:135], v[40:43], 0
v_mov_b32 v91, v89
s_nop 0
v_permlane32_swap_b32_e64 v88, v90
v_mfma_f32_16x16x32_bf16 a[36:39], a[132:135], v[44:47], 0
v_permlane32_swap_b32 v89, v91
s_nop 0
v_max_f32_e64 v88, v88, v90
v_mfma_f32_16x16x32_bf16 a[8:11], a[136:139], v[40:43], 0
v_max_f32 v89, v89, v91
v_mov_b32 v90, v88
v_mov_b32_e64 v91, v89
v_mfma_f32_16x16x32_bf16 a[40:43], a[136:139], v[44:47], 0
s_nop 0
v_permlane16_swap_b32 v88, v90
v_permlane16_swap_b32_e64 v89, v91
v_mfma_f32_16x16x32_bf16 a[12:15], a[140:143], v[40:43], 0
s_nop 0
v_max_f32 v88, v88, v90
v_max_f32_e64 v89, v89, v91
v_mfma_f32_16x16x32_bf16 a[44:47], a[140:143], v[44:47], 0
v_ceil_f32 v88, v88
v_ceil_f32 v89, v89
v_add_f32_e64 v88, s55, v88
v_mfma_f32_16x16x32_bf16 a[16:19], a[144:147], v[40:43], 0
v_add_f32 v89, s55, v89
v_sub_f32 v176, v176, v88
v_sub_f32_e64 v177, v177, v88
v_mfma_f32_16x16x32_bf16 a[48:51], a[144:147], v[44:47], 0
v_sub_f32 v178, v178, v88
v_sub_f32 v179, v179, v88
v_sub_f32_e64 v180, v180, v88
v_mfma_f32_16x16x32_bf16 a[20:23], a[148:151], v[40:43], 0
v_sub_f32 v181, v181, v88
v_sub_f32 v182, v182, v88
v_sub_f32_e64 v183, v183, v88
v_mfma_f32_16x16x32_bf16 a[52:55], a[148:151], v[44:47], 0
v_sub_f32 v184, v184, v89
v_sub_f32 v185, v185, v89
v_sub_f32_e64 v186, v186, v89
v_mfma_f32_16x16x32_bf16 a[24:27], a[152:155], v[40:43], 0
v_sub_f32 v187, v187, v89
v_sub_f32 v188, v188, v89
v_sub_f32_e64 v189, v189, v89
v_mfma_f32_16x16x32_bf16 a[56:59], a[152:155], v[44:47], 0
v_sub_f32 v190, v190, v89
v_sub_f32 v191, v191, v89
v_sub_f32_e64 v200, 0, v88
v_mfma_f32_16x16x32_bf16 a[28:31], a[156:159], v[40:43], 0
v_sub_f32 v201, 0, v88
v_sub_f32 v202, 0, v88
v_sub_f32_e64 v203, 0, v88
v_mfma_f32_16x16x32_bf16 a[60:63], a[156:159], v[44:47], 0
v_sub_f32 v204, 0, v89
v_sub_f32 v205, 0, v89
v_sub_f32 v206, 0, v89
v_sub_f32 v207, 0, v89
v_exp_f32 v72, v176
v_exp_f32 v73, v177
v_exp_f32_e64 v74, v178
v_cvt_pk_bf16_f32 v56, v72, v73
v_exp_f32 v75, v179
v_exp_f32 v72, v180
v_cvt_pk_bf16_f32 v57, v74, v75
v_exp_f32 v73, v181
v_exp_f32 v74, v182
v_cvt_pk_bf16_f32 v58, v72, v73
v_exp_f32 v75, v183
v_exp_f32 v72, v184
v_cvt_pk_bf16_f32 v59, v74, v75
v_exp_f32 v73, v185
v_exp_f32 v74, v186
v_cvt_pk_bf16_f32 v60, v72, v73
v_exp_f32 v75, v187
s_mov_b32 s58, 6
s_waitcnt vmcnt(15)
s_barrier
v_mfma_f32_16x16x32_bf16 v[160:163], a[224:227], v[32:35], v[192:195]
)"
R"(ds_read_b64_tr_b16 a[176:177], %[vVr] offset:9216
v_cvt_pk_bf16_f32 v61, v74, v75
v_mfma_f32_16x16x32_bf16 v[164:167], a[240:243], v[32:35], v[192:195]
v_exp_f32_e64 v72, v188
v_mfma_f32_16x16x32_bf16 v[160:163], a[228:231], v[64:67], v[160:163]
ds_read_b64_tr_b16 a[178:179], %[vVr] offset:13312
ds_read_b64_tr_b16 a[180:181], v140 offset:9216
v_mfma_f32_16x16x32_bf16 v[164:167], a[244:247], v[64:67], v[164:167]
v_exp_f32_e64 v73, v189
v_mfma_f32_16x16x32_bf16 v[160:163], a[232:235], v[96:99], v[160:163]
ds_read_b64_tr_b16 a[182:183], v140 offset:13312
v_cvt_pk_bf16_f32 v62, v72, v73
v_mfma_f32_16x16x32_bf16 v[164:167], a[248:251], v[96:99], v[164:167]
v_exp_f32_e64 v74, v190
v_mfma_f32_16x16x32_bf16 v[160:163], a[236:239], v[128:131], v[160:163]
v_mfma_f32_16x16x32_bf16 v[164:167], a[252:255], v[128:131], v[164:167]
v_exp_f32_e64 v75, v191
v_mfma_f32_16x16x32_bf16 v[168:171], a[224:227], v[36:39], v[196:199]
ds_read_b64_tr_b16 a[184:185], %[vVr] offset:9728
v_cvt_pk_bf16_f32 v63, v74, v75
v_mfma_f32_16x16x32_bf16 v[172:175], a[240:243], v[36:39], v[196:199]
s_nop 1
v_mfma_f32_16x16x32_bf16 v[168:171], a[228:231], v[68:71], v[168:171]
v_exp_f32 v72, v160
v_mfma_f32_16x16x32_bf16 v[172:175], a[244:247], v[68:71], v[172:175]
ds_read_b128 a[192:195], %[vKrd] offset:16384
ds_read_b128 a[208:211], %[vKrd] offset:20480
v_mfma_f32_16x16x32_bf16 v[168:171], a[232:235], v[100:103], v[168:171]
v_exp_f32_e64 v73, v161
v_smfmac_f32_16x16x64_bf16 v[220:223], v[212:215], v[56:63], v231
ds_read_b128 a[196:199], %[vKrd] offset:16896
v_cvt_pk_bf16_f32 v40, v72, v73
v_lshlrev_b32_e64 v108, 3, %[vX]
v_mfma_f32_16x16x32_bf16 v[172:175], a[248:251], v[100:103], v[172:175]
v_exp_f32_e64 v74, v162
v_mfma_f32_16x16x32_bf16 v[168:171], a[236:239], v[132:135], v[168:171]
ds_read_b128 a[212:215], %[vKrd] offset:20992
v_mfma_f32_16x16x32_bf16 v[172:175], a[252:255], v[132:135], v[172:175]
v_exp_f32_e64 v75, v163
v_mfma_f32_16x16x32_bf16 a[64:67], a[128:131], v[56:59], 0
ds_read_b64_tr_b16 a[186:187], %[vVr] offset:13824
v_cvt_pk_bf16_f32 v41, v74, v75
v_mfma_f32_16x16x32_bf16 a[96:99], a[128:131], v[60:63], 0
v_exp_f32_e64 v72, v164
v_mfma_f32_16x16x32_bf16 a[68:71], a[132:135], v[56:59], 0
v_min_u32 v108, 2176, v108
v_mfma_f32_16x16x32_bf16 a[100:103], a[132:135], v[60:63], 0
v_exp_f32_e64 v73, v165
v_mfma_f32_16x16x32_bf16 a[72:75], a[136:139], v[56:59], 0
s_add_u32 m0, %[sLw], 114688
v_cvt_pk_bf16_f32 v42, v72, v73
v_mfma_f32_16x16x32_bf16 a[104:107], a[136:139], v[60:63], 0
v_exp_f32_e64 v74, v166
v_mfma_f32_16x16x32_bf16 a[76:79], a[140:143], v[56:59], 0
buffer_load_dwordx4 %[vVs0], s[20:23], s56 offen offset:0 lds
v_mfma_f32_16x16x32_bf16 a[108:111], a[140:143], v[60:63], 0
v_exp_f32_e64 v75, v167
v_mfma_f32_16x16x32_bf16 a[80:83], a[144:147], v[56:59], 0
ds_read_b64_tr_b16 a[188:189], v140 offset:9728
v_cvt_pk_bf16_f32 v43, v74, v75
v_mfma_f32_16x16x32_bf16 a[112:115], a[144:147], v[60:63], 0
v_exp_f32_e64 v72, v168
v_mfma_f32_16x16x32_bf16 a[84:87], a[148:151], v[56:59], 0
buffer_load_dwordx4 %[vVs1], s[20:23], s56 offen offset:1024 lds
v_mfma_f32_16x16x32_bf16 a[116:119], a[148:151], v[60:63], 0
v_exp_f32_e64 v73, v169
v_mfma_f32_16x16x32_bf16 a[88:91], a[152:155], v[56:59], 0
ds_read_b64_tr_b16 a[190:191], v140 offset:13824
v_cvt_pk_bf16_f32 v44, v72, v73
v_mfma_f32_16x16x32_bf16 a[120:123], a[152:155], v[60:63], 0
v_exp_f32 v74, v170
v_mfma_f32_16x16x32_bf16 a[92:95], a[156:159], v[56:59], 0
s_waitcnt lgkmcnt(0)
global_load_dword v105, v108, s[28:29]
v_mfma_f32_16x16x32_bf16 a[124:127], a[156:159], v[60:63], 0
v_exp_f32 v75, v171
s_branch L_loop_%=
L_fs0_%=:
s_sub_u32 s69, s69, 1
s_cbranch_scc1 L_fsok0_%=
ds_read_b32 v211, v209 offset:0
s_waitcnt lgkmcnt(0)
v_cmp_lt_u32 vcc, v211, v210
s_cbranch_vccnz L_fs0_%=
s_branch L_fsok0_%=
L_fs1_%=:
s_sub_u32 s69, s69, 1
s_cbranch_scc1 L_fsok1_%=
ds_read_b32 v211, v209 offset:0
s_waitcnt lgkmcnt(0)
v_cmp_lt_u32 vcc, v211, v210
s_cbranch_vccnz L_fs1_%=
s_branch L_fsok1_%=
L_fs2_%=:
s_sub_u32 s69, s69, 1
s_cbranch_scc1 L_fsok2_%=
ds_read_b32 v211, v209 offset:0
s_waitcnt lgkmcnt(0)
v_cmp_lt_u32 vcc, v211, v210
LPa_3:
s_cbranch_execz LRa
s_cbranch_vccnz L_fs2_%=
s_branch L_fsok2_%=
L_fs3_%=:
s_sub_u32 s69, s69, 1
s_cbranch_scc1 L_fsok3_%=
ds_read_b32 v211, v209 offset:0
s_waitcnt lgkmcnt(0)
v_cmp_lt_u32_e64 vcc, v211, v210
s_cbranch_vccnz L_fs3_%=
s_branch L_fsok3_%=
L_loop_%=:
v_mfma_f32_16x16x32_bf16 v[176:179], a[224:227], v[48:51], v[200:203]
ds_read_b128 a[200:203], %[vKrd] offset:17408
v_cvt_pk_bf16_f32 v45, v74, v75
v_mfma_f32_16x16x32_bf16 v[180:183], a[240:243], v[48:51], v[200:203]
s_lshr_b32 s70, s86, 25
v_exp_f32 v72, v172
v_mfma_f32_16x16x32_bf16 v[176:179], a[228:231], v[80:83], v[176:179]
ds_read_b128 a[216:219], %[vKrd] offset:21504
ds_read_b128 a[204:207], %[vKrd] offset:17920
v_mfma_f32_16x16x32_bf16 v[180:183], a[244:247], v[80:83], v[180:183]
s_bfe_u32 s96, s86, 65560
v_exp_f32_e64 v73, v173
v_mfma_f32_16x16x32_bf16 v[176:179], a[232:235], v[112:115], v[176:179]
ds_read_b128 a[220:223], %[vKrd] offset:22016
v_cvt_pk_bf16_f32 v46, v72, v73
v_mfma_f32_16x16x32_bf16 v[180:183], a[248:251], v[112:115], v[180:183]
s_mov_b32 s93, s84
v_exp_f32 v74, v174
v_mfma_f32_16x16x32_bf16 v[176:179], a[236:239], v[144:147], v[176:179]
ds_read_b64_tr_b16 a[128:129], %[vVr] offset:16384
ds_read_b64_tr_b16 a[130:131], %[vVr] offset:20480
v_mfma_f32_16x16x32_bf16 v[180:183], a[252:255], v[144:147], v[180:183]
s_mov_b32 s94, s85
v_exp_f32 v75, v175
v_mfma_f32_16x16x32_bf16 v[184:187], a[224:227], v[52:55], v[204:207]
ds_read_b64_tr_b16 a[132:133], v140 offset:16384
v_cvt_pk_bf16_f32 v47, v74, v75
v_mfma_f32_16x16x32_bf16 v[188:191], a[240:243], v[52:55], v[204:207]
ds_read_b64_tr_b16 a[134:135], v140 offset:20480
v_mfma_f32_16x16x32_bf16 v[184:187], a[228:231], v[84:87], v[184:187]
v_exp_f32_e64 v72, v176
v_mfma_f32_16x16x32_bf16 v[188:191], a[244:247], v[84:87], v[188:191]
ds_read_b64_tr_b16 a[136:137], %[vVr] offset:16896
ds_read_b64_tr_b16 a[138:139], %[vVr] offset:20992
v_mfma_f32_16x16x32_bf16 v[184:187], a[232:235], v[116:119], v[184:187]
v_exp_f32_e64 v73, v177
v_smfmac_f32_16x16x64_bf16 v[216:219], v[212:215], v[40:47], v231
ds_read_b64_tr_b16 a[140:141], v140 offset:16896
v_cvt_pk_bf16_f32 v56, v72, v73
v_mfma_f32_16x16x32_bf16 v[188:191], a[248:251], v[116:119], v[188:191]
v_exp_f32_e64 v74, v178
v_mfma_f32_16x16x32_bf16 v[184:187], a[236:239], v[148:151], v[184:187]
ds_read_b64_tr_b16 a[142:143], v140 offset:20992
v_mfma_f32_16x16x32_bf16 v[188:191], a[252:255], v[148:151], v[188:191]
v_exp_f32_e64 v75, v179
v_mfma_f32_16x16x32_bf16 a[0:3], a[160:163], v[40:43], a[0:3]
v_cvt_pk_bf16_f32 v57, v74, v75
v_mad_u32_u24 v228, v224, s86, v225
v_mfma_f32_16x16x32_bf16 a[32:35], a[160:163], v[44:47], a[32:35]
v_exp_f32_e64 v72, v180
v_mfma_f32_16x16x32_bf16 a[4:7], a[164:167], v[40:43], a[4:7]
buffer_store_dwordx4 v[244:247], %[vX], %[sWS], s97 offen offset:0 sc1
v_mfma_f32_16x16x32_bf16 a[36:39], a[164:167], v[44:47], a[36:39]
v_exp_f32_e64 v73, v181
v_mfma_f32_16x16x32_bf16 a[8:11], a[168:171], v[40:43], a[8:11]
v_cvt_pk_bf16_f32 v58, v72, v73
v_mul_u32_u24_e64 v229, s96, v227
v_mfma_f32_16x16x32_bf16 a[40:43], a[168:171], v[44:47], a[40:43]
v_exp_f32_e64 v74, v182
v_mfma_f32_16x16x32_bf16 a[12:15], a[172:175], v[40:43], a[12:15]
buffer_load_dwordx4 %[vVs0], s[20:23], s56 offen offset:2048 lds
v_mfma_f32_16x16x32_bf16 a[44:47], a[172:175], v[44:47], a[44:47]
v_exp_f32_e64 v75, v183
v_mfma_f32_16x16x32_bf16 a[16:19], a[176:179], v[40:43], a[16:19]
s_add_u32 s97, s97, 1048576
v_cvt_pk_bf16_f32 v59, v74, v75
v_mfma_f32_16x16x32_bf16 a[48:51], a[176:179], v[44:47], a[48:51]
v_exp_f32_e64 v72, v184
v_mfma_f32_16x16x32_bf16 a[20:23], a[180:183], v[40:43], a[20:23]
buffer_load_dwordx4 %[vVs1], s[20:23], s56 offen offset:3072 lds
v_mfma_f32_16x16x32_bf16 a[52:55], a[180:183], v[44:47], a[52:55]
v_exp_f32_e64 v73, v185
v_mfma_f32_16x16x32_bf16 a[24:27], a[184:187], v[40:43], a[24:27]
s_add_u32 s56, s56, 16384
v_cvt_pk_bf16_f32 v60, v72, v73
v_mfma_f32_16x16x32_bf16 a[56:59], a[184:187], v[44:47], a[56:59]
v_exp_f32 v74, v186
v_mfma_f32_16x16x32_bf16 a[28:31], a[188:191], v[40:43], a[28:31]
s_waitcnt lgkmcnt(8)
v_mfma_f32_16x16x32_bf16 a[60:63], a[188:191], v[44:47], a[60:63]
v_exp_f32_e64 v75, v187
v_mfma_f32_16x16x32_bf16 v[160:163], a[192:195], v[32:35], v[192:195]
ds_read_b128 a[224:227], %[vKrd] offset:24576
v_cvt_pk_bf16_f32 v61, v74, v75
v_mfma_f32_16x16x32_bf16 v[164:167], a[208:211], v[32:35], v[192:195]
v_exp_f32_e64 v72, v188
v_mfma_f32_16x16x32_bf16 v[160:163], a[196:199], v[64:67], v[160:163]
ds_read_b128 a[240:243], %[vKrd] offset:28672
ds_read_b128 a[228:231], %[vKrd] offset:25088
)"
R"(v_mfma_f32_16x16x32_bf16 v[164:167], a[212:215], v[64:67], v[164:167]
s_lshr_b32 s60, %[sKV], 1
v_exp_f32 v73, v189
v_mfma_f32_16x16x32_bf16 v[160:163], a[200:203], v[96:99], v[160:163]
ds_read_b128 a[244:247], %[vKrd] offset:29184
v_cvt_pk_bf16_f32 v62, v72, v73
v_mfma_f32_16x16x32_bf16 v[164:167], a[216:219], v[96:99], v[164:167]
s_add_u32 s60, s60, %[sL]
v_exp_f32 v74, v190
v_mfma_f32_16x16x32_bf16 v[160:163], a[204:207], v[128:131], v[160:163]
ds_read_b64_tr_b16 a[144:145], %[vVr] offset:17408
ds_read_b64_tr_b16 a[146:147], %[vVr] offset:21504
v_mfma_f32_16x16x32_bf16 v[164:167], a[220:223], v[128:131], v[164:167]
s_add_u32 s92, s92, 32
v_exp_f32 v75, v191
v_mfma_f32_16x16x32_bf16 v[168:171], a[192:195], v[36:39], v[196:199]
ds_read_b64_tr_b16 a[148:149], v140 offset:17408
v_cvt_pk_bf16_f32 v63, v74, v75
v_mfma_f32_16x16x32_bf16 v[172:175], a[208:211], v[36:39], v[196:199]
ds_read_b64_tr_b16 a[150:151], v140 offset:21504
v_mfma_f32_16x16x32_bf16 v[168:171], a[196:199], v[68:71], v[168:171]
v_exp_f32_e64 v72, v160
v_mfma_f32_16x16x32_bf16 v[172:175], a[212:215], v[68:71], v[172:175]
ds_read_b64_tr_b16 a[152:153], %[vVr] offset:17920
ds_read_b64_tr_b16 a[154:155], %[vVr] offset:22016
v_mfma_f32_16x16x32_bf16 v[168:171], a[200:203], v[100:103], v[168:171]
v_exp_f32_e64 v73, v161
v_smfmac_f32_16x16x64_bf16 v[220:223], v[212:215], v[56:63], v231
ds_read_b64_tr_b16 a[156:157], v140 offset:17920
v_cvt_pk_bf16_f32 v40, v72, v73
v_mfma_f32_16x16x32_bf16 v[172:175], a[216:219], v[100:103], v[172:175]
v_exp_f32_e64 v74, v162
v_mfma_f32_16x16x32_bf16 v[168:171], a[204:207], v[132:135], v[168:171]
ds_read_b64_tr_b16 a[158:159], v140 offset:22016
v_mfma_f32_16x16x32_bf16 v[172:175], a[220:223], v[132:135], v[172:175]
v_exp_f32_e64 v75, v163
v_mfma_f32_16x16x32_bf16 a[64:67], a[160:163], v[56:59], a[64:67]
v_cvt_pk_bf16_f32 v41, v74, v75
v_sub_u32_e64 v229, v226, v229
v_mfma_f32_16x16x32_bf16 a[96:99], a[160:163], v[60:63], a[96:99]
v_exp_f32_e64 v72, v164
v_mfma_f32_16x16x32_bf16 a[68:71], a[164:167], v[56:59], a[68:71]
v_mfma_f32_16x16x32_bf16 a[100:103], a[164:167], v[60:63], a[100:103]
v_exp_f32 v73, v165
v_mfma_f32_16x16x32_bf16 a[72:75], a[168:171], v[56:59], a[72:75]
s_add_u32 m0, %[sLw], 0
v_cvt_pk_bf16_f32 v42, v72, v73
v_mfma_f32_16x16x32_bf16 a[104:107], a[168:171], v[60:63], a[104:107]
v_exp_f32_e64 v74, v166
v_mfma_f32_16x16x32_bf16 a[76:79], a[172:175], v[56:59], a[76:79]
buffer_load_dwordx4 %[vVs0], s[16:19], s56 offen offset:0 lds
v_mfma_f32_16x16x32_bf16 a[108:111], a[172:175], v[60:63], a[108:111]
v_exp_f32_e64 v75, v167
v_mfma_f32_16x16x32_bf16 a[80:83], a[176:179], v[56:59], a[80:83]
v_cvt_pk_bf16_f32 v43, v74, v75
v_mad_i32_i24 v228, v230, s70, v228
v_mfma_f32_16x16x32_bf16 a[112:115], a[176:179], v[60:63], a[112:115]
v_exp_f32_e64 v72, v168
v_mfma_f32_16x16x32_bf16 a[84:87], a[180:183], v[56:59], a[84:87]
buffer_load_dwordx4 %[vVs1], s[16:19], s56 offen offset:1024 lds
v_mfma_f32_16x16x32_bf16 a[116:119], a[180:183], v[60:63], a[116:119]
v_exp_f32_e64 v73, v169
v_mfma_f32_16x16x32_bf16 a[88:91], a[184:187], v[56:59], a[88:91]
v_cvt_pk_bf16_f32 v44, v72, v73
v_mfma_f32_16x16x32_bf16 a[120:123], a[184:187], v[60:63], a[120:123]
v_exp_f32_e64 v74, v170
v_mfma_f32_16x16x32_bf16 a[92:95], a[188:191], v[56:59], a[92:95]
v_mfma_f32_16x16x32_bf16 a[124:127], a[188:191], v[60:63], a[124:127]
v_exp_f32_e64 v75, v171
v_mfma_f32_16x16x32_bf16 v[176:179], a[192:195], v[48:51], v[200:203]
ds_read_b128 a[232:235], %[vKrd] offset:25600
v_cvt_pk_bf16_f32 v45, v74, v75
v_mfma_f32_16x16x32_bf16 v[180:183], a[208:211], v[48:51], v[200:203]
v_exp_f32_e64 v72, v172
v_mfma_f32_16x16x32_bf16 v[176:179], a[196:199], v[80:83], v[176:179]
ds_read_b128 a[248:251], %[vKrd] offset:29696
ds_read_b128 a[236:239], %[vKrd] offset:26112
v_mfma_f32_16x16x32_bf16 v[180:183], a[212:215], v[80:83], v[180:183]
v_exp_f32_e64 v73, v173
v_mfma_f32_16x16x32_bf16 v[176:179], a[200:203], v[112:115], v[176:179]
ds_read_b128 a[252:255], %[vKrd] offset:30208
v_cvt_pk_bf16_f32 v46, v72, v73
v_mfma_f32_16x16x32_bf16 v[180:183], a[216:219], v[112:115], v[180:183]
v_exp_f32_e64 v74, v174
v_mfma_f32_16x16x32_bf16 v[176:179], a[204:207], v[144:147], v[176:179]
ds_read_b64_tr_b16 a[160:161], %[vVr] offset:24576
ds_read_b64_tr_b16 a[162:163], %[vVr] offset:28672
v_mfma_f32_16x16x32_bf16 v[180:183], a[220:223], v[144:147], v[180:183]
v_exp_f32_e64 v75, v175
v_mfma_f32_16x16x32_bf16 v[184:187], a[192:195], v[52:55], v[204:207]
ds_read_b64_tr_b16 a[164:165], v140 offset:24576
v_cvt_pk_bf16_f32 v47, v74, v75
v_mfma_f32_16x16x32_bf16 v[188:191], a[208:211], v[52:55], v[204:207]
ds_read_b64_tr_b16 a[166:167], v140 offset:28672
ds_read_b64_tr_b16 a[168:169], %[vVr] offset:25088
v_mfma_f32_16x16x32_bf16 v[184:187], a[196:199], v[84:87], v[184:187]
v_exp_f32_e64 v72, v176
v_mfma_f32_16x16x32_bf16 v[188:191], a[212:215], v[84:87], v[188:191]
ds_read_b64_tr_b16 a[170:171], %[vVr] offset:29184
ds_read_b64_tr_b16 a[172:173], v140 offset:25088
v_mfma_f32_16x16x32_bf16 v[184:187], a[200:203], v[116:119], v[184:187]
v_exp_f32_e64 v73, v177
v_smfmac_f32_16x16x64_bf16 v[216:219], v[212:215], v[40:47], v231
ds_read_b64_tr_b16 a[174:175], v140 offset:29184
v_cvt_pk_bf16_f32 v56, v72, v73
v_mfma_f32_16x16x32_bf16 v[188:191], a[216:219], v[116:119], v[188:191]
v_exp_f32 v74, v178
v_mfma_f32_16x16x32_bf16 v[184:187], a[204:207], v[148:151], v[184:187]
s_waitcnt vmcnt(12) lgkmcnt(8)
ds_write_b32 v208, v210 offset:0
v_mfma_f32_16x16x32_bf16 v[188:191], a[220:223], v[148:151], v[188:191]
v_exp_f32_e64 v75, v179
v_mfma_f32_16x16x32_bf16 a[0:3], a[128:131], v[40:43], a[0:3]
v_cvt_pk_bf16_f32 v57, v74, v75
v_mfma_f32_16x16x32_bf16 a[32:35], a[128:131], v[44:47], a[32:35]
v_exp_f32_e64 v72, v180
v_mfma_f32_16x16x32_bf16 a[4:7], a[132:135], v[40:43], a[4:7]
buffer_store_dwordx4 v[244:247], %[vX], %[sWS], s97 offen offset:0 sc1
v_mfma_f32_16x16x32_bf16 a[36:39], a[132:135], v[44:47], a[36:39]
v_exp_f32_e64 v73, v181
v_mfma_f32_16x16x32_bf16 a[8:11], a[136:139], v[40:43], a[8:11]
v_cvt_pk_bf16_f32 v58, v72, v73
v_mfma_f32_16x16x32_bf16 a[40:43], a[136:139], v[44:47], a[40:43]
v_exp_f32_e64 v74, v182
v_mfma_f32_16x16x32_bf16 a[12:15], a[140:143], v[40:43], a[12:15]
buffer_load_dwordx4 %[vVs0], s[16:19], s56 offen offset:2048 lds
v_mfma_f32_16x16x32_bf16 a[44:47], a[140:143], v[44:47], a[44:47]
v_exp_f32_e64 v75, v183
v_mfma_f32_16x16x32_bf16 a[16:19], a[144:147], v[40:43], a[16:19]
s_add_u32 s97, s97, 1048576
v_cvt_pk_bf16_f32 v59, v74, v75
v_mfma_f32_16x16x32_bf16 a[48:51], a[144:147], v[44:47], a[48:51]
v_exp_f32_e64 v72, v184
v_mfma_f32_16x16x32_bf16 a[20:23], a[148:151], v[40:43], a[20:23]
buffer_load_dwordx4 %[vVs1], s[16:19], s56 offen offset:3072 lds
v_mfma_f32_16x16x32_bf16 a[52:55], a[148:151], v[44:47], a[52:55]
v_exp_f32_e64 v73, v185
v_mfma_f32_16x16x32_bf16 a[24:27], a[152:155], v[40:43], a[24:27]
s_add_u32 m0, s60, 131072
s_xor_b32 s97, s97, 8192
v_cvt_pk_bf16_f32 v60, v72, v73
v_mfma_f32_16x16x32_bf16 a[56:59], a[152:155], v[44:47], a[56:59]
v_exp_f32_e64 v74, v186
v_mfma_f32_16x16x32_bf16 a[28:31], a[156:159], v[40:43], a[28:31]
ds_read_b32 v211, v209 offset:0
v_mfma_f32_16x16x32_bf16 a[60:63], a[156:159], v[44:47], a[60:63]
v_exp_f32_e64 v75, v187
v_mfma_f32_16x16x32_bf16 v[160:163], a[224:227], v[32:35], v[192:195]
ds_read_b64_tr_b16 a[176:177], %[vVr] offset:25600
v_cvt_pk_bf16_f32 v61, v74, v75
v_mfma_f32_16x16x32_bf16 v[164:167], a[240:243], v[32:35], v[192:195]
v_exp_f32_e64 v72, v188
v_mfma_f32_16x16x32_bf16 v[160:163], a[228:231], v[64:67], v[160:163]
ds_read_b64_tr_b16 a[178:179], %[vVr] offset:29696
ds_read_b64_tr_b16 a[180:181], v140 offset:25600
v_mfma_f32_16x16x32_bf16 v[164:167], a[244:247], v[64:67], v[164:167]
v_exp_f32_e64 v73, v189
v_mfma_f32_16x16x32_bf16 v[160:163], a[232:235], v[96:99], v[160:163]
ds_read_b64_tr_b16 a[182:183], v140 offset:29696
v_cvt_pk_bf16_f32 v62, v72, v73
v_mfma_f32_16x16x32_bf16 v[164:167], a[248:251], v[96:99], v[164:167]
v_exp_f32_e64 v74, v190
v_mfma_f32_16x16x32_bf16 v[160:163], a[236:239], v[128:131], v[160:163]
s_waitcnt lgkmcnt(4)
v_cmp_lt_u32 vcc, v211, v210
v_mfma_f32_16x16x32_bf16 v[164:167], a[252:255], v[128:131], v[164:167]
v_exp_f32_e64 v75, v191
v_mfma_f32_16x16x32_bf16 v[168:171], a[224:227], v[36:39], v[196:199]
ds_read_b64_tr_b16 a[184:185], %[vVr] offset:26112
v_cvt_pk_bf16_f32 v63, v74, v75
v_mfma_f32_16x16x32_bf16 v[172:175], a[240:243], v[36:39], v[196:199]
s_cbranch_vccnz L_fs0_%=
L_fsok0_%=:
v_mfma_f32_16x16x32_bf16 v[168:171], a[228:231], v[68:71], v[168:171]
v_exp_f32 v72, v160
v_mfma_f32_16x16x32_bf16 v[172:175], a[244:247], v[68:71], v[172:175]
ds_read_b128 a[192:195], %[vKrd] offset:32768
ds_read_b128 a[208:211], %[vKrd] offset:36864
v_mfma_f32_16x16x32_bf16 v[168:171], a[232:235], v[100:103], v[168:171]
v_exp_f32_e64 v73, v161
v_smfmac_f32_16x16x64_bf16 v[220:223], v[212:215], v[56:63], v231
ds_read_b128 a[196:199], %[vKrd] offset:33280
v_cvt_pk_bf16_f32 v40, v72, v73
v_mfma_f32_16x16x32_bf16 v[172:175], a[248:251], v[100:103], v[172:175]
v_exp_f32_e64 v74, v162
v_mfma_f32_16x16x32_bf16 v[168:171], a[236:239], v[132:135], v[168:171]
ds_read_b128 a[212:215], %[vKrd] offset:37376
buffer_load_dwordx4 v228, s[80:83], 0 offen offset:0 lds
v_mfma_f32_16x16x32_bf16 v[172:175], a[252:255], v[132:135], v[172:175]
v_exp_f32_e64 v75, v163
v_mfma_f32_16x16x32_bf16 a[64:67], a[128:131], v[56:59], a[64:67]
ds_read_b64_tr_b16 a[186:187], %[vVr] offset:30208
v_cvt_pk_bf16_f32 v41, v74, v75
v_mfma_f32_16x16x32_bf16 a[96:99], a[128:131], v[60:63], a[96:99]
v_exp_f32_e64 v72, v164
v_mfma_f32_16x16x32_bf16 a[68:71], a[132:135], v[56:59], a[68:71]
buffer_load_dwordx4 v228, s[80:83], s87 offen offset:1024 lds
v_mfma_f32_16x16x32_bf16 a[100:103], a[132:135], v[60:63], a[100:103]
v_exp_f32_e64 v73, v165
v_mfma_f32_16x16x32_bf16 a[72:75], a[136:139], v[56:59], a[72:75]
)"
R"(s_add_u32 m0, %[sLw], 65536
v_cvt_pk_bf16_f32 v42, v72, v73
v_mfma_f32_16x16x32_bf16 a[104:107], a[136:139], v[60:63], a[104:107]
v_exp_f32_e64 v74, v166
v_mfma_f32_16x16x32_bf16 a[76:79], a[140:143], v[56:59], a[76:79]
buffer_load_dwordx4 %[vVs0], s[20:23], s56 offen offset:0 lds
v_mfma_f32_16x16x32_bf16 a[108:111], a[140:143], v[60:63], a[108:111]
v_exp_f32_e64 v75, v167
v_mfma_f32_16x16x32_bf16 a[80:83], a[144:147], v[56:59], a[80:83]
ds_read_b64_tr_b16 a[188:189], v140 offset:26112
v_cvt_pk_bf16_f32 v43, v74, v75
v_mfma_f32_16x16x32_bf16 a[112:115], a[144:147], v[60:63], a[112:115]
v_exp_f32_e64 v72, v168
v_mfma_f32_16x16x32_bf16 a[84:87], a[148:151], v[56:59], a[84:87]
buffer_load_dwordx4 %[vVs1], s[20:23], s56 offen offset:1024 lds
v_mfma_f32_16x16x32_bf16 a[116:119], a[148:151], v[60:63], a[116:119]
v_exp_f32_e64 v73, v169
v_mfma_f32_16x16x32_bf16 a[88:91], a[152:155], v[56:59], a[88:91]
ds_read_b64_tr_b16 a[190:191], v140 offset:30208
v_cvt_pk_bf16_f32 v44, v72, v73
v_mfma_f32_16x16x32_bf16 a[120:123], a[152:155], v[60:63], a[120:123]
v_exp_f32_e64 v74, v170
v_mfma_f32_16x16x32_bf16 a[92:95], a[156:159], v[56:59], a[92:95]
v_add_u32_e64 v210, 1, v210
v_mfma_f32_16x16x32_bf16 a[124:127], a[156:159], v[60:63], a[124:127]
v_exp_f32_e64 v75, v171
v_mfma_f32_16x16x32_bf16 v[176:179], a[224:227], v[48:51], v[200:203]
ds_read_b128 a[200:203], %[vKrd] offset:33792
v_cvt_pk_bf16_f32 v45, v74, v75
v_mfma_f32_16x16x32_bf16 v[180:183], a[240:243], v[48:51], v[200:203]
v_exp_f32_e64 v72, v172
v_mfma_f32_16x16x32_bf16 v[176:179], a[228:231], v[80:83], v[176:179]
ds_read_b128 a[216:219], %[vKrd] offset:37888
ds_read_b128 a[204:207], %[vKrd] offset:34304
v_mfma_f32_16x16x32_bf16 v[180:183], a[244:247], v[80:83], v[180:183]
v_exp_f32_e64 v73, v173
v_mfma_f32_16x16x32_bf16 v[176:179], a[232:235], v[112:115], v[176:179]
ds_read_b128 a[220:223], %[vKrd] offset:38400
v_cvt_pk_bf16_f32 v46, v72, v73
v_mfma_f32_16x16x32_bf16 v[180:183], a[248:251], v[112:115], v[180:183]
v_exp_f32_e64 v74, v174
v_mfma_f32_16x16x32_bf16 v[176:179], a[236:239], v[144:147], v[176:179]
ds_read_b64_tr_b16 a[128:129], %[vVr] offset:32768
ds_read_b64_tr_b16 a[130:131], %[vVr] offset:36864
v_mfma_f32_16x16x32_bf16 v[180:183], a[252:255], v[144:147], v[180:183]
v_exp_f32_e64 v75, v175
v_mfma_f32_16x16x32_bf16 v[184:187], a[224:227], v[52:55], v[204:207]
ds_read_b64_tr_b16 a[132:133], v140 offset:32768
v_cvt_pk_bf16_f32 v47, v74, v75
v_mfma_f32_16x16x32_bf16 v[188:191], a[240:243], v[52:55], v[204:207]
ds_read_b64_tr_b16 a[134:135], v140 offset:36864
v_mfma_f32_16x16x32_bf16 v[184:187], a[228:231], v[84:87], v[184:187]
v_exp_f32_e64 v72, v176
v_mfma_f32_16x16x32_bf16 v[188:191], a[244:247], v[84:87], v[188:191]
ds_read_b64_tr_b16 a[136:137], %[vVr] offset:33280
ds_read_b64_tr_b16 a[138:139], %[vVr] offset:37376
v_mfma_f32_16x16x32_bf16 v[184:187], a[232:235], v[116:119], v[184:187]
v_exp_f32_e64 v73, v177
v_smfmac_f32_16x16x64_bf16 v[216:219], v[212:215], v[40:47], v231
ds_read_b64_tr_b16 a[140:141], v140 offset:33280
v_cvt_pk_bf16_f32 v56, v72, v73
v_mfma_f32_16x16x32_bf16 v[188:191], a[248:251], v[116:119], v[188:191]
v_exp_f32 v74, v178
v_mfma_f32_16x16x32_bf16 v[184:187], a[236:239], v[148:151], v[184:187]
s_waitcnt lgkmcnt(11)
ds_read_b64_tr_b16 a[142:143], v140 offset:37376
v_mfma_f32_16x16x32_bf16 v[188:191], a[252:255], v[148:151], v[188:191]
v_exp_f32_e64 v75, v179
v_mfma_f32_16x16x32_bf16 a[0:3], a[160:163], v[40:43], a[0:3]
v_cvt_pk_bf16_f32 v57, v74, v75
v_mfma_f32_16x16x32_bf16 a[32:35], a[160:163], v[44:47], a[32:35]
v_exp_f32_e64 v72, v180
v_mfma_f32_16x16x32_bf16 a[4:7], a[164:167], v[40:43], a[4:7]
buffer_load_dwordx4 v[240:243], %[vSp], s[24:27], s52 offen offset:0
v_mfma_f32_16x16x32_bf16 a[36:39], a[164:167], v[44:47], a[36:39]
v_exp_f32_e64 v73, v181
v_mfma_f32_16x16x32_bf16 a[8:11], a[168:171], v[40:43], a[8:11]
v_cvt_pk_bf16_f32 v58, v72, v73
v_mfma_f32_16x16x32_bf16 a[40:43], a[168:171], v[44:47], a[40:43]
v_exp_f32_e64 v74, v182
v_mfma_f32_16x16x32_bf16 a[12:15], a[172:175], v[40:43], a[12:15]
buffer_load_dwordx4 %[vVs0], s[20:23], s56 offen offset:2048 lds
v_mfma_f32_16x16x32_bf16 a[44:47], a[172:175], v[44:47], a[44:47]
v_exp_f32_e64 v75, v183
v_mfma_f32_16x16x32_bf16 a[16:19], a[176:179], v[40:43], a[16:19]
s_add_u32 s52, s52, 524288
v_cvt_pk_bf16_f32 v59, v74, v75
v_mfma_f32_16x16x32_bf16 a[48:51], a[176:179], v[44:47], a[48:51]
v_exp_f32_e64 v72, v184
v_mfma_f32_16x16x32_bf16 a[20:23], a[180:183], v[40:43], a[20:23]
buffer_load_dwordx4 %[vVs1], s[20:23], s56 offen offset:3072 lds
v_mfma_f32_16x16x32_bf16 a[52:55], a[180:183], v[44:47], a[52:55]
v_exp_f32_e64 v73, v185
v_mfma_f32_16x16x32_bf16 a[24:27], a[184:187], v[40:43], a[24:27]
s_add_u32 s56, s56, 16384
v_cvt_pk_bf16_f32 v60, v72, v73
v_mfma_f32_16x16x32_bf16 a[56:59], a[184:187], v[44:47], a[56:59]
v_exp_f32 v74, v186
v_mfma_f32_16x16x32_bf16 a[28:31], a[188:191], v[40:43], a[28:31]
s_waitcnt lgkmcnt(8)
v_mfma_f32_16x16x32_bf16 a[60:63], a[188:191], v[44:47], a[60:63]
v_exp_f32_e64 v75, v187
v_mfma_f32_16x16x32_bf16 v[160:163], a[192:195], v[32:35], v[192:195]
ds_read_b128 a[224:227], %[vKrd] offset:40960
v_cvt_pk_bf16_f32 v61, v74, v75
v_mfma_f32_16x16x32_bf16 v[164:167], a[208:211], v[32:35], v[192:195]
v_exp_f32_e64 v72, v188
v_mfma_f32_16x16x32_bf16 v[160:163], a[196:199], v[64:67], v[160:163]
ds_read_b128 a[240:243], %[vKrd] offset:45056
ds_read_b128 a[228:231], %[vKrd] offset:41472
v_mfma_f32_16x16x32_bf16 v[164:167], a[212:215], v[64:67], v[164:167]
v_exp_f32_e64 v73, v189
v_mfma_f32_16x16x32_bf16 v[160:163], a[200:203], v[96:99], v[160:163]
ds_read_b128 a[244:247], %[vKrd] offset:45568
v_cvt_pk_bf16_f32 v62, v72, v73
v_mfma_f32_16x16x32_bf16 v[164:167], a[216:219], v[96:99], v[164:167]
v_exp_f32_e64 v74, v190
v_mfma_f32_16x16x32_bf16 v[160:163], a[204:207], v[128:131], v[160:163]
ds_read_b64_tr_b16 a[144:145], %[vVr] offset:33792
ds_read_b64_tr_b16 a[146:147], %[vVr] offset:37888
v_mfma_f32_16x16x32_bf16 v[164:167], a[220:223], v[128:131], v[164:167]
v_exp_f32_e64 v75, v191
v_mfma_f32_16x16x32_bf16 v[168:171], a[192:195], v[36:39], v[196:199]
ds_read_b64_tr_b16 a[148:149], v140 offset:33792
v_cvt_pk_bf16_f32 v63, v74, v75
v_mfma_f32_16x16x32_bf16 v[172:175], a[208:211], v[36:39], v[196:199]
ds_read_b64_tr_b16 a[150:151], v140 offset:37888
v_mfma_f32_16x16x32_bf16 v[168:171], a[196:199], v[68:71], v[168:171]
v_exp_f32_e64 v72, v160
v_mfma_f32_16x16x32_bf16 v[172:175], a[212:215], v[68:71], v[172:175]
ds_read_b64_tr_b16 a[152:153], %[vVr] offset:34304
ds_read_b64_tr_b16 a[154:155], %[vVr] offset:38400
v_mfma_f32_16x16x32_bf16 v[168:171], a[200:203], v[100:103], v[168:171]
v_exp_f32_e64 v73, v161
v_smfmac_f32_16x16x64_bf16 v[220:223], v[212:215], v[56:63], v231
ds_read_b64_tr_b16 a[156:157], v140 offset:34304
v_cvt_pk_bf16_f32 v40, v72, v73
v_mfma_f32_16x16x32_bf16 v[172:175], a[216:219], v[100:103], v[172:175]
v_exp_f32_e64 v74, v162
v_mfma_f32_16x16x32_bf16 v[168:171], a[204:207], v[132:135], v[168:171]
ds_read_b64_tr_b16 a[158:159], v140 offset:38400
v_mfma_f32_16x16x32_bf16 v[172:175], a[220:223], v[132:135], v[172:175]
v_exp_f32_e64 v75, v163
v_mfma_f32_16x16x32_bf16 a[64:67], a[160:163], v[56:59], a[64:67]
v_cvt_pk_bf16_f32 v41, v74, v75
v_mfma_f32_16x16x32_bf16 a[96:99], a[160:163], v[60:63], a[96:99]
v_exp_f32_e64 v72, v164
v_mfma_f32_16x16x32_bf16 a[68:71], a[164:167], v[56:59], a[68:71]
buffer_store_dwordx4 v[244:247], %[vX], %[sWS], s97 offen offset:0 sc1
v_mfma_f32_16x16x32_bf16 a[100:103], a[164:167], v[60:63], a[100:103]
v_exp_f32_e64 v73, v165
v_mfma_f32_16x16x32_bf16 a[72:75], a[168:171], v[56:59], a[72:75]
s_add_u32 m0, %[sLw], 16384
v_cvt_pk_bf16_f32 v42, v72, v73
v_mfma_f32_16x16x32_bf16 a[104:107], a[168:171], v[60:63], a[104:107]
v_exp_f32_e64 v74, v166
v_mfma_f32_16x16x32_bf16 a[76:79], a[172:175], v[56:59], a[76:79]
buffer_load_dwordx4 %[vVs0], s[16:19], s56 offen offset:0 lds
v_mfma_f32_16x16x32_bf16 a[108:111], a[172:175], v[60:63], a[108:111]
v_exp_f32_e64 v75, v167
v_mfma_f32_16x16x32_bf16 a[80:83], a[176:179], v[56:59], a[80:83]
s_add_u32 s97, s97, 1048576
v_cvt_pk_bf16_f32 v43, v74, v75
v_mfma_f32_16x16x32_bf16 a[112:115], a[176:179], v[60:63], a[112:115]
v_exp_f32_e64 v72, v168
v_mfma_f32_16x16x32_bf16 a[84:87], a[180:183], v[56:59], a[84:87]
buffer_load_dwordx4 %[vVs1], s[16:19], s56 offen offset:1024 lds
v_mfma_f32_16x16x32_bf16 a[116:119], a[180:183], v[60:63], a[116:119]
v_exp_f32_e64 v73, v169
v_mfma_f32_16x16x32_bf16 a[88:91], a[184:187], v[56:59], a[88:91]
v_cvt_pk_bf16_f32 v44, v72, v73
v_mfma_f32_16x16x32_bf16 a[120:123], a[184:187], v[60:63], a[120:123]
v_exp_f32_e64 v74, v170
v_mfma_f32_16x16x32_bf16 a[92:95], a[188:191], v[56:59], a[92:95]
v_mfma_f32_16x16x32_bf16 a[124:127], a[188:191], v[60:63], a[124:127]
v_exp_f32_e64 v75, v171
v_mfma_f32_16x16x32_bf16 v[176:179], a[192:195], v[48:51], v[200:203]
ds_read_b128 a[232:235], %[vKrd] offset:41984
v_cvt_pk_bf16_f32 v45, v74, v75
v_mfma_f32_16x16x32_bf16 v[180:183], a[208:211], v[48:51], v[200:203]
v_exp_f32_e64 v72, v172
v_mfma_f32_16x16x32_bf16 v[176:179], a[196:199], v[80:83], v[176:179]
ds_read_b128 a[248:251], %[vKrd] offset:46080
ds_read_b128 a[236:239], %[vKrd] offset:42496
v_mfma_f32_16x16x32_bf16 v[180:183], a[212:215], v[80:83], v[180:183]
v_exp_f32_e64 v73, v173
v_mfma_f32_16x16x32_bf16 v[176:179], a[200:203], v[112:115], v[176:179]
ds_read_b128 a[252:255], %[vKrd] offset:46592
v_cvt_pk_bf16_f32 v46, v72, v73
v_mfma_f32_16x16x32_bf16 v[180:183], a[216:219], v[112:115], v[180:183]
v_exp_f32_e64 v74, v174
v_mfma_f32_16x16x32_bf16 v[176:179], a[204:207], v[144:147], v[176:179]
ds_read_b64_tr_b16 a[160:161], %[vVr] offset:40960
ds_read_b64_tr_b16 a[162:163], %[vVr] offset:45056
v_mfma_f32_16x16x32_bf16 v[180:183], a[220:223], v[144:147], v[180:183]
v_exp_f32_e64 v75, v175
v_mfma_f32_16x16x32_bf16 v[184:187], a[192:195], v[52:55], v[204:207]
ds_read_b64_tr_b16 a[164:165], v140 offset:40960
v_cvt_pk_bf16_f32 v47, v74, v75
v_mfma_f32_16x16x32_bf16 v[188:191], a[208:211], v[52:55], v[204:207]
)"
R"(ds_read_b64_tr_b16 a[166:167], v140 offset:45056
ds_read_b64_tr_b16 a[168:169], %[vVr] offset:41472
v_mfma_f32_16x16x32_bf16 v[184:187], a[196:199], v[84:87], v[184:187]
v_exp_f32_e64 v72, v176
v_mfma_f32_16x16x32_bf16 v[188:191], a[212:215], v[84:87], v[188:191]
ds_read_b64_tr_b16 a[170:171], %[vVr] offset:45568
ds_read_b64_tr_b16 a[172:173], v140 offset:41472
v_mfma_f32_16x16x32_bf16 v[184:187], a[200:203], v[116:119], v[184:187]
v_exp_f32_e64 v73, v177
v_smfmac_f32_16x16x64_bf16 v[216:219], v[212:215], v[40:47], v231
ds_read_b64_tr_b16 a[174:175], v140 offset:45568
v_cvt_pk_bf16_f32 v56, v72, v73
v_mfma_f32_16x16x32_bf16 v[188:191], a[216:219], v[116:119], v[188:191]
v_exp_f32 v74, v178
v_mfma_f32_16x16x32_bf16 v[184:187], a[204:207], v[148:151], v[184:187]
s_waitcnt vmcnt(15) lgkmcnt(8)
ds_write_b32 v208, v210 offset:0
v_mfma_f32_16x16x32_bf16 v[188:191], a[220:223], v[148:151], v[188:191]
v_exp_f32_e64 v75, v179
v_mfma_f32_16x16x32_bf16 a[0:3], a[128:131], v[40:43], a[0:3]
v_cvt_pk_bf16_f32 v57, v74, v75
v_mfma_f32_16x16x32_bf16 a[32:35], a[128:131], v[44:47], a[32:35]
v_exp_f32_e64 v72, v180
v_mfma_f32_16x16x32_bf16 a[4:7], a[132:135], v[40:43], a[4:7]
s_buffer_load_dwordx8 s[80:87], %[sWS], s92
v_mfma_f32_16x16x32_bf16 a[36:39], a[132:135], v[44:47], a[36:39]
v_exp_f32_e64 v73, v181
v_mfma_f32_16x16x32_bf16 a[8:11], a[136:139], v[40:43], a[8:11]
v_cvt_pk_bf16_f32 v58, v72, v73
v_mfma_f32_16x16x32_bf16 a[40:43], a[136:139], v[44:47], a[40:43]
v_exp_f32 v74, v182
v_mfma_f32_16x16x32_bf16 a[12:15], a[140:143], v[40:43], a[12:15]
LPa_4:
s_cbranch_execz LRa
buffer_load_dwordx4 %[vVs0], s[16:19], s56 offen offset:2048 lds
v_mfma_f32_16x16x32_bf16 a[44:47], a[140:143], v[44:47], a[44:47]
v_exp_f32_e64 v75, v183
v_mfma_f32_16x16x32_bf16 a[16:19], a[144:147], v[40:43], a[16:19]
v_cvt_pk_bf16_f32 v59, v74, v75
v_mfma_f32_16x16x32_bf16 a[48:51], a[144:147], v[44:47], a[48:51]
v_exp_f32_e64 v72, v184
v_mfma_f32_16x16x32_bf16 a[20:23], a[148:151], v[40:43], a[20:23]
buffer_load_dwordx4 %[vVs1], s[16:19], s56 offen offset:3072 lds
v_mfma_f32_16x16x32_bf16 a[52:55], a[148:151], v[44:47], a[52:55]
v_exp_f32_e64 v73, v185
v_mfma_f32_16x16x32_bf16 a[24:27], a[152:155], v[40:43], a[24:27]
v_cvt_pk_bf16_f32 v60, v72, v73
v_mfma_f32_16x16x32_bf16 a[56:59], a[152:155], v[44:47], a[56:59]
v_exp_f32_e64 v74, v186
v_mfma_f32_16x16x32_bf16 a[28:31], a[156:159], v[40:43], a[28:31]
ds_read_b32 v211, v209 offset:0
v_mfma_f32_16x16x32_bf16 a[60:63], a[156:159], v[44:47], a[60:63]
v_exp_f32_e64 v75, v187
v_mfma_f32_16x16x32_bf16 v[160:163], a[224:227], v[32:35], v[192:195]
ds_read_b64_tr_b16 a[176:177], %[vVr] offset:41984
v_cvt_pk_bf16_f32 v61, v74, v75
v_mfma_f32_16x16x32_bf16 v[164:167], a[240:243], v[32:35], v[192:195]
v_exp_f32_e64 v72, v188
v_mfma_f32_16x16x32_bf16 v[160:163], a[228:231], v[64:67], v[160:163]
ds_read_b64_tr_b16 a[178:179], %[vVr] offset:46080
ds_read_b64_tr_b16 a[180:181], v140 offset:41984
v_mfma_f32_16x16x32_bf16 v[164:167], a[244:247], v[64:67], v[164:167]
v_exp_f32_e64 v73, v189
v_mfma_f32_16x16x32_bf16 v[160:163], a[232:235], v[96:99], v[160:163]
ds_read_b64_tr_b16 a[182:183], v140 offset:46080
v_cvt_pk_bf16_f32 v62, v72, v73
v_mfma_f32_16x16x32_bf16 v[164:167], a[248:251], v[96:99], v[164:167]
v_exp_f32_e64 v74, v190
v_mfma_f32_16x16x32_bf16 v[160:163], a[236:239], v[128:131], v[160:163]
s_waitcnt lgkmcnt(4)
v_cmp_lt_u32 vcc, v211, v210
v_mfma_f32_16x16x32_bf16 v[164:167], a[252:255], v[128:131], v[164:167]
v_exp_f32_e64 v75, v191
v_mfma_f32_16x16x32_bf16 v[168:171], a[224:227], v[36:39], v[196:199]
ds_read_b64_tr_b16 a[184:185], %[vVr] offset:42496
v_cvt_pk_bf16_f32 v63, v74, v75
v_mfma_f32_16x16x32_bf16 v[172:175], a[240:243], v[36:39], v[196:199]
s_cbranch_vccnz L_fs1_%=
L_fsok1_%=:
v_mfma_f32_16x16x32_bf16 v[168:171], a[228:231], v[68:71], v[168:171]
v_exp_f32 v72, v160
v_mfma_f32_16x16x32_bf16 v[172:175], a[244:247], v[68:71], v[172:175]
ds_read_b128 a[192:195], %[vKrd] offset:49152
ds_read_b128 a[208:211], %[vKrd] offset:53248
v_mfma_f32_16x16x32_bf16 v[168:171], a[232:235], v[100:103], v[168:171]
v_exp_f32_e64 v73, v161
v_smfmac_f32_16x16x64_bf16 v[220:223], v[212:215], v[56:63], v231
ds_read_b128 a[196:199], %[vKrd] offset:49664
v_cvt_pk_bf16_f32 v40, v72, v73
v_mfma_f32_16x16x32_bf16 v[172:175], a[248:251], v[100:103], v[172:175]
v_exp_f32_e64 v74, v162
v_mfma_f32_16x16x32_bf16 v[168:171], a[236:239], v[132:135], v[168:171]
ds_read_b128 a[212:215], %[vKrd] offset:53760
v_mfma_f32_16x16x32_bf16 v[172:175], a[252:255], v[132:135], v[172:175]
v_exp_f32_e64 v75, v163
v_mfma_f32_16x16x32_bf16 a[64:67], a[128:131], v[56:59], a[64:67]
ds_read_b64_tr_b16 a[186:187], %[vVr] offset:46592
v_cvt_pk_bf16_f32 v41, v74, v75
v_mfma_f32_16x16x32_bf16 a[96:99], a[128:131], v[60:63], a[96:99]
v_exp_f32_e64 v72, v164
v_mfma_f32_16x16x32_bf16 a[68:71], a[132:135], v[56:59], a[68:71]
buffer_store_dwordx4 v[244:247], %[vX], %[sWS], s97 offen offset:0 sc1
v_mfma_f32_16x16x32_bf16 a[100:103], a[132:135], v[60:63], a[100:103]
v_exp_f32_e64 v73, v165
v_mfma_f32_16x16x32_bf16 a[72:75], a[136:139], v[56:59], a[72:75]
s_add_u32 m0, %[sLw], 81920
v_cvt_pk_bf16_f32 v42, v72, v73
v_mfma_f32_16x16x32_bf16 a[104:107], a[136:139], v[60:63], a[104:107]
v_exp_f32_e64 v74, v166
v_mfma_f32_16x16x32_bf16 a[76:79], a[140:143], v[56:59], a[76:79]
buffer_load_dwordx4 %[vVs0], s[20:23], s56 offen offset:0 lds
v_mfma_f32_16x16x32_bf16 a[108:111], a[140:143], v[60:63], a[108:111]
v_exp_f32_e64 v75, v167
v_mfma_f32_16x16x32_bf16 a[80:83], a[144:147], v[56:59], a[80:83]
s_add_u32 s97, s97, 1048576
ds_read_b64_tr_b16 a[188:189], v140 offset:42496
v_cvt_pk_bf16_f32 v43, v74, v75
v_mfma_f32_16x16x32_bf16 a[112:115], a[144:147], v[60:63], a[112:115]
v_exp_f32_e64 v72, v168
v_mfma_f32_16x16x32_bf16 a[84:87], a[148:151], v[56:59], a[84:87]
buffer_load_dwordx4 %[vVs1], s[20:23], s56 offen offset:1024 lds
v_mfma_f32_16x16x32_bf16 a[116:119], a[148:151], v[60:63], a[116:119]
v_exp_f32_e64 v73, v169
v_mfma_f32_16x16x32_bf16 a[88:91], a[152:155], v[56:59], a[88:91]
s_xor_b32 s97, s97, 8192
ds_read_b64_tr_b16 a[190:191], v140 offset:46592
v_cvt_pk_bf16_f32 v44, v72, v73
v_mfma_f32_16x16x32_bf16 a[120:123], a[152:155], v[60:63], a[120:123]
v_exp_f32_e64 v74, v170
v_mfma_f32_16x16x32_bf16 a[92:95], a[156:159], v[56:59], a[92:95]
v_add_u32_e64 v210, 1, v210
v_mfma_f32_16x16x32_bf16 a[124:127], a[156:159], v[60:63], a[124:127]
v_exp_f32_e64 v75, v171
v_mfma_f32_16x16x32_bf16 v[176:179], a[224:227], v[48:51], v[200:203]
ds_read_b128 a[200:203], %[vKrd] offset:50176
v_cvt_pk_bf16_f32 v45, v74, v75
v_mfma_f32_16x16x32_bf16 v[180:183], a[240:243], v[48:51], v[200:203]
v_exp_f32_e64 v72, v172
v_mfma_f32_16x16x32_bf16 v[176:179], a[228:231], v[80:83], v[176:179]
ds_read_b128 a[216:219], %[vKrd] offset:54272
ds_read_b128 a[204:207], %[vKrd] offset:50688
v_mfma_f32_16x16x32_bf16 v[180:183], a[244:247], v[80:83], v[180:183]
v_exp_f32_e64 v73, v173
v_mfma_f32_16x16x32_bf16 v[176:179], a[232:235], v[112:115], v[176:179]
ds_read_b128 a[220:223], %[vKrd] offset:54784
v_cvt_pk_bf16_f32 v46, v72, v73
v_mfma_f32_16x16x32_bf16 v[180:183], a[248:251], v[112:115], v[180:183]
v_exp_f32_e64 v74, v174
v_mfma_f32_16x16x32_bf16 v[176:179], a[236:239], v[144:147], v[176:179]
ds_read_b64_tr_b16 a[128:129], %[vVr] offset:49152
ds_read_b64_tr_b16 a[130:131], %[vVr] offset:53248
v_mfma_f32_16x16x32_bf16 v[180:183], a[252:255], v[144:147], v[180:183]
v_exp_f32_e64 v75, v175
v_mfma_f32_16x16x32_bf16 v[184:187], a[224:227], v[52:55], v[204:207]
ds_read_b64_tr_b16 a[132:133], v140 offset:49152
v_cvt_pk_bf16_f32 v47, v74, v75
v_mfma_f32_16x16x32_bf16 v[188:191], a[240:243], v[52:55], v[204:207]
ds_read_b64_tr_b16 a[134:135], v140 offset:53248
v_mfma_f32_16x16x32_bf16 v[184:187], a[228:231], v[84:87], v[184:187]
v_exp_f32_e64 v72, v176
v_mfma_f32_16x16x32_bf16 v[188:191], a[244:247], v[84:87], v[188:191]
ds_read_b64_tr_b16 a[136:137], %[vVr] offset:49664
ds_read_b64_tr_b16 a[138:139], %[vVr] offset:53760
v_mfma_f32_16x16x32_bf16 v[184:187], a[232:235], v[116:119], v[184:187]
v_exp_f32_e64 v73, v177
v_smfmac_f32_16x16x64_bf16 v[216:219], v[212:215], v[40:47], v231
ds_read_b64_tr_b16 a[140:141], v140 offset:49664
v_cvt_pk_bf16_f32 v56, v72, v73
v_mfma_f32_16x16x32_bf16 v[188:191], a[248:251], v[116:119], v[188:191]
v_exp_f32 v74, v178
v_mfma_f32_16x16x32_bf16 v[184:187], a[236:239], v[148:151], v[184:187]
s_waitcnt lgkmcnt(11)
ds_read_b64_tr_b16 a[142:143], v140 offset:53760
v_mfma_f32_16x16x32_bf16 v[188:191], a[252:255], v[148:151], v[188:191]
v_exp_f32_e64 v75, v179
v_mfma_f32_16x16x32_bf16 a[0:3], a[160:163], v[40:43], a[0:3]
v_cvt_pk_bf16_f32 v57, v74, v75
v_mfma_f32_16x16x32_bf16 a[32:35], a[160:163], v[44:47], a[32:35]
v_exp_f32_e64 v72, v180
v_mfma_f32_16x16x32_bf16 a[4:7], a[164:167], v[40:43], a[4:7]
v_mfma_f32_16x16x32_bf16 a[36:39], a[164:167], v[44:47], a[36:39]
v_exp_f32_e64 v73, v181
v_mfma_f32_16x16x32_bf16 a[8:11], a[168:171], v[40:43], a[8:11]
v_cvt_pk_bf16_f32 v58, v72, v73
v_mfma_f32_16x16x32_bf16 a[40:43], a[168:171], v[44:47], a[40:43]
v_exp_f32_e64 v74, v182
v_mfma_f32_16x16x32_bf16 a[12:15], a[172:175], v[40:43], a[12:15]
buffer_load_dwordx4 %[vVs0], s[20:23], s56 offen offset:2048 lds
v_mfma_f32_16x16x32_bf16 a[44:47], a[172:175], v[44:47], a[44:47]
v_exp_f32_e64 v75, v183
v_mfma_f32_16x16x32_bf16 a[16:19], a[176:179], v[40:43], a[16:19]
v_cvt_pk_bf16_f32 v59, v74, v75
v_mfma_f32_16x16x32_bf16 a[48:51], a[176:179], v[44:47], a[48:51]
v_exp_f32_e64 v72, v184
v_mfma_f32_16x16x32_bf16 a[20:23], a[180:183], v[40:43], a[20:23]
buffer_load_dwordx4 %[vVs1], s[20:23], s56 offen offset:3072 lds
v_mfma_f32_16x16x32_bf16 a[52:55], a[180:183], v[44:47], a[52:55]
v_exp_f32_e64 v73, v185
v_mfma_f32_16x16x32_bf16 a[24:27], a[184:187], v[40:43], a[24:27]
s_add_u32 s56, s56, 16384
v_cvt_pk_bf16_f32 v60, v72, v73
)"
R"(v_mfma_f32_16x16x32_bf16 a[56:59], a[184:187], v[44:47], a[56:59]
v_exp_f32 v74, v186
v_mfma_f32_16x16x32_bf16 a[28:31], a[188:191], v[40:43], a[28:31]
s_waitcnt lgkmcnt(8)
v_mfma_f32_16x16x32_bf16 a[60:63], a[188:191], v[44:47], a[60:63]
v_exp_f32_e64 v75, v187
v_mfma_f32_16x16x32_bf16 v[160:163], a[192:195], v[32:35], v[192:195]
ds_read_b128 a[224:227], %[vKrd] offset:57344
v_cvt_pk_bf16_f32 v61, v74, v75
v_mfma_f32_16x16x32_bf16 v[164:167], a[208:211], v[32:35], v[192:195]
v_exp_f32_e64 v72, v188
v_mfma_f32_16x16x32_bf16 v[160:163], a[196:199], v[64:67], v[160:163]
ds_read_b128 a[240:243], %[vKrd] offset:61440
ds_read_b128 a[228:231], %[vKrd] offset:57856
v_mfma_f32_16x16x32_bf16 v[164:167], a[212:215], v[64:67], v[164:167]
v_exp_f32_e64 v73, v189
v_mfma_f32_16x16x32_bf16 v[160:163], a[200:203], v[96:99], v[160:163]
ds_read_b128 a[244:247], %[vKrd] offset:61952
v_cvt_pk_bf16_f32 v62, v72, v73
v_mfma_f32_16x16x32_bf16 v[164:167], a[216:219], v[96:99], v[164:167]
v_exp_f32_e64 v74, v190
v_mfma_f32_16x16x32_bf16 v[160:163], a[204:207], v[128:131], v[160:163]
ds_read_b64_tr_b16 a[144:145], %[vVr] offset:50176
ds_read_b64_tr_b16 a[146:147], %[vVr] offset:54272
v_mfma_f32_16x16x32_bf16 v[164:167], a[220:223], v[128:131], v[164:167]
v_exp_f32_e64 v75, v191
v_mfma_f32_16x16x32_bf16 v[168:171], a[192:195], v[36:39], v[196:199]
ds_read_b64_tr_b16 a[148:149], v140 offset:50176
v_cvt_pk_bf16_f32 v63, v74, v75
v_mfma_f32_16x16x32_bf16 v[172:175], a[208:211], v[36:39], v[196:199]
ds_read_b64_tr_b16 a[150:151], v140 offset:54272
v_mfma_f32_16x16x32_bf16 v[168:171], a[196:199], v[68:71], v[168:171]
v_exp_f32_e64 v72, v160
v_mfma_f32_16x16x32_bf16 v[172:175], a[212:215], v[68:71], v[172:175]
ds_read_b64_tr_b16 a[152:153], %[vVr] offset:50688
ds_read_b64_tr_b16 a[154:155], %[vVr] offset:54784
v_mfma_f32_16x16x32_bf16 v[168:171], a[200:203], v[100:103], v[168:171]
v_exp_f32_e64 v73, v161
v_smfmac_f32_16x16x64_bf16 v[220:223], v[212:215], v[56:63], v231
ds_read_b64_tr_b16 a[156:157], v140 offset:50688
v_cvt_pk_bf16_f32 v40, v72, v73
v_mfma_f32_16x16x32_bf16 v[172:175], a[216:219], v[100:103], v[172:175]
v_exp_f32_e64 v74, v162
v_mfma_f32_16x16x32_bf16 v[168:171], a[204:207], v[132:135], v[168:171]
ds_read_b64_tr_b16 a[158:159], v140 offset:54784
v_mfma_f32_16x16x32_bf16 v[172:175], a[220:223], v[132:135], v[172:175]
v_exp_f32_e64 v75, v163
v_mfma_f32_16x16x32_bf16 a[64:67], a[160:163], v[56:59], a[64:67]
v_cvt_pk_bf16_f32 v41, v74, v75
v_mfma_f32_16x16x32_bf16 a[96:99], a[160:163], v[60:63], a[96:99]
v_exp_f32_e64 v72, v164
v_mfma_f32_16x16x32_bf16 a[68:71], a[164:167], v[56:59], a[68:71]
global_load_dword v105, v104, s[98:99]
v_mfma_f32_16x16x32_bf16 a[100:103], a[164:167], v[60:63], a[100:103]
v_exp_f32_e64 v73, v165
v_mfma_f32_16x16x32_bf16 a[72:75], a[168:171], v[56:59], a[72:75]
s_add_u32 m0, %[sLw], 32768
v_cvt_pk_bf16_f32 v42, v72, v73
v_mfma_f32_16x16x32_bf16 a[104:107], a[168:171], v[60:63], a[104:107]
v_exp_f32_e64 v74, v166
v_mfma_f32_16x16x32_bf16 a[76:79], a[172:175], v[56:59], a[76:79]
buffer_load_dwordx4 %[vVs0], s[16:19], s56 offen offset:0 lds
v_mfma_f32_16x16x32_bf16 a[108:111], a[172:175], v[60:63], a[108:111]
v_exp_f32_e64 v75, v167
v_mfma_f32_16x16x32_bf16 a[80:83], a[176:179], v[56:59], a[80:83]
v_cvt_pk_bf16_f32 v43, v74, v75
v_mfma_f32_16x16x32_bf16 a[112:115], a[176:179], v[60:63], a[112:115]
v_exp_f32_e64 v72, v168
v_mfma_f32_16x16x32_bf16 a[84:87], a[180:183], v[56:59], a[84:87]
buffer_load_dwordx4 %[vVs1], s[16:19], s56 offen offset:1024 lds
v_mfma_f32_16x16x32_bf16 a[116:119], a[180:183], v[60:63], a[116:119]
v_exp_f32_e64 v73, v169
v_mfma_f32_16x16x32_bf16 a[88:91], a[184:187], v[56:59], a[88:91]
v_cvt_pk_bf16_f32 v44, v72, v73
v_mfma_f32_16x16x32_bf16 a[120:123], a[184:187], v[60:63], a[120:123]
v_exp_f32_e64 v74, v170
v_mfma_f32_16x16x32_bf16 a[92:95], a[188:191], v[56:59], a[92:95]
v_mfma_f32_16x16x32_bf16 a[124:127], a[188:191], v[60:63], a[124:127]
v_exp_f32_e64 v75, v171
v_mfma_f32_16x16x32_bf16 v[176:179], a[192:195], v[48:51], v[200:203]
ds_read_b128 a[232:235], %[vKrd] offset:58368
v_cvt_pk_bf16_f32 v45, v74, v75
v_mfma_f32_16x16x32_bf16 v[180:183], a[208:211], v[48:51], v[200:203]
v_exp_f32_e64 v72, v172
v_mfma_f32_16x16x32_bf16 v[176:179], a[196:199], v[80:83], v[176:179]
ds_read_b128 a[248:251], %[vKrd] offset:62464
ds_read_b128 a[236:239], %[vKrd] offset:58880
v_mfma_f32_16x16x32_bf16 v[180:183], a[212:215], v[80:83], v[180:183]
v_exp_f32_e64 v73, v173
v_mfma_f32_16x16x32_bf16 v[176:179], a[200:203], v[112:115], v[176:179]
ds_read_b128 a[252:255], %[vKrd] offset:62976
v_cvt_pk_bf16_f32 v46, v72, v73
v_mfma_f32_16x16x32_bf16 v[180:183], a[216:219], v[112:115], v[180:183]
v_exp_f32_e64 v74, v174
v_mfma_f32_16x16x32_bf16 v[176:179], a[204:207], v[144:147], v[176:179]
ds_read_b64_tr_b16 a[160:161], %[vVr] offset:57344
ds_read_b64_tr_b16 a[162:163], %[vVr] offset:61440
v_mfma_f32_16x16x32_bf16 v[180:183], a[220:223], v[144:147], v[180:183]
v_exp_f32_e64 v75, v175
v_mfma_f32_16x16x32_bf16 v[184:187], a[192:195], v[52:55], v[204:207]
ds_read_b64_tr_b16 a[164:165], v140 offset:57344
v_cvt_pk_bf16_f32 v47, v74, v75
v_mfma_f32_16x16x32_bf16 v[188:191], a[208:211], v[52:55], v[204:207]
ds_read_b64_tr_b16 a[166:167], v140 offset:61440
ds_read_b64_tr_b16 a[168:169], %[vVr] offset:57856
v_mfma_f32_16x16x32_bf16 v[184:187], a[196:199], v[84:87], v[184:187]
v_exp_f32_e64 v72, v176
v_mfma_f32_16x16x32_bf16 v[188:191], a[212:215], v[84:87], v[188:191]
ds_read_b64_tr_b16 a[170:171], %[vVr] offset:61952
ds_read_b64_tr_b16 a[172:173], v140 offset:57856
v_mfma_f32_16x16x32_bf16 v[184:187], a[200:203], v[116:119], v[184:187]
v_exp_f32_e64 v73, v177
v_smfmac_f32_16x16x64_bf16 v[216:219], v[212:215], v[40:47], v231
ds_read_b64_tr_b16 a[174:175], v140 offset:61952
v_cvt_pk_bf16_f32 v56, v72, v73
v_mfma_f32_16x16x32_bf16 v[188:191], a[216:219], v[116:119], v[188:191]
v_exp_f32 v74, v178
v_mfma_f32_16x16x32_bf16 v[184:187], a[204:207], v[148:151], v[184:187]
s_waitcnt vmcnt(13) lgkmcnt(8)
ds_write_b32 v208, v210 offset:0
v_mfma_f32_16x16x32_bf16 v[188:191], a[220:223], v[148:151], v[188:191]
v_exp_f32_e64 v75, v179
v_mfma_f32_16x16x32_bf16 a[0:3], a[128:131], v[40:43], a[0:3]
ds_read_b64_tr_b16 v[232:233], v229 offset:0
v_cvt_pk_bf16_f32 v57, v74, v75
v_mfma_f32_16x16x32_bf16 a[32:35], a[128:131], v[44:47], a[32:35]
v_exp_f32_e64 v72, v180
v_mfma_f32_16x16x32_bf16 a[4:7], a[132:135], v[40:43], a[4:7]
buffer_store_dwordx4 v[240:243], %[vX], %[sWS], s53 offen offset:0 sc1
v_mfma_f32_16x16x32_bf16 a[36:39], a[132:135], v[44:47], a[36:39]
v_exp_f32_e64 v73, v181
v_mfma_f32_16x16x32_bf16 a[8:11], a[136:139], v[40:43], a[8:11]
ds_read_b64_tr_b16 v[234:235], v229 offset:256
v_cvt_pk_bf16_f32 v58, v72, v73
v_mfma_f32_16x16x32_bf16 a[40:43], a[136:139], v[44:47], a[40:43]
v_exp_f32_e64 v74, v182
v_mfma_f32_16x16x32_bf16 a[12:15], a[140:143], v[40:43], a[12:15]
buffer_load_dwordx4 %[vVs0], s[16:19], s56 offen offset:2048 lds
v_mfma_f32_16x16x32_bf16 a[44:47], a[140:143], v[44:47], a[44:47]
v_exp_f32_e64 v75, v183
v_mfma_f32_16x16x32_bf16 a[16:19], a[144:147], v[40:43], a[16:19]
s_add_u32 s53, s53, 4194304
ds_read_b64_tr_b16 v[236:237], v229 offset:8
v_cvt_pk_bf16_f32 v59, v74, v75
v_mfma_f32_16x16x32_bf16 a[48:51], a[144:147], v[44:47], a[48:51]
v_exp_f32_e64 v72, v184
v_mfma_f32_16x16x32_bf16 a[20:23], a[148:151], v[40:43], a[20:23]
buffer_load_dwordx4 %[vVs1], s[16:19], s56 offen offset:3072 lds
v_mfma_f32_16x16x32_bf16 a[52:55], a[148:151], v[44:47], a[52:55]
v_exp_f32_e64 v73, v185
v_mfma_f32_16x16x32_bf16 a[24:27], a[152:155], v[40:43], a[24:27]
ds_read_b64_tr_b16 v[238:239], v229 offset:264
v_cvt_pk_bf16_f32 v60, v72, v73
v_mfma_f32_16x16x32_bf16 a[56:59], a[152:155], v[44:47], a[56:59]
v_exp_f32_e64 v74, v186
v_mfma_f32_16x16x32_bf16 a[28:31], a[156:159], v[40:43], a[28:31]
ds_read_b32 v211, v209 offset:0
v_mfma_f32_16x16x32_bf16 a[60:63], a[156:159], v[44:47], a[60:63]
v_exp_f32_e64 v75, v187
v_mfma_f32_16x16x32_bf16 v[160:163], a[224:227], v[32:35], v[192:195]
ds_read_b64_tr_b16 a[176:177], %[vVr] offset:58368
v_cvt_pk_bf16_f32 v61, v74, v75
v_mfma_f32_16x16x32_bf16 v[164:167], a[240:243], v[32:35], v[192:195]
v_exp_f32_e64 v72, v188
v_mfma_f32_16x16x32_bf16 v[160:163], a[228:231], v[64:67], v[160:163]
ds_read_b64_tr_b16 a[178:179], %[vVr] offset:62464
ds_read_b64_tr_b16 a[180:181], v140 offset:58368
v_mfma_f32_16x16x32_bf16 v[164:167], a[244:247], v[64:67], v[164:167]
v_exp_f32_e64 v73, v189
v_mfma_f32_16x16x32_bf16 v[160:163], a[232:235], v[96:99], v[160:163]
ds_read_b64_tr_b16 a[182:183], v140 offset:62464
v_cvt_pk_bf16_f32 v62, v72, v73
v_mfma_f32_16x16x32_bf16 v[164:167], a[248:251], v[96:99], v[164:167]
v_exp_f32_e64 v74, v190
v_mfma_f32_16x16x32_bf16 v[160:163], a[236:239], v[128:131], v[160:163]
s_waitcnt lgkmcnt(4)
v_cmp_lt_u32 vcc, v211, v210
v_mfma_f32_16x16x32_bf16 v[164:167], a[252:255], v[128:131], v[164:167]
v_exp_f32_e64 v75, v191
v_mfma_f32_16x16x32_bf16 v[168:171], a[224:227], v[36:39], v[196:199]
ds_read_b64_tr_b16 a[184:185], %[vVr] offset:58880
v_cvt_pk_bf16_f32 v63, v74, v75
v_mfma_f32_16x16x32_bf16 v[172:175], a[240:243], v[36:39], v[196:199]
s_cbranch_vccnz L_fs2_%=
L_fsok2_%=:
v_mfma_f32_16x16x32_bf16 v[168:171], a[228:231], v[68:71], v[168:171]
v_exp_f32 v72, v160
v_mfma_f32_16x16x32_bf16 v[172:175], a[244:247], v[68:71], v[172:175]
ds_read_b128 a[192:195], %[vKrd] offset:0
ds_read_b128 a[208:211], %[vKrd] offset:4096
v_mfma_f32_16x16x32_bf16 v[168:171], a[232:235], v[100:103], v[168:171]
v_exp_f32_e64 v73, v161
v_smfmac_f32_16x16x64_bf16 v[220:223], v[212:215], v[56:63], v231
ds_read_b128 a[196:199], %[vKrd] offset:512
v_cvt_pk_bf16_f32 v40, v72, v73
v_mfma_f32_16x16x32_bf16 v[172:175], a[248:251], v[100:103], v[172:175]
v_exp_f32_e64 v74, v162
v_mfma_f32_16x16x32_bf16 v[168:171], a[236:239], v[132:135], v[168:171]
ds_read_b128 a[212:215], %[vKrd] offset:4608
v_mfma_f32_16x16x32_bf16 v[172:175], a[252:255], v[132:135], v[172:175]
)"
R"(v_exp_f32_e64 v75, v163
v_mfma_f32_16x16x32_bf16 a[64:67], a[128:131], v[56:59], a[64:67]
ds_read_b64_tr_b16 a[186:187], %[vVr] offset:62976
v_cvt_pk_bf16_f32 v41, v74, v75
v_mfma_f32_16x16x32_bf16 a[96:99], a[128:131], v[60:63], a[96:99]
v_exp_f32_e64 v72, v164
v_mfma_f32_16x16x32_bf16 a[68:71], a[132:135], v[56:59], a[68:71]
buffer_store_dword v106, off, %[sWS], s68 sc1
v_mfma_f32_16x16x32_bf16 a[100:103], a[132:135], v[60:63], a[100:103]
v_exp_f32_e64 v73, v165
v_mfma_f32_16x16x32_bf16 a[72:75], a[136:139], v[56:59], a[72:75]
s_add_u32 m0, %[sLw], 98304
v_cvt_pk_bf16_f32 v42, v72, v73
v_mfma_f32_16x16x32_bf16 a[104:107], a[136:139], v[60:63], a[104:107]
v_exp_f32_e64 v74, v166
v_mfma_f32_16x16x32_bf16 a[76:79], a[140:143], v[56:59], a[76:79]
buffer_load_dwordx4 %[vVs0], s[20:23], s56 offen offset:0 lds
v_mfma_f32_16x16x32_bf16 a[108:111], a[140:143], v[60:63], a[108:111]
v_exp_f32_e64 v75, v167
v_mfma_f32_16x16x32_bf16 a[80:83], a[144:147], v[56:59], a[80:83]
ds_read_b64_tr_b16 a[188:189], v140 offset:58880
v_cvt_pk_bf16_f32 v43, v74, v75
v_mfma_f32_16x16x32_bf16 a[112:115], a[144:147], v[60:63], a[112:115]
v_exp_f32_e64 v72, v168
v_mfma_f32_16x16x32_bf16 a[84:87], a[148:151], v[56:59], a[84:87]
buffer_load_dwordx4 %[vVs1], s[20:23], s56 offen offset:1024 lds
v_mfma_f32_16x16x32_bf16 a[116:119], a[148:151], v[60:63], a[116:119]
v_exp_f32_e64 v73, v169
v_mfma_f32_16x16x32_bf16 a[88:91], a[152:155], v[56:59], a[88:91]
ds_read_b64_tr_b16 a[190:191], v140 offset:62976
v_cvt_pk_bf16_f32 v44, v72, v73
v_mfma_f32_16x16x32_bf16 a[120:123], a[152:155], v[60:63], a[120:123]
v_exp_f32_e64 v74, v170
v_mfma_f32_16x16x32_bf16 a[92:95], a[156:159], v[56:59], a[92:95]
v_add_u32_e64 v210, 1, v210
v_mfma_f32_16x16x32_bf16 a[124:127], a[156:159], v[60:63], a[124:127]
v_exp_f32_e64 v75, v171
v_mfma_f32_16x16x32_bf16 v[176:179], a[224:227], v[48:51], v[200:203]
ds_read_b128 a[200:203], %[vKrd] offset:1024
v_cvt_pk_bf16_f32 v45, v74, v75
v_mfma_f32_16x16x32_bf16 v[180:183], a[240:243], v[48:51], v[200:203]
v_exp_f32_e64 v72, v172
v_mfma_f32_16x16x32_bf16 v[176:179], a[228:231], v[80:83], v[176:179]
ds_read_b128 a[216:219], %[vKrd] offset:5120
ds_read_b128 a[204:207], %[vKrd] offset:1536
v_mfma_f32_16x16x32_bf16 v[180:183], a[244:247], v[80:83], v[180:183]
v_exp_f32_e64 v73, v173
v_mfma_f32_16x16x32_bf16 v[176:179], a[232:235], v[112:115], v[176:179]
ds_read_b128 a[220:223], %[vKrd] offset:5632
v_cvt_pk_bf16_f32 v46, v72, v73
v_mfma_f32_16x16x32_bf16 v[180:183], a[248:251], v[112:115], v[180:183]
v_exp_f32_e64 v74, v174
v_mfma_f32_16x16x32_bf16 v[176:179], a[236:239], v[144:147], v[176:179]
ds_read_b64_tr_b16 a[128:129], %[vVr] offset:0
ds_read_b64_tr_b16 a[130:131], %[vVr] offset:4096
v_mfma_f32_16x16x32_bf16 v[180:183], a[252:255], v[144:147], v[180:183]
v_exp_f32_e64 v75, v175
v_mfma_f32_16x16x32_bf16 v[184:187], a[224:227], v[52:55], v[204:207]
ds_read_b64_tr_b16 a[132:133], v140 offset:0
v_cvt_pk_bf16_f32 v47, v74, v75
v_mfma_f32_16x16x32_bf16 v[188:191], a[240:243], v[52:55], v[204:207]
ds_read_b64_tr_b16 a[134:135], v140 offset:4096
v_mfma_f32_16x16x32_bf16 v[184:187], a[228:231], v[84:87], v[184:187]
v_exp_f32_e64 v72, v176
v_mfma_f32_16x16x32_bf16 v[188:191], a[244:247], v[84:87], v[188:191]
ds_read_b64_tr_b16 a[136:137], %[vVr] offset:512
ds_read_b64_tr_b16 a[138:139], %[vVr] offset:4608
v_mfma_f32_16x16x32_bf16 v[184:187], a[232:235], v[116:119], v[184:187]
v_exp_f32_e64 v73, v177
v_smfmac_f32_16x16x64_bf16 v[216:219], v[212:215], v[40:47], v231
ds_read_b64_tr_b16 a[140:141], v140 offset:512
v_cvt_pk_bf16_f32 v56, v72, v73
v_mfma_f32_16x16x32_bf16 v[188:191], a[248:251], v[116:119], v[188:191]
v_exp_f32 v74, v178
v_mfma_f32_16x16x32_bf16 v[184:187], a[236:239], v[148:151], v[184:187]
s_waitcnt lgkmcnt(11)
ds_read_b64_tr_b16 a[142:143], v140 offset:4608
v_mfma_f32_16x16x32_bf16 v[188:191], a[252:255], v[148:151], v[188:191]
v_exp_f32_e64 v75, v179
v_mfma_f32_16x16x32_bf16 a[0:3], a[160:163], v[40:43], a[0:3]
v_cvt_pk_bf16_f32 v57, v74, v75
v_mfma_f32_16x16x32_bf16 a[32:35], a[160:163], v[44:47], a[32:35]
v_exp_f32_e64 v72, v180
v_mfma_f32_16x16x32_bf16 a[4:7], a[164:167], v[40:43], a[4:7]
buffer_store_dwordx4 v[232:235], %[vX], %[sWS], s93 offen offset:0 sc1
v_mfma_f32_16x16x32_bf16 a[36:39], a[164:167], v[44:47], a[36:39]
v_exp_f32_e64 v73, v181
v_mfma_f32_16x16x32_bf16 a[8:11], a[168:171], v[40:43], a[8:11]
v_cvt_pk_bf16_f32 v58, v72, v73
v_mfma_f32_16x16x32_bf16 a[40:43], a[168:171], v[44:47], a[40:43]
v_exp_f32_e64 v74, v182
v_mfma_f32_16x16x32_bf16 a[12:15], a[172:175], v[40:43], a[12:15]
buffer_load_dwordx4 %[vVs0], s[20:23], s56 offen offset:2048 lds
v_mfma_f32_16x16x32_bf16 a[44:47], a[172:175], v[44:47], a[44:47]
v_exp_f32_e64 v75, v183
v_mfma_f32_16x16x32_bf16 a[16:19], a[176:179], v[40:43], a[16:19]
v_cvt_pk_bf16_f32 v59, v74, v75
v_mfma_f32_16x16x32_bf16 a[48:51], a[176:179], v[44:47], a[48:51]
v_exp_f32_e64 v72, v184
v_mfma_f32_16x16x32_bf16 a[20:23], a[180:183], v[40:43], a[20:23]
buffer_load_dwordx4 %[vVs1], s[20:23], s56 offen offset:3072 lds
v_mfma_f32_16x16x32_bf16 a[52:55], a[180:183], v[44:47], a[52:55]
v_exp_f32_e64 v73, v185
v_mfma_f32_16x16x32_bf16 a[24:27], a[184:187], v[40:43], a[24:27]
s_add_u32 s56, s56, 16384
v_cvt_pk_bf16_f32 v60, v72, v73
v_mfma_f32_16x16x32_bf16 a[56:59], a[184:187], v[44:47], a[56:59]
v_exp_f32 v74, v186
v_mfma_f32_16x16x32_bf16 a[28:31], a[188:191], v[40:43], a[28:31]
s_waitcnt lgkmcnt(8)
v_mfma_f32_16x16x32_bf16 a[60:63], a[188:191], v[44:47], a[60:63]
v_exp_f32_e64 v75, v187
v_mfma_f32_16x16x32_bf16 v[160:163], a[192:195], v[32:35], v[192:195]
ds_read_b128 a[224:227], %[vKrd] offset:8192
v_cvt_pk_bf16_f32 v61, v74, v75
v_mfma_f32_16x16x32_bf16 v[164:167], a[208:211], v[32:35], v[192:195]
v_exp_f32_e64 v72, v188
v_mfma_f32_16x16x32_bf16 v[160:163], a[196:199], v[64:67], v[160:163]
ds_read_b128 a[240:243], %[vKrd] offset:12288
ds_read_b128 a[228:231], %[vKrd] offset:8704
v_mfma_f32_16x16x32_bf16 v[164:167], a[212:215], v[64:67], v[164:167]
v_exp_f32_e64 v73, v189
v_mfma_f32_16x16x32_bf16 v[160:163], a[200:203], v[96:99], v[160:163]
ds_read_b128 a[244:247], %[vKrd] offset:12800
v_cvt_pk_bf16_f32 v62, v72, v73
v_mfma_f32_16x16x32_bf16 v[164:167], a[216:219], v[96:99], v[164:167]
v_exp_f32_e64 v74, v190
v_mfma_f32_16x16x32_bf16 v[160:163], a[204:207], v[128:131], v[160:163]
ds_read_b64_tr_b16 a[144:145], %[vVr] offset:1024
ds_read_b64_tr_b16 a[146:147], %[vVr] offset:5120
v_mfma_f32_16x16x32_bf16 v[164:167], a[220:223], v[128:131], v[164:167]
v_exp_f32_e64 v75, v191
v_mfma_f32_16x16x32_bf16 v[168:171], a[192:195], v[36:39], v[196:199]
ds_read_b64_tr_b16 a[148:149], v140 offset:1024
v_cvt_pk_bf16_f32 v63, v74, v75
v_mfma_f32_16x16x32_bf16 v[172:175], a[208:211], v[36:39], v[196:199]
ds_read_b64_tr_b16 a[150:151], v140 offset:5120
v_mfma_f32_16x16x32_bf16 v[168:171], a[196:199], v[68:71], v[168:171]
v_exp_f32_e64 v72, v160
v_mfma_f32_16x16x32_bf16 v[172:175], a[212:215], v[68:71], v[172:175]
ds_read_b64_tr_b16 a[152:153], %[vVr] offset:1536
ds_read_b64_tr_b16 a[154:155], %[vVr] offset:5632
v_mfma_f32_16x16x32_bf16 v[168:171], a[200:203], v[100:103], v[168:171]
v_exp_f32_e64 v73, v161
v_smfmac_f32_16x16x64_bf16 v[220:223], v[212:215], v[56:63], v231
ds_read_b64_tr_b16 a[156:157], v140 offset:1536
v_cvt_pk_bf16_f32 v40, v72, v73
v_mfma_f32_16x16x32_bf16 v[172:175], a[216:219], v[100:103], v[172:175]
v_exp_f32_e64 v74, v162
v_mfma_f32_16x16x32_bf16 v[168:171], a[204:207], v[132:135], v[168:171]
ds_read_b64_tr_b16 a[158:159], v140 offset:5632
v_mfma_f32_16x16x32_bf16 v[172:175], a[220:223], v[132:135], v[172:175]
v_exp_f32_e64 v75, v163
v_mfma_f32_16x16x32_bf16 a[64:67], a[160:163], v[56:59], a[64:67]
v_cvt_pk_bf16_f32 v41, v74, v75
v_add_u32_e64 v106, 1, v106
v_mfma_f32_16x16x32_bf16 a[96:99], a[160:163], v[60:63], a[96:99]
LPa_5:
s_cbranch_execz LRa
v_exp_f32 v72, v164
v_mfma_f32_16x16x32_bf16 a[68:71], a[164:167], v[56:59], a[68:71]
v_mfma_f32_16x16x32_bf16 a[100:103], a[164:167], v[60:63], a[100:103]
v_exp_f32_e64 v73, v165
v_mfma_f32_16x16x32_bf16 a[72:75], a[168:171], v[56:59], a[72:75]
s_add_u32 m0, %[sLw], 49152
v_cvt_pk_bf16_f32 v42, v72, v73
v_mfma_f32_16x16x32_bf16 a[104:107], a[168:171], v[60:63], a[104:107]
v_exp_f32_e64 v74, v166
v_mfma_f32_16x16x32_bf16 a[76:79], a[172:175], v[56:59], a[76:79]
buffer_load_dwordx4 %[vVs0], s[16:19], s56 offen offset:0 lds
v_mfma_f32_16x16x32_bf16 a[108:111], a[172:175], v[60:63], a[108:111]
v_exp_f32_e64 v75, v167
v_mfma_f32_16x16x32_bf16 a[80:83], a[176:179], v[56:59], a[80:83]
v_cvt_pk_bf16_f32 v43, v74, v75
v_mfma_f32_16x16x32_bf16 a[112:115], a[176:179], v[60:63], a[112:115]
v_exp_f32_e64 v72, v168
v_mfma_f32_16x16x32_bf16 a[84:87], a[180:183], v[56:59], a[84:87]
buffer_load_dwordx4 %[vVs1], s[16:19], s56 offen offset:1024 lds
v_mfma_f32_16x16x32_bf16 a[116:119], a[180:183], v[60:63], a[116:119]
v_exp_f32_e64 v73, v169
v_mfma_f32_16x16x32_bf16 a[88:91], a[184:187], v[56:59], a[88:91]
v_cvt_pk_bf16_f32 v44, v72, v73
v_mfma_f32_16x16x32_bf16 a[120:123], a[184:187], v[60:63], a[120:123]
v_exp_f32_e64 v74, v170
v_mfma_f32_16x16x32_bf16 a[92:95], a[188:191], v[56:59], a[92:95]
v_mfma_f32_16x16x32_bf16 a[124:127], a[188:191], v[60:63], a[124:127]
v_exp_f32_e64 v75, v171
v_mfma_f32_16x16x32_bf16 v[176:179], a[192:195], v[48:51], v[200:203]
ds_read_b128 a[232:235], %[vKrd] offset:9216
v_cvt_pk_bf16_f32 v45, v74, v75
v_mfma_f32_16x16x32_bf16 v[180:183], a[208:211], v[48:51], v[200:203]
v_exp_f32_e64 v72, v172
v_mfma_f32_16x16x32_bf16 v[176:179], a[196:199], v[80:83], v[176:179]
ds_read_b128 a[248:251], %[vKrd] offset:13312
ds_read_b128 a[236:239], %[vKrd] offset:9728
v_mfma_f32_16x16x32_bf16 v[180:183], a[212:215], v[80:83], v[180:183]
v_exp_f32_e64 v73, v173
v_mfma_f32_16x16x32_bf16 v[176:179], a[200:203], v[112:115], v[176:179]
ds_read_b128 a[252:255], %[vKrd] offset:13824
v_cvt_pk_bf16_f32 v46, v72, v73
v_mfma_f32_16x16x32_bf16 v[180:183], a[216:219], v[112:115], v[180:183]
)"
R"(v_exp_f32_e64 v74, v174
v_mfma_f32_16x16x32_bf16 v[176:179], a[204:207], v[144:147], v[176:179]
ds_read_b64_tr_b16 a[160:161], %[vVr] offset:8192
ds_read_b64_tr_b16 a[162:163], %[vVr] offset:12288
v_mfma_f32_16x16x32_bf16 v[180:183], a[220:223], v[144:147], v[180:183]
v_exp_f32_e64 v75, v175
v_mfma_f32_16x16x32_bf16 v[184:187], a[192:195], v[52:55], v[204:207]
ds_read_b64_tr_b16 a[164:165], v140 offset:8192
v_cvt_pk_bf16_f32 v47, v74, v75
v_mfma_f32_16x16x32_bf16 v[188:191], a[208:211], v[52:55], v[204:207]
ds_read_b64_tr_b16 a[166:167], v140 offset:12288
ds_read_b64_tr_b16 a[168:169], %[vVr] offset:8704
v_mfma_f32_16x16x32_bf16 v[184:187], a[196:199], v[84:87], v[184:187]
v_exp_f32_e64 v72, v176
v_mfma_f32_16x16x32_bf16 v[188:191], a[212:215], v[84:87], v[188:191]
ds_read_b64_tr_b16 a[170:171], %[vVr] offset:12800
ds_read_b64_tr_b16 a[172:173], v140 offset:8704
v_mfma_f32_16x16x32_bf16 v[184:187], a[200:203], v[116:119], v[184:187]
v_exp_f32_e64 v73, v177
v_smfmac_f32_16x16x64_bf16 v[216:219], v[212:215], v[40:47], v231
ds_read_b64_tr_b16 a[174:175], v140 offset:12800
v_cvt_pk_bf16_f32 v56, v72, v73
v_mfma_f32_16x16x32_bf16 v[188:191], a[216:219], v[116:119], v[188:191]
v_exp_f32 v74, v178
v_mfma_f32_16x16x32_bf16 v[184:187], a[204:207], v[148:151], v[184:187]
s_waitcnt vmcnt(14) lgkmcnt(8)
ds_write_b32 v208, v210 offset:0
v_mfma_f32_16x16x32_bf16 v[188:191], a[220:223], v[148:151], v[188:191]
v_exp_f32_e64 v75, v179
v_mfma_f32_16x16x32_bf16 a[0:3], a[128:131], v[40:43], a[0:3]
v_cvt_pk_bf16_f32 v57, v74, v75
v_mfma_f32_16x16x32_bf16 a[32:35], a[128:131], v[44:47], a[32:35]
v_exp_f32_e64 v72, v180
v_mfma_f32_16x16x32_bf16 a[4:7], a[132:135], v[40:43], a[4:7]
buffer_store_dwordx4 v[236:239], %[vX], %[sWS], s94 offen offset:0 sc1
v_mfma_f32_16x16x32_bf16 a[36:39], a[132:135], v[44:47], a[36:39]
v_exp_f32_e64 v73, v181
v_mfma_f32_16x16x32_bf16 a[8:11], a[136:139], v[40:43], a[8:11]
v_cvt_pk_bf16_f32 v58, v72, v73
v_mfma_f32_16x16x32_bf16 a[40:43], a[136:139], v[44:47], a[40:43]
v_exp_f32_e64 v74, v182
v_mfma_f32_16x16x32_bf16 a[12:15], a[140:143], v[40:43], a[12:15]
buffer_load_dwordx4 %[vVs0], s[16:19], s56 offen offset:2048 lds
v_mfma_f32_16x16x32_bf16 a[44:47], a[140:143], v[44:47], a[44:47]
v_exp_f32_e64 v75, v183
v_mfma_f32_16x16x32_bf16 a[16:19], a[144:147], v[40:43], a[16:19]
v_cvt_pk_bf16_f32 v59, v74, v75
v_mfma_f32_16x16x32_bf16 a[48:51], a[144:147], v[44:47], a[48:51]
v_exp_f32_e64 v72, v184
v_mfma_f32_16x16x32_bf16 a[20:23], a[148:151], v[40:43], a[20:23]
buffer_load_dwordx4 %[vVs1], s[16:19], s56 offen offset:3072 lds
v_mfma_f32_16x16x32_bf16 a[52:55], a[148:151], v[44:47], a[52:55]
v_exp_f32_e64 v73, v185
v_mfma_f32_16x16x32_bf16 a[24:27], a[152:155], v[40:43], a[24:27]
v_cvt_pk_bf16_f32 v60, v72, v73
v_mfma_f32_16x16x32_bf16 a[56:59], a[152:155], v[44:47], a[56:59]
v_exp_f32_e64 v74, v186
v_mfma_f32_16x16x32_bf16 a[28:31], a[156:159], v[40:43], a[28:31]
ds_read_b32 v211, v209 offset:0
v_mfma_f32_16x16x32_bf16 a[60:63], a[156:159], v[44:47], a[60:63]
v_exp_f32_e64 v75, v187
v_mfma_f32_16x16x32_bf16 v[160:163], a[224:227], v[32:35], v[192:195]
ds_read_b64_tr_b16 a[176:177], %[vVr] offset:9216
v_cvt_pk_bf16_f32 v61, v74, v75
v_mfma_f32_16x16x32_bf16 v[164:167], a[240:243], v[32:35], v[192:195]
v_exp_f32_e64 v72, v188
v_mfma_f32_16x16x32_bf16 v[160:163], a[228:231], v[64:67], v[160:163]
ds_read_b64_tr_b16 a[178:179], %[vVr] offset:13312
ds_read_b64_tr_b16 a[180:181], v140 offset:9216
v_mfma_f32_16x16x32_bf16 v[164:167], a[244:247], v[64:67], v[164:167]
v_exp_f32_e64 v73, v189
v_mfma_f32_16x16x32_bf16 v[160:163], a[232:235], v[96:99], v[160:163]
ds_read_b64_tr_b16 a[182:183], v140 offset:13312
v_cvt_pk_bf16_f32 v62, v72, v73
v_mfma_f32_16x16x32_bf16 v[164:167], a[248:251], v[96:99], v[164:167]
v_exp_f32_e64 v74, v190
v_mfma_f32_16x16x32_bf16 v[160:163], a[236:239], v[128:131], v[160:163]
s_waitcnt lgkmcnt(4)
v_cmp_lt_u32 vcc, v211, v210
v_mfma_f32_16x16x32_bf16 v[164:167], a[252:255], v[128:131], v[164:167]
v_exp_f32_e64 v75, v191
v_mfma_f32_16x16x32_bf16 v[168:171], a[224:227], v[36:39], v[196:199]
ds_read_b64_tr_b16 a[184:185], %[vVr] offset:9728
v_cvt_pk_bf16_f32 v63, v74, v75
v_mfma_f32_16x16x32_bf16 v[172:175], a[240:243], v[36:39], v[196:199]
s_cbranch_vccnz L_fs3_%=
L_fsok3_%=:
v_mfma_f32_16x16x32_bf16 v[168:171], a[228:231], v[68:71], v[168:171]
v_exp_f32 v72, v160
v_mfma_f32_16x16x32_bf16 v[172:175], a[244:247], v[68:71], v[172:175]
ds_read_b128 a[192:195], %[vKrd] offset:16384
ds_read_b128 a[208:211], %[vKrd] offset:20480
v_mfma_f32_16x16x32_bf16 v[168:171], a[232:235], v[100:103], v[168:171]
v_exp_f32_e64 v73, v161
v_smfmac_f32_16x16x64_bf16 v[220:223], v[212:215], v[56:63], v231
ds_read_b128 a[196:199], %[vKrd] offset:16896
v_cvt_pk_bf16_f32 v40, v72, v73
v_mfma_f32_16x16x32_bf16 v[172:175], a[248:251], v[100:103], v[172:175]
v_exp_f32_e64 v74, v162
v_mfma_f32_16x16x32_bf16 v[168:171], a[236:239], v[132:135], v[168:171]
ds_read_b128 a[212:215], %[vKrd] offset:20992
v_mfma_f32_16x16x32_bf16 v[172:175], a[252:255], v[132:135], v[172:175]
v_exp_f32_e64 v75, v163
v_mfma_f32_16x16x32_bf16 a[64:67], a[128:131], v[56:59], a[64:67]
ds_read_b64_tr_b16 a[186:187], %[vVr] offset:13824
v_cvt_pk_bf16_f32 v41, v74, v75
v_mfma_f32_16x16x32_bf16 a[96:99], a[128:131], v[60:63], a[96:99]
v_exp_f32_e64 v72, v164
v_mfma_f32_16x16x32_bf16 a[68:71], a[132:135], v[56:59], a[68:71]
v_mfma_f32_16x16x32_bf16 a[100:103], a[132:135], v[60:63], a[100:103]
v_exp_f32_e64 v73, v165
v_mfma_f32_16x16x32_bf16 a[72:75], a[136:139], v[56:59], a[72:75]
s_add_u32 m0, %[sLw], 114688
v_cvt_pk_bf16_f32 v42, v72, v73
v_mfma_f32_16x16x32_bf16 a[104:107], a[136:139], v[60:63], a[104:107]
v_exp_f32_e64 v74, v166
v_mfma_f32_16x16x32_bf16 a[76:79], a[140:143], v[56:59], a[76:79]
buffer_load_dwordx4 %[vVs0], s[20:23], s56 offen offset:0 lds
v_mfma_f32_16x16x32_bf16 a[108:111], a[140:143], v[60:63], a[108:111]
v_exp_f32_e64 v75, v167
v_mfma_f32_16x16x32_bf16 a[80:83], a[144:147], v[56:59], a[80:83]
ds_read_b64_tr_b16 a[188:189], v140 offset:9728
v_cvt_pk_bf16_f32 v43, v74, v75
v_mfma_f32_16x16x32_bf16 a[112:115], a[144:147], v[60:63], a[112:115]
v_exp_f32_e64 v72, v168
v_mfma_f32_16x16x32_bf16 a[84:87], a[148:151], v[56:59], a[84:87]
buffer_load_dwordx4 %[vVs1], s[20:23], s56 offen offset:1024 lds
v_mfma_f32_16x16x32_bf16 a[116:119], a[148:151], v[60:63], a[116:119]
v_exp_f32_e64 v73, v169
v_mfma_f32_16x16x32_bf16 a[88:91], a[152:155], v[56:59], a[88:91]
ds_read_b64_tr_b16 a[190:191], v140 offset:13824
v_cvt_pk_bf16_f32 v44, v72, v73
v_mfma_f32_16x16x32_bf16 a[120:123], a[152:155], v[60:63], a[120:123]
v_exp_f32 v74, v170
v_mfma_f32_16x16x32_bf16 a[92:95], a[156:159], v[56:59], a[92:95]
s_waitcnt lgkmcnt(0)
v_add_u32 v210, 1, v210
s_sub_u32 s58, s58, 1
v_mfma_f32_16x16x32_bf16 a[124:127], a[156:159], v[60:63], a[124:127]
v_exp_f32_e64 v75, v171
s_cbranch_scc0 L_loop_%=
L_tail:
s_mov_b64 exec, 0
s_cmp_eq_u32 %[sKV], 0
s_cbranch_scc1 LPc_0
s_cmp_eq_u32 %[sKV], 4096
s_cbranch_scc1 LPc_1
s_cmp_eq_u32 %[sKV], 8192
s_cbranch_scc1 LPc_2
s_branch LPc_3
LRc:
s_mov_b64 exec, -1
v_mfma_f32_16x16x32_bf16 v[176:179], a[224:227], v[48:51], v[200:203]
ds_read_b128 a[200:203], %[vKrd] offset:17408
v_cvt_pk_bf16_f32 v45, v74, v75
v_mfma_f32_16x16x32_bf16 v[180:183], a[240:243], v[48:51], v[200:203]
v_exp_f32_e64 v72, v172
v_mfma_f32_16x16x32_bf16 v[176:179], a[228:231], v[80:83], v[176:179]
ds_read_b128 a[216:219], %[vKrd] offset:21504
ds_read_b128 a[204:207], %[vKrd] offset:17920
v_mfma_f32_16x16x32_bf16 v[180:183], a[244:247], v[80:83], v[180:183]
v_exp_f32_e64 v73, v173
v_mfma_f32_16x16x32_bf16 v[176:179], a[232:235], v[112:115], v[176:179]
ds_read_b128 a[220:223], %[vKrd] offset:22016
v_cvt_pk_bf16_f32 v46, v72, v73
v_mfma_f32_16x16x32_bf16 v[180:183], a[248:251], v[112:115], v[180:183]
v_exp_f32_e64 v74, v174
v_mfma_f32_16x16x32_bf16 v[176:179], a[236:239], v[144:147], v[176:179]
ds_read_b64_tr_b16 a[128:129], %[vVr] offset:16384
ds_read_b64_tr_b16 a[130:131], %[vVr] offset:20480
v_mfma_f32_16x16x32_bf16 v[180:183], a[252:255], v[144:147], v[180:183]
v_exp_f32_e64 v75, v175
v_mfma_f32_16x16x32_bf16 v[184:187], a[224:227], v[52:55], v[204:207]
ds_read_b64_tr_b16 a[132:133], v140 offset:16384
v_cvt_pk_bf16_f32 v47, v74, v75
v_mfma_f32_16x16x32_bf16 v[188:191], a[240:243], v[52:55], v[204:207]
ds_read_b64_tr_b16 a[134:135], v140 offset:20480
v_mfma_f32_16x16x32_bf16 v[184:187], a[228:231], v[84:87], v[184:187]
v_exp_f32_e64 v72, v176
v_mfma_f32_16x16x32_bf16 v[188:191], a[244:247], v[84:87], v[188:191]
ds_read_b64_tr_b16 a[136:137], %[vVr] offset:16896
ds_read_b64_tr_b16 a[138:139], %[vVr] offset:20992
v_mfma_f32_16x16x32_bf16 v[184:187], a[232:235], v[116:119], v[184:187]
v_exp_f32_e64 v73, v177
v_smfmac_f32_16x16x64_bf16 v[216:219], v[212:215], v[40:47], v231
ds_read_b64_tr_b16 a[140:141], v140 offset:16896
v_cvt_pk_bf16_f32 v56, v72, v73
v_mfma_f32_16x16x32_bf16 v[188:191], a[248:251], v[116:119], v[188:191]
v_exp_f32_e64 v74, v178
v_mfma_f32_16x16x32_bf16 v[184:187], a[236:239], v[148:151], v[184:187]
ds_read_b64_tr_b16 a[142:143], v140 offset:20992
v_mfma_f32_16x16x32_bf16 v[188:191], a[252:255], v[148:151], v[188:191]
v_exp_f32_e64 v75, v179
v_mfma_f32_16x16x32_bf16 a[0:3], a[160:163], v[40:43], a[0:3]
v_cvt_pk_bf16_f32 v57, v74, v75
v_mfma_f32_16x16x32_bf16 a[32:35], a[160:163], v[44:47], a[32:35]
v_exp_f32_e64 v72, v180
v_mfma_f32_16x16x32_bf16 a[4:7], a[164:167], v[40:43], a[4:7]
buffer_load_dwordx4 v[232:235], %[vX], %[sWS], s37 offen offset:0 sc1
v_mfma_f32_16x16x32_bf16 a[36:39], a[164:167], v[44:47], a[36:39]
)"
R"(v_exp_f32_e64 v73, v181
v_mfma_f32_16x16x32_bf16 a[8:11], a[168:171], v[40:43], a[8:11]
v_cvt_pk_bf16_f32 v58, v72, v73
v_mfma_f32_16x16x32_bf16 a[40:43], a[168:171], v[44:47], a[40:43]
v_exp_f32_e64 v74, v182
v_mfma_f32_16x16x32_bf16 a[12:15], a[172:175], v[40:43], a[12:15]
buffer_load_dwordx4 %[vVs0], s[20:23], s56 offen offset:2048 lds
v_mfma_f32_16x16x32_bf16 a[44:47], a[172:175], v[44:47], a[44:47]
v_exp_f32_e64 v75, v183
v_mfma_f32_16x16x32_bf16 a[16:19], a[176:179], v[40:43], a[16:19]
v_cvt_pk_bf16_f32 v59, v74, v75
v_mfma_f32_16x16x32_bf16 a[48:51], a[176:179], v[44:47], a[48:51]
v_exp_f32_e64 v72, v184
v_mfma_f32_16x16x32_bf16 a[20:23], a[180:183], v[40:43], a[20:23]
buffer_load_dwordx4 %[vVs1], s[20:23], s56 offen offset:3072 lds
v_mfma_f32_16x16x32_bf16 a[52:55], a[180:183], v[44:47], a[52:55]
v_exp_f32_e64 v73, v185
v_mfma_f32_16x16x32_bf16 a[24:27], a[184:187], v[40:43], a[24:27]
s_add_u32 s56, s56, 16384
v_cvt_pk_bf16_f32 v60, v72, v73
v_mfma_f32_16x16x32_bf16 a[56:59], a[184:187], v[44:47], a[56:59]
v_exp_f32 v74, v186
v_mfma_f32_16x16x32_bf16 a[28:31], a[188:191], v[40:43], a[28:31]
s_waitcnt lgkmcnt(8)
v_mfma_f32_16x16x32_bf16 a[60:63], a[188:191], v[44:47], a[60:63]
v_exp_f32_e64 v75, v187
v_mfma_f32_16x16x32_bf16 v[160:163], a[192:195], v[32:35], v[192:195]
ds_read_b128 a[224:227], %[vKrd] offset:24576
v_cvt_pk_bf16_f32 v61, v74, v75
v_mfma_f32_16x16x32_bf16 v[164:167], a[208:211], v[32:35], v[192:195]
v_exp_f32_e64 v72, v188
v_mfma_f32_16x16x32_bf16 v[160:163], a[196:199], v[64:67], v[160:163]
ds_read_b128 a[240:243], %[vKrd] offset:28672
ds_read_b128 a[228:231], %[vKrd] offset:25088
v_mfma_f32_16x16x32_bf16 v[164:167], a[212:215], v[64:67], v[164:167]
v_exp_f32_e64 v73, v189
v_mfma_f32_16x16x32_bf16 v[160:163], a[200:203], v[96:99], v[160:163]
ds_read_b128 a[244:247], %[vKrd] offset:29184
v_cvt_pk_bf16_f32 v62, v72, v73
v_mfma_f32_16x16x32_bf16 v[164:167], a[216:219], v[96:99], v[164:167]
v_exp_f32_e64 v74, v190
v_mfma_f32_16x16x32_bf16 v[160:163], a[204:207], v[128:131], v[160:163]
ds_read_b64_tr_b16 a[144:145], %[vVr] offset:17408
ds_read_b64_tr_b16 a[146:147], %[vVr] offset:21504
v_mfma_f32_16x16x32_bf16 v[164:167], a[220:223], v[128:131], v[164:167]
v_exp_f32_e64 v75, v191
v_mfma_f32_16x16x32_bf16 v[168:171], a[192:195], v[36:39], v[196:199]
ds_read_b64_tr_b16 a[148:149], v140 offset:17408
v_cvt_pk_bf16_f32 v63, v74, v75
v_mfma_f32_16x16x32_bf16 v[172:175], a[208:211], v[36:39], v[196:199]
ds_read_b64_tr_b16 a[150:151], v140 offset:21504
v_mfma_f32_16x16x32_bf16 v[168:171], a[196:199], v[68:71], v[168:171]
v_exp_f32_e64 v72, v160
v_mfma_f32_16x16x32_bf16 v[172:175], a[212:215], v[68:71], v[172:175]
ds_read_b64_tr_b16 a[152:153], %[vVr] offset:17920
ds_read_b64_tr_b16 a[154:155], %[vVr] offset:22016
v_mfma_f32_16x16x32_bf16 v[168:171], a[200:203], v[100:103], v[168:171]
v_exp_f32_e64 v73, v161
v_smfmac_f32_16x16x64_bf16 v[220:223], v[212:215], v[56:63], v231
ds_read_b64_tr_b16 a[156:157], v140 offset:17920
v_cvt_pk_bf16_f32 v40, v72, v73
v_mfma_f32_16x16x32_bf16 v[172:175], a[216:219], v[100:103], v[172:175]
v_exp_f32_e64 v74, v162
v_mfma_f32_16x16x32_bf16 v[168:171], a[204:207], v[132:135], v[168:171]
ds_read_b64_tr_b16 a[158:159], v140 offset:22016
v_mfma_f32_16x16x32_bf16 v[172:175], a[220:223], v[132:135], v[172:175]
v_exp_f32_e64 v75, v163
v_mfma_f32_16x16x32_bf16 a[64:67], a[160:163], v[56:59], a[64:67]
v_cvt_pk_bf16_f32 v41, v74, v75
v_mfma_f32_16x16x32_bf16 a[96:99], a[160:163], v[60:63], a[96:99]
v_exp_f32_e64 v72, v164
v_mfma_f32_16x16x32_bf16 a[68:71], a[164:167], v[56:59], a[68:71]
v_add_u32_e64 v107, s38, %[vX]
v_mfma_f32_16x16x32_bf16 a[100:103], a[164:167], v[60:63], a[100:103]
v_exp_f32_e64 v73, v165
v_mfma_f32_16x16x32_bf16 a[72:75], a[168:171], v[56:59], a[72:75]
v_cvt_pk_bf16_f32 v42, v72, v73
v_mfma_f32_16x16x32_bf16 a[104:107], a[168:171], v[60:63], a[104:107]
v_exp_f32_e64 v74, v166
v_mfma_f32_16x16x32_bf16 a[76:79], a[172:175], v[56:59], a[76:79]
v_add_u32_e64 v136, -1, v225
v_mfma_f32_16x16x32_bf16 a[108:111], a[172:175], v[60:63], a[108:111]
v_exp_f32_e64 v75, v167
v_mfma_f32_16x16x32_bf16 a[80:83], a[176:179], v[56:59], a[80:83]
v_cvt_pk_bf16_f32 v43, v74, v75
v_mfma_f32_16x16x32_bf16 a[112:115], a[176:179], v[60:63], a[112:115]
v_exp_f32_e64 v72, v168
v_mfma_f32_16x16x32_bf16 a[84:87], a[180:183], v[56:59], a[84:87]
v_ashrrev_i32_e64 v136, 31, v136
v_mfma_f32_16x16x32_bf16 a[116:119], a[180:183], v[60:63], a[116:119]
v_exp_f32_e64 v73, v169
v_mfma_f32_16x16x32_bf16 a[88:91], a[184:187], v[56:59], a[88:91]
v_cvt_pk_bf16_f32 v44, v72, v73
v_mfma_f32_16x16x32_bf16 a[120:123], a[184:187], v[60:63], a[120:123]
v_exp_f32_e64 v74, v170
v_mfma_f32_16x16x32_bf16 a[92:95], a[188:191], v[56:59], a[92:95]
v_and_b32 v139, 1065369472, v136
v_mfma_f32_16x16x32_bf16 a[124:127], a[188:191], v[60:63], a[124:127]
v_exp_f32_e64 v75, v171
v_mfma_f32_16x16x32_bf16 v[176:179], a[192:195], v[48:51], v[200:203]
ds_read_b128 a[232:235], %[vKrd] offset:25600
v_cvt_pk_bf16_f32 v45, v74, v75
v_mfma_f32_16x16x32_bf16 v[180:183], a[208:211], v[48:51], v[200:203]
v_exp_f32_e64 v72, v172
v_mfma_f32_16x16x32_bf16 v[176:179], a[196:199], v[80:83], v[176:179]
ds_read_b128 a[248:251], %[vKrd] offset:29696
ds_read_b128 a[236:239], %[vKrd] offset:26112
v_mfma_f32_16x16x32_bf16 v[180:183], a[212:215], v[80:83], v[180:183]
v_exp_f32_e64 v73, v173
v_mfma_f32_16x16x32_bf16 v[176:179], a[200:203], v[112:115], v[176:179]
ds_read_b128 a[252:255], %[vKrd] offset:30208
v_cvt_pk_bf16_f32 v46, v72, v73
v_mfma_f32_16x16x32_bf16 v[180:183], a[216:219], v[112:115], v[180:183]
v_exp_f32_e64 v74, v174
v_mfma_f32_16x16x32_bf16 v[176:179], a[204:207], v[144:147], v[176:179]
ds_read_b64_tr_b16 a[160:161], %[vVr] offset:24576
ds_read_b64_tr_b16 a[162:163], %[vVr] offset:28672
v_mfma_f32_16x16x32_bf16 v[180:183], a[220:223], v[144:147], v[180:183]
v_exp_f32_e64 v75, v175
v_mfma_f32_16x16x32_bf16 v[184:187], a[192:195], v[52:55], v[204:207]
ds_read_b64_tr_b16 a[164:165], v140 offset:24576
v_cvt_pk_bf16_f32 v47, v74, v75
v_mfma_f32_16x16x32_bf16 v[188:191], a[208:211], v[52:55], v[204:207]
ds_read_b64_tr_b16 a[166:167], v140 offset:28672
v_mfma_f32_16x16x32_bf16 v[184:187], a[196:199], v[84:87], v[184:187]
v_exp_f32_e64 v72, v176
v_mfma_f32_16x16x32_bf16 v[188:191], a[212:215], v[84:87], v[188:191]
ds_read_b64_tr_b16 a[168:169], %[vVr] offset:25088
ds_read_b64_tr_b16 a[170:171], %[vVr] offset:29184
v_mfma_f32_16x16x32_bf16 v[184:187], a[200:203], v[116:119], v[184:187]
v_exp_f32_e64 v73, v177
v_smfmac_f32_16x16x64_bf16 v[216:219], v[212:215], v[40:47], v231
ds_read_b64_tr_b16 a[172:173], v140 offset:25088
v_cvt_pk_bf16_f32 v56, v72, v73
v_mfma_f32_16x16x32_bf16 v[188:191], a[216:219], v[116:119], v[188:191]
v_exp_f32 v74, v178
v_mfma_f32_16x16x32_bf16 v[184:187], a[204:207], v[148:151], v[184:187]
s_waitcnt lgkmcnt(11)
ds_read_b64_tr_b16 a[174:175], v140 offset:29184
v_mfma_f32_16x16x32_bf16 v[188:191], a[220:223], v[148:151], v[188:191]
v_exp_f32_e64 v75, v179
v_mfma_f32_16x16x32_bf16 a[0:3], a[128:131], v[40:43], a[0:3]
v_cvt_pk_bf16_f32 v57, v74, v75
v_mfma_f32_16x16x32_bf16 a[32:35], a[128:131], v[44:47], a[32:35]
v_exp_f32_e64 v72, v180
v_mfma_f32_16x16x32_bf16 a[4:7], a[132:135], v[40:43], a[4:7]
v_and_b32 v138, 1065369472, v136
v_mfma_f32_16x16x32_bf16 a[36:39], a[132:135], v[44:47], a[36:39]
v_exp_f32_e64 v73, v181
v_mfma_f32_16x16x32_bf16 a[8:11], a[136:139], v[40:43], a[8:11]
v_cvt_pk_bf16_f32 v58, v72, v73
v_mfma_f32_16x16x32_bf16 a[40:43], a[136:139], v[44:47], a[40:43]
v_exp_f32_e64 v74, v182
v_mfma_f32_16x16x32_bf16 a[12:15], a[140:143], v[40:43], a[12:15]
v_and_b32 v137, 1065369472, v136
v_mfma_f32_16x16x32_bf16 a[44:47], a[140:143], v[44:47], a[44:47]
v_exp_f32_e64 v75, v183
v_mfma_f32_16x16x32_bf16 a[16:19], a[144:147], v[40:43], a[16:19]
v_cvt_pk_bf16_f32 v59, v74, v75
v_mfma_f32_16x16x32_bf16 a[48:51], a[144:147], v[44:47], a[48:51]
v_exp_f32_e64 v72, v184
v_mfma_f32_16x16x32_bf16 a[20:23], a[148:151], v[40:43], a[20:23]
v_and_b32 v136, 1065369472, v136
v_mfma_f32_16x16x32_bf16 a[52:55], a[148:151], v[44:47], a[52:55]
v_exp_f32_e64 v73, v185
v_mfma_f32_16x16x32_bf16 a[24:27], a[152:155], v[40:43], a[24:27]
v_cvt_pk_bf16_f32 v60, v72, v73
v_mfma_f32_16x16x32_bf16 a[56:59], a[152:155], v[44:47], a[56:59]
v_exp_f32_e64 v74, v186
v_mfma_f32_16x16x32_bf16 a[28:31], a[156:159], v[40:43], a[28:31]
s_waitcnt vmcnt(10) lgkmcnt(8)
s_barrier
v_mfma_f32_16x16x32_bf16 a[60:63], a[156:159], v[44:47], a[60:63]
v_exp_f32_e64 v75, v187
v_mfma_f32_16x16x32_bf16 v[160:163], a[224:227], v[32:35], v[192:195]
ds_read_b128 a[192:195], %[vKrd] offset:32768
v_cvt_pk_bf16_f32 v61, v74, v75
v_mfma_f32_16x16x32_bf16 v[164:167], a[240:243], v[32:35], v[192:195]
v_exp_f32_e64 v72, v188
v_mfma_f32_16x16x32_bf16 v[160:163], a[228:231], v[64:67], v[160:163]
ds_read_b128 a[208:211], %[vKrd] offset:36864
ds_read_b128 a[196:199], %[vKrd] offset:33280
v_mfma_f32_16x16x32_bf16 v[164:167], a[244:247], v[64:67], v[164:167]
v_exp_f32_e64 v73, v189
v_mfma_f32_16x16x32_bf16 v[160:163], a[232:235], v[96:99], v[160:163]
ds_read_b128 a[212:215], %[vKrd] offset:37376
v_cvt_pk_bf16_f32 v62, v72, v73
v_mfma_f32_16x16x32_bf16 v[164:167], a[248:251], v[96:99], v[164:167]
v_exp_f32_e64 v74, v190
v_mfma_f32_16x16x32_bf16 v[160:163], a[236:239], v[128:131], v[160:163]
ds_read_b64_tr_b16 a[176:177], %[vVr] offset:25600
ds_read_b64_tr_b16 a[178:179], %[vVr] offset:29696
v_mfma_f32_16x16x32_bf16 v[164:167], a[252:255], v[128:131], v[164:167]
v_exp_f32_e64 v75, v191
v_mfma_f32_16x16x32_bf16 v[168:171], a[224:227], v[36:39], v[196:199]
ds_read_b64_tr_b16 a[180:181], v140 offset:25600
v_cvt_pk_bf16_f32 v63, v74, v75
v_mfma_f32_16x16x32_bf16 v[172:175], a[240:243], v[36:39], v[196:199]
ds_read_b64_tr_b16 a[182:183], v140 offset:29696
v_mfma_f32_16x16x32_bf16 v[168:171], a[228:231], v[68:71], v[168:171]
)"
R"(v_exp_f32_e64 v72, v160
v_mfma_f32_16x16x32_bf16 v[172:175], a[244:247], v[68:71], v[172:175]
ds_read_b64_tr_b16 a[184:185], %[vVr] offset:26112
ds_read_b64_tr_b16 a[186:187], %[vVr] offset:30208
v_mfma_f32_16x16x32_bf16 v[168:171], a[232:235], v[100:103], v[168:171]
v_exp_f32_e64 v73, v161
v_smfmac_f32_16x16x64_bf16 v[220:223], v[212:215], v[56:63], v231
ds_read_b64_tr_b16 a[188:189], v140 offset:26112
v_cvt_pk_bf16_f32 v40, v72, v73
v_mfma_f32_16x16x32_bf16 v[172:175], a[248:251], v[100:103], v[172:175]
v_exp_f32_e64 v74, v162
v_mfma_f32_16x16x32_bf16 v[168:171], a[236:239], v[132:135], v[168:171]
ds_read_b64_tr_b16 a[190:191], v140 offset:30208
v_mfma_f32_16x16x32_bf16 v[172:175], a[252:255], v[132:135], v[172:175]
v_exp_f32_e64 v75, v163
v_mfma_f32_16x16x32_bf16 a[64:67], a[128:131], v[56:59], a[64:67]
v_cvt_pk_bf16_f32 v41, v74, v75
v_mfma_f32_16x16x32_bf16 a[96:99], a[128:131], v[60:63], a[96:99]
v_exp_f32_e64 v72, v164
v_mfma_f32_16x16x32_bf16 a[68:71], a[132:135], v[56:59], a[68:71]
v_mfma_f32_16x16x32_bf16 a[100:103], a[132:135], v[60:63], a[100:103]
v_exp_f32_e64 v73, v165
v_mfma_f32_16x16x32_bf16 a[72:75], a[136:139], v[56:59], a[72:75]
v_cvt_pk_bf16_f32 v42, v72, v73
v_mfma_f32_16x16x32_bf16 a[104:107], a[136:139], v[60:63], a[104:107]
v_exp_f32_e64 v74, v166
v_mfma_f32_16x16x32_bf16 a[76:79], a[140:143], v[56:59], a[76:79]
v_mfma_f32_16x16x32_bf16 a[108:111], a[140:143], v[60:63], a[108:111]
v_exp_f32_e64 v75, v167
v_mfma_f32_16x16x32_bf16 a[80:83], a[144:147], v[56:59], a[80:83]
v_cvt_pk_bf16_f32 v43, v74, v75
v_mfma_f32_16x16x32_bf16 a[112:115], a[144:147], v[60:63], a[112:115]
v_exp_f32_e64 v72, v168
v_mfma_f32_16x16x32_bf16 a[84:87], a[148:151], v[56:59], a[84:87]
v_mfma_f32_16x16x32_bf16 a[116:119], a[148:151], v[60:63], a[116:119]
v_exp_f32_e64 v73, v169
v_mfma_f32_16x16x32_bf16 a[88:91], a[152:155], v[56:59], a[88:91]
v_cvt_pk_bf16_f32 v44, v72, v73
v_mfma_f32_16x16x32_bf16 a[120:123], a[152:155], v[60:63], a[120:123]
v_exp_f32_e64 v74, v170
v_mfma_f32_16x16x32_bf16 a[92:95], a[156:159], v[56:59], a[92:95]
v_mfma_f32_16x16x32_bf16 a[124:127], a[156:159], v[60:63], a[124:127]
v_exp_f32_e64 v75, v171
v_mfma_f32_16x16x32_bf16 v[176:179], a[224:227], v[48:51], v[200:203]
ds_read_b128 a[200:203], %[vKrd] offset:33792
v_cvt_pk_bf16_f32 v45, v74, v75
v_mfma_f32_16x16x32_bf16 v[180:183], a[240:243], v[48:51], v[200:203]
v_exp_f32_e64 v72, v172
v_mfma_f32_16x16x32_bf16 v[176:179], a[228:231], v[80:83], v[176:179]
ds_read_b128 a[216:219], %[vKrd] offset:37888
ds_read_b128 a[204:207], %[vKrd] offset:34304
v_mfma_f32_16x16x32_bf16 v[180:183], a[244:247], v[80:83], v[180:183]
v_exp_f32_e64 v73, v173
v_mfma_f32_16x16x32_bf16 v[176:179], a[232:235], v[112:115], v[176:179]
ds_read_b128 a[220:223], %[vKrd] offset:38400
v_cvt_pk_bf16_f32 v46, v72, v73
v_mfma_f32_16x16x32_bf16 v[180:183], a[248:251], v[112:115], v[180:183]
v_exp_f32_e64 v74, v174
v_mfma_f32_16x16x32_bf16 v[176:179], a[236:239], v[144:147], v[176:179]
ds_read_b64_tr_b16 a[128:129], %[vVr] offset:32768
ds_read_b64_tr_b16 a[130:131], %[vVr] offset:36864
v_mfma_f32_16x16x32_bf16 v[180:183], a[252:255], v[144:147], v[180:183]
v_exp_f32_e64 v75, v175
v_mfma_f32_16x16x32_bf16 v[184:187], a[224:227], v[52:55], v[204:207]
ds_read_b64_tr_b16 a[132:133], v140 offset:32768
v_cvt_pk_bf16_f32 v47, v74, v75
v_mfma_f32_16x16x32_bf16 v[188:191], a[240:243], v[52:55], v[204:207]
ds_read_b64_tr_b16 a[134:135], v140 offset:36864
v_mfma_f32_16x16x32_bf16 v[184:187], a[228:231], v[84:87], v[184:187]
v_exp_f32_e64 v72, v176
v_mfma_f32_16x16x32_bf16 v[188:191], a[244:247], v[84:87], v[188:191]
ds_read_b64_tr_b16 a[136:137], %[vVr] offset:33280
ds_read_b64_tr_b16 a[138:139], %[vVr] offset:37376
v_mfma_f32_16x16x32_bf16 v[184:187], a[232:235], v[116:119], v[184:187]
v_exp_f32_e64 v73, v177
v_smfmac_f32_16x16x64_bf16 v[216:219], v[212:215], v[40:47], v231
ds_read_b64_tr_b16 a[140:141], v140 offset:33280
v_cvt_pk_bf16_f32 v56, v72, v73
v_mfma_f32_16x16x32_bf16 v[188:191], a[248:251], v[116:119], v[188:191]
v_exp_f32 v74, v178
v_mfma_f32_16x16x32_bf16 v[184:187], a[236:239], v[148:151], v[184:187]
s_waitcnt lgkmcnt(11)
ds_read_b64_tr_b16 a[142:143], v140 offset:37376
v_mfma_f32_16x16x32_bf16 v[188:191], a[252:255], v[148:151], v[188:191]
v_exp_f32 v75, v179
v_mfma_f32_16x16x32_bf16 a[0:3], a[160:163], v[40:43], a[0:3]
LPc_0:
s_cbranch_execz LPc_4
v_cvt_pk_bf16_f32 v57, v74, v75
v_mfma_f32_16x16x32_bf16 a[32:35], a[160:163], v[44:47], a[32:35]
v_exp_f32_e64 v72, v180
v_mfma_f32_16x16x32_bf16 a[4:7], a[164:167], v[40:43], a[4:7]
buffer_load_dwordx4 v[240:243], %[vSp], s[24:27], s52 offen offset:0
v_mfma_f32_16x16x32_bf16 a[36:39], a[164:167], v[44:47], a[36:39]
v_exp_f32_e64 v73, v181
v_mfma_f32_16x16x32_bf16 a[8:11], a[168:171], v[40:43], a[8:11]
v_cvt_pk_bf16_f32 v58, v72, v73
v_mfma_f32_16x16x32_bf16 a[40:43], a[168:171], v[44:47], a[40:43]
v_exp_f32_e64 v74, v182
v_mfma_f32_16x16x32_bf16 a[12:15], a[172:175], v[40:43], a[12:15]
v_mfma_f32_16x16x32_bf16 a[44:47], a[172:175], v[44:47], a[44:47]
v_exp_f32_e64 v75, v183
v_mfma_f32_16x16x32_bf16 a[16:19], a[176:179], v[40:43], a[16:19]
s_add_u32 s52, s52, 524288
v_cvt_pk_bf16_f32 v59, v74, v75
v_mfma_f32_16x16x32_bf16 a[48:51], a[176:179], v[44:47], a[48:51]
v_exp_f32_e64 v72, v184
v_mfma_f32_16x16x32_bf16 a[20:23], a[180:183], v[40:43], a[20:23]
v_mfma_f32_16x16x32_bf16 a[52:55], a[180:183], v[44:47], a[52:55]
v_exp_f32_e64 v73, v185
v_mfma_f32_16x16x32_bf16 a[24:27], a[184:187], v[40:43], a[24:27]
v_cvt_pk_bf16_f32 v60, v72, v73
v_mfma_f32_16x16x32_bf16 a[56:59], a[184:187], v[44:47], a[56:59]
v_exp_f32 v74, v186
v_mfma_f32_16x16x32_bf16 a[28:31], a[188:191], v[40:43], a[28:31]
s_waitcnt lgkmcnt(8)
v_mfma_f32_16x16x32_bf16 a[60:63], a[188:191], v[44:47], a[60:63]
v_exp_f32_e64 v75, v187
v_mfma_f32_16x16x32_bf16 v[160:163], a[192:195], v[32:35], v[192:195]
ds_read_b128 a[224:227], %[vKrd] offset:40960
v_cvt_pk_bf16_f32 v61, v74, v75
v_mfma_f32_16x16x32_bf16 v[164:167], a[208:211], v[32:35], v[192:195]
v_exp_f32_e64 v72, v188
v_mfma_f32_16x16x32_bf16 v[160:163], a[196:199], v[64:67], v[160:163]
ds_read_b128 a[240:243], %[vKrd] offset:45056
ds_read_b128 a[228:231], %[vKrd] offset:41472
v_mfma_f32_16x16x32_bf16 v[164:167], a[212:215], v[64:67], v[164:167]
v_exp_f32_e64 v73, v189
v_mfma_f32_16x16x32_bf16 v[160:163], a[200:203], v[96:99], v[160:163]
ds_read_b128 a[244:247], %[vKrd] offset:45568
v_cvt_pk_bf16_f32 v62, v72, v73
v_mfma_f32_16x16x32_bf16 v[164:167], a[216:219], v[96:99], v[164:167]
v_exp_f32_e64 v74, v190
v_mfma_f32_16x16x32_bf16 v[160:163], a[204:207], v[128:131], v[160:163]
ds_read_b64_tr_b16 a[144:145], %[vVr] offset:33792
ds_read_b64_tr_b16 a[146:147], %[vVr] offset:37888
v_mfma_f32_16x16x32_bf16 v[164:167], a[220:223], v[128:131], v[164:167]
v_exp_f32_e64 v75, v191
v_mfma_f32_16x16x32_bf16 v[168:171], a[192:195], v[36:39], v[196:199]
ds_read_b64_tr_b16 a[148:149], v140 offset:33792
v_cvt_pk_bf16_f32 v63, v74, v75
v_mfma_f32_16x16x32_bf16 v[172:175], a[208:211], v[36:39], v[196:199]
ds_read_b64_tr_b16 a[150:151], v140 offset:37888
v_mfma_f32_16x16x32_bf16 v[168:171], a[196:199], v[68:71], v[168:171]
v_exp_f32_e64 v72, v160
v_mfma_f32_16x16x32_bf16 v[172:175], a[212:215], v[68:71], v[172:175]
ds_read_b64_tr_b16 a[152:153], %[vVr] offset:34304
ds_read_b64_tr_b16 a[154:155], %[vVr] offset:38400
v_mfma_f32_16x16x32_bf16 v[168:171], a[200:203], v[100:103], v[168:171]
v_exp_f32_e64 v73, v161
v_smfmac_f32_16x16x64_bf16 v[220:223], v[212:215], v[56:63], v231
ds_read_b64_tr_b16 a[156:157], v140 offset:34304
v_cvt_pk_bf16_f32 v40, v72, v73
v_mfma_f32_16x16x32_bf16 v[172:175], a[216:219], v[100:103], v[172:175]
v_exp_f32_e64 v74, v162
v_mfma_f32_16x16x32_bf16 v[168:171], a[204:207], v[132:135], v[168:171]
ds_read_b64_tr_b16 a[158:159], v140 offset:38400
v_mfma_f32_16x16x32_bf16 v[172:175], a[220:223], v[132:135], v[172:175]
v_exp_f32 v75, v163
v_mfma_f32_16x16x32_bf16 a[64:67], a[160:163], v[56:59], a[64:67]
s_waitcnt vmcnt(3)
v_cvt_pk_bf16_f32 v41, v74, v75
v_min3_u32 v236, v232, v233, v234
v_mfma_f32_16x16x32_bf16 a[96:99], a[160:163], v[60:63], a[96:99]
v_exp_f32_e64 v72, v164
v_mfma_f32_16x16x32_bf16 a[68:71], a[164:167], v[56:59], a[68:71]
v_mfma_f32_16x16x32_bf16 a[100:103], a[164:167], v[60:63], a[100:103]
v_exp_f32_e64 v73, v165
v_mfma_f32_16x16x32_bf16 a[72:75], a[168:171], v[56:59], a[72:75]
v_cvt_pk_bf16_f32 v42, v72, v73
v_min_u32_e64 v236, v236, v235
v_mfma_f32_16x16x32_bf16 a[104:107], a[168:171], v[60:63], a[104:107]
v_exp_f32_e64 v74, v166
v_mfma_f32_16x16x32_bf16 a[76:79], a[172:175], v[56:59], a[76:79]
v_cmp_gt_u32 vcc, 3, v236
s_cbranch_vccnz L_fas_%=
L_faok_%=:
v_mfma_f32_16x16x32_bf16 a[108:111], a[172:175], v[60:63], a[108:111]
v_exp_f32_e64 v75, v167
v_mfma_f32_16x16x32_bf16 a[80:83], a[176:179], v[56:59], a[80:83]
v_cvt_pk_bf16_f32 v43, v74, v75
v_mfma_f32_16x16x32_bf16 a[112:115], a[176:179], v[60:63], a[112:115]
s_mul_i32 s60, s39, 36864
v_exp_f32 v72, v168
v_mfma_f32_16x16x32_bf16 a[84:87], a[180:183], v[56:59], a[84:87]
s_add_u32 s60, s60, s40
v_mfma_f32_16x16x32_bf16 a[116:119], a[180:183], v[60:63], a[116:119]
v_exp_f32_e64 v73, v169
v_mfma_f32_16x16x32_bf16 a[88:91], a[184:187], v[56:59], a[88:91]
v_cvt_pk_bf16_f32 v44, v72, v73
v_mfma_f32_16x16x32_bf16 a[120:123], a[184:187], v[60:63], a[120:123]
v_exp_f32_e64 v74, v170
v_mfma_f32_16x16x32_bf16 a[92:95], a[188:191], v[56:59], a[92:95]
v_mfma_f32_16x16x32_bf16 a[124:127], a[188:191], v[60:63], a[124:127]
v_exp_f32_e64 v75, v171
v_mfma_f32_16x16x32_bf16 v[176:179], a[192:195], v[48:51], v[200:203]
ds_read_b128 a[232:235], %[vKrd] offset:41984
v_cvt_pk_bf16_f32 v45, v74, v75
v_mfma_f32_16x16x32_bf16 v[180:183], a[208:211], v[48:51], v[200:203]
v_exp_f32_e64 v72, v172
v_mfma_f32_16x16x32_bf16 v[176:179], a[196:199], v[80:83], v[176:179]
)"
R"(ds_read_b128 a[248:251], %[vKrd] offset:46080
ds_read_b128 a[236:239], %[vKrd] offset:42496
v_mfma_f32_16x16x32_bf16 v[180:183], a[212:215], v[80:83], v[180:183]
v_exp_f32_e64 v73, v173
v_mfma_f32_16x16x32_bf16 v[176:179], a[200:203], v[112:115], v[176:179]
ds_read_b128 a[252:255], %[vKrd] offset:46592
v_cvt_pk_bf16_f32 v46, v72, v73
v_mfma_f32_16x16x32_bf16 v[180:183], a[216:219], v[112:115], v[180:183]
v_exp_f32_e64 v74, v174
v_mfma_f32_16x16x32_bf16 v[176:179], a[204:207], v[144:147], v[176:179]
ds_read_b64_tr_b16 a[160:161], %[vVr] offset:40960
ds_read_b64_tr_b16 a[162:163], %[vVr] offset:45056
v_mfma_f32_16x16x32_bf16 v[180:183], a[220:223], v[144:147], v[180:183]
v_exp_f32_e64 v75, v175
v_mfma_f32_16x16x32_bf16 v[184:187], a[192:195], v[52:55], v[204:207]
ds_read_b64_tr_b16 a[164:165], v140 offset:40960
v_cvt_pk_bf16_f32 v47, v74, v75
v_mfma_f32_16x16x32_bf16 v[188:191], a[208:211], v[52:55], v[204:207]
ds_read_b64_tr_b16 a[166:167], v140 offset:45056
v_mfma_f32_16x16x32_bf16 v[184:187], a[196:199], v[84:87], v[184:187]
v_exp_f32_e64 v72, v176
v_mfma_f32_16x16x32_bf16 v[188:191], a[212:215], v[84:87], v[188:191]
ds_read_b64_tr_b16 a[168:169], %[vVr] offset:41472
ds_read_b64_tr_b16 a[170:171], %[vVr] offset:45568
v_mfma_f32_16x16x32_bf16 v[184:187], a[200:203], v[116:119], v[184:187]
v_exp_f32_e64 v73, v177
v_smfmac_f32_16x16x64_bf16 v[216:219], v[212:215], v[40:47], v231
ds_read_b64_tr_b16 a[172:173], v140 offset:41472
v_cvt_pk_bf16_f32 v56, v72, v73
v_mfma_f32_16x16x32_bf16 v[188:191], a[216:219], v[116:119], v[188:191]
v_exp_f32 v74, v178
v_mfma_f32_16x16x32_bf16 v[184:187], a[204:207], v[148:151], v[184:187]
s_waitcnt lgkmcnt(11)
ds_read_b64_tr_b16 a[174:175], v140 offset:45568
v_mfma_f32_16x16x32_bf16 v[188:191], a[220:223], v[148:151], v[188:191]
v_exp_f32_e64 v75, v179
v_mfma_f32_16x16x32_bf16 a[0:3], a[128:131], v[40:43], a[0:3]
v_cvt_pk_bf16_f32 v57, v74, v75
v_lshlrev_b32_e64 v108, 3, %[vX]
v_mfma_f32_16x16x32_bf16 a[32:35], a[128:131], v[44:47], a[32:35]
v_exp_f32_e64 v72, v180
v_mfma_f32_16x16x32_bf16 a[4:7], a[132:135], v[40:43], a[4:7]
v_mfma_f32_16x16x32_bf16 a[36:39], a[132:135], v[44:47], a[36:39]
v_exp_f32_e64 v73, v181
v_mfma_f32_16x16x32_bf16 a[8:11], a[136:139], v[40:43], a[8:11]
v_cvt_pk_bf16_f32 v58, v72, v73
v_mfma_f32_16x16x32_bf16 a[40:43], a[136:139], v[44:47], a[40:43]
v_exp_f32_e64 v74, v182
v_mfma_f32_16x16x32_bf16 a[12:15], a[140:143], v[40:43], a[12:15]
buffer_load_dword v105, v108, %[sWS], s41 offen offset:0 sc1
v_mfma_f32_16x16x32_bf16 a[44:47], a[140:143], v[44:47], a[44:47]
v_exp_f32_e64 v75, v183
v_mfma_f32_16x16x32_bf16 a[16:19], a[144:147], v[40:43], a[16:19]
v_cvt_pk_bf16_f32 v59, v74, v75
v_mfma_f32_16x16x32_bf16 a[48:51], a[144:147], v[44:47], a[48:51]
v_exp_f32_e64 v72, v184
v_mfma_f32_16x16x32_bf16 a[20:23], a[148:151], v[40:43], a[20:23]
buffer_load_dword v105, v108, %[sWS], s41 offen offset:1024 sc1
v_mfma_f32_16x16x32_bf16 a[52:55], a[148:151], v[44:47], a[52:55]
v_exp_f32_e64 v73, v185
v_mfma_f32_16x16x32_bf16 a[24:27], a[152:155], v[40:43], a[24:27]
v_cvt_pk_bf16_f32 v60, v72, v73
v_mfma_f32_16x16x32_bf16 a[56:59], a[152:155], v[44:47], a[56:59]
v_exp_f32_e64 v74, v186
v_mfma_f32_16x16x32_bf16 a[28:31], a[156:159], v[40:43], a[28:31]
s_waitcnt vmcnt(3) lgkmcnt(8)
s_barrier
v_mfma_f32_16x16x32_bf16 a[60:63], a[156:159], v[44:47], a[60:63]
v_exp_f32_e64 v75, v187
v_mfma_f32_16x16x32_bf16 v[160:163], a[224:227], v[32:35], v[192:195]
ds_read_b128 a[192:195], %[vKrd] offset:49152
v_cvt_pk_bf16_f32 v61, v74, v75
v_mfma_f32_16x16x32_bf16 v[164:167], a[240:243], v[32:35], v[192:195]
v_exp_f32_e64 v72, v188
v_mfma_f32_16x16x32_bf16 v[160:163], a[228:231], v[64:67], v[160:163]
ds_read_b128 a[208:211], %[vKrd] offset:53248
ds_read_b128 a[196:199], %[vKrd] offset:49664
v_mfma_f32_16x16x32_bf16 v[164:167], a[244:247], v[64:67], v[164:167]
v_exp_f32_e64 v73, v189
v_mfma_f32_16x16x32_bf16 v[160:163], a[232:235], v[96:99], v[160:163]
ds_read_b128 a[212:215], %[vKrd] offset:53760
v_cvt_pk_bf16_f32 v62, v72, v73
v_mfma_f32_16x16x32_bf16 v[164:167], a[248:251], v[96:99], v[164:167]
v_exp_f32_e64 v74, v190
v_mfma_f32_16x16x32_bf16 v[160:163], a[236:239], v[128:131], v[160:163]
ds_read_b64_tr_b16 a[176:177], %[vVr] offset:41984
ds_read_b64_tr_b16 a[178:179], %[vVr] offset:46080
v_mfma_f32_16x16x32_bf16 v[164:167], a[252:255], v[128:131], v[164:167]
v_exp_f32_e64 v75, v191
v_mfma_f32_16x16x32_bf16 v[168:171], a[224:227], v[36:39], v[196:199]
ds_read_b64_tr_b16 a[180:181], v140 offset:41984
v_cvt_pk_bf16_f32 v63, v74, v75
v_mfma_f32_16x16x32_bf16 v[172:175], a[240:243], v[36:39], v[196:199]
ds_read_b64_tr_b16 a[182:183], v140 offset:46080
v_mfma_f32_16x16x32_bf16 v[168:171], a[228:231], v[68:71], v[168:171]
v_exp_f32_e64 v72, v160
v_mfma_f32_16x16x32_bf16 v[172:175], a[244:247], v[68:71], v[172:175]
ds_read_b64_tr_b16 a[184:185], %[vVr] offset:42496
ds_read_b64_tr_b16 a[186:187], %[vVr] offset:46592
v_mfma_f32_16x16x32_bf16 v[168:171], a[232:235], v[100:103], v[168:171]
v_exp_f32_e64 v73, v161
v_smfmac_f32_16x16x64_bf16 v[220:223], v[212:215], v[56:63], v231
ds_read_b64_tr_b16 a[188:189], v140 offset:42496
v_cvt_pk_bf16_f32 v40, v72, v73
v_mfma_f32_16x16x32_bf16 v[172:175], a[248:251], v[100:103], v[172:175]
v_exp_f32_e64 v74, v162
v_mfma_f32_16x16x32_bf16 v[168:171], a[236:239], v[132:135], v[168:171]
ds_read_b64_tr_b16 a[190:191], v140 offset:46592
v_mfma_f32_16x16x32_bf16 v[172:175], a[252:255], v[132:135], v[172:175]
v_exp_f32_e64 v75, v163
v_mfma_f32_16x16x32_bf16 a[64:67], a[128:131], v[56:59], a[64:67]
v_cvt_pk_bf16_f32 v41, v74, v75
v_mfma_f32_16x16x32_bf16 a[96:99], a[128:131], v[60:63], a[96:99]
v_exp_f32_e64 v72, v164
v_mfma_f32_16x16x32_bf16 a[68:71], a[132:135], v[56:59], a[68:71]
v_mfma_f32_16x16x32_bf16 a[100:103], a[132:135], v[60:63], a[100:103]
v_exp_f32 v73, v165
v_mfma_f32_16x16x32_bf16 a[72:75], a[136:139], v[56:59], a[72:75]
s_add_u32 m0, s42, 0
v_cvt_pk_bf16_f32 v42, v72, v73
v_mfma_f32_16x16x32_bf16 a[104:107], a[136:139], v[60:63], a[104:107]
v_exp_f32_e64 v74, v166
v_mfma_f32_16x16x32_bf16 a[76:79], a[140:143], v[56:59], a[76:79]
buffer_load_dwordx4 %[vX], %[sWS], s60 offen offset:0 sc1 lds
v_mfma_f32_16x16x32_bf16 a[108:111], a[140:143], v[60:63], a[108:111]
v_exp_f32_e64 v75, v167
v_mfma_f32_16x16x32_bf16 a[80:83], a[144:147], v[56:59], a[80:83]
v_cvt_pk_bf16_f32 v43, v74, v75
v_mfma_f32_16x16x32_bf16 a[112:115], a[144:147], v[60:63], a[112:115]
v_exp_f32_e64 v72, v168
v_mfma_f32_16x16x32_bf16 a[84:87], a[148:151], v[56:59], a[84:87]
buffer_load_dwordx4 %[vX], %[sWS], s60 offen offset:1024 sc1 lds
v_mfma_f32_16x16x32_bf16 a[116:119], a[148:151], v[60:63], a[116:119]
v_exp_f32_e64 v73, v169
v_mfma_f32_16x16x32_bf16 a[88:91], a[152:155], v[56:59], a[88:91]
v_cvt_pk_bf16_f32 v44, v72, v73
v_mfma_f32_16x16x32_bf16 a[120:123], a[152:155], v[60:63], a[120:123]
v_exp_f32_e64 v74, v170
v_mfma_f32_16x16x32_bf16 a[92:95], a[156:159], v[56:59], a[92:95]
v_mfma_f32_16x16x32_bf16 a[124:127], a[156:159], v[60:63], a[124:127]
v_exp_f32_e64 v75, v171
v_mfma_f32_16x16x32_bf16 v[176:179], a[224:227], v[48:51], v[200:203]
ds_read_b128 a[200:203], %[vKrd] offset:50176
v_cvt_pk_bf16_f32 v45, v74, v75
v_mfma_f32_16x16x32_bf16 v[180:183], a[240:243], v[48:51], v[200:203]
v_exp_f32_e64 v72, v172
v_mfma_f32_16x16x32_bf16 v[176:179], a[228:231], v[80:83], v[176:179]
ds_read_b128 a[216:219], %[vKrd] offset:54272
ds_read_b128 a[204:207], %[vKrd] offset:50688
v_mfma_f32_16x16x32_bf16 v[180:183], a[244:247], v[80:83], v[180:183]
v_exp_f32_e64 v73, v173
v_mfma_f32_16x16x32_bf16 v[176:179], a[232:235], v[112:115], v[176:179]
ds_read_b128 a[220:223], %[vKrd] offset:54784
v_cvt_pk_bf16_f32 v46, v72, v73
v_mfma_f32_16x16x32_bf16 v[180:183], a[248:251], v[112:115], v[180:183]
v_exp_f32_e64 v74, v174
v_mfma_f32_16x16x32_bf16 v[176:179], a[236:239], v[144:147], v[176:179]
ds_read_b64_tr_b16 a[128:129], %[vVr] offset:49152
ds_read_b64_tr_b16 a[130:131], %[vVr] offset:53248
v_mfma_f32_16x16x32_bf16 v[180:183], a[252:255], v[144:147], v[180:183]
v_exp_f32_e64 v75, v175
v_mfma_f32_16x16x32_bf16 v[184:187], a[224:227], v[52:55], v[204:207]
ds_read_b64_tr_b16 a[132:133], v140 offset:49152
v_cvt_pk_bf16_f32 v47, v74, v75
v_mfma_f32_16x16x32_bf16 v[188:191], a[240:243], v[52:55], v[204:207]
ds_read_b64_tr_b16 a[134:135], v140 offset:53248
v_mfma_f32_16x16x32_bf16 v[184:187], a[228:231], v[84:87], v[184:187]
v_exp_f32_e64 v72, v176
v_mfma_f32_16x16x32_bf16 v[188:191], a[244:247], v[84:87], v[188:191]
ds_read_b64_tr_b16 a[136:137], %[vVr] offset:49664
ds_read_b64_tr_b16 a[138:139], %[vVr] offset:53760
v_mfma_f32_16x16x32_bf16 v[184:187], a[232:235], v[116:119], v[184:187]
v_exp_f32_e64 v73, v177
v_smfmac_f32_16x16x64_bf16 v[216:219], v[212:215], v[40:47], v231
ds_read_b64_tr_b16 a[140:141], v140 offset:49664
v_cvt_pk_bf16_f32 v56, v72, v73
v_mfma_f32_16x16x32_bf16 v[188:191], a[248:251], v[116:119], v[188:191]
v_exp_f32 v74, v178
v_mfma_f32_16x16x32_bf16 v[184:187], a[236:239], v[148:151], v[184:187]
s_waitcnt lgkmcnt(11)
ds_read_b64_tr_b16 a[142:143], v140 offset:53760
v_mfma_f32_16x16x32_bf16 v[188:191], a[252:255], v[148:151], v[188:191]
v_exp_f32_e64 v75, v179
v_mfma_f32_16x16x32_bf16 a[0:3], a[160:163], v[40:43], a[0:3]
ds_read_b128 a[224:227], %[vKrd] offset:57344
v_cvt_pk_bf16_f32 v57, v74, v75
v_mfma_f32_16x16x32_bf16 a[32:35], a[160:163], v[44:47], a[32:35]
v_exp_f32_e64 v72, v180
v_mfma_f32_16x16x32_bf16 a[4:7], a[164:167], v[40:43], a[4:7]
v_mfma_f32_16x16x32_bf16 a[36:39], a[164:167], v[44:47], a[36:39]
v_exp_f32_e64 v73, v181
v_mfma_f32_16x16x32_bf16 a[8:11], a[168:171], v[40:43], a[8:11]
ds_read_b128 a[240:243], %[vKrd] offset:61440
v_cvt_pk_bf16_f32 v58, v72, v73
v_mfma_f32_16x16x32_bf16 a[40:43], a[168:171], v[44:47], a[40:43]
v_exp_f32_e64 v74, v182
v_mfma_f32_16x16x32_bf16 a[12:15], a[172:175], v[40:43], a[12:15]
buffer_load_dwordx4 %[vX], %[sWS], s60 offen offset:2048 sc1 lds
v_mfma_f32_16x16x32_bf16 a[44:47], a[172:175], v[44:47], a[44:47]
v_exp_f32_e64 v75, v183
)"
R"(v_mfma_f32_16x16x32_bf16 a[16:19], a[176:179], v[40:43], a[16:19]
ds_read_b128 a[228:231], %[vKrd] offset:57856
v_cvt_pk_bf16_f32 v59, v74, v75
v_mfma_f32_16x16x32_bf16 a[48:51], a[176:179], v[44:47], a[48:51]
v_exp_f32_e64 v72, v184
v_mfma_f32_16x16x32_bf16 a[20:23], a[180:183], v[40:43], a[20:23]
buffer_load_dwordx4 %[vX], %[sWS], s60 offen offset:3072 sc1 lds
v_mfma_f32_16x16x32_bf16 a[52:55], a[180:183], v[44:47], a[52:55]
v_exp_f32_e64 v73, v185
v_mfma_f32_16x16x32_bf16 a[24:27], a[184:187], v[40:43], a[24:27]
ds_read_b128 a[244:247], %[vKrd] offset:61952
v_cvt_pk_bf16_f32 v60, v72, v73
v_mfma_f32_16x16x32_bf16 a[56:59], a[184:187], v[44:47], a[56:59]
v_exp_f32_e64 v74, v186
v_mfma_f32_16x16x32_bf16 a[28:31], a[188:191], v[40:43], a[28:31]
v_mfma_f32_16x16x32_bf16 a[60:63], a[188:191], v[44:47], a[60:63]
v_exp_f32 v75, v187
s_waitcnt lgkmcnt(0)
v_mfma_f32_16x16x32_bf16 v[160:163], a[192:195], v[32:35], v[192:195]
ds_read_b128 a[232:235], %[vKrd] offset:58368
v_cvt_pk_bf16_f32 v61, v74, v75
v_mfma_f32_16x16x32_bf16 v[164:167], a[208:211], v[32:35], v[192:195]
v_exp_f32_e64 v72, v188
v_mfma_f32_16x16x32_bf16 v[160:163], a[196:199], v[64:67], v[160:163]
ds_read_b128 a[248:251], %[vKrd] offset:62464
ds_read_b128 a[236:239], %[vKrd] offset:58880
v_mfma_f32_16x16x32_bf16 v[164:167], a[212:215], v[64:67], v[164:167]
v_exp_f32_e64 v73, v189
v_mfma_f32_16x16x32_bf16 v[160:163], a[200:203], v[96:99], v[160:163]
ds_read_b128 a[252:255], %[vKrd] offset:62976
v_cvt_pk_bf16_f32 v62, v72, v73
v_mfma_f32_16x16x32_bf16 v[164:167], a[216:219], v[96:99], v[164:167]
v_exp_f32_e64 v74, v190
v_mfma_f32_16x16x32_bf16 v[160:163], a[204:207], v[128:131], v[160:163]
ds_read_b64_tr_b16 a[144:145], %[vVr] offset:50176
ds_read_b64_tr_b16 a[146:147], %[vVr] offset:54272
v_mfma_f32_16x16x32_bf16 v[164:167], a[220:223], v[128:131], v[164:167]
v_exp_f32_e64 v75, v191
v_mfma_f32_16x16x32_bf16 v[168:171], a[224:227], v[32:35], v[192:195]
ds_read_b64_tr_b16 a[148:149], v140 offset:50176
v_cvt_pk_bf16_f32 v63, v74, v75
v_mfma_f32_16x16x32_bf16 v[172:175], a[240:243], v[32:35], v[192:195]
ds_read_b64_tr_b16 a[150:151], v140 offset:54272
v_smfmac_f32_16x16x64_bf16 v[220:223], v[212:215], v[56:63], v231
v_exp_f32_e64 v72, v160
v_mfma_f32_16x16x32_bf16 v[168:171], a[228:231], v[64:67], v[168:171]
ds_read_b64_tr_b16 a[152:153], %[vVr] offset:50688
ds_read_b64_tr_b16 a[154:155], %[vVr] offset:54784
v_mfma_f32_16x16x32_bf16 v[172:175], a[244:247], v[64:67], v[172:175]
s_waitcnt lgkmcnt(6)
v_exp_f32 v73, v161
v_mfma_f32_16x16x32_bf16 v[168:171], a[232:235], v[96:99], v[168:171]
ds_read_b64_tr_b16 a[156:157], v140 offset:50688
v_cvt_pk_bf16_f32 v40, v72, v73
v_mfma_f32_16x16x32_bf16 v[172:175], a[248:251], v[96:99], v[172:175]
v_exp_f32_e64 v74, v162
v_mfma_f32_16x16x32_bf16 v[168:171], a[236:239], v[128:131], v[168:171]
ds_read_b64_tr_b16 a[158:159], v140 offset:54784
v_add_f32_e64 v120, v216, v217
v_mfma_f32_16x16x32_bf16 v[172:175], a[252:255], v[128:131], v[172:175]
v_exp_f32_e64 v75, v163
v_mfma_f32_16x16x32_bf16 a[64:67], a[160:163], v[56:59], a[64:67]
v_cvt_pk_bf16_f32 v41, v74, v75
v_mfma_f32_16x16x32_bf16 a[96:99], a[160:163], v[60:63], a[96:99]
v_exp_f32_e64 v72, v164
v_mfma_f32_16x16x32_bf16 a[68:71], a[164:167], v[56:59], a[68:71]
global_load_dword v105, v104, s[98:99]
v_add_f32_e64 v124, v218, v219
v_mfma_f32_16x16x32_bf16 a[100:103], a[164:167], v[60:63], a[100:103]
v_exp_f32_e64 v73, v165
v_mfma_f32_16x16x32_bf16 a[72:75], a[168:171], v[56:59], a[72:75]
s_add_u32 m0, s42, 4096
s_add_u32 s60, s60, 4096
ds_read_b64_tr_b16 a[160:161], %[vVr] offset:57344
v_cvt_pk_bf16_f32 v42, v72, v73
v_mfma_f32_16x16x32_bf16 a[104:107], a[168:171], v[60:63], a[104:107]
v_exp_f32_e64 v74, v166
v_mfma_f32_16x16x32_bf16 a[76:79], a[172:175], v[56:59], a[76:79]
buffer_load_dwordx4 %[vX], %[sWS], s60 offen offset:0 sc1 lds
v_mfma_f32_16x16x32_bf16 a[108:111], a[172:175], v[60:63], a[108:111]
v_exp_f32_e64 v75, v167
v_mfma_f32_16x16x32_bf16 a[80:83], a[176:179], v[56:59], a[80:83]
ds_read_b64_tr_b16 a[162:163], %[vVr] offset:61440
v_cvt_pk_bf16_f32 v43, v74, v75
v_mfma_f32_16x16x32_bf16 a[112:115], a[176:179], v[60:63], a[112:115]
v_exp_f32_e64 v72, v168
v_mfma_f32_16x16x32_bf16 a[84:87], a[180:183], v[56:59], a[84:87]
buffer_load_dwordx4 %[vX], %[sWS], s60 offen offset:1024 sc1 lds
v_mfma_f32_16x16x32_bf16 a[116:119], a[180:183], v[60:63], a[116:119]
v_exp_f32_e64 v73, v169
v_mfma_f32_16x16x32_bf16 a[88:91], a[184:187], v[56:59], a[88:91]
ds_read_b64_tr_b16 a[164:165], v140 offset:57344
v_cvt_pk_bf16_f32 v44, v72, v73
v_mfma_f32_16x16x32_bf16 a[120:123], a[184:187], v[60:63], a[120:123]
v_exp_f32_e64 v74, v170
v_mfma_f32_16x16x32_bf16 a[92:95], a[188:191], v[56:59], a[92:95]
ds_read_b64_tr_b16 a[166:167], v140 offset:61440
v_mfma_f32_16x16x32_bf16 a[124:127], a[188:191], v[60:63], a[124:127]
v_exp_f32_e64 v75, v171
v_mfma_f32_16x16x32_bf16 v[176:179], a[192:195], v[36:39], v[196:199]
ds_read_b64_tr_b16 a[168:169], %[vVr] offset:57856
v_cvt_pk_bf16_f32 v45, v74, v75
v_mfma_f32_16x16x32_bf16 v[180:183], a[208:211], v[36:39], v[196:199]
v_exp_f32_e64 v72, v172
v_mfma_f32_16x16x32_bf16 v[176:179], a[196:199], v[68:71], v[176:179]
ds_read_b64_tr_b16 a[170:171], %[vVr] offset:61952
ds_read_b64_tr_b16 a[172:173], v140 offset:57856
v_mfma_f32_16x16x32_bf16 v[180:183], a[212:215], v[68:71], v[180:183]
v_exp_f32_e64 v73, v173
v_mfma_f32_16x16x32_bf16 v[176:179], a[200:203], v[100:103], v[176:179]
ds_read_b64_tr_b16 a[174:175], v140 offset:61952
v_cvt_pk_bf16_f32 v46, v72, v73
v_mfma_f32_16x16x32_bf16 v[180:183], a[216:219], v[100:103], v[180:183]
v_exp_f32_e64 v74, v174
v_mfma_f32_16x16x32_bf16 v[176:179], a[204:207], v[132:135], v[176:179]
ds_read_b64_tr_b16 a[176:177], %[vVr] offset:58368
ds_read_b64_tr_b16 a[178:179], %[vVr] offset:62464
v_mfma_f32_16x16x32_bf16 v[180:183], a[220:223], v[132:135], v[180:183]
v_exp_f32_e64 v75, v175
v_mfma_f32_16x16x32_bf16 v[184:187], a[224:227], v[36:39], v[196:199]
ds_read_b64_tr_b16 a[180:181], v140 offset:58368
v_cvt_pk_bf16_f32 v47, v74, v75
v_mfma_f32_16x16x32_bf16 v[188:191], a[240:243], v[36:39], v[196:199]
ds_read_b64_tr_b16 a[182:183], v140 offset:62464
v_mfma_f32_16x16x32_bf16 v[120:123], v[136:139], v[40:43], v[120:123]
ds_read_b64_tr_b16 a[184:185], %[vVr] offset:58880
ds_read_b64_tr_b16 a[186:187], %[vVr] offset:62976
v_mfma_f32_16x16x32_bf16 v[120:123], v[136:139], v[44:47], v[120:123]
v_exp_f32_e64 v72, v176
v_mfma_f32_16x16x32_bf16 v[184:187], a[228:231], v[68:71], v[184:187]
ds_read_b64_tr_b16 a[188:189], v140 offset:58880
ds_read_b64_tr_b16 a[190:191], v140 offset:62976
v_mfma_f32_16x16x32_bf16 v[188:191], a[244:247], v[68:71], v[188:191]
v_exp_f32_e64 v73, v177
v_mfma_f32_16x16x32_bf16 v[184:187], a[232:235], v[100:103], v[184:187]
v_cvt_pk_bf16_f32 v56, v72, v73
v_mfma_f32_16x16x32_bf16 v[188:191], a[248:251], v[100:103], v[188:191]
v_exp_f32_e64 v74, v178
v_mfma_f32_16x16x32_bf16 v[184:187], a[236:239], v[132:135], v[184:187]
s_waitcnt lgkmcnt(15)
s_waitcnt vmcnt(9)
v_add_f32_e64 v92, v220, v221
v_mfma_f32_16x16x32_bf16 v[188:191], a[252:255], v[132:135], v[188:191]
v_exp_f32 v75, v179
v_mfma_f32_16x16x32_bf16 a[0:3], a[128:131], v[40:43], a[0:3]
s_xor_b32 s72, s43, s71
v_cvt_pk_bf16_f32 v57, v74, v75
v_mfma_f32_16x16x32_bf16 a[4:7], a[132:135], v[40:43], a[4:7]
v_exp_f32_e64 v72, v180
v_mfma_f32_16x16x32_bf16 a[8:11], a[136:139], v[40:43], a[8:11]
buffer_store_dwordx4 v[240:243], %[vX], %[sWS], s53 offen offset:0 sc1
v_add_f32_e64 v76, v222, v223
v_mfma_f32_16x16x32_bf16 a[12:15], a[140:143], v[40:43], a[12:15]
v_exp_f32_e64 v73, v181
v_mfma_f32_16x16x32_bf16 a[16:19], a[144:147], v[40:43], a[16:19]
s_add_u32 s73, s72, 4096
v_cvt_pk_bf16_f32 v58, v72, v73
v_mfma_f32_16x16x32_bf16 a[20:23], a[148:151], v[40:43], a[20:23]
v_exp_f32_e64 v74, v182
v_mfma_f32_16x16x32_bf16 a[24:27], a[152:155], v[40:43], a[24:27]
buffer_load_dwordx4 %[vX], %[sWS], s60 offen offset:2048 sc1 lds
v_mfma_f32_16x16x32_bf16 a[28:31], a[156:159], v[40:43], a[28:31]
s_waitcnt lgkmcnt(0)
v_exp_f32 v75, v183
v_mfma_f32_16x16x32_bf16 a[0:3], a[160:163], v[44:47], a[0:3]
s_add_u32 s74, s72, 16384
s_add_u32 s53, s53, 4194304
v_cvt_pk_bf16_f32 v59, v74, v75
v_mfma_f32_16x16x32_bf16 a[4:7], a[164:167], v[44:47], a[4:7]
v_exp_f32_e64 v72, v184
v_mfma_f32_16x16x32_bf16 a[8:11], a[168:171], v[44:47], a[8:11]
buffer_load_dwordx4 %[vX], %[sWS], s60 offen offset:3072 sc1 lds
v_mfma_f32_16x16x32_bf16 a[12:15], a[172:175], v[44:47], a[12:15]
v_exp_f32_e64 v73, v185
v_mfma_f32_16x16x32_bf16 a[16:19], a[176:179], v[44:47], a[16:19]
s_add_u32 s75, s72, 20480
v_cvt_pk_bf16_f32 v60, v72, v73
v_mfma_f32_16x16x32_bf16 a[20:23], a[180:183], v[44:47], a[20:23]
v_exp_f32_e64 v74, v186
v_mfma_f32_16x16x32_bf16 a[24:27], a[184:187], v[44:47], a[24:27]
v_mfma_f32_16x16x32_bf16 a[28:31], a[188:191], v[44:47], a[28:31]
v_exp_f32_e64 v75, v187
v_mfma_f32_16x16x32_bf16 v[160:163], a[192:195], v[48:51], v[200:203]
v_cvt_pk_bf16_f32 v61, v74, v75
v_rcp_f32_e64 v192, v120
v_mul_f32 v120, 0x76800000, v120
v_mfma_f32_16x16x32_bf16 v[164:167], a[208:211], v[48:51], v[200:203]
v_exp_f32_e64 v72, v188
v_accvgpr_read_b32 v208, a0
v_accvgpr_read_b32 v209, a1
v_mfma_f32_16x16x32_bf16 v[160:163], a[196:199], v[80:83], v[160:163]
v_accvgpr_read_b32 v210, a2
v_accvgpr_read_b32 v211, a3
v_accvgpr_read_b32 v212, a4
v_accvgpr_read_b32 v213, a5
v_mfma_f32_16x16x32_bf16 v[164:167], a[212:215], v[80:83], v[164:167]
v_exp_f32_e64 v73, v189
v_accvgpr_read_b32 v214, a6
v_accvgpr_read_b32 v215, a7
v_mfma_f32_16x16x32_bf16 v[160:163], a[200:203], v[112:115], v[160:163]
)"
R"(v_cvt_pk_bf16_f32 v62, v72, v73
v_accvgpr_read_b32 v216, a8
v_accvgpr_read_b32 v217, a9
v_accvgpr_read_b32 v218, a10
v_mfma_f32_16x16x32_bf16 v[164:167], a[216:219], v[112:115], v[164:167]
v_exp_f32 v74, v190
v_accvgpr_read_b32 v219, a11
v_accvgpr_read_b32 v220, a12
v_mfma_f32_16x16x32_bf16 v[160:163], a[204:207], v[144:147], v[160:163]
v_accvgpr_read_b32 v221, a13
v_accvgpr_read_b32 v222, a14
v_accvgpr_read_b32 v223, a15
LPc_1:
s_cbranch_execz LPc_5
v_pk_mul_f32 v[208:209], v[208:209], v[192:193] op_sel_hi:[1,0]
v_pk_mul_f32 v[210:211], v[210:211], v[192:193] op_sel_hi:[1,0]
v_pk_mul_f32 v[212:213], v[212:213], v[192:193] op_sel_hi:[1,0]
v_pk_mul_f32 v[214:215], v[214:215], v[192:193] op_sel_hi:[1,0]
v_pk_mul_f32 v[216:217], v[216:217], v[192:193] op_sel_hi:[1,0]
v_pk_mul_f32 v[218:219], v[218:219], v[192:193] op_sel_hi:[1,0]
v_pk_mul_f32 v[220:221], v[220:221], v[192:193] op_sel_hi:[1,0]
v_pk_mul_f32 v[222:223], v[222:223], v[192:193] op_sel_hi:[1,0]
v_pk_mul_f32 v[152:153], v[208:209], v[208:209]
v_pk_fma_f32 v[152:153], v[210:211], v[210:211], v[152:153]
v_pk_fma_f32 v[152:153], v[212:213], v[212:213], v[152:153]
v_pk_fma_f32 v[152:153], v[214:215], v[214:215], v[152:153]
v_pk_fma_f32 v[152:153], v[216:217], v[216:217], v[152:153]
v_pk_fma_f32 v[152:153], v[218:219], v[218:219], v[152:153]
v_pk_fma_f32 v[152:153], v[220:221], v[220:221], v[152:153]
v_pk_fma_f32 v[152:153], v[222:223], v[222:223], v[152:153]
v_mfma_f32_16x16x32_bf16 v[164:167], a[220:223], v[144:147], v[164:167]
v_exp_f32_e64 v75, v191
v_cvt_pk_bf16_f32 v208, v208, v209
v_cvt_pk_bf16_f32 v209, v210, v211
v_mfma_f32_16x16x32_bf16 v[168:171], a[224:227], v[48:51], v[200:203]
v_cvt_pk_bf16_f32 v63, v74, v75
v_cvt_pk_bf16_f32 v210, v212, v213
v_cvt_pk_bf16_f32 v211, v214, v215
v_mfma_f32_16x16x32_bf16 v[172:175], a[240:243], v[48:51], v[200:203]
buffer_store_dwordx4 v[208:211], %[vX], %[sWS], s72 offen offset:0 sc1
ds_write_b128 v107, v[208:211] offset:0
v_cvt_pk_bf16_f32 v216, v216, v217
v_mfma_f32_16x16x32_bf16 v[124:127], v[136:139], v[56:59], v[124:127]
v_cvt_pk_bf16_f32 v217, v218, v219
v_cvt_pk_bf16_f32 v218, v220, v221
v_cvt_pk_bf16_f32 v219, v222, v223
v_mfma_f32_16x16x32_bf16 v[124:127], v[136:139], v[60:63], v[124:127]
v_exp_f32_e64 v72, v160
buffer_store_dwordx4 v[216:219], %[vX], %[sWS], s73 offen offset:0 sc1
v_mfma_f32_16x16x32_bf16 v[168:171], a[228:231], v[80:83], v[168:171]
ds_write_b128 v107, v[216:219] offset:4096
v_accvgpr_read_b32 v224, a16
v_accvgpr_read_b32 v225, a17
v_accvgpr_read_b32 v226, a18
v_mfma_f32_16x16x32_bf16 v[172:175], a[244:247], v[80:83], v[172:175]
v_exp_f32_e64 v73, v161
v_accvgpr_read_b32 v227, a19
v_accvgpr_read_b32 v228, a20
v_mfma_f32_16x16x32_bf16 v[168:171], a[232:235], v[112:115], v[168:171]
v_cvt_pk_bf16_f32 v40, v72, v73
v_accvgpr_read_b32 v229, a21
v_accvgpr_read_b32 v230, a22
v_accvgpr_read_b32 v231, a23
v_mfma_f32_16x16x32_bf16 v[172:175], a[248:251], v[112:115], v[172:175]
v_exp_f32_e64 v74, v162
v_accvgpr_read_b32 v232, a24
v_accvgpr_read_b32 v233, a25
v_mfma_f32_16x16x32_bf16 v[168:171], a[236:239], v[144:147], v[168:171]
v_accvgpr_read_b32 v234, a26
v_accvgpr_read_b32 v235, a27
v_accvgpr_read_b32 v236, a28
v_accvgpr_read_b32 v237, a29
v_mfma_f32_16x16x32_bf16 v[172:175], a[252:255], v[144:147], v[172:175]
v_exp_f32_e64 v75, v163
v_accvgpr_read_b32 v238, a30
v_accvgpr_read_b32 v239, a31
v_pk_mul_f32 v[224:225], v[224:225], v[192:193] op_sel_hi:[1,0]
v_pk_mul_f32 v[226:227], v[226:227], v[192:193] op_sel_hi:[1,0]
v_pk_mul_f32 v[228:229], v[228:229], v[192:193] op_sel_hi:[1,0]
v_pk_mul_f32 v[230:231], v[230:231], v[192:193] op_sel_hi:[1,0]
v_pk_mul_f32 v[232:233], v[232:233], v[192:193] op_sel_hi:[1,0]
v_pk_mul_f32 v[234:235], v[234:235], v[192:193] op_sel_hi:[1,0]
v_pk_mul_f32 v[236:237], v[236:237], v[192:193] op_sel_hi:[1,0]
v_pk_mul_f32 v[238:239], v[238:239], v[192:193] op_sel_hi:[1,0]
v_pk_fma_f32 v[152:153], v[224:225], v[224:225], v[152:153]
v_pk_fma_f32 v[152:153], v[226:227], v[226:227], v[152:153]
v_pk_fma_f32 v[152:153], v[228:229], v[228:229], v[152:153]
v_pk_fma_f32 v[152:153], v[230:231], v[230:231], v[152:153]
v_pk_fma_f32 v[152:153], v[232:233], v[232:233], v[152:153]
v_pk_fma_f32 v[152:153], v[234:235], v[234:235], v[152:153]
v_pk_fma_f32 v[152:153], v[236:237], v[236:237], v[152:153]
v_pk_fma_f32 v[152:153], v[238:239], v[238:239], v[152:153]
v_mfma_f32_16x16x32_bf16 a[32:35], a[128:131], v[56:59], a[32:35]
v_cvt_pk_bf16_f32 v41, v74, v75
v_cvt_pk_bf16_f32 v224, v224, v225
v_cvt_pk_bf16_f32 v225, v226, v227
v_cvt_pk_bf16_f32 v226, v228, v229
v_mfma_f32_16x16x32_bf16 a[36:39], a[132:135], v[56:59], a[36:39]
v_exp_f32_e64 v72, v164
v_cvt_pk_bf16_f32 v227, v230, v231
v_mfma_f32_16x16x32_bf16 a[40:43], a[136:139], v[56:59], a[40:43]
buffer_store_dwordx4 v[224:227], %[vX], %[sWS], s74 offen offset:0 sc1
ds_write_b128 v107, v[224:227] offset:8192
v_cvt_pk_bf16_f32 v232, v232, v233
v_mfma_f32_16x16x32_bf16 a[44:47], a[140:143], v[56:59], a[44:47]
v_exp_f32_e64 v73, v165
v_cvt_pk_bf16_f32 v233, v234, v235
v_cvt_pk_bf16_f32 v234, v236, v237
v_mfma_f32_16x16x32_bf16 a[48:51], a[144:147], v[56:59], a[48:51]
s_add_u32 m0, s42, 8192
s_add_u32 s60, s60, 4096
v_cvt_pk_bf16_f32 v42, v72, v73
v_cvt_pk_bf16_f32 v235, v238, v239
v_mfma_f32_16x16x32_bf16 a[52:55], a[148:151], v[56:59], a[52:55]
v_exp_f32_e64 v74, v166
buffer_store_dwordx4 v[232:235], %[vX], %[sWS], s75 offen offset:0 sc1
v_mfma_f32_16x16x32_bf16 a[56:59], a[152:155], v[56:59], a[56:59]
buffer_load_dwordx4 %[vX], %[sWS], s60 offen offset:0 sc1 lds
ds_write_b128 v107, v[232:235] offset:12288
v_mfma_f32_16x16x32_bf16 a[60:63], a[156:159], v[56:59], a[60:63]
v_exp_f32_e64 v75, v167
v_mfma_f32_16x16x32_bf16 a[32:35], a[160:163], v[60:63], a[32:35]
v_cvt_pk_bf16_f32 v43, v74, v75
v_mfma_f32_16x16x32_bf16 a[36:39], a[164:167], v[60:63], a[36:39]
v_exp_f32_e64 v72, v168
v_mfma_f32_16x16x32_bf16 a[40:43], a[168:171], v[60:63], a[40:43]
v_mfma_f32_16x16x32_bf16 a[44:47], a[172:175], v[60:63], a[44:47]
v_exp_f32_e64 v73, v169
v_mfma_f32_16x16x32_bf16 a[48:51], a[176:179], v[60:63], a[48:51]
v_cvt_pk_bf16_f32 v44, v72, v73
v_mfma_f32_16x16x32_bf16 a[52:55], a[180:183], v[60:63], a[52:55]
v_exp_f32_e64 v74, v170
v_mfma_f32_16x16x32_bf16 a[56:59], a[184:187], v[60:63], a[56:59]
v_mfma_f32_16x16x32_bf16 a[60:63], a[188:191], v[60:63], a[60:63]
v_exp_f32_e64 v75, v171
v_mfma_f32_16x16x32_bf16 v[176:179], a[192:195], v[52:55], v[204:207]
v_cvt_pk_bf16_f32 v45, v74, v75
v_rcp_f32_e64 v194, v124
v_mul_f32 v124, 0x76800000, v124
v_mfma_f32_16x16x32_bf16 v[180:183], a[208:211], v[52:55], v[204:207]
v_exp_f32_e64 v72, v172
v_accvgpr_read_b32 v208, a32
v_accvgpr_read_b32 v209, a33
v_mfma_f32_16x16x32_bf16 v[176:179], a[196:199], v[84:87], v[176:179]
v_accvgpr_read_b32 v210, a34
v_accvgpr_read_b32 v211, a35
v_accvgpr_read_b32 v212, a36
v_accvgpr_read_b32 v213, a37
v_mfma_f32_16x16x32_bf16 v[180:183], a[212:215], v[84:87], v[180:183]
v_exp_f32_e64 v73, v173
v_accvgpr_read_b32 v214, a38
v_accvgpr_read_b32 v215, a39
v_mfma_f32_16x16x32_bf16 v[176:179], a[200:203], v[116:119], v[176:179]
v_cvt_pk_bf16_f32 v46, v72, v73
v_accvgpr_read_b32 v216, a40
v_accvgpr_read_b32 v217, a41
v_accvgpr_read_b32 v218, a42
v_mfma_f32_16x16x32_bf16 v[180:183], a[216:219], v[116:119], v[180:183]
v_exp_f32_e64 v74, v174
v_accvgpr_read_b32 v219, a43
v_accvgpr_read_b32 v220, a44
v_mfma_f32_16x16x32_bf16 v[176:179], a[204:207], v[148:151], v[176:179]
v_accvgpr_read_b32 v221, a45
v_accvgpr_read_b32 v222, a46
v_accvgpr_read_b32 v223, a47
v_pk_mul_f32 v[208:209], v[208:209], v[194:195] op_sel_hi:[1,0]
v_pk_mul_f32 v[210:211], v[210:211], v[194:195] op_sel_hi:[1,0]
v_pk_mul_f32 v[212:213], v[212:213], v[194:195] op_sel_hi:[1,0]
v_pk_mul_f32 v[214:215], v[214:215], v[194:195] op_sel_hi:[1,0]
v_pk_mul_f32 v[216:217], v[216:217], v[194:195] op_sel_hi:[1,0]
v_pk_mul_f32 v[218:219], v[218:219], v[194:195] op_sel_hi:[1,0]
v_pk_mul_f32 v[220:221], v[220:221], v[194:195] op_sel_hi:[1,0]
v_pk_mul_f32 v[222:223], v[222:223], v[194:195] op_sel_hi:[1,0]
v_pk_mul_f32 v[154:155], v[208:209], v[208:209]
v_pk_fma_f32 v[154:155], v[210:211], v[210:211], v[154:155]
v_pk_fma_f32 v[154:155], v[212:213], v[212:213], v[154:155]
v_pk_fma_f32 v[154:155], v[214:215], v[214:215], v[154:155]
v_pk_fma_f32 v[154:155], v[216:217], v[216:217], v[154:155]
v_pk_fma_f32 v[154:155], v[218:219], v[218:219], v[154:155]
v_pk_fma_f32 v[154:155], v[220:221], v[220:221], v[154:155]
v_pk_fma_f32 v[154:155], v[222:223], v[222:223], v[154:155]
v_mfma_f32_16x16x32_bf16 v[180:183], a[220:223], v[148:151], v[180:183]
v_exp_f32_e64 v75, v175
v_cvt_pk_bf16_f32 v208, v208, v209
v_cvt_pk_bf16_f32 v209, v210, v211
v_mfma_f32_16x16x32_bf16 v[184:187], a[224:227], v[52:55], v[204:207]
v_cvt_pk_bf16_f32 v47, v74, v75
v_cvt_pk_bf16_f32 v210, v212, v213
v_cvt_pk_bf16_f32 v211, v214, v215
v_mfma_f32_16x16x32_bf16 v[188:191], a[240:243], v[52:55], v[204:207]
buffer_store_dwordx4 v[208:211], %[vX], %[sWS], s72 offen offset:1024 sc1
ds_write_b128 v107, v[208:211] offset:1024
v_cvt_pk_bf16_f32 v216, v216, v217
v_mfma_f32_16x16x32_bf16 v[92:95], v[136:139], v[40:43], v[92:95]
v_cvt_pk_bf16_f32 v217, v218, v219
v_cvt_pk_bf16_f32 v218, v220, v221
v_cvt_pk_bf16_f32 v219, v222, v223
v_mfma_f32_16x16x32_bf16 v[92:95], v[136:139], v[44:47], v[92:95]
v_exp_f32_e64 v72, v176
buffer_store_dwordx4 v[216:219], %[vX], %[sWS], s73 offen offset:1024 sc1
v_mfma_f32_16x16x32_bf16 v[184:187], a[228:231], v[84:87], v[184:187]
)"
R"(ds_write_b128 v107, v[216:219] offset:5120
v_accvgpr_read_b32 v224, a48
v_accvgpr_read_b32 v225, a49
v_accvgpr_read_b32 v226, a50
v_mfma_f32_16x16x32_bf16 v[188:191], a[244:247], v[84:87], v[188:191]
v_exp_f32_e64 v73, v177
v_accvgpr_read_b32 v227, a51
v_accvgpr_read_b32 v228, a52
v_mfma_f32_16x16x32_bf16 v[184:187], a[232:235], v[116:119], v[184:187]
v_cvt_pk_bf16_f32 v56, v72, v73
v_accvgpr_read_b32 v229, a53
v_accvgpr_read_b32 v230, a54
v_accvgpr_read_b32 v231, a55
v_mfma_f32_16x16x32_bf16 v[188:191], a[248:251], v[116:119], v[188:191]
v_exp_f32_e64 v74, v178
v_accvgpr_read_b32 v232, a56
v_accvgpr_read_b32 v233, a57
v_mfma_f32_16x16x32_bf16 v[184:187], a[236:239], v[148:151], v[184:187]
v_accvgpr_read_b32 v234, a58
v_accvgpr_read_b32 v235, a59
v_accvgpr_read_b32 v236, a60
v_mfma_f32_16x16x32_bf16 v[188:191], a[252:255], v[148:151], v[188:191]
v_exp_f32_e64 v75, v179
v_accvgpr_read_b32 v237, a61
v_accvgpr_read_b32 v238, a62
v_mfma_f32_16x16x32_bf16 a[64:67], a[128:131], v[40:43], a[64:67]
v_cvt_pk_bf16_f32 v57, v74, v75
v_accvgpr_read_b32 v239, a63
v_pk_mul_f32 v[224:225], v[224:225], v[194:195] op_sel_hi:[1,0]
v_pk_mul_f32 v[226:227], v[226:227], v[194:195] op_sel_hi:[1,0]
v_pk_mul_f32 v[228:229], v[228:229], v[194:195] op_sel_hi:[1,0]
v_pk_mul_f32 v[230:231], v[230:231], v[194:195] op_sel_hi:[1,0]
v_pk_mul_f32 v[232:233], v[232:233], v[194:195] op_sel_hi:[1,0]
v_pk_mul_f32 v[234:235], v[234:235], v[194:195] op_sel_hi:[1,0]
v_pk_mul_f32 v[236:237], v[236:237], v[194:195] op_sel_hi:[1,0]
v_pk_mul_f32 v[238:239], v[238:239], v[194:195] op_sel_hi:[1,0]
v_pk_fma_f32 v[154:155], v[224:225], v[224:225], v[154:155]
v_pk_fma_f32 v[154:155], v[226:227], v[226:227], v[154:155]
v_pk_fma_f32 v[154:155], v[228:229], v[228:229], v[154:155]
v_pk_fma_f32 v[154:155], v[230:231], v[230:231], v[154:155]
v_pk_fma_f32 v[154:155], v[232:233], v[232:233], v[154:155]
v_pk_fma_f32 v[154:155], v[234:235], v[234:235], v[154:155]
v_pk_fma_f32 v[154:155], v[236:237], v[236:237], v[154:155]
v_pk_fma_f32 v[154:155], v[238:239], v[238:239], v[154:155]
v_mfma_f32_16x16x32_bf16 a[68:71], a[132:135], v[40:43], a[68:71]
v_exp_f32_e64 v72, v180
v_cvt_pk_bf16_f32 v224, v224, v225
v_cvt_pk_bf16_f32 v225, v226, v227
v_mfma_f32_16x16x32_bf16 a[72:75], a[136:139], v[40:43], a[72:75]
v_cvt_pk_bf16_f32 v226, v228, v229
v_cvt_pk_bf16_f32 v227, v230, v231
buffer_store_dwordx4 v[224:227], %[vX], %[sWS], s74 offen offset:1024 sc1
v_mfma_f32_16x16x32_bf16 a[76:79], a[140:143], v[40:43], a[76:79]
v_exp_f32_e64 v73, v181
ds_write_b128 v107, v[224:227] offset:9216
v_cvt_pk_bf16_f32 v232, v232, v233
v_mfma_f32_16x16x32_bf16 a[80:83], a[144:147], v[40:43], a[80:83]
v_cvt_pk_bf16_f32 v58, v72, v73
v_cvt_pk_bf16_f32 v233, v234, v235
v_cvt_pk_bf16_f32 v234, v236, v237
v_cvt_pk_bf16_f32 v235, v238, v239
v_mfma_f32_16x16x32_bf16 a[84:87], a[148:151], v[40:43], a[84:87]
v_exp_f32_e64 v74, v182
buffer_store_dwordx4 v[232:235], %[vX], %[sWS], s75 offen offset:1024 sc1
v_mfma_f32_16x16x32_bf16 a[88:91], a[152:155], v[40:43], a[88:91]
ds_write_b128 v107, v[232:235] offset:13312
v_mfma_f32_16x16x32_bf16 a[92:95], a[156:159], v[40:43], a[92:95]
v_exp_f32_e64 v75, v183
v_mfma_f32_16x16x32_bf16 a[64:67], a[160:163], v[44:47], a[64:67]
v_cvt_pk_bf16_f32 v59, v74, v75
v_mfma_f32_16x16x32_bf16 a[68:71], a[164:167], v[44:47], a[68:71]
v_exp_f32_e64 v72, v184
v_mfma_f32_16x16x32_bf16 a[72:75], a[168:171], v[44:47], a[72:75]
v_mfma_f32_16x16x32_bf16 a[76:79], a[172:175], v[44:47], a[76:79]
v_exp_f32_e64 v73, v185
v_mfma_f32_16x16x32_bf16 a[80:83], a[176:179], v[44:47], a[80:83]
v_cvt_pk_bf16_f32 v60, v72, v73
v_mfma_f32_16x16x32_bf16 a[84:87], a[180:183], v[44:47], a[84:87]
v_exp_f32_e64 v74, v186
v_mfma_f32_16x16x32_bf16 a[88:91], a[184:187], v[44:47], a[88:91]
v_mfma_f32_16x16x32_bf16 a[92:95], a[188:191], v[44:47], a[92:95]
v_exp_f32 v75, v187
v_exp_f32 v72, v188
v_rcp_f32_e64 v196, v92
v_cvt_pk_bf16_f32 v61, v74, v75
v_exp_f32_e64 v73, v189
v_mul_f32 v92, 0x76800000, v92
v_exp_f32_e64 v74, v190
v_accvgpr_read_b32 v208, a64
v_cvt_pk_bf16_f32 v62, v72, v73
v_exp_f32_e64 v75, v191
v_accvgpr_read_b32 v209, a65
v_accvgpr_read_b32 v210, a66
v_cvt_pk_bf16_f32 v63, v74, v75
v_accvgpr_read_b32 v211, a67
v_accvgpr_read_b32 v212, a68
v_mfma_f32_16x16x32_bf16 v[76:79], v[136:139], v[56:59], v[76:79]
v_mfma_f32_16x16x32_bf16 v[76:79], v[136:139], v[60:63], v[76:79]
v_accvgpr_read_b32 v213, a69
v_accvgpr_read_b32 v214, a70
v_accvgpr_read_b32 v215, a71
v_accvgpr_read_b32 v216, a72
v_accvgpr_read_b32 v217, a73
v_accvgpr_read_b32 v218, a74
v_accvgpr_read_b32 v219, a75
v_accvgpr_read_b32 v220, a76
v_mfma_f32_16x16x32_bf16 a[96:99], a[128:131], v[56:59], a[96:99]
v_accvgpr_read_b32 v221, a77
v_accvgpr_read_b32 v222, a78
v_mfma_f32_16x16x32_bf16 a[100:103], a[132:135], v[56:59], a[100:103]
v_accvgpr_read_b32 v223, a79
v_pk_mul_f32 v[208:209], v[208:209], v[196:197] op_sel_hi:[1,0]
v_pk_mul_f32 v[210:211], v[210:211], v[196:197] op_sel_hi:[1,0]
v_pk_mul_f32 v[212:213], v[212:213], v[196:197] op_sel_hi:[1,0]
v_pk_mul_f32 v[214:215], v[214:215], v[196:197] op_sel_hi:[1,0]
v_pk_mul_f32 v[216:217], v[216:217], v[196:197] op_sel_hi:[1,0]
v_pk_mul_f32 v[218:219], v[218:219], v[196:197] op_sel_hi:[1,0]
v_pk_mul_f32 v[220:221], v[220:221], v[196:197] op_sel_hi:[1,0]
v_pk_mul_f32 v[222:223], v[222:223], v[196:197] op_sel_hi:[1,0]
v_pk_mul_f32 v[156:157], v[208:209], v[208:209]
v_pk_fma_f32 v[156:157], v[210:211], v[210:211], v[156:157]
v_pk_fma_f32 v[156:157], v[212:213], v[212:213], v[156:157]
v_pk_fma_f32 v[156:157], v[214:215], v[214:215], v[156:157]
v_pk_fma_f32 v[156:157], v[216:217], v[216:217], v[156:157]
v_pk_fma_f32 v[156:157], v[218:219], v[218:219], v[156:157]
v_pk_fma_f32 v[156:157], v[220:221], v[220:221], v[156:157]
v_pk_fma_f32 v[156:157], v[222:223], v[222:223], v[156:157]
v_mfma_f32_16x16x32_bf16 a[104:107], a[136:139], v[56:59], a[104:107]
v_cvt_pk_bf16_f32 v208, v208, v209
v_cvt_pk_bf16_f32 v209, v210, v211
v_mfma_f32_16x16x32_bf16 a[108:111], a[140:143], v[56:59], a[108:111]
v_cvt_pk_bf16_f32 v210, v212, v213
v_cvt_pk_bf16_f32 v211, v214, v215
v_mfma_f32_16x16x32_bf16 a[112:115], a[144:147], v[56:59], a[112:115]
buffer_store_dwordx4 v[208:211], %[vX], %[sWS], s72 offen offset:2048 sc1
v_mfma_f32_16x16x32_bf16 a[116:119], a[148:151], v[56:59], a[116:119]
ds_write_b128 v107, v[208:211] offset:2048
v_cvt_pk_bf16_f32 v216, v216, v217
v_mfma_f32_16x16x32_bf16 a[120:123], a[152:155], v[56:59], a[120:123]
v_cvt_pk_bf16_f32 v217, v218, v219
v_cvt_pk_bf16_f32 v218, v220, v221
v_mfma_f32_16x16x32_bf16 a[124:127], a[156:159], v[56:59], a[124:127]
v_cvt_pk_bf16_f32 v219, v222, v223
v_mfma_f32_16x16x32_bf16 a[96:99], a[160:163], v[60:63], a[96:99]
buffer_store_dwordx4 v[216:219], %[vX], %[sWS], s73 offen offset:2048 sc1
v_mfma_f32_16x16x32_bf16 a[100:103], a[164:167], v[60:63], a[100:103]
ds_write_b128 v107, v[216:219] offset:6144
v_accvgpr_read_b32 v224, a80
v_mfma_f32_16x16x32_bf16 a[104:107], a[168:171], v[60:63], a[104:107]
v_accvgpr_read_b32 v225, a81
v_accvgpr_read_b32 v226, a82
v_mfma_f32_16x16x32_bf16 a[108:111], a[172:175], v[60:63], a[108:111]
v_accvgpr_read_b32 v227, a83
v_accvgpr_read_b32 v228, a84
v_mfma_f32_16x16x32_bf16 a[112:115], a[176:179], v[60:63], a[112:115]
v_accvgpr_read_b32 v229, a85
v_accvgpr_read_b32 v230, a86
v_mfma_f32_16x16x32_bf16 a[116:119], a[180:183], v[60:63], a[116:119]
v_accvgpr_read_b32 v231, a87
v_accvgpr_read_b32 v232, a88
v_mfma_f32_16x16x32_bf16 a[120:123], a[184:187], v[60:63], a[120:123]
v_accvgpr_read_b32 v233, a89
v_accvgpr_read_b32 v234, a90
v_mfma_f32_16x16x32_bf16 a[124:127], a[188:191], v[60:63], a[124:127]
v_accvgpr_read_b32 v235, a91
v_accvgpr_read_b32 v236, a92
v_accvgpr_read_b32 v237, a93
v_accvgpr_read_b32 v238, a94
v_accvgpr_read_b32 v239, a95
v_pk_mul_f32 v[224:225], v[224:225], v[196:197] op_sel_hi:[1,0]
v_pk_mul_f32 v[226:227], v[226:227], v[196:197] op_sel_hi:[1,0]
v_pk_mul_f32 v[228:229], v[228:229], v[196:197] op_sel_hi:[1,0]
v_pk_mul_f32 v[230:231], v[230:231], v[196:197] op_sel_hi:[1,0]
v_pk_mul_f32 v[232:233], v[232:233], v[196:197] op_sel_hi:[1,0]
v_pk_mul_f32 v[234:235], v[234:235], v[196:197] op_sel_hi:[1,0]
v_pk_mul_f32 v[236:237], v[236:237], v[196:197] op_sel_hi:[1,0]
v_pk_mul_f32 v[238:239], v[238:239], v[196:197] op_sel_hi:[1,0]
v_pk_fma_f32 v[156:157], v[224:225], v[224:225], v[156:157]
v_pk_fma_f32 v[156:157], v[226:227], v[226:227], v[156:157]
v_pk_fma_f32 v[156:157], v[228:229], v[228:229], v[156:157]
v_pk_fma_f32 v[156:157], v[230:231], v[230:231], v[156:157]
v_pk_fma_f32 v[156:157], v[232:233], v[232:233], v[156:157]
v_pk_fma_f32 v[156:157], v[234:235], v[234:235], v[156:157]
v_pk_fma_f32 v[156:157], v[236:237], v[236:237], v[156:157]
v_pk_fma_f32 v[156:157], v[238:239], v[238:239], v[156:157]
v_cvt_pk_bf16_f32 v224, v224, v225
v_cvt_pk_bf16_f32 v225, v226, v227
v_cvt_pk_bf16_f32 v226, v228, v229
v_cvt_pk_bf16_f32 v227, v230, v231
buffer_store_dwordx4 v[224:227], %[vX], %[sWS], s74 offen offset:2048 sc1
ds_write_b128 v107, v[224:227] offset:10240
v_cvt_pk_bf16_f32 v232, v232, v233
v_cvt_pk_bf16_f32 v233, v234, v235
v_cvt_pk_bf16_f32 v234, v236, v237
v_cvt_pk_bf16_f32 v235, v238, v239
buffer_store_dwordx4 v[232:235], %[vX], %[sWS], s75 offen offset:2048 sc1
ds_write_b128 v107, v[232:235] offset:14336
v_rcp_f32_e64 v198, v76
v_mul_f32 v76, 0x76800000, v76
v_accvgpr_read_b32 v160, a96
)"
R"(v_accvgpr_read_b32 v161, a97
v_accvgpr_read_b32 v162, a98
v_accvgpr_read_b32 v163, a99
v_accvgpr_read_b32 v164, a100
v_accvgpr_read_b32 v165, a101
v_accvgpr_read_b32 v166, a102
v_accvgpr_read_b32 v167, a103
v_accvgpr_read_b32 v168, a104
v_accvgpr_read_b32 v169, a105
v_accvgpr_read_b32 v170, a106
v_accvgpr_read_b32 v171, a107
v_accvgpr_read_b32 v172, a108
v_accvgpr_read_b32 v173, a109
v_accvgpr_read_b32 v174, a110
v_accvgpr_read_b32 v175, a111
v_pk_mul_f32 v[160:161], v[160:161], v[198:199] op_sel_hi:[1,0]
v_pk_mul_f32 v[162:163], v[162:163], v[198:199] op_sel_hi:[1,0]
v_pk_mul_f32 v[164:165], v[164:165], v[198:199] op_sel_hi:[1,0]
v_pk_mul_f32 v[166:167], v[166:167], v[198:199] op_sel_hi:[1,0]
v_pk_mul_f32 v[168:169], v[168:169], v[198:199] op_sel_hi:[1,0]
v_pk_mul_f32 v[170:171], v[170:171], v[198:199] op_sel_hi:[1,0]
v_pk_mul_f32 v[172:173], v[172:173], v[198:199] op_sel_hi:[1,0]
v_pk_mul_f32 v[174:175], v[174:175], v[198:199] op_sel_hi:[1,0]
v_pk_mul_f32 v[158:159], v[160:161], v[160:161]
v_pk_fma_f32 v[158:159], v[162:163], v[162:163], v[158:159]
v_pk_fma_f32 v[158:159], v[164:165], v[164:165], v[158:159]
v_pk_fma_f32 v[158:159], v[166:167], v[166:167], v[158:159]
v_pk_fma_f32 v[158:159], v[168:169], v[168:169], v[158:159]
v_pk_fma_f32 v[158:159], v[170:171], v[170:171], v[158:159]
v_pk_fma_f32 v[158:159], v[172:173], v[172:173], v[158:159]
v_pk_fma_f32 v[158:159], v[174:175], v[174:175], v[158:159]
v_cvt_pk_bf16_f32 v160, v160, v161
v_cvt_pk_bf16_f32 v161, v162, v163
v_cvt_pk_bf16_f32 v162, v164, v165
v_cvt_pk_bf16_f32 v163, v166, v167
buffer_store_dwordx4 v[160:163], %[vX], %[sWS], s72 offen offset:3072 sc1
ds_write_b128 v107, v[160:163] offset:3072
v_cvt_pk_bf16_f32 v168, v168, v169
v_cvt_pk_bf16_f32 v169, v170, v171
v_cvt_pk_bf16_f32 v170, v172, v173
v_cvt_pk_bf16_f32 v171, v174, v175
buffer_store_dwordx4 v[168:171], %[vX], %[sWS], s73 offen offset:3072 sc1
ds_write_b128 v107, v[168:171] offset:7168
v_accvgpr_read_b32 v176, a112
v_accvgpr_read_b32 v177, a113
v_accvgpr_read_b32 v178, a114
v_accvgpr_read_b32 v179, a115
v_accvgpr_read_b32 v180, a116
v_accvgpr_read_b32 v181, a117
v_accvgpr_read_b32 v182, a118
v_accvgpr_read_b32 v183, a119
v_accvgpr_read_b32 v184, a120
v_accvgpr_read_b32 v185, a121
v_accvgpr_read_b32 v186, a122
v_accvgpr_read_b32 v187, a123
v_accvgpr_read_b32 v188, a124
v_accvgpr_read_b32 v189, a125
v_accvgpr_read_b32 v190, a126
v_accvgpr_read_b32 v191, a127
v_pk_mul_f32 v[176:177], v[176:177], v[198:199] op_sel_hi:[1,0]
v_pk_mul_f32 v[178:179], v[178:179], v[198:199] op_sel_hi:[1,0]
v_pk_mul_f32 v[180:181], v[180:181], v[198:199] op_sel_hi:[1,0]
v_pk_mul_f32 v[182:183], v[182:183], v[198:199] op_sel_hi:[1,0]
v_pk_mul_f32 v[184:185], v[184:185], v[198:199] op_sel_hi:[1,0]
v_pk_mul_f32 v[186:187], v[186:187], v[198:199] op_sel_hi:[1,0]
v_pk_mul_f32 v[188:189], v[188:189], v[198:199] op_sel_hi:[1,0]
v_pk_mul_f32 v[190:191], v[190:191], v[198:199] op_sel_hi:[1,0]
v_pk_fma_f32 v[158:159], v[176:177], v[176:177], v[158:159]
v_pk_fma_f32 v[158:159], v[178:179], v[178:179], v[158:159]
v_pk_fma_f32 v[158:159], v[180:181], v[180:181], v[158:159]
v_pk_fma_f32 v[158:159], v[182:183], v[182:183], v[158:159]
v_pk_fma_f32 v[158:159], v[184:185], v[184:185], v[158:159]
v_pk_fma_f32 v[158:159], v[186:187], v[186:187], v[158:159]
v_pk_fma_f32 v[158:159], v[188:189], v[188:189], v[158:159]
v_pk_fma_f32 v[158:159], v[190:191], v[190:191], v[158:159]
v_cvt_pk_bf16_f32 v176, v176, v177
v_cvt_pk_bf16_f32 v177, v178, v179
v_cvt_pk_bf16_f32 v178, v180, v181
v_cvt_pk_bf16_f32 v179, v182, v183
buffer_store_dwordx4 v[176:179], %[vX], %[sWS], s74 offen offset:3072 sc1
ds_write_b128 v107, v[176:179] offset:11264
v_cvt_pk_bf16_f32 v184, v184, v185
v_cvt_pk_bf16_f32 v185, v186, v187
v_cvt_pk_bf16_f32 v186, v188, v189
v_cvt_pk_bf16_f32 v187, v190, v191
buffer_store_dwordx4 v[184:187], %[vX], %[sWS], s75 offen offset:3072 sc1
ds_write_b128 v107, v[184:187] offset:15360
v_add_f32 v152, v152, v153
v_add_f32 v154, v154, v155
v_add_f32 v156, v156, v157
v_add_f32 v158, v158, v159
v_add_f32 v153, v152, v154
v_add_f32 v155, v156, v158
v_add_f32 v153, v153, v155
v_mul_f32 v152, v152, v120
v_mul_f32 v154, v154, v124
v_mul_f32 v156, v156, v92
v_mul_f32 v158, v158, v76
v_mul_f32 v152, v152, v120
v_mul_f32 v154, v154, v124
v_mul_f32 v156, v156, v92
v_mul_f32_e64 v158, v158, v76
v_min3_f32 v152, v152, v154, v156
v_min3_f32 v192, v192, v194, v196
v_min_f32 v192, v192, v198
v_mul_f32 v192, s54, v192
v_min3_f32 v152, v152, v158, v192
v_mov_b32 v24, 519
s_lshr_b32 s60, %[sKV], 10
s_add_u32 s61, %[sL], 81920
v_mov_b32 v27, s61
v_add_u32 v25, s60, v27
v_cmp_class_f32 vcc, v153, v24
v_cmp_nle_f32 s[62:63], 1.0, v152
s_nop 1
s_or_b64 vcc, vcc, s[62:63]
s_cselect_b32 s60, 1, 0
v_mov_b32 v26, s60
ds_write_b32 v25, v26 offset:0
s_waitcnt vmcnt(12) lgkmcnt(0)
s_barrier
ds_read_b128 v[24:27], v27 offset:0
s_waitcnt lgkmcnt(0)
v_or_b32 v24, v24, v25
v_or3_b32 v24, v24, v26, v27
s_nop 0
v_readfirstlane_b32 s60, v24
s_nop 3
s_andn2_b32 s60, s60, s59
s_cbranch_scc0 L_done_%=
s_waitcnt vmcnt(0) lgkmcnt(0)
s_mov_b32 s55, 0x41800000
s_mov_b32 s56, %[sKV]
s_mov_b32 s58, 31
v_accvgpr_write_b32 a0, 0
v_accvgpr_write_b32 a1, 0
v_accvgpr_write_b32 a2, 0
v_accvgpr_write_b32 a3, 0
v_accvgpr_write_b32 a4, 0
v_accvgpr_write_b32 a5, 0
v_accvgpr_write_b32 a6, 0
v_accvgpr_write_b32 a7, 0
v_accvgpr_write_b32 a8, 0
v_accvgpr_write_b32 a9, 0
v_accvgpr_write_b32 a10, 0
v_accvgpr_write_b32 a11, 0
v_accvgpr_write_b32 a12, 0
v_accvgpr_write_b32 a13, 0
v_accvgpr_write_b32 a14, 0
v_accvgpr_write_b32 a15, 0
v_accvgpr_write_b32 a16, 0
v_accvgpr_write_b32 a17, 0
v_accvgpr_write_b32 a18, 0
v_accvgpr_write_b32 a19, 0
v_accvgpr_write_b32 a20, 0
v_accvgpr_write_b32 a21, 0
v_accvgpr_write_b32 a22, 0
v_accvgpr_write_b32 a23, 0
v_accvgpr_write_b32 a24, 0
v_accvgpr_write_b32 a25, 0
v_accvgpr_write_b32 a26, 0
v_accvgpr_write_b32 a27, 0
v_accvgpr_write_b32 a28, 0
v_accvgpr_write_b32 a29, 0
v_accvgpr_write_b32 a30, 0
v_accvgpr_write_b32 a31, 0
v_accvgpr_write_b32 a32, 0
v_accvgpr_write_b32 a33, 0
v_accvgpr_write_b32 a34, 0
v_accvgpr_write_b32 a35, 0
v_accvgpr_write_b32 a36, 0
v_accvgpr_write_b32 a37, 0
v_accvgpr_write_b32 a38, 0
v_accvgpr_write_b32 a39, 0
v_accvgpr_write_b32 a40, 0
v_accvgpr_write_b32 a41, 0
v_accvgpr_write_b32 a42, 0
v_accvgpr_write_b32 a43, 0
v_accvgpr_write_b32 a44, 0
v_accvgpr_write_b32 a45, 0
v_accvgpr_write_b32 a46, 0
v_accvgpr_write_b32 a47, 0
v_accvgpr_write_b32 a48, 0
v_accvgpr_write_b32 a49, 0
v_accvgpr_write_b32 a50, 0
v_accvgpr_write_b32 a51, 0
v_accvgpr_write_b32 a52, 0
v_accvgpr_write_b32 a53, 0
v_accvgpr_write_b32 a54, 0
v_accvgpr_write_b32 a55, 0
v_accvgpr_write_b32 a56, 0
v_accvgpr_write_b32 a57, 0
v_accvgpr_write_b32 a58, 0
v_accvgpr_write_b32 a59, 0
v_accvgpr_write_b32 a60, 0
v_accvgpr_write_b32 a61, 0
v_accvgpr_write_b32 a62, 0
v_accvgpr_write_b32 a63, 0
v_accvgpr_write_b32 a64, 0
v_accvgpr_write_b32 a65, 0
)"
R"(v_accvgpr_write_b32 a66, 0
v_accvgpr_write_b32 a67, 0
v_accvgpr_write_b32 a68, 0
v_accvgpr_write_b32 a69, 0
v_accvgpr_write_b32 a70, 0
v_accvgpr_write_b32 a71, 0
v_accvgpr_write_b32 a72, 0
v_accvgpr_write_b32 a73, 0
v_accvgpr_write_b32 a74, 0
v_accvgpr_write_b32 a75, 0
v_accvgpr_write_b32 a76, 0
v_accvgpr_write_b32 a77, 0
v_accvgpr_write_b32 a78, 0
v_accvgpr_write_b32 a79, 0
v_accvgpr_write_b32 a80, 0
v_accvgpr_write_b32 a81, 0
v_accvgpr_write_b32 a82, 0
v_accvgpr_write_b32 a83, 0
v_accvgpr_write_b32 a84, 0
v_accvgpr_write_b32 a85, 0
v_accvgpr_write_b32 a86, 0
v_accvgpr_write_b32 a87, 0
v_accvgpr_write_b32 a88, 0
v_accvgpr_write_b32 a89, 0
v_accvgpr_write_b32 a90, 0
v_accvgpr_write_b32 a91, 0
v_accvgpr_write_b32 a92, 0
v_accvgpr_write_b32 a93, 0
v_accvgpr_write_b32 a94, 0
v_accvgpr_write_b32 a95, 0
v_accvgpr_write_b32 a96, 0
v_accvgpr_write_b32 a97, 0
v_accvgpr_write_b32 a98, 0
v_accvgpr_write_b32 a99, 0
v_accvgpr_write_b32 a100, 0
v_accvgpr_write_b32 a101, 0
v_accvgpr_write_b32 a102, 0
v_accvgpr_write_b32 a103, 0
v_accvgpr_write_b32 a104, 0
v_accvgpr_write_b32 a105, 0
v_accvgpr_write_b32 a106, 0
v_accvgpr_write_b32 a107, 0
v_accvgpr_write_b32 a108, 0
v_accvgpr_write_b32 a109, 0
v_accvgpr_write_b32 a110, 0
v_accvgpr_write_b32 a111, 0
v_accvgpr_write_b32 a112, 0
v_accvgpr_write_b32 a113, 0
v_accvgpr_write_b32 a114, 0
v_accvgpr_write_b32 a115, 0
v_accvgpr_write_b32 a116, 0
v_accvgpr_write_b32 a117, 0
v_accvgpr_write_b32 a118, 0
v_accvgpr_write_b32 a119, 0
v_accvgpr_write_b32 a120, 0
v_accvgpr_write_b32 a121, 0
v_accvgpr_write_b32 a122, 0
v_accvgpr_write_b32 a123, 0
v_accvgpr_write_b32 a124, 0
v_accvgpr_write_b32 a125, 0
v_accvgpr_write_b32 a126, 0
v_accvgpr_write_b32 a127, 0
v_mov_b32 v200, 4286578687
v_mov_b32 v120, 0
v_mov_b32 v121, 0
v_mov_b32 v122, 0
v_mov_b32 v123, 0
v_mov_b32 v201, 4286578687
v_mov_b32 v124, 0
v_mov_b32 v125, 0
v_mov_b32 v126, 0
v_mov_b32 v127, 0
v_mov_b32 v202, 4286578687
v_mov_b32 v92, 0
v_mov_b32 v93, 0
v_mov_b32 v94, 0
v_mov_b32 v95, 0
v_mov_b32 v203, 4286578687
v_mov_b32 v76, 0
v_mov_b32 v77, 0
v_mov_b32_e64 v78, 0
v_mov_b32 v79, 0
L_p2_%=:
s_barrier
s_add_u32 m0, %[sLw], 49152
s_nop 0
buffer_load_dwordx4 %[vVs0], s[16:19], s56 offen offset:0 lds
buffer_load_dwordx4 %[vVs1], s[16:19], s56 offen offset:1024 lds
buffer_load_dwordx4 %[vVs0], s[16:19], s56 offen offset:2048 lds
buffer_load_dwordx4 %[vVs1], s[16:19], s56 offen offset:3072 lds
s_add_u32 m0, %[sLw], 114688
s_nop 0
buffer_load_dwordx4 %[vVs0], s[20:23], s56 offen offset:0 lds
buffer_load_dwordx4 %[vVs1], s[20:23], s56 offen offset:1024 lds
buffer_load_dwordx4 %[vVs0], s[20:23], s56 offen offset:2048 lds
buffer_load_dwordx4 %[vVs1], s[20:23], s56 offen offset:3072 lds
s_add_u32 s56, s56, 16384
s_waitcnt vmcnt(0)
s_barrier
ds_read_b128 a[192:195], %[vKrd] offset:49152
ds_read_b128 a[208:211], %[vKrd] offset:53248
ds_read_b128 a[196:199], %[vKrd] offset:49664
ds_read_b128 a[212:215], %[vKrd] offset:53760
ds_read_b128 a[200:203], %[vKrd] offset:50176
ds_read_b128 a[216:219], %[vKrd] offset:54272
ds_read_b128 a[204:207], %[vKrd] offset:50688
ds_read_b128 a[220:223], %[vKrd] offset:54784
s_waitcnt lgkmcnt(0)
ds_read_b64_tr_b16 a[128:129], %[vVr] offset:49152
ds_read_b64_tr_b16 a[130:131], %[vVr] offset:53248
ds_read_b64_tr_b16 a[132:133], v140 offset:49152
ds_read_b64_tr_b16 a[134:135], v140 offset:53248
ds_read_b64_tr_b16 a[136:137], %[vVr] offset:49664
ds_read_b64_tr_b16 a[138:139], %[vVr] offset:53760
ds_read_b64_tr_b16 a[140:141], v140 offset:49664
ds_read_b64_tr_b16 a[142:143], v140 offset:53760
s_waitcnt lgkmcnt(0)
ds_read_b64_tr_b16 a[144:145], %[vVr] offset:50176
ds_read_b64_tr_b16 a[146:147], %[vVr] offset:54272
ds_read_b64_tr_b16 a[148:149], v140 offset:50176
ds_read_b64_tr_b16 a[150:151], v140 offset:54272
ds_read_b64_tr_b16 a[152:153], %[vVr] offset:50688
ds_read_b64_tr_b16 a[154:155], %[vVr] offset:54784
ds_read_b64_tr_b16 a[156:157], v140 offset:50688
ds_read_b64_tr_b16 a[158:159], v140 offset:54784
s_waitcnt lgkmcnt(0)
ds_read_b128 a[224:227], %[vKrd] offset:57344
ds_read_b128 a[240:243], %[vKrd] offset:61440
ds_read_b128 a[228:231], %[vKrd] offset:57856
ds_read_b128 a[244:247], %[vKrd] offset:61952
ds_read_b128 a[232:235], %[vKrd] offset:58368
ds_read_b128 a[248:251], %[vKrd] offset:62464
ds_read_b128 a[236:239], %[vKrd] offset:58880
ds_read_b128 a[252:255], %[vKrd] offset:62976
s_waitcnt lgkmcnt(0)
ds_read_b64_tr_b16 a[160:161], %[vVr] offset:57344
ds_read_b64_tr_b16 a[162:163], %[vVr] offset:61440
ds_read_b64_tr_b16 a[164:165], v140 offset:57344
ds_read_b64_tr_b16 a[166:167], v140 offset:61440
ds_read_b64_tr_b16 a[168:169], %[vVr] offset:57856
ds_read_b64_tr_b16 a[170:171], %[vVr] offset:61952
ds_read_b64_tr_b16 a[172:173], v140 offset:57856
ds_read_b64_tr_b16 a[174:175], v140 offset:61952
s_waitcnt lgkmcnt(0)
ds_read_b64_tr_b16 a[176:177], %[vVr] offset:58368
ds_read_b64_tr_b16 a[178:179], %[vVr] offset:62464
ds_read_b64_tr_b16 a[180:181], v140 offset:58368
ds_read_b64_tr_b16 a[182:183], v140 offset:62464
ds_read_b64_tr_b16 a[184:185], %[vVr] offset:58880
ds_read_b64_tr_b16 a[186:187], %[vVr] offset:62976
ds_read_b64_tr_b16 a[188:189], v140 offset:58880
ds_read_b64_tr_b16 a[190:191], v140 offset:62976
s_waitcnt lgkmcnt(0)
v_mfma_f32_16x16x32_bf16 v[160:163], a[192:195], v[32:35], 0
v_mfma_f32_16x16x32_bf16 v[164:167], a[208:211], v[32:35], 0
v_mfma_f32_16x16x32_bf16 v[160:163], a[196:199], v[64:67], v[160:163]
v_mfma_f32_16x16x32_bf16 v[164:167], a[212:215], v[64:67], v[164:167]
v_mfma_f32_16x16x32_bf16 v[160:163], a[200:203], v[96:99], v[160:163]
v_mfma_f32_16x16x32_bf16 v[164:167], a[216:219], v[96:99], v[164:167]
v_mfma_f32_16x16x32_bf16 v[160:163], a[204:207], v[128:131], v[160:163]
v_mfma_f32_16x16x32_bf16 v[164:167], a[220:223], v[128:131], v[164:167]
v_mfma_f32_16x16x32_bf16 v[168:171], a[224:227], v[32:35], 0
v_mfma_f32_16x16x32_bf16 v[172:175], a[240:243], v[32:35], 0
v_mfma_f32_16x16x32_bf16 v[168:171], a[228:231], v[64:67], v[168:171]
v_mfma_f32_16x16x32_bf16 v[172:175], a[244:247], v[64:67], v[172:175]
v_mfma_f32_16x16x32_bf16 v[168:171], a[232:235], v[96:99], v[168:171]
v_mfma_f32_16x16x32_bf16 v[172:175], a[248:251], v[96:99], v[172:175]
v_mfma_f32_16x16x32_bf16 v[168:171], a[236:239], v[128:131], v[168:171]
v_mfma_f32_16x16x32_bf16 v[172:175], a[252:255], v[128:131], v[172:175]
s_nop 7
v_max3_f32 v88, v160, v161, v162
v_max3_f32 v90, v163, v164, v165
v_max3_f32 v89, v168, v169, v170
v_max3_f32 v91, v171, v172, v173
v_max3_f32 v88, v88, v166, v167
v_max3_f32 v89, v89, v174, v175
v_max3_f32 v88, v88, v89, v90
v_max_f32 v88, v88, v91
v_mov_b32 v90, v88
s_nop 1
v_permlane32_swap_b32 v88, v90
s_nop 0
v_max_f32 v88, v88, v90
v_mov_b32 v90, v88
s_nop 1
v_permlane16_swap_b32 v88, v90
s_nop 0
v_max_f32 v88, v88, v90
v_ceil_f32 v88, v88
v_add_f32 v88, s55, v88
v_max_f32 v88, v200, v88
v_sub_f32 v90, v200, v88
v_mov_b32 v200, v88
v_cvt_i32_f32 v90, v90
v_ldexp_f32 v120, v120, v90
v_accvgpr_read_b32 v72, a0
v_ldexp_f32 v72, v72, v90
v_accvgpr_write_b32 a0, v72
v_accvgpr_read_b32 v73, a1
v_ldexp_f32 v73, v73, v90
)"
R"(v_accvgpr_write_b32 a1, v73
v_accvgpr_read_b32 v74, a2
v_ldexp_f32 v74, v74, v90
v_accvgpr_write_b32 a2, v74
v_accvgpr_read_b32 v75, a3
v_ldexp_f32 v75, v75, v90
v_accvgpr_write_b32 a3, v75
v_accvgpr_read_b32 v72, a4
v_ldexp_f32 v72, v72, v90
v_accvgpr_write_b32 a4, v72
v_accvgpr_read_b32 v73, a5
v_ldexp_f32 v73, v73, v90
v_accvgpr_write_b32 a5, v73
v_accvgpr_read_b32 v74, a6
v_ldexp_f32 v74, v74, v90
v_accvgpr_write_b32 a6, v74
v_accvgpr_read_b32 v75, a7
v_ldexp_f32 v75, v75, v90
v_accvgpr_write_b32 a7, v75
v_accvgpr_read_b32 v72, a8
v_ldexp_f32 v72, v72, v90
v_accvgpr_write_b32 a8, v72
v_accvgpr_read_b32 v73, a9
v_ldexp_f32 v73, v73, v90
v_accvgpr_write_b32 a9, v73
v_accvgpr_read_b32 v74, a10
v_ldexp_f32 v74, v74, v90
v_accvgpr_write_b32 a10, v74
v_accvgpr_read_b32 v75, a11
v_ldexp_f32 v75, v75, v90
v_accvgpr_write_b32 a11, v75
v_accvgpr_read_b32 v72, a12
v_ldexp_f32 v72, v72, v90
v_accvgpr_write_b32 a12, v72
v_accvgpr_read_b32 v73, a13
v_ldexp_f32 v73, v73, v90
v_accvgpr_write_b32 a13, v73
v_accvgpr_read_b32 v74, a14
v_ldexp_f32 v74, v74, v90
v_accvgpr_write_b32 a14, v74
v_accvgpr_read_b32 v75, a15
v_ldexp_f32 v75, v75, v90
v_accvgpr_write_b32 a15, v75
v_accvgpr_read_b32 v72, a16
v_ldexp_f32 v72, v72, v90
v_accvgpr_write_b32 a16, v72
v_accvgpr_read_b32 v73, a17
v_ldexp_f32 v73, v73, v90
v_accvgpr_write_b32 a17, v73
v_accvgpr_read_b32 v74, a18
v_ldexp_f32 v74, v74, v90
v_accvgpr_write_b32 a18, v74
v_accvgpr_read_b32 v75, a19
v_ldexp_f32 v75, v75, v90
v_accvgpr_write_b32 a19, v75
v_accvgpr_read_b32 v72, a20
v_ldexp_f32 v72, v72, v90
v_accvgpr_write_b32 a20, v72
v_accvgpr_read_b32 v73, a21
v_ldexp_f32 v73, v73, v90
v_accvgpr_write_b32 a21, v73
v_accvgpr_read_b32 v74, a22
v_ldexp_f32 v74, v74, v90
v_accvgpr_write_b32 a22, v74
v_accvgpr_read_b32 v75, a23
v_ldexp_f32 v75, v75, v90
v_accvgpr_write_b32 a23, v75
v_accvgpr_read_b32 v72, a24
v_ldexp_f32 v72, v72, v90
v_accvgpr_write_b32 a24, v72
v_accvgpr_read_b32 v73, a25
v_ldexp_f32 v73, v73, v90
v_accvgpr_write_b32 a25, v73
v_accvgpr_read_b32 v74, a26
v_ldexp_f32 v74, v74, v90
v_accvgpr_write_b32 a26, v74
v_accvgpr_read_b32 v75, a27
v_ldexp_f32 v75, v75, v90
v_accvgpr_write_b32 a27, v75
v_accvgpr_read_b32 v72, a28
v_ldexp_f32 v72, v72, v90
v_accvgpr_write_b32 a28, v72
v_accvgpr_read_b32 v73, a29
v_ldexp_f32 v73, v73, v90
v_accvgpr_write_b32 a29, v73
v_accvgpr_read_b32 v74, a30
v_ldexp_f32 v74, v74, v90
v_accvgpr_write_b32 a30, v74
v_accvgpr_read_b32 v75, a31
v_ldexp_f32 v75, v75, v90
v_accvgpr_write_b32 a31, v75
v_sub_f32 v160, v160, v88
v_sub_f32 v161, v161, v88
v_sub_f32 v162, v162, v88
v_sub_f32 v163, v163, v88
v_sub_f32 v164, v164, v88
v_sub_f32 v165, v165, v88
v_sub_f32 v166, v166, v88
v_sub_f32 v167, v167, v88
v_sub_f32 v168, v168, v88
v_sub_f32 v169, v169, v88
v_sub_f32 v170, v170, v88
v_sub_f32 v171, v171, v88
v_sub_f32 v172, v172, v88
v_sub_f32 v173, v173, v88
v_sub_f32 v174, v174, v88
v_sub_f32 v175, v175, v88
v_exp_f32 v160, v160
v_exp_f32 v161, v161
v_exp_f32 v162, v162
v_exp_f32 v163, v163
v_exp_f32 v164, v164
v_exp_f32 v165, v165
v_exp_f32 v166, v166
v_exp_f32 v167, v167
v_exp_f32 v168, v168
v_exp_f32 v169, v169
v_exp_f32 v170, v170
v_exp_f32 v171, v171
v_exp_f32 v172, v172
v_exp_f32 v173, v173
v_exp_f32 v174, v174
v_exp_f32 v175, v175
s_nop 0
v_cvt_pk_bf16_f32 v40, v160, v161
v_cvt_pk_bf16_f32 v41, v162, v163
v_cvt_pk_bf16_f32 v42, v164, v165
v_cvt_pk_bf16_f32 v43, v166, v167
v_cvt_pk_bf16_f32 v44, v168, v169
v_cvt_pk_bf16_f32 v45, v170, v171
v_cvt_pk_bf16_f32 v46, v172, v173
v_cvt_pk_bf16_f32 v47, v174, v175
s_nop 1
v_mfma_f32_16x16x32_bf16 v[120:123], v[136:139], v[40:43], v[120:123]
v_mfma_f32_16x16x32_bf16 v[120:123], v[136:139], v[44:47], v[120:123]
v_mfma_f32_16x16x32_bf16 a[0:3], a[128:131], v[40:43], a[0:3]
v_mfma_f32_16x16x32_bf16 a[4:7], a[132:135], v[40:43], a[4:7]
v_mfma_f32_16x16x32_bf16 a[8:11], a[136:139], v[40:43], a[8:11]
v_mfma_f32_16x16x32_bf16 a[12:15], a[140:143], v[40:43], a[12:15]
v_mfma_f32_16x16x32_bf16 a[16:19], a[144:147], v[40:43], a[16:19]
v_mfma_f32_16x16x32_bf16 a[20:23], a[148:151], v[40:43], a[20:23]
v_mfma_f32_16x16x32_bf16 a[24:27], a[152:155], v[40:43], a[24:27]
v_mfma_f32_16x16x32_bf16 a[28:31], a[156:159], v[40:43], a[28:31]
v_mfma_f32_16x16x32_bf16 a[0:3], a[160:163], v[44:47], a[0:3]
v_mfma_f32_16x16x32_bf16 a[4:7], a[164:167], v[44:47], a[4:7]
v_mfma_f32_16x16x32_bf16 a[8:11], a[168:171], v[44:47], a[8:11]
v_mfma_f32_16x16x32_bf16 a[12:15], a[172:175], v[44:47], a[12:15]
v_mfma_f32_16x16x32_bf16 a[16:19], a[176:179], v[44:47], a[16:19]
v_mfma_f32_16x16x32_bf16 a[20:23], a[180:183], v[44:47], a[20:23]
v_mfma_f32_16x16x32_bf16 a[24:27], a[184:187], v[44:47], a[24:27]
v_mfma_f32_16x16x32_bf16 a[28:31], a[188:191], v[44:47], a[28:31]
v_mfma_f32_16x16x32_bf16 v[176:179], a[192:195], v[36:39], 0
v_mfma_f32_16x16x32_bf16 v[180:183], a[208:211], v[36:39], 0
v_mfma_f32_16x16x32_bf16 v[176:179], a[196:199], v[68:71], v[176:179]
v_mfma_f32_16x16x32_bf16 v[180:183], a[212:215], v[68:71], v[180:183]
v_mfma_f32_16x16x32_bf16 v[176:179], a[200:203], v[100:103], v[176:179]
v_mfma_f32_16x16x32_bf16 v[180:183], a[216:219], v[100:103], v[180:183]
v_mfma_f32_16x16x32_bf16 v[176:179], a[204:207], v[132:135], v[176:179]
v_mfma_f32_16x16x32_bf16 v[180:183], a[220:223], v[132:135], v[180:183]
v_mfma_f32_16x16x32_bf16 v[184:187], a[224:227], v[36:39], 0
v_mfma_f32_16x16x32_bf16 v[188:191], a[240:243], v[36:39], 0
v_mfma_f32_16x16x32_bf16 v[184:187], a[228:231], v[68:71], v[184:187]
v_mfma_f32_16x16x32_bf16 v[188:191], a[244:247], v[68:71], v[188:191]
v_mfma_f32_16x16x32_bf16 v[184:187], a[232:235], v[100:103], v[184:187]
v_mfma_f32_16x16x32_bf16 v[188:191], a[248:251], v[100:103], v[188:191]
v_mfma_f32_16x16x32_bf16 v[184:187], a[236:239], v[132:135], v[184:187]
v_mfma_f32_16x16x32_bf16 v[188:191], a[252:255], v[132:135], v[188:191]
s_nop 7
v_max3_f32 v88, v176, v177, v178
v_max3_f32 v90, v179, v180, v181
v_max3_f32 v89, v184, v185, v186
v_max3_f32 v91, v187, v188, v189
v_max3_f32 v88, v88, v182, v183
v_max3_f32 v89, v89, v190, v191
v_max3_f32 v88, v88, v89, v90
v_max_f32 v88, v88, v91
v_mov_b32 v90, v88
s_nop 1
v_permlane32_swap_b32 v88, v90
s_nop 0
v_max_f32 v88, v88, v90
v_mov_b32 v90, v88
s_nop 1
v_permlane16_swap_b32 v88, v90
s_nop 0
v_max_f32 v88, v88, v90
v_ceil_f32 v88, v88
v_add_f32 v88, s55, v88
v_max_f32 v88, v201, v88
v_sub_f32 v90, v201, v88
v_mov_b32 v201, v88
v_cvt_i32_f32 v90, v90
v_ldexp_f32 v124, v124, v90
v_accvgpr_read_b32 v72, a32
v_ldexp_f32 v72, v72, v90
v_accvgpr_write_b32 a32, v72
v_accvgpr_read_b32 v73, a33
v_ldexp_f32 v73, v73, v90
v_accvgpr_write_b32 a33, v73
v_accvgpr_read_b32 v74, a34
)"
R"(v_ldexp_f32 v74, v74, v90
v_accvgpr_write_b32 a34, v74
v_accvgpr_read_b32 v75, a35
v_ldexp_f32 v75, v75, v90
v_accvgpr_write_b32 a35, v75
v_accvgpr_read_b32 v72, a36
v_ldexp_f32 v72, v72, v90
v_accvgpr_write_b32 a36, v72
v_accvgpr_read_b32 v73, a37
v_ldexp_f32 v73, v73, v90
v_accvgpr_write_b32 a37, v73
v_accvgpr_read_b32 v74, a38
v_ldexp_f32 v74, v74, v90
v_accvgpr_write_b32 a38, v74
v_accvgpr_read_b32 v75, a39
v_ldexp_f32 v75, v75, v90
v_accvgpr_write_b32 a39, v75
v_accvgpr_read_b32 v72, a40
v_ldexp_f32 v72, v72, v90
v_accvgpr_write_b32 a40, v72
v_accvgpr_read_b32 v73, a41
v_ldexp_f32 v73, v73, v90
v_accvgpr_write_b32 a41, v73
v_accvgpr_read_b32 v74, a42
v_ldexp_f32 v74, v74, v90
v_accvgpr_write_b32 a42, v74
v_accvgpr_read_b32 v75, a43
v_ldexp_f32 v75, v75, v90
v_accvgpr_write_b32 a43, v75
v_accvgpr_read_b32 v72, a44
v_ldexp_f32 v72, v72, v90
v_accvgpr_write_b32 a44, v72
v_accvgpr_read_b32 v73, a45
v_ldexp_f32 v73, v73, v90
v_accvgpr_write_b32 a45, v73
v_accvgpr_read_b32 v74, a46
v_ldexp_f32 v74, v74, v90
v_accvgpr_write_b32 a46, v74
v_accvgpr_read_b32 v75, a47
v_ldexp_f32 v75, v75, v90
v_accvgpr_write_b32 a47, v75
v_accvgpr_read_b32 v72, a48
v_ldexp_f32 v72, v72, v90
v_accvgpr_write_b32 a48, v72
v_accvgpr_read_b32 v73, a49
v_ldexp_f32 v73, v73, v90
v_accvgpr_write_b32 a49, v73
v_accvgpr_read_b32 v74, a50
v_ldexp_f32 v74, v74, v90
v_accvgpr_write_b32 a50, v74
v_accvgpr_read_b32 v75, a51
v_ldexp_f32 v75, v75, v90
v_accvgpr_write_b32 a51, v75
v_accvgpr_read_b32 v72, a52
v_ldexp_f32 v72, v72, v90
v_accvgpr_write_b32 a52, v72
v_accvgpr_read_b32 v73, a53
v_ldexp_f32 v73, v73, v90
v_accvgpr_write_b32 a53, v73
v_accvgpr_read_b32 v74, a54
v_ldexp_f32 v74, v74, v90
v_accvgpr_write_b32 a54, v74
v_accvgpr_read_b32 v75, a55
v_ldexp_f32 v75, v75, v90
v_accvgpr_write_b32 a55, v75
v_accvgpr_read_b32 v72, a56
v_ldexp_f32 v72, v72, v90
v_accvgpr_write_b32 a56, v72
v_accvgpr_read_b32 v73, a57
v_ldexp_f32 v73, v73, v90
v_accvgpr_write_b32 a57, v73
v_accvgpr_read_b32 v74, a58
v_ldexp_f32 v74, v74, v90
v_accvgpr_write_b32 a58, v74
v_accvgpr_read_b32 v75, a59
v_ldexp_f32 v75, v75, v90
v_accvgpr_write_b32 a59, v75
v_accvgpr_read_b32 v72, a60
v_ldexp_f32 v72, v72, v90
v_accvgpr_write_b32 a60, v72
v_accvgpr_read_b32 v73, a61
v_ldexp_f32 v73, v73, v90
v_accvgpr_write_b32 a61, v73
v_accvgpr_read_b32 v74, a62
v_ldexp_f32 v74, v74, v90
v_accvgpr_write_b32 a62, v74
v_accvgpr_read_b32 v75, a63
v_ldexp_f32 v75, v75, v90
v_accvgpr_write_b32 a63, v75
v_sub_f32 v176, v176, v88
v_sub_f32 v177, v177, v88
v_sub_f32 v178, v178, v88
v_sub_f32 v179, v179, v88
v_sub_f32 v180, v180, v88
v_sub_f32 v181, v181, v88
v_sub_f32 v182, v182, v88
v_sub_f32 v183, v183, v88
v_sub_f32 v184, v184, v88
v_sub_f32 v185, v185, v88
v_sub_f32 v186, v186, v88
v_sub_f32 v187, v187, v88
v_sub_f32 v188, v188, v88
v_sub_f32 v189, v189, v88
v_sub_f32 v190, v190, v88
v_sub_f32 v191, v191, v88
v_exp_f32 v176, v176
v_exp_f32 v177, v177
v_exp_f32 v178, v178
v_exp_f32 v179, v179
v_exp_f32 v180, v180
v_exp_f32 v181, v181
v_exp_f32 v182, v182
v_exp_f32 v183, v183
v_exp_f32 v184, v184
v_exp_f32 v185, v185
v_exp_f32 v186, v186
v_exp_f32 v187, v187
v_exp_f32 v188, v188
v_exp_f32 v189, v189
v_exp_f32 v190, v190
v_exp_f32 v191, v191
s_nop 0
v_cvt_pk_bf16_f32 v56, v176, v177
v_cvt_pk_bf16_f32 v57, v178, v179
v_cvt_pk_bf16_f32 v58, v180, v181
v_cvt_pk_bf16_f32 v59, v182, v183
v_cvt_pk_bf16_f32 v60, v184, v185
v_cvt_pk_bf16_f32 v61, v186, v187
v_cvt_pk_bf16_f32 v62, v188, v189
v_cvt_pk_bf16_f32 v63, v190, v191
s_nop 1
v_mfma_f32_16x16x32_bf16 v[124:127], v[136:139], v[56:59], v[124:127]
v_mfma_f32_16x16x32_bf16 v[124:127], v[136:139], v[60:63], v[124:127]
v_mfma_f32_16x16x32_bf16 a[32:35], a[128:131], v[56:59], a[32:35]
v_mfma_f32_16x16x32_bf16 a[36:39], a[132:135], v[56:59], a[36:39]
v_mfma_f32_16x16x32_bf16 a[40:43], a[136:139], v[56:59], a[40:43]
v_mfma_f32_16x16x32_bf16 a[44:47], a[140:143], v[56:59], a[44:47]
v_mfma_f32_16x16x32_bf16 a[48:51], a[144:147], v[56:59], a[48:51]
v_mfma_f32_16x16x32_bf16 a[52:55], a[148:151], v[56:59], a[52:55]
v_mfma_f32_16x16x32_bf16 a[56:59], a[152:155], v[56:59], a[56:59]
v_mfma_f32_16x16x32_bf16 a[60:63], a[156:159], v[56:59], a[60:63]
v_mfma_f32_16x16x32_bf16 a[32:35], a[160:163], v[60:63], a[32:35]
v_mfma_f32_16x16x32_bf16 a[36:39], a[164:167], v[60:63], a[36:39]
v_mfma_f32_16x16x32_bf16 a[40:43], a[168:171], v[60:63], a[40:43]
v_mfma_f32_16x16x32_bf16 a[44:47], a[172:175], v[60:63], a[44:47]
v_mfma_f32_16x16x32_bf16 a[48:51], a[176:179], v[60:63], a[48:51]
v_mfma_f32_16x16x32_bf16 a[52:55], a[180:183], v[60:63], a[52:55]
v_mfma_f32_16x16x32_bf16 a[56:59], a[184:187], v[60:63], a[56:59]
v_mfma_f32_16x16x32_bf16 a[60:63], a[188:191], v[60:63], a[60:63]
v_mfma_f32_16x16x32_bf16 v[160:163], a[192:195], v[48:51], 0
v_mfma_f32_16x16x32_bf16 v[164:167], a[208:211], v[48:51], 0
v_mfma_f32_16x16x32_bf16 v[160:163], a[196:199], v[80:83], v[160:163]
v_mfma_f32_16x16x32_bf16 v[164:167], a[212:215], v[80:83], v[164:167]
v_mfma_f32_16x16x32_bf16 v[160:163], a[200:203], v[112:115], v[160:163]
v_mfma_f32_16x16x32_bf16 v[164:167], a[216:219], v[112:115], v[164:167]
v_mfma_f32_16x16x32_bf16 v[160:163], a[204:207], v[144:147], v[160:163]
v_mfma_f32_16x16x32_bf16 v[164:167], a[220:223], v[144:147], v[164:167]
v_mfma_f32_16x16x32_bf16 v[168:171], a[224:227], v[48:51], 0
v_mfma_f32_16x16x32_bf16 v[172:175], a[240:243], v[48:51], 0
v_mfma_f32_16x16x32_bf16 v[168:171], a[228:231], v[80:83], v[168:171]
v_mfma_f32_16x16x32_bf16 v[172:175], a[244:247], v[80:83], v[172:175]
v_mfma_f32_16x16x32_bf16 v[168:171], a[232:235], v[112:115], v[168:171]
v_mfma_f32_16x16x32_bf16 v[172:175], a[248:251], v[112:115], v[172:175]
v_mfma_f32_16x16x32_bf16 v[168:171], a[236:239], v[144:147], v[168:171]
v_mfma_f32_16x16x32_bf16 v[172:175], a[252:255], v[144:147], v[172:175]
s_nop 7
v_max3_f32 v88, v160, v161, v162
v_max3_f32 v90, v163, v164, v165
v_max3_f32 v89, v168, v169, v170
v_max3_f32 v91, v171, v172, v173
v_max3_f32 v88, v88, v166, v167
v_max3_f32 v89, v89, v174, v175
v_max3_f32 v88, v88, v89, v90
v_max_f32 v88, v88, v91
v_mov_b32 v90, v88
s_nop 1
v_permlane32_swap_b32 v88, v90
s_nop 0
v_max_f32 v88, v88, v90
v_mov_b32 v90, v88
s_nop 1
v_permlane16_swap_b32 v88, v90
s_nop 0
v_max_f32 v88, v88, v90
v_ceil_f32 v88, v88
v_add_f32 v88, s55, v88
v_max_f32 v88, v202, v88
v_sub_f32 v90, v202, v88
v_mov_b32 v202, v88
v_cvt_i32_f32 v90, v90
v_ldexp_f32 v92, v92, v90
v_accvgpr_read_b32 v72, a64
v_ldexp_f32 v72, v72, v90
v_accvgpr_write_b32 a64, v72
v_accvgpr_read_b32 v73, a65
v_ldexp_f32 v73, v73, v90
v_accvgpr_write_b32 a65, v73
v_accvgpr_read_b32 v74, a66
v_ldexp_f32 v74, v74, v90
v_accvgpr_write_b32 a66, v74
)"
R"(v_accvgpr_read_b32 v75, a67
v_ldexp_f32 v75, v75, v90
v_accvgpr_write_b32 a67, v75
v_accvgpr_read_b32 v72, a68
v_ldexp_f32 v72, v72, v90
v_accvgpr_write_b32 a68, v72
v_accvgpr_read_b32 v73, a69
v_ldexp_f32 v73, v73, v90
v_accvgpr_write_b32 a69, v73
v_accvgpr_read_b32 v74, a70
v_ldexp_f32 v74, v74, v90
v_accvgpr_write_b32 a70, v74
v_accvgpr_read_b32 v75, a71
v_ldexp_f32 v75, v75, v90
v_accvgpr_write_b32 a71, v75
v_accvgpr_read_b32 v72, a72
v_ldexp_f32 v72, v72, v90
v_accvgpr_write_b32 a72, v72
v_accvgpr_read_b32 v73, a73
v_ldexp_f32 v73, v73, v90
v_accvgpr_write_b32 a73, v73
v_accvgpr_read_b32 v74, a74
v_ldexp_f32 v74, v74, v90
v_accvgpr_write_b32 a74, v74
v_accvgpr_read_b32 v75, a75
v_ldexp_f32 v75, v75, v90
v_accvgpr_write_b32 a75, v75
v_accvgpr_read_b32 v72, a76
v_ldexp_f32 v72, v72, v90
v_accvgpr_write_b32 a76, v72
v_accvgpr_read_b32 v73, a77
v_ldexp_f32 v73, v73, v90
v_accvgpr_write_b32 a77, v73
v_accvgpr_read_b32 v74, a78
v_ldexp_f32 v74, v74, v90
v_accvgpr_write_b32 a78, v74
v_accvgpr_read_b32 v75, a79
v_ldexp_f32 v75, v75, v90
v_accvgpr_write_b32 a79, v75
v_accvgpr_read_b32 v72, a80
v_ldexp_f32 v72, v72, v90
v_accvgpr_write_b32 a80, v72
v_accvgpr_read_b32 v73, a81
v_ldexp_f32 v73, v73, v90
v_accvgpr_write_b32 a81, v73
v_accvgpr_read_b32 v74, a82
v_ldexp_f32 v74, v74, v90
v_accvgpr_write_b32 a82, v74
v_accvgpr_read_b32 v75, a83
v_ldexp_f32 v75, v75, v90
v_accvgpr_write_b32 a83, v75
v_accvgpr_read_b32 v72, a84
v_ldexp_f32 v72, v72, v90
v_accvgpr_write_b32 a84, v72
v_accvgpr_read_b32 v73, a85
v_ldexp_f32 v73, v73, v90
v_accvgpr_write_b32 a85, v73
v_accvgpr_read_b32 v74, a86
v_ldexp_f32 v74, v74, v90
v_accvgpr_write_b32 a86, v74
v_accvgpr_read_b32 v75, a87
v_ldexp_f32 v75, v75, v90
v_accvgpr_write_b32 a87, v75
v_accvgpr_read_b32 v72, a88
v_ldexp_f32 v72, v72, v90
v_accvgpr_write_b32 a88, v72
v_accvgpr_read_b32 v73, a89
v_ldexp_f32 v73, v73, v90
v_accvgpr_write_b32 a89, v73
v_accvgpr_read_b32 v74, a90
v_ldexp_f32 v74, v74, v90
v_accvgpr_write_b32 a90, v74
v_accvgpr_read_b32 v75, a91
v_ldexp_f32 v75, v75, v90
v_accvgpr_write_b32 a91, v75
v_accvgpr_read_b32 v72, a92
v_ldexp_f32 v72, v72, v90
v_accvgpr_write_b32 a92, v72
v_accvgpr_read_b32 v73, a93
v_ldexp_f32 v73, v73, v90
v_accvgpr_write_b32 a93, v73
v_accvgpr_read_b32 v74, a94
v_ldexp_f32 v74, v74, v90
v_accvgpr_write_b32 a94, v74
v_accvgpr_read_b32 v75, a95
v_ldexp_f32 v75, v75, v90
v_accvgpr_write_b32 a95, v75
v_sub_f32 v160, v160, v88
v_sub_f32 v161, v161, v88
v_sub_f32 v162, v162, v88
v_sub_f32 v163, v163, v88
v_sub_f32 v164, v164, v88
v_sub_f32 v165, v165, v88
v_sub_f32 v166, v166, v88
v_sub_f32 v167, v167, v88
v_sub_f32 v168, v168, v88
v_sub_f32 v169, v169, v88
v_sub_f32 v170, v170, v88
v_sub_f32 v171, v171, v88
v_sub_f32 v172, v172, v88
v_sub_f32 v173, v173, v88
v_sub_f32 v174, v174, v88
v_sub_f32 v175, v175, v88
v_exp_f32 v160, v160
v_exp_f32 v161, v161
v_exp_f32 v162, v162
v_exp_f32 v163, v163
v_exp_f32 v164, v164
v_exp_f32 v165, v165
v_exp_f32 v166, v166
v_exp_f32 v167, v167
v_exp_f32 v168, v168
v_exp_f32 v169, v169
v_exp_f32 v170, v170
v_exp_f32 v171, v171
v_exp_f32 v172, v172
v_exp_f32 v173, v173
v_exp_f32 v174, v174
v_exp_f32 v175, v175
s_nop 0
v_cvt_pk_bf16_f32 v40, v160, v161
v_cvt_pk_bf16_f32 v41, v162, v163
v_cvt_pk_bf16_f32 v42, v164, v165
v_cvt_pk_bf16_f32 v43, v166, v167
v_cvt_pk_bf16_f32 v44, v168, v169
v_cvt_pk_bf16_f32 v45, v170, v171
v_cvt_pk_bf16_f32 v46, v172, v173
v_cvt_pk_bf16_f32 v47, v174, v175
s_nop 1
v_mfma_f32_16x16x32_bf16 v[92:95], v[136:139], v[40:43], v[92:95]
v_mfma_f32_16x16x32_bf16 v[92:95], v[136:139], v[44:47], v[92:95]
v_mfma_f32_16x16x32_bf16 a[64:67], a[128:131], v[40:43], a[64:67]
v_mfma_f32_16x16x32_bf16 a[68:71], a[132:135], v[40:43], a[68:71]
v_mfma_f32_16x16x32_bf16 a[72:75], a[136:139], v[40:43], a[72:75]
v_mfma_f32_16x16x32_bf16 a[76:79], a[140:143], v[40:43], a[76:79]
v_mfma_f32_16x16x32_bf16 a[80:83], a[144:147], v[40:43], a[80:83]
v_mfma_f32_16x16x32_bf16 a[84:87], a[148:151], v[40:43], a[84:87]
v_mfma_f32_16x16x32_bf16 a[88:91], a[152:155], v[40:43], a[88:91]
v_mfma_f32_16x16x32_bf16 a[92:95], a[156:159], v[40:43], a[92:95]
v_mfma_f32_16x16x32_bf16 a[64:67], a[160:163], v[44:47], a[64:67]
v_mfma_f32_16x16x32_bf16 a[68:71], a[164:167], v[44:47], a[68:71]
v_mfma_f32_16x16x32_bf16 a[72:75], a[168:171], v[44:47], a[72:75]
v_mfma_f32_16x16x32_bf16 a[76:79], a[172:175], v[44:47], a[76:79]
v_mfma_f32_16x16x32_bf16 a[80:83], a[176:179], v[44:47], a[80:83]
v_mfma_f32_16x16x32_bf16 a[84:87], a[180:183], v[44:47], a[84:87]
v_mfma_f32_16x16x32_bf16 a[88:91], a[184:187], v[44:47], a[88:91]
v_mfma_f32_16x16x32_bf16 a[92:95], a[188:191], v[44:47], a[92:95]
v_mfma_f32_16x16x32_bf16 v[176:179], a[192:195], v[52:55], 0
v_mfma_f32_16x16x32_bf16 v[180:183], a[208:211], v[52:55], 0
v_mfma_f32_16x16x32_bf16 v[176:179], a[196:199], v[84:87], v[176:179]
v_mfma_f32_16x16x32_bf16 v[180:183], a[212:215], v[84:87], v[180:183]
v_mfma_f32_16x16x32_bf16 v[176:179], a[200:203], v[116:119], v[176:179]
v_mfma_f32_16x16x32_bf16 v[180:183], a[216:219], v[116:119], v[180:183]
v_mfma_f32_16x16x32_bf16 v[176:179], a[204:207], v[148:151], v[176:179]
v_mfma_f32_16x16x32_bf16 v[180:183], a[220:223], v[148:151], v[180:183]
v_mfma_f32_16x16x32_bf16 v[184:187], a[224:227], v[52:55], 0
v_mfma_f32_16x16x32_bf16 v[188:191], a[240:243], v[52:55], 0
v_mfma_f32_16x16x32_bf16 v[184:187], a[228:231], v[84:87], v[184:187]
v_mfma_f32_16x16x32_bf16 v[188:191], a[244:247], v[84:87], v[188:191]
v_mfma_f32_16x16x32_bf16 v[184:187], a[232:235], v[116:119], v[184:187]
v_mfma_f32_16x16x32_bf16 v[188:191], a[248:251], v[116:119], v[188:191]
v_mfma_f32_16x16x32_bf16 v[184:187], a[236:239], v[148:151], v[184:187]
v_mfma_f32_16x16x32_bf16 v[188:191], a[252:255], v[148:151], v[188:191]
s_nop 7
v_max3_f32 v88, v176, v177, v178
v_max3_f32 v90, v179, v180, v181
v_max3_f32 v89, v184, v185, v186
v_max3_f32 v91, v187, v188, v189
v_max3_f32 v88, v88, v182, v183
v_max3_f32 v89, v89, v190, v191
v_max3_f32 v88, v88, v89, v90
v_max_f32 v88, v88, v91
v_mov_b32 v90, v88
s_nop 1
v_permlane32_swap_b32 v88, v90
s_nop 0
v_max_f32 v88, v88, v90
v_mov_b32 v90, v88
s_nop 1
v_permlane16_swap_b32 v88, v90
s_nop 0
v_max_f32 v88, v88, v90
v_ceil_f32 v88, v88
v_add_f32 v88, s55, v88
v_max_f32 v88, v203, v88
v_sub_f32 v90, v203, v88
v_mov_b32 v203, v88
v_cvt_i32_f32 v90, v90
v_ldexp_f32 v76, v76, v90
v_accvgpr_read_b32 v72, a96
v_ldexp_f32 v72, v72, v90
v_accvgpr_write_b32 a96, v72
v_accvgpr_read_b32 v73, a97
v_ldexp_f32 v73, v73, v90
v_accvgpr_write_b32 a97, v73
v_accvgpr_read_b32 v74, a98
v_ldexp_f32 v74, v74, v90
v_accvgpr_write_b32 a98, v74
v_accvgpr_read_b32 v75, a99
v_ldexp_f32 v75, v75, v90
)"
R"(v_accvgpr_write_b32 a99, v75
v_accvgpr_read_b32 v72, a100
v_ldexp_f32 v72, v72, v90
v_accvgpr_write_b32 a100, v72
v_accvgpr_read_b32 v73, a101
v_ldexp_f32 v73, v73, v90
v_accvgpr_write_b32 a101, v73
v_accvgpr_read_b32 v74, a102
v_ldexp_f32 v74, v74, v90
v_accvgpr_write_b32 a102, v74
v_accvgpr_read_b32 v75, a103
v_ldexp_f32 v75, v75, v90
v_accvgpr_write_b32 a103, v75
v_accvgpr_read_b32 v72, a104
v_ldexp_f32 v72, v72, v90
v_accvgpr_write_b32 a104, v72
v_accvgpr_read_b32 v73, a105
v_ldexp_f32 v73, v73, v90
v_accvgpr_write_b32 a105, v73
v_accvgpr_read_b32 v74, a106
v_ldexp_f32 v74, v74, v90
v_accvgpr_write_b32 a106, v74
v_accvgpr_read_b32 v75, a107
v_ldexp_f32 v75, v75, v90
v_accvgpr_write_b32 a107, v75
v_accvgpr_read_b32 v72, a108
v_ldexp_f32 v72, v72, v90
v_accvgpr_write_b32 a108, v72
v_accvgpr_read_b32 v73, a109
v_ldexp_f32 v73, v73, v90
v_accvgpr_write_b32 a109, v73
v_accvgpr_read_b32 v74, a110
v_ldexp_f32 v74, v74, v90
v_accvgpr_write_b32 a110, v74
v_accvgpr_read_b32 v75, a111
v_ldexp_f32 v75, v75, v90
v_accvgpr_write_b32 a111, v75
v_accvgpr_read_b32 v72, a112
v_ldexp_f32 v72, v72, v90
v_accvgpr_write_b32 a112, v72
v_accvgpr_read_b32 v73, a113
v_ldexp_f32 v73, v73, v90
v_accvgpr_write_b32 a113, v73
v_accvgpr_read_b32 v74, a114
v_ldexp_f32 v74, v74, v90
v_accvgpr_write_b32 a114, v74
v_accvgpr_read_b32 v75, a115
v_ldexp_f32 v75, v75, v90
v_accvgpr_write_b32 a115, v75
v_accvgpr_read_b32 v72, a116
v_ldexp_f32 v72, v72, v90
v_accvgpr_write_b32 a116, v72
v_accvgpr_read_b32 v73, a117
v_ldexp_f32 v73, v73, v90
v_accvgpr_write_b32 a117, v73
v_accvgpr_read_b32 v74, a118
v_ldexp_f32 v74, v74, v90
v_accvgpr_write_b32 a118, v74
v_accvgpr_read_b32 v75, a119
v_ldexp_f32 v75, v75, v90
v_accvgpr_write_b32 a119, v75
v_accvgpr_read_b32 v72, a120
v_ldexp_f32 v72, v72, v90
v_accvgpr_write_b32 a120, v72
v_accvgpr_read_b32 v73, a121
v_ldexp_f32 v73, v73, v90
v_accvgpr_write_b32 a121, v73
v_accvgpr_read_b32 v74, a122
v_ldexp_f32 v74, v74, v90
v_accvgpr_write_b32 a122, v74
v_accvgpr_read_b32 v75, a123
v_ldexp_f32 v75, v75, v90
v_accvgpr_write_b32 a123, v75
v_accvgpr_read_b32 v72, a124
v_ldexp_f32 v72, v72, v90
v_accvgpr_write_b32 a124, v72
v_accvgpr_read_b32 v73, a125
v_ldexp_f32 v73, v73, v90
v_accvgpr_write_b32 a125, v73
v_accvgpr_read_b32 v74, a126
v_ldexp_f32 v74, v74, v90
v_accvgpr_write_b32 a126, v74
v_accvgpr_read_b32 v75, a127
v_ldexp_f32 v75, v75, v90
v_accvgpr_write_b32 a127, v75
v_sub_f32 v176, v176, v88
v_sub_f32 v177, v177, v88
v_sub_f32 v178, v178, v88
v_sub_f32 v179, v179, v88
v_sub_f32 v180, v180, v88
v_sub_f32 v181, v181, v88
v_sub_f32 v182, v182, v88
v_sub_f32 v183, v183, v88
v_sub_f32 v184, v184, v88
v_sub_f32 v185, v185, v88
v_sub_f32 v186, v186, v88
v_sub_f32 v187, v187, v88
v_sub_f32 v188, v188, v88
v_sub_f32 v189, v189, v88
v_sub_f32 v190, v190, v88
v_sub_f32 v191, v191, v88
v_exp_f32 v176, v176
v_exp_f32 v177, v177
v_exp_f32 v178, v178
v_exp_f32 v179, v179
v_exp_f32 v180, v180
v_exp_f32 v181, v181
v_exp_f32 v182, v182
v_exp_f32 v183, v183
v_exp_f32 v184, v184
v_exp_f32 v185, v185
v_exp_f32 v186, v186
v_exp_f32 v187, v187
v_exp_f32 v188, v188
v_exp_f32 v189, v189
v_exp_f32 v190, v190
v_exp_f32 v191, v191
s_nop 0
v_cvt_pk_bf16_f32 v56, v176, v177
v_cvt_pk_bf16_f32 v57, v178, v179
v_cvt_pk_bf16_f32 v58, v180, v181
v_cvt_pk_bf16_f32 v59, v182, v183
v_cvt_pk_bf16_f32 v60, v184, v185
v_cvt_pk_bf16_f32 v61, v186, v187
v_cvt_pk_bf16_f32 v62, v188, v189
v_cvt_pk_bf16_f32 v63, v190, v191
s_nop 1
v_mfma_f32_16x16x32_bf16 v[76:79], v[136:139], v[56:59], v[76:79]
v_mfma_f32_16x16x32_bf16 v[76:79], v[136:139], v[60:63], v[76:79]
v_mfma_f32_16x16x32_bf16 a[96:99], a[128:131], v[56:59], a[96:99]
v_mfma_f32_16x16x32_bf16 a[100:103], a[132:135], v[56:59], a[100:103]
v_mfma_f32_16x16x32_bf16 a[104:107], a[136:139], v[56:59], a[104:107]
v_mfma_f32_16x16x32_bf16 a[108:111], a[140:143], v[56:59], a[108:111]
v_mfma_f32_16x16x32_bf16 a[112:115], a[144:147], v[56:59], a[112:115]
v_mfma_f32_16x16x32_bf16 a[116:119], a[148:151], v[56:59], a[116:119]
v_mfma_f32_16x16x32_bf16 a[120:123], a[152:155], v[56:59], a[120:123]
v_mfma_f32_16x16x32_bf16 a[124:127], a[156:159], v[56:59], a[124:127]
v_mfma_f32_16x16x32_bf16 a[96:99], a[160:163], v[60:63], a[96:99]
v_mfma_f32_16x16x32_bf16 a[100:103], a[164:167], v[60:63], a[100:103]
v_mfma_f32_16x16x32_bf16 a[104:107], a[168:171], v[60:63], a[104:107]
v_mfma_f32_16x16x32_bf16 a[108:111], a[172:175], v[60:63], a[108:111]
v_mfma_f32_16x16x32_bf16 a[112:115], a[176:179], v[60:63], a[112:115]
v_mfma_f32_16x16x32_bf16 a[116:119], a[180:183], v[60:63], a[116:119]
v_mfma_f32_16x16x32_bf16 a[120:123], a[184:187], v[60:63], a[120:123]
v_mfma_f32_16x16x32_bf16 a[124:127], a[188:191], v[60:63], a[124:127]
s_sub_u32 s58, s58, 1
s_cbranch_scc0 L_p2_%=
s_nop 7
v_rcp_f32 v192, v120
v_mul_f32 v120, 0x76800000, v120
v_accvgpr_read_b32 v160, a0
v_accvgpr_read_b32 v161, a1
v_accvgpr_read_b32 v162, a2
v_accvgpr_read_b32 v163, a3
v_accvgpr_read_b32 v164, a4
v_accvgpr_read_b32 v165, a5
v_accvgpr_read_b32 v166, a6
v_accvgpr_read_b32 v167, a7
v_accvgpr_read_b32 v168, a8
v_accvgpr_read_b32 v169, a9
v_accvgpr_read_b32 v170, a10
v_accvgpr_read_b32 v171, a11
v_accvgpr_read_b32 v172, a12
v_accvgpr_read_b32 v173, a13
v_accvgpr_read_b32 v174, a14
v_accvgpr_read_b32 v175, a15
v_pk_mul_f32 v[160:161], v[160:161], v[192:193] op_sel_hi:[1,0]
v_pk_mul_f32 v[162:163], v[162:163], v[192:193] op_sel_hi:[1,0]
v_pk_mul_f32 v[164:165], v[164:165], v[192:193] op_sel_hi:[1,0]
v_pk_mul_f32 v[166:167], v[166:167], v[192:193] op_sel_hi:[1,0]
v_pk_mul_f32 v[168:169], v[168:169], v[192:193] op_sel_hi:[1,0]
v_pk_mul_f32 v[170:171], v[170:171], v[192:193] op_sel_hi:[1,0]
v_pk_mul_f32 v[172:173], v[172:173], v[192:193] op_sel_hi:[1,0]
v_pk_mul_f32 v[174:175], v[174:175], v[192:193] op_sel_hi:[1,0]
v_pk_mul_f32 v[152:153], v[160:161], v[160:161]
v_pk_fma_f32 v[152:153], v[162:163], v[162:163], v[152:153]
v_pk_fma_f32 v[152:153], v[164:165], v[164:165], v[152:153]
v_pk_fma_f32 v[152:153], v[166:167], v[166:167], v[152:153]
v_pk_fma_f32 v[152:153], v[168:169], v[168:169], v[152:153]
v_pk_fma_f32 v[152:153], v[170:171], v[170:171], v[152:153]
v_pk_fma_f32 v[152:153], v[172:173], v[172:173], v[152:153]
v_pk_fma_f32 v[152:153], v[174:175], v[174:175], v[152:153]
v_cvt_pk_bf16_f32 v160, v160, v161
v_cvt_pk_bf16_f32 v161, v162, v163
v_cvt_pk_bf16_f32 v162, v164, v165
v_cvt_pk_bf16_f32 v163, v166, v167
buffer_store_dwordx4 v[160:163], %[vX], %[sWS], s72 offen offset:0 sc1
ds_write_b128 v107, v[160:163] offset:0
v_cvt_pk_bf16_f32 v168, v168, v169
v_cvt_pk_bf16_f32 v169, v170, v171
v_cvt_pk_bf16_f32 v170, v172, v173
v_cvt_pk_bf16_f32 v171, v174, v175
buffer_store_dwordx4 v[168:171], %[vX], %[sWS], s73 offen offset:0 sc1
ds_write_b128 v107, v[168:171] offset:4096
v_accvgpr_read_b32 v176, a16
v_accvgpr_read_b32 v177, a17
v_accvgpr_read_b32 v178, a18
v_accvgpr_read_b32 v179, a19
v_accvgpr_read_b32 v180, a20
v_accvgpr_read_b32 v181, a21
)"
R"(v_accvgpr_read_b32 v182, a22
v_accvgpr_read_b32 v183, a23
v_accvgpr_read_b32 v184, a24
v_accvgpr_read_b32 v185, a25
v_accvgpr_read_b32 v186, a26
v_accvgpr_read_b32 v187, a27
v_accvgpr_read_b32 v188, a28
v_accvgpr_read_b32 v189, a29
v_accvgpr_read_b32 v190, a30
v_accvgpr_read_b32 v191, a31
v_pk_mul_f32 v[176:177], v[176:177], v[192:193] op_sel_hi:[1,0]
v_pk_mul_f32 v[178:179], v[178:179], v[192:193] op_sel_hi:[1,0]
v_pk_mul_f32 v[180:181], v[180:181], v[192:193] op_sel_hi:[1,0]
v_pk_mul_f32 v[182:183], v[182:183], v[192:193] op_sel_hi:[1,0]
v_pk_mul_f32 v[184:185], v[184:185], v[192:193] op_sel_hi:[1,0]
v_pk_mul_f32 v[186:187], v[186:187], v[192:193] op_sel_hi:[1,0]
v_pk_mul_f32 v[188:189], v[188:189], v[192:193] op_sel_hi:[1,0]
v_pk_mul_f32 v[190:191], v[190:191], v[192:193] op_sel_hi:[1,0]
v_pk_fma_f32 v[152:153], v[176:177], v[176:177], v[152:153]
v_pk_fma_f32 v[152:153], v[178:179], v[178:179], v[152:153]
v_pk_fma_f32 v[152:153], v[180:181], v[180:181], v[152:153]
v_pk_fma_f32 v[152:153], v[182:183], v[182:183], v[152:153]
v_pk_fma_f32 v[152:153], v[184:185], v[184:185], v[152:153]
v_pk_fma_f32 v[152:153], v[186:187], v[186:187], v[152:153]
v_pk_fma_f32 v[152:153], v[188:189], v[188:189], v[152:153]
v_pk_fma_f32 v[152:153], v[190:191], v[190:191], v[152:153]
v_cvt_pk_bf16_f32 v176, v176, v177
v_cvt_pk_bf16_f32 v177, v178, v179
v_cvt_pk_bf16_f32 v178, v180, v181
v_cvt_pk_bf16_f32 v179, v182, v183
buffer_store_dwordx4 v[176:179], %[vX], %[sWS], s74 offen offset:0 sc1
ds_write_b128 v107, v[176:179] offset:8192
v_cvt_pk_bf16_f32 v184, v184, v185
v_cvt_pk_bf16_f32 v185, v186, v187
v_cvt_pk_bf16_f32 v186, v188, v189
v_cvt_pk_bf16_f32 v187, v190, v191
buffer_store_dwordx4 v[184:187], %[vX], %[sWS], s75 offen offset:0 sc1
ds_write_b128 v107, v[184:187] offset:12288
v_rcp_f32_e64 v194, v124
v_mul_f32 v124, 0x76800000, v124
v_accvgpr_read_b32 v208, a32
v_accvgpr_read_b32 v209, a33
v_accvgpr_read_b32 v210, a34
v_accvgpr_read_b32 v211, a35
v_accvgpr_read_b32 v212, a36
v_accvgpr_read_b32 v213, a37
v_accvgpr_read_b32 v214, a38
v_accvgpr_read_b32 v215, a39
v_accvgpr_read_b32 v216, a40
v_accvgpr_read_b32 v217, a41
v_accvgpr_read_b32 v218, a42
v_accvgpr_read_b32 v219, a43
v_accvgpr_read_b32 v220, a44
v_accvgpr_read_b32 v221, a45
v_accvgpr_read_b32 v222, a46
v_accvgpr_read_b32 v223, a47
v_pk_mul_f32 v[208:209], v[208:209], v[194:195] op_sel_hi:[1,0]
v_pk_mul_f32 v[210:211], v[210:211], v[194:195] op_sel_hi:[1,0]
v_pk_mul_f32 v[212:213], v[212:213], v[194:195] op_sel_hi:[1,0]
v_pk_mul_f32 v[214:215], v[214:215], v[194:195] op_sel_hi:[1,0]
v_pk_mul_f32 v[216:217], v[216:217], v[194:195] op_sel_hi:[1,0]
v_pk_mul_f32 v[218:219], v[218:219], v[194:195] op_sel_hi:[1,0]
v_pk_mul_f32 v[220:221], v[220:221], v[194:195] op_sel_hi:[1,0]
v_pk_mul_f32 v[222:223], v[222:223], v[194:195] op_sel_hi:[1,0]
v_pk_mul_f32 v[154:155], v[208:209], v[208:209]
v_pk_fma_f32 v[154:155], v[210:211], v[210:211], v[154:155]
v_pk_fma_f32 v[154:155], v[212:213], v[212:213], v[154:155]
v_pk_fma_f32 v[154:155], v[214:215], v[214:215], v[154:155]
v_pk_fma_f32 v[154:155], v[216:217], v[216:217], v[154:155]
v_pk_fma_f32 v[154:155], v[218:219], v[218:219], v[154:155]
v_pk_fma_f32 v[154:155], v[220:221], v[220:221], v[154:155]
v_pk_fma_f32 v[154:155], v[222:223], v[222:223], v[154:155]
v_cvt_pk_bf16_f32 v208, v208, v209
v_cvt_pk_bf16_f32 v209, v210, v211
v_cvt_pk_bf16_f32 v210, v212, v213
v_cvt_pk_bf16_f32 v211, v214, v215
buffer_store_dwordx4 v[208:211], %[vX], %[sWS], s72 offen offset:1024 sc1
ds_write_b128 v107, v[208:211] offset:1024
v_cvt_pk_bf16_f32 v216, v216, v217
v_cvt_pk_bf16_f32 v217, v218, v219
v_cvt_pk_bf16_f32 v218, v220, v221
v_cvt_pk_bf16_f32 v219, v222, v223
buffer_store_dwordx4 v[216:219], %[vX], %[sWS], s73 offen offset:1024 sc1
ds_write_b128 v107, v[216:219] offset:5120
v_accvgpr_read_b32 v224, a48
v_accvgpr_read_b32 v225, a49
v_accvgpr_read_b32 v226, a50
v_accvgpr_read_b32 v227, a51
v_accvgpr_read_b32 v228, a52
v_accvgpr_read_b32 v229, a53
v_accvgpr_read_b32 v230, a54
v_accvgpr_read_b32 v231, a55
v_accvgpr_read_b32 v232, a56
v_accvgpr_read_b32 v233, a57
v_accvgpr_read_b32 v234, a58
v_accvgpr_read_b32 v235, a59
v_accvgpr_read_b32 v236, a60
v_accvgpr_read_b32 v237, a61
v_accvgpr_read_b32 v238, a62
v_accvgpr_read_b32 v239, a63
v_pk_mul_f32 v[224:225], v[224:225], v[194:195] op_sel_hi:[1,0]
v_pk_mul_f32 v[226:227], v[226:227], v[194:195] op_sel_hi:[1,0]
v_pk_mul_f32 v[228:229], v[228:229], v[194:195] op_sel_hi:[1,0]
v_pk_mul_f32 v[230:231], v[230:231], v[194:195] op_sel_hi:[1,0]
v_pk_mul_f32 v[232:233], v[232:233], v[194:195] op_sel_hi:[1,0]
v_pk_mul_f32 v[234:235], v[234:235], v[194:195] op_sel_hi:[1,0]
v_pk_mul_f32 v[236:237], v[236:237], v[194:195] op_sel_hi:[1,0]
v_pk_mul_f32 v[238:239], v[238:239], v[194:195] op_sel_hi:[1,0]
v_pk_fma_f32 v[154:155], v[224:225], v[224:225], v[154:155]
v_pk_fma_f32 v[154:155], v[226:227], v[226:227], v[154:155]
v_pk_fma_f32 v[154:155], v[228:229], v[228:229], v[154:155]
v_pk_fma_f32 v[154:155], v[230:231], v[230:231], v[154:155]
v_pk_fma_f32 v[154:155], v[232:233], v[232:233], v[154:155]
v_pk_fma_f32 v[154:155], v[234:235], v[234:235], v[154:155]
v_pk_fma_f32 v[154:155], v[236:237], v[236:237], v[154:155]
v_pk_fma_f32 v[154:155], v[238:239], v[238:239], v[154:155]
v_cvt_pk_bf16_f32 v224, v224, v225
v_cvt_pk_bf16_f32 v225, v226, v227
v_cvt_pk_bf16_f32 v226, v228, v229
v_cvt_pk_bf16_f32 v227, v230, v231
buffer_store_dwordx4 v[224:227], %[vX], %[sWS], s74 offen offset:1024 sc1
ds_write_b128 v107, v[224:227] offset:9216
v_cvt_pk_bf16_f32 v232, v232, v233
v_cvt_pk_bf16_f32 v233, v234, v235
v_cvt_pk_bf16_f32 v234, v236, v237
v_cvt_pk_bf16_f32 v235, v238, v239
buffer_store_dwordx4 v[232:235], %[vX], %[sWS], s75 offen offset:1024 sc1
ds_write_b128 v107, v[232:235] offset:13312
v_rcp_f32_e64 v196, v92
v_mul_f32 v92, 0x76800000, v92
v_accvgpr_read_b32 v160, a64
v_accvgpr_read_b32 v161, a65
v_accvgpr_read_b32 v162, a66
v_accvgpr_read_b32 v163, a67
v_accvgpr_read_b32 v164, a68
v_accvgpr_read_b32 v165, a69
v_accvgpr_read_b32 v166, a70
v_accvgpr_read_b32 v167, a71
v_accvgpr_read_b32 v168, a72
v_accvgpr_read_b32 v169, a73
v_accvgpr_read_b32 v170, a74
v_accvgpr_read_b32 v171, a75
v_accvgpr_read_b32 v172, a76
v_accvgpr_read_b32 v173, a77
v_accvgpr_read_b32 v174, a78
v_accvgpr_read_b32 v175, a79
v_pk_mul_f32 v[160:161], v[160:161], v[196:197] op_sel_hi:[1,0]
v_pk_mul_f32 v[162:163], v[162:163], v[196:197] op_sel_hi:[1,0]
v_pk_mul_f32 v[164:165], v[164:165], v[196:197] op_sel_hi:[1,0]
v_pk_mul_f32 v[166:167], v[166:167], v[196:197] op_sel_hi:[1,0]
v_pk_mul_f32 v[168:169], v[168:169], v[196:197] op_sel_hi:[1,0]
v_pk_mul_f32 v[170:171], v[170:171], v[196:197] op_sel_hi:[1,0]
v_pk_mul_f32 v[172:173], v[172:173], v[196:197] op_sel_hi:[1,0]
v_pk_mul_f32 v[174:175], v[174:175], v[196:197] op_sel_hi:[1,0]
v_pk_mul_f32 v[156:157], v[160:161], v[160:161]
v_pk_fma_f32 v[156:157], v[162:163], v[162:163], v[156:157]
v_pk_fma_f32 v[156:157], v[164:165], v[164:165], v[156:157]
v_pk_fma_f32 v[156:157], v[166:167], v[166:167], v[156:157]
v_pk_fma_f32 v[156:157], v[168:169], v[168:169], v[156:157]
v_pk_fma_f32 v[156:157], v[170:171], v[170:171], v[156:157]
v_pk_fma_f32 v[156:157], v[172:173], v[172:173], v[156:157]
v_pk_fma_f32 v[156:157], v[174:175], v[174:175], v[156:157]
v_cvt_pk_bf16_f32 v160, v160, v161
v_cvt_pk_bf16_f32 v161, v162, v163
v_cvt_pk_bf16_f32 v162, v164, v165
v_cvt_pk_bf16_f32 v163, v166, v167
buffer_store_dwordx4 v[160:163], %[vX], %[sWS], s72 offen offset:2048 sc1
ds_write_b128 v107, v[160:163] offset:2048
v_cvt_pk_bf16_f32 v168, v168, v169
v_cvt_pk_bf16_f32 v169, v170, v171
v_cvt_pk_bf16_f32 v170, v172, v173
v_cvt_pk_bf16_f32 v171, v174, v175
buffer_store_dwordx4 v[168:171], %[vX], %[sWS], s73 offen offset:2048 sc1
ds_write_b128 v107, v[168:171] offset:6144
v_accvgpr_read_b32 v176, a80
v_accvgpr_read_b32 v177, a81
v_accvgpr_read_b32 v178, a82
v_accvgpr_read_b32 v179, a83
v_accvgpr_read_b32 v180, a84
v_accvgpr_read_b32 v181, a85
v_accvgpr_read_b32 v182, a86
v_accvgpr_read_b32 v183, a87
v_accvgpr_read_b32 v184, a88
v_accvgpr_read_b32 v185, a89
v_accvgpr_read_b32 v186, a90
v_accvgpr_read_b32 v187, a91
v_accvgpr_read_b32 v188, a92
v_accvgpr_read_b32 v189, a93
v_accvgpr_read_b32 v190, a94
v_accvgpr_read_b32 v191, a95
v_pk_mul_f32 v[176:177], v[176:177], v[196:197] op_sel_hi:[1,0]
v_pk_mul_f32 v[178:179], v[178:179], v[196:197] op_sel_hi:[1,0]
v_pk_mul_f32 v[180:181], v[180:181], v[196:197] op_sel_hi:[1,0]
v_pk_mul_f32 v[182:183], v[182:183], v[196:197] op_sel_hi:[1,0]
v_pk_mul_f32 v[184:185], v[184:185], v[196:197] op_sel_hi:[1,0]
v_pk_mul_f32 v[186:187], v[186:187], v[196:197] op_sel_hi:[1,0]
v_pk_mul_f32 v[188:189], v[188:189], v[196:197] op_sel_hi:[1,0]
v_pk_mul_f32 v[190:191], v[190:191], v[196:197] op_sel_hi:[1,0]
v_pk_fma_f32 v[156:157], v[176:177], v[176:177], v[156:157]
v_pk_fma_f32 v[156:157], v[178:179], v[178:179], v[156:157]
)"
R"(v_pk_fma_f32 v[156:157], v[180:181], v[180:181], v[156:157]
v_pk_fma_f32 v[156:157], v[182:183], v[182:183], v[156:157]
v_pk_fma_f32 v[156:157], v[184:185], v[184:185], v[156:157]
v_pk_fma_f32 v[156:157], v[186:187], v[186:187], v[156:157]
v_pk_fma_f32 v[156:157], v[188:189], v[188:189], v[156:157]
v_pk_fma_f32 v[156:157], v[190:191], v[190:191], v[156:157]
v_cvt_pk_bf16_f32 v176, v176, v177
v_cvt_pk_bf16_f32 v177, v178, v179
v_cvt_pk_bf16_f32 v178, v180, v181
v_cvt_pk_bf16_f32 v179, v182, v183
buffer_store_dwordx4 v[176:179], %[vX], %[sWS], s74 offen offset:2048 sc1
ds_write_b128 v107, v[176:179] offset:10240
v_cvt_pk_bf16_f32 v184, v184, v185
v_cvt_pk_bf16_f32 v185, v186, v187
v_cvt_pk_bf16_f32 v186, v188, v189
v_cvt_pk_bf16_f32 v187, v190, v191
buffer_store_dwordx4 v[184:187], %[vX], %[sWS], s75 offen offset:2048 sc1
ds_write_b128 v107, v[184:187] offset:14336
v_rcp_f32_e64 v198, v76
v_mul_f32 v76, 0x76800000, v76
v_accvgpr_read_b32 v208, a96
v_accvgpr_read_b32 v209, a97
v_accvgpr_read_b32 v210, a98
v_accvgpr_read_b32 v211, a99
v_accvgpr_read_b32 v212, a100
v_accvgpr_read_b32 v213, a101
v_accvgpr_read_b32 v214, a102
v_accvgpr_read_b32 v215, a103
v_accvgpr_read_b32 v216, a104
v_accvgpr_read_b32 v217, a105
v_accvgpr_read_b32 v218, a106
v_accvgpr_read_b32 v219, a107
v_accvgpr_read_b32 v220, a108
v_accvgpr_read_b32 v221, a109
v_accvgpr_read_b32 v222, a110
v_accvgpr_read_b32 v223, a111
v_pk_mul_f32 v[208:209], v[208:209], v[198:199] op_sel_hi:[1,0]
v_pk_mul_f32 v[210:211], v[210:211], v[198:199] op_sel_hi:[1,0]
v_pk_mul_f32 v[212:213], v[212:213], v[198:199] op_sel_hi:[1,0]
v_pk_mul_f32 v[214:215], v[214:215], v[198:199] op_sel_hi:[1,0]
v_pk_mul_f32 v[216:217], v[216:217], v[198:199] op_sel_hi:[1,0]
v_pk_mul_f32 v[218:219], v[218:219], v[198:199] op_sel_hi:[1,0]
v_pk_mul_f32 v[220:221], v[220:221], v[198:199] op_sel_hi:[1,0]
v_pk_mul_f32 v[222:223], v[222:223], v[198:199] op_sel_hi:[1,0]
v_pk_mul_f32 v[158:159], v[208:209], v[208:209]
v_pk_fma_f32 v[158:159], v[210:211], v[210:211], v[158:159]
v_pk_fma_f32 v[158:159], v[212:213], v[212:213], v[158:159]
v_pk_fma_f32 v[158:159], v[214:215], v[214:215], v[158:159]
v_pk_fma_f32 v[158:159], v[216:217], v[216:217], v[158:159]
v_pk_fma_f32 v[158:159], v[218:219], v[218:219], v[158:159]
v_pk_fma_f32 v[158:159], v[220:221], v[220:221], v[158:159]
v_pk_fma_f32 v[158:159], v[222:223], v[222:223], v[158:159]
v_cvt_pk_bf16_f32 v208, v208, v209
v_cvt_pk_bf16_f32 v209, v210, v211
v_cvt_pk_bf16_f32 v210, v212, v213
v_cvt_pk_bf16_f32 v211, v214, v215
buffer_store_dwordx4 v[208:211], %[vX], %[sWS], s72 offen offset:3072 sc1
ds_write_b128 v107, v[208:211] offset:3072
v_cvt_pk_bf16_f32 v216, v216, v217
v_cvt_pk_bf16_f32 v217, v218, v219
v_cvt_pk_bf16_f32 v218, v220, v221
v_cvt_pk_bf16_f32 v219, v222, v223
buffer_store_dwordx4 v[216:219], %[vX], %[sWS], s73 offen offset:3072 sc1
ds_write_b128 v107, v[216:219] offset:7168
v_accvgpr_read_b32 v224, a112
v_accvgpr_read_b32 v225, a113
v_accvgpr_read_b32 v226, a114
v_accvgpr_read_b32 v227, a115
v_accvgpr_read_b32 v228, a116
v_accvgpr_read_b32 v229, a117
v_accvgpr_read_b32 v230, a118
v_accvgpr_read_b32 v231, a119
v_accvgpr_read_b32 v232, a120
v_accvgpr_read_b32 v233, a121
v_accvgpr_read_b32 v234, a122
v_accvgpr_read_b32 v235, a123
v_accvgpr_read_b32 v236, a124
v_accvgpr_read_b32 v237, a125
v_accvgpr_read_b32 v238, a126
v_accvgpr_read_b32 v239, a127
v_pk_mul_f32 v[224:225], v[224:225], v[198:199] op_sel_hi:[1,0]
v_pk_mul_f32 v[226:227], v[226:227], v[198:199] op_sel_hi:[1,0]
v_pk_mul_f32 v[228:229], v[228:229], v[198:199] op_sel_hi:[1,0]
v_pk_mul_f32 v[230:231], v[230:231], v[198:199] op_sel_hi:[1,0]
v_pk_mul_f32 v[232:233], v[232:233], v[198:199] op_sel_hi:[1,0]
v_pk_mul_f32 v[234:235], v[234:235], v[198:199] op_sel_hi:[1,0]
v_pk_mul_f32 v[236:237], v[236:237], v[198:199] op_sel_hi:[1,0]
v_pk_mul_f32 v[238:239], v[238:239], v[198:199] op_sel_hi:[1,0]
v_pk_fma_f32 v[158:159], v[224:225], v[224:225], v[158:159]
v_pk_fma_f32 v[158:159], v[226:227], v[226:227], v[158:159]
v_pk_fma_f32 v[158:159], v[228:229], v[228:229], v[158:159]
v_pk_fma_f32 v[158:159], v[230:231], v[230:231], v[158:159]
v_pk_fma_f32 v[158:159], v[232:233], v[232:233], v[158:159]
v_pk_fma_f32 v[158:159], v[234:235], v[234:235], v[158:159]
v_pk_fma_f32 v[158:159], v[236:237], v[236:237], v[158:159]
v_pk_fma_f32 v[158:159], v[238:239], v[238:239], v[158:159]
v_cvt_pk_bf16_f32 v224, v224, v225
v_cvt_pk_bf16_f32 v225, v226, v227
v_cvt_pk_bf16_f32 v226, v228, v229
v_cvt_pk_bf16_f32 v227, v230, v231
buffer_store_dwordx4 v[224:227], %[vX], %[sWS], s74 offen offset:3072 sc1
ds_write_b128 v107, v[224:227] offset:11264
v_cvt_pk_bf16_f32 v232, v232, v233
v_cvt_pk_bf16_f32 v233, v234, v235
v_cvt_pk_bf16_f32 v234, v236, v237
v_cvt_pk_bf16_f32 v235, v238, v239
buffer_store_dwordx4 v[232:235], %[vX], %[sWS], s75 offen offset:3072 sc1
ds_write_b128 v107, v[232:235] offset:15360
s_waitcnt vmcnt(0) lgkmcnt(0)
s_barrier
s_branch L_done_%=
L_half_%=:
s_waitcnt vmcnt(0)
s_mov_b32 s76, s72
s_mul_i32 s57, s76, 1179648
s_add_u32 s57, s57, s33
buffer_load_dwordx4 v[160:163], %[vX], %[sWS], s57 offen offset:0
buffer_load_dwordx4 v[164:167], %[vX], %[sWS], s57 offen offset:1024
buffer_load_dwordx4 v[168:171], %[vX], %[sWS], s57 offen offset:2048
buffer_load_dwordx4 v[172:175], %[vX], %[sWS], s57 offen offset:3072
s_add_u32 s57, s57, 4096
buffer_load_dwordx4 v[176:179], %[vX], %[sWS], s57 offen offset:0
buffer_load_dwordx4 v[180:183], %[vX], %[sWS], s57 offen offset:1024
buffer_load_dwordx4 v[184:187], %[vX], %[sWS], s57 offen offset:2048
buffer_load_dwordx4 v[188:191], %[vX], %[sWS], s57 offen offset:3072
s_bfe_u32 s70, s32, 327688
s_lshl_b32 s96, s70, 11
s_add_u32 s96, s96, s57
s_add_u32 s96, s96, 24576
s_cmp_lt_u32 s70, 22
s_cselect_b32 s96, s96, 0x7f000000
buffer_load_dwordx4 v[72:75], %[vX], %[sWS], s96 offen offset:0
buffer_load_dwordx4 v[76:79], %[vX], %[sWS], s96 offen offset:1024
s_waitcnt vmcnt(0)
s_branch L_halfok_%=
L_hs_%=:
s_cmp_eq_u32 s69, 0
s_cbranch_scc1 L_hsok_%=
s_sub_u32 s69, s69, 1
v_and_b32 v30, 384, %[vX]
v_bfe_u32 v24, %[vX], 2, 5
v_or_b32_e64 v30, v30, v24
buffer_load_dword v30, v30, %[sWS], s70 offen offset:32 sc1
s_waitcnt vmcnt(0)
v_cmp_eq_u32 vcc, 0, v30
s_cbranch_vccnz L_hs_%=
s_branch L_hsok_%=
L_fas_%=:
s_cmp_eq_u32 s69, 0
s_cbranch_scc1 L_faok_%=
s_sub_u32 s69, s69, 1
buffer_load_dwordx4 v[232:235], %[vX], %[sWS], s37 offen offset:0 sc1
s_waitcnt vmcnt(0)
v_min3_u32 v236, v232, v233, v234
v_min_u32 v236, v236, v235
v_cmp_gt_u32 vcc, 3, v236
s_cbranch_vccnz L_fas_%=
s_branch L_faok_%=
L_done_%=:
LPc_2:
s_cbranch_execz LPc_6
v_add_u32 v112, %[sL], %[vX]
v_add_u32 v114, s44, %[vX]
ds_read_b128 a[72:75], v114 offset:0
ds_read_b128 a[76:79], v114 offset:1024
ds_read_b128 a[0:3], v112 offset:0
ds_read_b128 a[4:7], v112 offset:1024
ds_read_b128 a[8:11], v112 offset:2048
ds_read_b128 a[12:15], v112 offset:3072
ds_read_b128 a[16:19], v112 offset:4096
ds_read_b128 a[20:23], v112 offset:5120
ds_read_b128 a[24:27], v112 offset:6144
ds_read_b128 a[28:31], v112 offset:7168
ds_read_b128 a[32:35], v112 offset:8192
s_add_u32 m0, s42, 36864
s_add_u32 s54, s39, 1
s_and_b32 s54, s54, 7
s_mul_i32 s54, s54, 36864
s_add_u32 s48, s54, s40
buffer_load_dwordx4 %[vX], %[sWS], s48 offen offset:0 sc1 lds
buffer_load_dwordx4 %[vX], %[sWS], s48 offen offset:1024 sc1 lds
buffer_load_dwordx4 %[vX], %[sWS], s48 offen offset:2048 sc1 lds
buffer_load_dwordx4 %[vX], %[sWS], s48 offen offset:3072 sc1 lds
s_add_u32 m0, s42, 40960
s_add_u32 s48, s48, 4096
buffer_load_dwordx4 %[vX], %[sWS], s48 offen offset:0 sc1 lds
buffer_load_dwordx4 %[vX], %[sWS], s48 offen offset:1024 sc1 lds
buffer_load_dwordx4 %[vX], %[sWS], s48 offen offset:2048 sc1 lds
buffer_load_dwordx4 %[vX], %[sWS], s48 offen offset:3072 sc1 lds
s_add_u32 m0, s42, 45056
s_add_u32 s48, s48, 4096
buffer_load_dwordx4 %[vX], %[sWS], s48 offen offset:0 sc1 lds
ds_read_b128 a[80:83], v114 offset:4096
ds_read_b128 a[84:87], v114 offset:5120
ds_read_b128 a[88:91], v114 offset:8192
ds_read_b128 a[92:95], v114 offset:9216
ds_read_b128 a[96:99], v114 offset:12288
ds_read_b128 a[100:103], v114 offset:13312
s_xor_b32 s72, s45, s71
s_xor_b32 s73, s46, s71
v_add_u32 v113, 36864, v112
)"
R"(v_mov_b32_e64 v115, 1
v_bfe_u32 v116, %[vX], 4, 3
v_min_u32_e64 v116, 6, v116
v_add3_u32 v116, v116, s39, 1
v_and_b32_e64 v116, 7, v116
v_lshlrev_b32 v116, 2, v116
s_waitcnt lgkmcnt(6)
v_mfma_f32_16x16x32_bf16 v[40:43], a[0:3], a[72:75], 0
ds_read_b128 a[36:39], v112 offset:9216
v_mfma_f32_16x16x32_bf16 v[76:79], a[0:3], a[76:79], 0
ds_read_b128 a[40:43], v112 offset:10240
v_mfma_f32_16x16x32_bf16 v[44:47], a[4:7], a[72:75], 0
v_mfma_f32_16x16x32_bf16 v[80:83], a[4:7], a[76:79], 0
ds_read_b128 a[44:47], v112 offset:11264
v_mfma_f32_16x16x32_bf16 v[48:51], a[8:11], a[72:75], 0
ds_read_b128 a[48:51], v112 offset:12288
v_mfma_f32_16x16x32_bf16 v[84:87], a[8:11], a[76:79], 0
v_mfma_f32_16x16x32_bf16 v[52:55], a[12:15], a[72:75], 0
ds_read_b128 a[52:55], v112 offset:13312
v_mfma_f32_16x16x32_bf16 v[88:91], a[12:15], a[76:79], 0
ds_read_b128 a[56:59], v112 offset:14336
v_mfma_f32_16x16x32_bf16 v[56:59], a[16:19], a[72:75], 0
v_mfma_f32_16x16x32_bf16 v[92:95], a[16:19], a[76:79], 0
ds_read_b128 a[60:63], v112 offset:15360
v_mfma_f32_16x16x32_bf16 v[60:63], a[20:23], a[72:75], 0
ds_read_b128 a[64:67], v112 offset:16384
v_mfma_f32_16x16x32_bf16 v[96:99], a[20:23], a[76:79], 0
v_mfma_f32_16x16x32_bf16 v[64:67], a[24:27], a[72:75], 0
ds_read_b128 a[68:71], v112 offset:17408
v_mfma_f32_16x16x32_bf16 v[100:103], a[24:27], a[76:79], 0
v_mfma_f32_16x16x32_bf16 v[68:71], a[28:31], a[72:75], 0
v_mfma_f32_16x16x32_bf16 v[104:107], a[28:31], a[76:79], 0
v_mfma_f32_16x16x32_bf16 v[72:75], a[32:35], a[72:75], 0
v_mfma_f32_16x16x32_bf16 v[108:111], a[32:35], a[76:79], 0
s_waitcnt lgkmcnt(0)
v_mfma_f32_16x16x32_bf16 v[40:43], a[36:39], a[80:83], v[40:43]
ds_read_b128 a[0:3], v112 offset:18432
v_mfma_f32_16x16x32_bf16 v[76:79], a[36:39], a[84:87], v[76:79]
ds_read_b128 a[4:7], v112 offset:19456
v_mfma_f32_16x16x32_bf16 v[44:47], a[40:43], a[80:83], v[44:47]
v_mfma_f32_16x16x32_bf16 v[80:83], a[40:43], a[84:87], v[80:83]
ds_read_b128 a[8:11], v112 offset:20480
v_mfma_f32_16x16x32_bf16 v[48:51], a[44:47], a[80:83], v[48:51]
ds_read_b128 a[12:15], v112 offset:21504
v_mfma_f32_16x16x32_bf16 v[84:87], a[44:47], a[84:87], v[84:87]
v_mfma_f32_16x16x32_bf16 v[52:55], a[48:51], a[80:83], v[52:55]
ds_read_b128 a[16:19], v112 offset:22528
v_mfma_f32_16x16x32_bf16 v[88:91], a[48:51], a[84:87], v[88:91]
ds_read_b128 a[20:23], v112 offset:23552
v_mfma_f32_16x16x32_bf16 v[56:59], a[52:55], a[80:83], v[56:59]
v_mfma_f32_16x16x32_bf16 v[92:95], a[52:55], a[84:87], v[92:95]
ds_read_b128 a[24:27], v112 offset:24576
v_mfma_f32_16x16x32_bf16 v[60:63], a[56:59], a[80:83], v[60:63]
ds_read_b128 a[28:31], v112 offset:25600
v_mfma_f32_16x16x32_bf16 v[96:99], a[56:59], a[84:87], v[96:99]
v_mfma_f32_16x16x32_bf16 v[64:67], a[60:63], a[80:83], v[64:67]
s_waitcnt vmcnt(9)
ds_read_b128 a[32:35], v112 offset:26624
buffer_store_dword v115, off, %[sWS], s72 sc1
v_mfma_f32_16x16x32_bf16 v[100:103], a[60:63], a[84:87], v[100:103]
v_mfma_f32_16x16x32_bf16 v[68:71], a[64:67], a[80:83], v[68:71]
v_mfma_f32_16x16x32_bf16 v[104:107], a[64:67], a[84:87], v[104:107]
v_mfma_f32_16x16x32_bf16 v[72:75], a[68:71], a[80:83], v[72:75]
v_mfma_f32_16x16x32_bf16 v[108:111], a[68:71], a[84:87], v[108:111]
s_waitcnt lgkmcnt(0)
v_mfma_f32_16x16x32_bf16 v[40:43], a[0:3], a[88:91], v[40:43]
ds_read_b128 a[36:39], v112 offset:27648
v_mfma_f32_16x16x32_bf16 v[76:79], a[0:3], a[92:95], v[76:79]
ds_read_b128 a[40:43], v112 offset:28672
v_mfma_f32_16x16x32_bf16 v[44:47], a[4:7], a[88:91], v[44:47]
v_mfma_f32_16x16x32_bf16 v[80:83], a[4:7], a[92:95], v[80:83]
ds_read_b128 a[44:47], v112 offset:29696
v_mfma_f32_16x16x32_bf16 v[48:51], a[8:11], a[88:91], v[48:51]
ds_read_b128 a[48:51], v112 offset:30720
v_mfma_f32_16x16x32_bf16 v[84:87], a[8:11], a[92:95], v[84:87]
v_mfma_f32_16x16x32_bf16 v[52:55], a[12:15], a[88:91], v[52:55]
ds_read_b128 a[52:55], v112 offset:31744
v_mfma_f32_16x16x32_bf16 v[88:91], a[12:15], a[92:95], v[88:91]
ds_read_b128 a[56:59], v112 offset:32768
v_mfma_f32_16x16x32_bf16 v[56:59], a[16:19], a[88:91], v[56:59]
v_mfma_f32_16x16x32_bf16 v[92:95], a[16:19], a[92:95], v[92:95]
ds_read_b128 a[60:63], v112 offset:33792
v_mfma_f32_16x16x32_bf16 v[60:63], a[20:23], a[88:91], v[60:63]
ds_read_b128 a[64:67], v112 offset:34816
v_mfma_f32_16x16x32_bf16 v[96:99], a[20:23], a[92:95], v[96:99]
v_mfma_f32_16x16x32_bf16 v[64:67], a[24:27], a[88:91], v[64:67]
ds_read_b128 a[68:71], v112 offset:35840
v_mfma_f32_16x16x32_bf16 v[100:103], a[24:27], a[92:95], v[100:103]
v_mfma_f32_16x16x32_bf16 v[68:71], a[28:31], a[88:91], v[68:71]
v_mfma_f32_16x16x32_bf16 v[104:107], a[28:31], a[92:95], v[104:107]
v_mfma_f32_16x16x32_bf16 v[72:75], a[32:35], a[88:91], v[72:75]
v_mfma_f32_16x16x32_bf16 v[108:111], a[32:35], a[92:95], v[108:111]
s_waitcnt vmcnt(1) lgkmcnt(0)
s_barrier
v_mfma_f32_16x16x32_bf16 v[40:43], a[36:39], a[96:99], v[40:43]
s_add_u32 m0, s42, 73728
ds_read_b128 a[0:3], v113 offset:0
v_mfma_f32_16x16x32_bf16 v[76:79], a[36:39], a[100:103], v[76:79]
ds_read_b128 a[4:7], v113 offset:1024
v_mfma_f32_16x16x32_bf16 v[44:47], a[40:43], a[96:99], v[44:47]
s_add_u32 s54, s39, 2
s_and_b32 s54, s54, 7
s_mul_i32 s54, s54, 36864
s_add_u32 s48, s54, s40
buffer_load_dwordx4 %[vX], %[sWS], s48 offen offset:0 sc1 lds
v_mfma_f32_16x16x32_bf16 v[80:83], a[40:43], a[100:103], v[80:83]
ds_read_b128 a[8:11], v113 offset:2048
v_mfma_f32_16x16x32_bf16 v[48:51], a[44:47], a[96:99], v[48:51]
ds_read_b128 a[12:15], v113 offset:3072
v_mfma_f32_16x16x32_bf16 v[84:87], a[44:47], a[100:103], v[84:87]
buffer_load_dwordx4 %[vX], %[sWS], s48 offen offset:1024 sc1 lds
v_mfma_f32_16x16x32_bf16 v[52:55], a[48:51], a[96:99], v[52:55]
ds_read_b128 a[16:19], v113 offset:4096
v_mfma_f32_16x16x32_bf16 v[88:91], a[48:51], a[100:103], v[88:91]
ds_read_b128 a[20:23], v113 offset:5120
v_mfma_f32_16x16x32_bf16 v[56:59], a[52:55], a[96:99], v[56:59]
buffer_load_dwordx4 %[vX], %[sWS], s48 offen offset:2048 sc1 lds
v_mfma_f32_16x16x32_bf16 v[92:95], a[52:55], a[100:103], v[92:95]
ds_read_b128 a[24:27], v113 offset:6144
v_mfma_f32_16x16x32_bf16 v[60:63], a[56:59], a[96:99], v[60:63]
ds_read_b128 a[28:31], v113 offset:7168
v_mfma_f32_16x16x32_bf16 v[96:99], a[56:59], a[100:103], v[96:99]
buffer_load_dwordx4 %[vX], %[sWS], s48 offen offset:3072 sc1 lds
v_mfma_f32_16x16x32_bf16 v[64:67], a[60:63], a[96:99], v[64:67]
s_add_u32 m0, s42, 77824
ds_read_b128 a[32:35], v113 offset:8192
v_mfma_f32_16x16x32_bf16 v[100:103], a[60:63], a[100:103], v[100:103]
s_add_u32 s48, s48, 4096
buffer_load_dwordx4 %[vX], %[sWS], s48 offen offset:0 sc1 lds
v_mfma_f32_16x16x32_bf16 v[68:71], a[64:67], a[96:99], v[68:71]
buffer_load_dwordx4 %[vX], %[sWS], s48 offen offset:1024 sc1 lds
v_mfma_f32_16x16x32_bf16 v[104:107], a[64:67], a[100:103], v[104:107]
buffer_load_dwordx4 %[vX], %[sWS], s48 offen offset:2048 sc1 lds
v_mfma_f32_16x16x32_bf16 v[72:75], a[68:71], a[96:99], v[72:75]
buffer_load_dwordx4 %[vX], %[sWS], s48 offen offset:3072 sc1 lds
v_mfma_f32_16x16x32_bf16 v[108:111], a[68:71], a[100:103], v[108:111]
s_add_u32 m0, s42, 81920
s_add_u32 s48, s48, 4096
buffer_load_dwordx4 %[vX], %[sWS], s48 offen offset:0 sc1 lds
buffer_load_dword v117, v116, %[sWS], s73 offen offset:0 sc1
s_waitcnt lgkmcnt(0)
L_end0_%=:
L_pe1:
)"
    : "+{s16}"(r_s16), "+{s17}"(r_s17), "+{s18}"(r_s18), "+{s19}"(r_s19), "+{s20}"(r_s20), "+{s21}"(r_s21), "+{s22}"(r_s22), "+{s23}"(r_s23), "+{s24}"(r_s24), "+{s25}"(r_s25), "+{s26}"(r_s26), "+{s27}"(r_s27), "+{s28}"(r_s28), "+{s29}"(r_s29), "+{s30}"(r_s30), "+{s31}"(r_s31), "+{s32}"(r_s32), "+{s33}"(r_s33), "+{s34}"(r_s34), "+{s35}"(r_s35), "+{s36}"(r_s36), "+{s37}"(r_s37), "+{s38}"(r_s38), "+{s39}"(r_s39), "+{s40}"(r_s40), "+{s41}"(r_s41), "+{s42}"(r_s42), "+{s43}"(r_s43), "+{s44}"(r_s44), "+{s45}"(r_s45), "+{s46}"(r_s46), "+{s47}"(r_s47), "+{s57}"(r_s57), "+{s60}"(r_s60), "+{s76}"(r_s76), "+{v[32:63]}"(r_v32), "+{v[64:95]}"(r_v64), "+{v[160:191]}"(r_v160), "=&{s69}"(r_s69), "=&{s71}"(r_s71), "=&{s73}"(r_s73), "=&{v[40:47]}"(r_v40), "=&{v[48:55]}"(r_v48), "=&{v[56:63]}"(r_v56), "=&{v[96:127]}"(r_v96), "+v"(tid), "+s"(bid), [vKrd] "=&v"(zvKrd), [vSp] "=&v"(zvSp), [vVr] "=&v"(zvVr), [vVs0] "=&v"(zvVs0), [vVs1] "=&v"(zvVs1)
    : [sKV] "s"(sKV), [sL] "s"(sL), [sLw] "s"(sLw_), [sW] "s"(sW_), [sWS] "s"(sWS_), [vAS] "v"(vAS), [vTr] "v"(vTr), [vTw2] "v"(vTw2), [vWs] "v"(vWs_), [vX] "v"(vX_)
    : "memory", "vcc", "scc", "m0","v24","v25","v26","v27","v28","v29","v30","v31","v128","v129","v130","v131","v132","v133","v134","v135","v136","v137","v138","v139","v140","v141","v142","v143","v144","v145","v146","v147","v148","v149","v150","v151","v152","v153","v154","v155","v156","v157","v158","v159","v192","v193","v194","v195","v196","v197","v198","v199","v200","v201","v202","v203","v204","v205","v206","v207","v208","v209","v210","v211","v212","v213","v214","v215","v216","v217","v218","v219","v220","v221","v222","v223","v224","v225","v226","v227","v228","v229","v230","v231","v232","v233","v234","v235","v236","v237","v238","v239","v240","v241","v242","v243","v244","v245","v246","v247","v248","v249","v250","v251","v252","v253","v254","v255","a0","a1","a2","a3","a4","a5","a6","a7","a8","a9","a10","a11","a12","a13","a14","a15","a16","a17","a18","a19","a20","a21","a22","a23","a24","a25","a26","a27","a28","a29","a30","a31","a32","a33","a34","a35","a36","a37","a38","a39","a40","a41","a42","a43","a44","a45","a46","a47","a48","a49","a50","a51","a52","a53","a54","a55","a56","a57","a58","a59","a60","a61","a62","a63","a64","a65","a66","a67","a68","a69","a70","a71","a72","a73","a74","a75","a76","a77","a78","a79","a80","a81","a82","a83","a84","a85","a86","a87","a88","a89","a90","a91","a92","a93","a94","a95","a96","a97","a98","a99","a100","a101","a102","a103","a104","a105","a106","a107","a108","a109","a110","a111","a112","a113","a114","a115","a116","a117","a118","a119","a120","a121","a122","a123","a124","a125","a126","a127","a128","a129","a130","a131","a132","a133","a134","a135","a136","a137","a138","a139","a140","a141","a142","a143","a144","a145","a146","a147","a148","a149","a150","a151","a152","a153","a154","a155","a156","a157","a158","a159","a160","a161","a162","a163","a164","a165","a166","a167","a168","a169","a170","a171","a172","a173","a174","a175","a176","a177","a178","a179","a180","a181","a182","a183","a184","a185","a186","a187","a188","a189","a190","a191","a192","a193","a194","a195","a196","a197","a198","a199","a200","a201","a202","a203","a204","a205","a206","a207","a208","a209","a210","a211","a212","a213","a214","a215","a216","a217","a218","a219","a220","a221","a222","a223","a224","a225","a226","a227","a228","a229","a230","a231","a232","a233","a234","a235","a236","a237","a238","a239","a240","a241","a242","a243","a244","a245","a246","a247","a248","a249","a250","a251","a252","a253","a254","a255","s48","s52","s53","s54","s55","s56","s58","s59","s61","s62","s63","s68","s70","s72","s74","s75","s80","s81","s82","s83","s84","s85","s86","s87","s92","s93","s94","s96","s97","s98","s99");
  }
  bid = RFL(bid);
  {
  const unsigned hp = RFL(r_s71) >> 13, half = RFL(r_s76);
  const unsigned lane = tid & 63, w = RFL(tid >> 6), b = w & 1, hf = w >> 1;
  const unsigned xcd = bid & 7, jj = bid >> 3, kq = (xcd ^ KQX) & 3, rb = half * 4 + (jj >> 3), mt = 2 * rb + (jj & 1), nt = (jj >> 1) & 3, sHl = RFL(r_s39);
  const unsigned ldsb = 0, OOB = 0x7f000000u, vCp = CODE_OFF;
  const srd_t sWS = make_srd(ws, WS_END);
  const unsigned sLx = RFL(r_s42), s2Xo = RFL(r_s40), sFBr = nr(WS_FB, hp) + (((2 * mt + hf) & 15) * 4 + kq) * 128;
  const unsigned r64 = (RFL(r_s46) >> 9) & 15, gw4 = RFL(r_s31) & 4095;
  const unsigned vY2 = nr(WS_O + kq * 4 * MB + r64 * 262144, hp) + b * 2048 + lane * 16, sHl15 = sHl << 15;
  const unsigned ct = kq * 4 + nt, j = lane & 15, g = lane >> 4, sg = mt * 4 + w;
  const unsigned vL16 = (lane * 16), vL8 = lane * 8;
  const unsigned vXr0 = ldsb + vL16, vXr1 = vXr0 + 36864, vXr2 = vXr0 + 2 * 36864, vXr3 = vXr0 + 3 * 36864;
  const unsigned bLo = ((unsigned)(size_t)bsp), bHi = ((unsigned)((size_t)bsp >> 32)), oLo = ((unsigned)(size_t)out), oHi = ((unsigned)((size_t)out >> 32)), sPo3 = WS_PAR + 131072 + gw4 * 32, sPo4 = WS_PAR + 262144 + gw4 * 8;
  const srd_t sXin = make_srd(x, S_ * D_ * 2), sB = make_srd((void*)(bLo | (size_t)bHi << 32), S_ * 2), sOut = make_srd((void*)(oLo | (size_t)oHi << 32), S_ * D_ * 2);
  const unsigned vU1 = vXr2 + (b * 9 + hf * 4) * 1024, vV1 = vXr2 + (b * 9 + 8) * 1024, vX2 = ldsb + 4 * 36864 + 1024 + tid * 16;
  const unsigned s = half * 1024 + (r64 >> 1) * 128 + kq * 32 + rowperm(b, j), d0 = nt * 144 + 64 * hf + 8 * g, d8 = nt * 144 + 128 + 4 * g, m8 = (hf - 1) & OOB;
  const unsigned vR = (s * D_ + d0) * 2, vR8 = (s * D_ + d8) * 2 | m8;
  const unsigned vG = WS_MOD + (2 * D_ + d0) * 4, vG8 = WS_MOD + (2 * D_ + d8) * 4, vG58 = vG8 + 3 * D_ * 4, vRc = vL16 + hf * 2048;
  const unsigned vFC = WS_FB + (tid >> 1) * 128 + (tid & 1) * 16;
  const unsigned sOwn = w ^ kq, tl = ((mt & 7) * 4 + nt) * 4, sx = (kq - w - 1) & 3, sSnd = nr(WS_E2 + sx * 2 * MB + (tl + w) * 16384, hp), sRcv = nr(WS_E2 + (tl + kq) * 16384, hp) + b * 4096;
  const unsigned er = nr(WS_E2R, hp) + tl * 8192, sSnd2 = er + hf * 16384 + b * 4096 + sx * 1024, sRcv2 = (er + (kq >> 1) * 16384 + (kq & 1) * 4096 + b * 512), sRcv5 = sRcv2 - MB;
  const unsigned fc = bid, strip = fc / 9, fn = fc - strip * 9, fc2 = 256 + (fc & 31), strip2 = fc2 / 9, fn2 = fc2 - strip2 * 9, m2 = fc < 32 ? 0 : OOB;
  const unsigned sJob = RFL(win_stride(strip, fn) * 1024), sJob2 = RFL(fc < 32 ? win_stride(strip2, fn2) * 1024 : 0), wb = WS_PK + (unsigned)PK_WIN + win_base(strip, fn) * 1024, wb2 = WS_PK + (unsigned)PK_WIN + win_base(strip2, fn2) * 1024;
  const unsigned vW = wb + vL16, vW2 = wb2 + vL16, vT = WS_MOD + 13824 + (lane & 3) * 1152 + g * 16;
  const unsigned pk = ((sOwn - 1) & 3) * 8 + (lane >> 3), mp = pk < 18 ? 0 : OOB, vWp = (wb + pk * sJob + (lane & 7) * 128) | mp, vWp2 = (wb2 + pk * sJob2 + (lane & 7) * 128) | mp | m2;
  const unsigned vPk = pk, vPl = (lane & 7) * 128 | mp, sWb = wb, sWb2 = wb2 | m2;
  const unsigned mj = (j | sOwn) == 0 ? 0 : OOB;
  const unsigned yb = nr(WS_Y, hp) + ((mt & 7) * 4 + kq) * 16384, ybb = yb + b * 1024, k0 = (nt * 144 + 64 * hf) >> 5, dg = ((nt & 1) * 2 + g), yl = (j * 16 + (dg & 3) * 256), hAS = (g * 32 | mj), hSt = (rowperm(0, j) * 64 | (g == 0 ? 0 : OOB)), hF8 = (j * 16 + (g >> 1) * 256 + (g & 1) * 8);
  const unsigned aS1 = (WS_AS + fc * 128), aS2 = ((WS_AS + fc2 * 128) | m2), ySb = (yb + 24 * MB + 4096 + b * 256 + (2 * nt + hf) * 8);
  const unsigned Y0 = (ybb + yoff(k0)), Y1 = (ybb + yoff(k0 + 1)), Y2 = (ybb + yoff(k0 + 2)), Y8 = ((ybb + yoff((nt * 144 + 128) >> 5) + (nt & 1) * 512) | m8);
  unsigned yAS = aS1 | hAS, yAS2 = aS2 | hAS, ySt = ySb + hSt;
  unsigned yF = yl + (dg < 4 ? Y0 : Y1), yF2 = yl + (dg < 4 ? Y1 : Y2), yF8 = Y8 + hF8, yU1 = vU1, yV1 = vV1, yX2 = vX2, yRc = vRc, yL8 = vL8;
  const unsigned ck8 = 18 * (j >> 2) + 4 * (j & 2) + 4; unsigned yCan = yoff(ck8 >> 2) + (j & 1) * 1024 + (ck8 & 3) * 256;
  const unsigned long long sMsk = hf * 0x3f800000u; const unsigned sInv = RFL(__float_as_uint(hf ? 1.f / 80 : 1.f / 64));
  const unsigned tbl = ldsb + 4 * 36864 + w * 256, vOwn = vXr1 + w * 9216;
  const unsigned s3Yo = (nr(WS_Y, hp) + (sg & 31) * 16384), vS = s3Yo + 24 * MB + 4096 + (lane & 31) * 64, ctb = (WS_AS + ct * 1152), vA2 = ctb + 16 * 1152 + j * 8, vA1 = ctb + g * 32;
  const unsigned vTw = tbl + (lane & 31) * 4, vTr = tbl + g * 32, vT1 = tbl + rowperm(0, j) * 4;
  const unsigned ex = (nr(WS_EX, hp) + (mt & 7) * 32768 + w * 256), hEw = ((ct >> 3) * 16384 + (ct & 7) * 1024 + g * 64 + (j >> 2) * 16), vEw4 = ex + hEw, hEr = ((lane >> 5) * 16384 + (lane & 31) * 8), vEr = ex + hEr;
  const unsigned sMt16 = ((mt & 7) << 16), fz = (nr(WS_FZ, hp) + (ct * 4 + w) * 128), vFl = fz + (j & 7) * 4, vFs = lane == 0 ? fz + (sMt16 >> 14) : OOB;
  const unsigned vB = half * 2048 + (sMt16 >> 8) + w * 64 + 2 * rowperm(0, j), y4b = (nr(WS_SP, hp) + (mt & 7) * 4 * MB + w * 2048), vY4 = y4b + vL16;
  const unsigned s3Xo = WS_PK + (unsigned)PK_WIN + ct * 324 * 1024 + w * 9216, sXo4 = nr(WS_Z2 + (ct * 32 + w) * 16384, hp), sXr4 = nr(WS_Z2 + 8 * MB, hp) + (ct >> 1) * 16384 + (ct & 1) * 4096 + w * 1024;
  const unsigned sUo = nr(WS_U + ((sg & 31) * 4 + kq) * 65536, hp), sUr = (nr(WS_UR, hp) + ((sg & 31) * 2 + (kq >> 1)) * 16384 + (kq & 1) * 4096), sNt = nt, vU8 = sUr + (nt >> 1) * 2048 + (nt & 1) * 512 + hF8;
  const unsigned vG5 = vG + 3 * D_ * 4;
  const unsigned hw = w < 3 ? 5120 * w : 13312, sLh = ldsb + hw, x5 = WS_PK + (unsigned)PK_WOUT + (nt * 72 + kq * 18) * 9216, s5X4 = x5 + 16 * 9216 + hw;
  const unsigned s5X0 = x5 + (4 * nt + w) * 9216, s5X1 = x5 + (4 * ((nt + 1 + hf) & 3) + b) * 9216, s5X2 = x5 + (4 * ((nt + 3 - 2 * hf) & 3) + w) * 9216, s5X3 = x5 + (4 * ((nt + 2 + hf) & 3) + 2 + b) * 9216;
  asm volatile(
R"(L_pb2:
s_nop 4
.p2align 3
s_buffer_load_dwordx16 s[16:31], %[sWS], %[sPo3] offset:0
s_buffer_load_dwordx16 s[32:47], %[sWS], %[sPo3] offset:64
s_buffer_load_dwordx4 s[80:83], %[sWS], %[sPo4]
s_mov_b32 s56, 4608
s_getpc_b64 s[98:99]
s_add_u32 s98, s98, L_g34@rel32@lo+4
s_addc_u32 s99, s99, L_g34@rel32@hi+12
v_min_u32 v234, code_end_lbl-L_g34-4, %[vCp]
s_mov_b32 s94, 0x000f000f
s_mov_b32 s95, s94
s_waitcnt vmcnt(0)
v_cmp_eq_u32 vcc, 0, v117
s_cbranch_vccnz L_sl0_%=
L_ok0_%=:
s_add_u32 s55, %[sHl15], 32768
s_and_b32 s55, s55, 262143
buffer_load_dwordx4 a[104:107], %[vY2], %[sWS], s55 offen offset:0 sc1
buffer_load_dwordx4 a[108:111], %[vY2], %[sWS], s55 offen offset:1024 sc1
s_add_u32 s55, %[sHl15], 36864
s_and_b32 s55, s55, 262143
buffer_load_dwordx4 a[112:115], %[vY2], %[sWS], s55 offen offset:0 sc1
buffer_load_dwordx4 a[116:119], %[vY2], %[sWS], s55 offen offset:1024 sc1
s_add_u32 s55, %[sHl15], 49152
s_and_b32 s55, s55, 262143
LPc_3:
s_cbranch_execz LPc_7
buffer_load_dwordx4 a[120:123], %[vY2], %[sWS], s55 offen offset:0 sc1
buffer_load_dwordx4 a[124:127], %[vY2], %[sWS], s55 offen offset:1024 sc1
s_add_u32 s55, %[sHl15], 53248
s_and_b32 s55, s55, 262143
buffer_load_dwordx4 a[128:131], %[vY2], %[sWS], s55 offen offset:0 sc1
buffer_load_dwordx4 a[132:135], %[vY2], %[sWS], s55 offen offset:1024 sc1
s_add_u32 s55, %[sHl15], 65536
s_and_b32 s55, s55, 262143
buffer_load_dwordx4 a[136:139], %[vY2], %[sWS], s55 offen offset:0 sc1
buffer_load_dwordx4 a[140:143], %[vY2], %[sWS], s55 offen offset:1024 sc1
s_waitcnt vmcnt(8)
v_mfma_f32_16x16x32_bf16 v[40:43], a[0:3], a[104:107], v[40:43]
s_add_u32 m0, %[sLx], 110592
ds_read_b128 a[36:39], %[vXr1] offset:9216
v_mfma_f32_16x16x32_bf16 v[76:79], a[0:3], a[108:111], v[76:79]
ds_read_b128 a[40:43], %[vXr1] offset:10240
v_mfma_f32_16x16x32_bf16 v[44:47], a[4:7], a[104:107], v[44:47]
s_add_u32 s54, %[sHl], 3
s_and_b32 s54, s54, 7
s_mul_i32 s54, s54, 36864
s_add_u32 s48, s54, %[s2Xo]
buffer_load_dwordx4 %[vL16], %[sWS], s48 offen offset:0 sc1 lds
v_mfma_f32_16x16x32_bf16 v[80:83], a[4:7], a[108:111], v[80:83]
ds_read_b128 a[44:47], %[vXr1] offset:11264
v_mfma_f32_16x16x32_bf16 v[48:51], a[8:11], a[104:107], v[48:51]
ds_read_b128 a[48:51], %[vXr1] offset:12288
v_mfma_f32_16x16x32_bf16 v[84:87], a[8:11], a[108:111], v[84:87]
buffer_load_dwordx4 %[vL16], %[sWS], s48 offen offset:1024 sc1 lds
v_mfma_f32_16x16x32_bf16 v[52:55], a[12:15], a[104:107], v[52:55]
ds_read_b128 a[52:55], %[vXr1] offset:13312
v_mfma_f32_16x16x32_bf16 v[88:91], a[12:15], a[108:111], v[88:91]
ds_read_b128 a[56:59], %[vXr1] offset:14336
v_mfma_f32_16x16x32_bf16 v[56:59], a[16:19], a[104:107], v[56:59]
buffer_load_dwordx4 %[vL16], %[sWS], s48 offen offset:2048 sc1 lds
v_mfma_f32_16x16x32_bf16 v[92:95], a[16:19], a[108:111], v[92:95]
ds_read_b128 a[60:63], %[vXr1] offset:15360
v_mfma_f32_16x16x32_bf16 v[60:63], a[20:23], a[104:107], v[60:63]
ds_read_b128 a[64:67], %[vXr1] offset:16384
v_mfma_f32_16x16x32_bf16 v[96:99], a[20:23], a[108:111], v[96:99]
buffer_load_dwordx4 %[vL16], %[sWS], s48 offen offset:3072 sc1 lds
v_mfma_f32_16x16x32_bf16 v[64:67], a[24:27], a[104:107], v[64:67]
s_add_u32 m0, %[sLx], 114688
ds_read_b128 a[68:71], %[vXr1] offset:17408
v_mfma_f32_16x16x32_bf16 v[100:103], a[24:27], a[108:111], v[100:103]
s_add_u32 s48, s48, 4096
buffer_load_dwordx4 %[vL16], %[sWS], s48 offen offset:0 sc1 lds
v_mfma_f32_16x16x32_bf16 v[68:71], a[28:31], a[104:107], v[68:71]
buffer_load_dwordx4 %[vL16], %[sWS], s48 offen offset:1024 sc1 lds
v_mfma_f32_16x16x32_bf16 v[104:107], a[28:31], a[108:111], v[104:107]
buffer_load_dwordx4 %[vL16], %[sWS], s48 offen offset:2048 sc1 lds
v_mfma_f32_16x16x32_bf16 v[72:75], a[32:35], a[104:107], v[72:75]
buffer_load_dwordx4 %[vL16], %[sWS], s48 offen offset:3072 sc1 lds
v_mfma_f32_16x16x32_bf16 v[108:111], a[32:35], a[108:111], v[108:111]
s_waitcnt vmcnt(14) lgkmcnt(0)
v_mfma_f32_16x16x32_bf16 v[40:43], a[36:39], a[112:115], v[40:43]
s_add_u32 m0, %[sLx], 118784
ds_read_b128 a[0:3], %[vXr1] offset:18432
v_mfma_f32_16x16x32_bf16 v[76:79], a[36:39], a[116:119], v[76:79]
s_add_u32 s48, s48, 4096
ds_read_b128 a[4:7], %[vXr1] offset:19456
v_mfma_f32_16x16x32_bf16 v[44:47], a[40:43], a[112:115], v[44:47]
buffer_load_dwordx4 %[vL16], %[sWS], s48 offen offset:0 sc1 lds
v_mfma_f32_16x16x32_bf16 v[80:83], a[40:43], a[116:119], v[80:83]
s_add_u32 s55, %[sHl15], 69632
ds_read_b128 a[8:11], %[vXr1] offset:20480
v_mfma_f32_16x16x32_bf16 v[48:51], a[44:47], a[112:115], v[48:51]
s_and_b32 s55, s55, 262143
ds_read_b128 a[12:15], %[vXr1] offset:21504
v_mfma_f32_16x16x32_bf16 v[84:87], a[44:47], a[116:119], v[84:87]
buffer_load_dwordx4 a[144:147], %[vY2], %[sWS], s55 offen offset:0 sc1
v_mfma_f32_16x16x32_bf16 v[52:55], a[48:51], a[112:115], v[52:55]
ds_read_b128 a[16:19], %[vXr1] offset:22528
v_mfma_f32_16x16x32_bf16 v[88:91], a[48:51], a[116:119], v[88:91]
ds_read_b128 a[20:23], %[vXr1] offset:23552
v_mfma_f32_16x16x32_bf16 v[56:59], a[52:55], a[112:115], v[56:59]
v_mfma_f32_16x16x32_bf16 v[92:95], a[52:55], a[116:119], v[92:95]
ds_read_b128 a[24:27], %[vXr1] offset:24576
v_mfma_f32_16x16x32_bf16 v[60:63], a[56:59], a[112:115], v[60:63]
ds_read_b128 a[28:31], %[vXr1] offset:25600
v_mfma_f32_16x16x32_bf16 v[96:99], a[56:59], a[116:119], v[96:99]
buffer_load_dwordx4 a[148:151], %[vY2], %[sWS], s55 offen offset:1024 sc1
v_mfma_f32_16x16x32_bf16 v[64:67], a[60:63], a[112:115], v[64:67]
s_add_u32 m0, %[sLx], 0
ds_read_b128 a[32:35], %[vXr1] offset:26624
v_mfma_f32_16x16x32_bf16 v[100:103], a[60:63], a[116:119], v[100:103]
v_mfma_f32_16x16x32_bf16 v[68:71], a[64:67], a[112:115], v[68:71]
s_add_u32 s54, %[sHl], 4
s_and_b32 s54, s54, 7
s_mul_i32 s54, s54, 36864
s_add_u32 s48, s54, %[s2Xo]
buffer_load_dwordx4 %[vL16], %[sWS], s48 offen offset:0 sc1 lds
v_mfma_f32_16x16x32_bf16 v[104:107], a[64:67], a[116:119], v[104:107]
v_mfma_f32_16x16x32_bf16 v[72:75], a[68:71], a[112:115], v[72:75]
buffer_load_dwordx4 %[vL16], %[sWS], s48 offen offset:1024 sc1 lds
v_mfma_f32_16x16x32_bf16 v[108:111], a[68:71], a[116:119], v[108:111]
s_waitcnt vmcnt(17) lgkmcnt(0)
v_mfma_f32_16x16x32_bf16 v[40:43], a[0:3], a[120:123], v[40:43]
s_add_u32 s55, %[sHl15], 81920
ds_read_b128 a[36:39], %[vXr1] offset:27648
v_mfma_f32_16x16x32_bf16 v[76:79], a[0:3], a[124:127], v[76:79]
s_and_b32 s55, s55, 262143
ds_read_b128 a[40:43], %[vXr1] offset:28672
v_mfma_f32_16x16x32_bf16 v[44:47], a[4:7], a[120:123], v[44:47]
buffer_load_dwordx4 a[152:155], %[vY2], %[sWS], s55 offen offset:0 sc1
v_mfma_f32_16x16x32_bf16 v[80:83], a[4:7], a[124:127], v[80:83]
ds_read_b128 a[44:47], %[vXr1] offset:29696
v_mfma_f32_16x16x32_bf16 v[48:51], a[8:11], a[120:123], v[48:51]
ds_read_b128 a[48:51], %[vXr1] offset:30720
v_mfma_f32_16x16x32_bf16 v[84:87], a[8:11], a[124:127], v[84:87]
v_mfma_f32_16x16x32_bf16 v[52:55], a[12:15], a[120:123], v[52:55]
ds_read_b128 a[52:55], %[vXr1] offset:31744
v_mfma_f32_16x16x32_bf16 v[88:91], a[12:15], a[124:127], v[88:91]
ds_read_b128 a[56:59], %[vXr1] offset:32768
v_mfma_f32_16x16x32_bf16 v[56:59], a[16:19], a[120:123], v[56:59]
buffer_load_dwordx4 a[156:159], %[vY2], %[sWS], s55 offen offset:1024 sc1
v_mfma_f32_16x16x32_bf16 v[92:95], a[16:19], a[124:127], v[92:95]
ds_read_b128 a[60:63], %[vXr1] offset:33792
v_mfma_f32_16x16x32_bf16 v[60:63], a[20:23], a[120:123], v[60:63]
ds_read_b128 a[64:67], %[vXr1] offset:34816
v_mfma_f32_16x16x32_bf16 v[96:99], a[20:23], a[124:127], v[96:99]
v_mfma_f32_16x16x32_bf16 v[64:67], a[24:27], a[120:123], v[64:67]
ds_read_b128 a[68:71], %[vXr1] offset:35840
v_mfma_f32_16x16x32_bf16 v[100:103], a[24:27], a[124:127], v[100:103]
buffer_load_dwordx4 %[vL16], %[sWS], s48 offen offset:2048 sc1 lds
v_mfma_f32_16x16x32_bf16 v[68:71], a[28:31], a[120:123], v[68:71]
v_mfma_f32_16x16x32_bf16 v[104:107], a[28:31], a[124:127], v[104:107]
buffer_load_dwordx4 %[vL16], %[sWS], s48 offen offset:3072 sc1 lds
v_mfma_f32_16x16x32_bf16 v[72:75], a[32:35], a[120:123], v[72:75]
v_mfma_f32_16x16x32_bf16 v[108:111], a[32:35], a[124:127], v[108:111]
s_waitcnt vmcnt(19) lgkmcnt(0)
s_barrier
v_mfma_f32_16x16x32_bf16 v[40:43], a[36:39], a[128:131], v[40:43]
s_add_u32 s55, %[sHl15], 86016
ds_read_b128 a[0:3], %[vXr2] offset:0
v_mfma_f32_16x16x32_bf16 v[76:79], a[36:39], a[132:135], v[76:79]
s_and_b32 s55, s55, 262143
ds_read_b128 a[4:7], %[vXr2] offset:1024
v_mfma_f32_16x16x32_bf16 v[44:47], a[40:43], a[128:131], v[44:47]
buffer_load_dwordx4 a[160:163], %[vY2], %[sWS], s55 offen offset:0 sc1
v_mfma_f32_16x16x32_bf16 v[80:83], a[40:43], a[132:135], v[80:83]
ds_read_b128 a[8:11], %[vXr2] offset:2048
v_mfma_f32_16x16x32_bf16 v[48:51], a[44:47], a[128:131], v[48:51]
ds_read_b128 a[12:15], %[vXr2] offset:3072
v_mfma_f32_16x16x32_bf16 v[84:87], a[44:47], a[132:135], v[84:87]
v_mfma_f32_16x16x32_bf16 v[52:55], a[48:51], a[128:131], v[52:55]
ds_read_b128 a[16:19], %[vXr2] offset:4096
v_mfma_f32_16x16x32_bf16 v[88:91], a[48:51], a[132:135], v[88:91]
ds_read_b128 a[20:23], %[vXr2] offset:5120
v_mfma_f32_16x16x32_bf16 v[56:59], a[52:55], a[128:131], v[56:59]
buffer_load_dwordx4 a[164:167], %[vY2], %[sWS], s55 offen offset:1024 sc1
v_mfma_f32_16x16x32_bf16 v[92:95], a[52:55], a[132:135], v[92:95]
ds_read_b128 a[24:27], %[vXr2] offset:6144
v_mfma_f32_16x16x32_bf16 v[60:63], a[56:59], a[128:131], v[60:63]
ds_read_b128 a[28:31], %[vXr2] offset:7168
v_mfma_f32_16x16x32_bf16 v[96:99], a[56:59], a[132:135], v[96:99]
s_add_u32 m0, %[sLx], 4096
v_mfma_f32_16x16x32_bf16 v[64:67], a[60:63], a[128:131], v[64:67]
s_add_u32 s48, s48, 4096
ds_read_b128 a[32:35], %[vXr2] offset:8192
v_mfma_f32_16x16x32_bf16 v[100:103], a[60:63], a[132:135], v[100:103]
buffer_load_dwordx4 %[vL16], %[sWS], s48 offen offset:0 sc1 lds
v_mfma_f32_16x16x32_bf16 v[68:71], a[64:67], a[128:131], v[68:71]
v_mfma_f32_16x16x32_bf16 v[104:107], a[64:67], a[132:135], v[104:107]
buffer_load_dwordx4 %[vL16], %[sWS], s48 offen offset:1024 sc1 lds
v_mfma_f32_16x16x32_bf16 v[72:75], a[68:71], a[128:131], v[72:75]
v_mfma_f32_16x16x32_bf16 v[108:111], a[68:71], a[132:135], v[108:111]
s_waitcnt vmcnt(21) lgkmcnt(0)
v_mfma_f32_16x16x32_bf16 v[40:43], a[0:3], a[136:139], v[40:43]
s_add_u32 s55, %[sHl15], 98304
ds_read_b128 a[36:39], %[vXr2] offset:9216
v_mfma_f32_16x16x32_bf16 v[76:79], a[0:3], a[140:143], v[76:79]
s_and_b32 s55, s55, 262143
)"
R"(ds_read_b128 a[40:43], %[vXr2] offset:10240
v_mfma_f32_16x16x32_bf16 v[44:47], a[4:7], a[136:139], v[44:47]
buffer_load_dwordx4 a[72:75], %[vY2], %[sWS], s55 offen offset:0 sc1
v_mfma_f32_16x16x32_bf16 v[80:83], a[4:7], a[140:143], v[80:83]
ds_read_b128 a[44:47], %[vXr2] offset:11264
v_mfma_f32_16x16x32_bf16 v[48:51], a[8:11], a[136:139], v[48:51]
ds_read_b128 a[48:51], %[vXr2] offset:12288
v_mfma_f32_16x16x32_bf16 v[84:87], a[8:11], a[140:143], v[84:87]
v_mfma_f32_16x16x32_bf16 v[52:55], a[12:15], a[136:139], v[52:55]
ds_read_b128 a[52:55], %[vXr2] offset:13312
v_mfma_f32_16x16x32_bf16 v[88:91], a[12:15], a[140:143], v[88:91]
ds_read_b128 a[56:59], %[vXr2] offset:14336
v_mfma_f32_16x16x32_bf16 v[56:59], a[16:19], a[136:139], v[56:59]
buffer_load_dwordx4 a[76:79], %[vY2], %[sWS], s55 offen offset:1024 sc1
v_mfma_f32_16x16x32_bf16 v[92:95], a[16:19], a[140:143], v[92:95]
ds_read_b128 a[60:63], %[vXr2] offset:15360
v_mfma_f32_16x16x32_bf16 v[60:63], a[20:23], a[136:139], v[60:63]
ds_read_b128 a[64:67], %[vXr2] offset:16384
v_mfma_f32_16x16x32_bf16 v[96:99], a[20:23], a[140:143], v[96:99]
v_mfma_f32_16x16x32_bf16 v[64:67], a[24:27], a[136:139], v[64:67]
ds_read_b128 a[68:71], %[vXr2] offset:17408
v_mfma_f32_16x16x32_bf16 v[100:103], a[24:27], a[140:143], v[100:103]
buffer_load_dwordx4 %[vL16], %[sWS], s48 offen offset:2048 sc1 lds
v_mfma_f32_16x16x32_bf16 v[68:71], a[28:31], a[136:139], v[68:71]
v_mfma_f32_16x16x32_bf16 v[104:107], a[28:31], a[140:143], v[104:107]
buffer_load_dwordx4 %[vL16], %[sWS], s48 offen offset:3072 sc1 lds
v_mfma_f32_16x16x32_bf16 v[72:75], a[32:35], a[136:139], v[72:75]
v_mfma_f32_16x16x32_bf16 v[108:111], a[32:35], a[140:143], v[108:111]
s_waitcnt vmcnt(14) lgkmcnt(0)
v_mfma_f32_16x16x32_bf16 v[40:43], a[36:39], a[144:147], v[40:43]
s_add_u32 m0, %[sLx], 8192
ds_read_b128 a[0:3], %[vXr2] offset:18432
v_mfma_f32_16x16x32_bf16 v[76:79], a[36:39], a[148:151], v[76:79]
s_add_u32 s48, s48, 4096
ds_read_b128 a[4:7], %[vXr2] offset:19456
v_mfma_f32_16x16x32_bf16 v[44:47], a[40:43], a[144:147], v[44:47]
buffer_load_dwordx4 %[vL16], %[sWS], s48 offen offset:0 sc1 lds
v_mfma_f32_16x16x32_bf16 v[80:83], a[40:43], a[148:151], v[80:83]
s_add_u32 s55, %[sHl15], 102400
ds_read_b128 a[8:11], %[vXr2] offset:20480
v_mfma_f32_16x16x32_bf16 v[48:51], a[44:47], a[144:147], v[48:51]
s_and_b32 s55, s55, 262143
ds_read_b128 a[12:15], %[vXr2] offset:21504
v_mfma_f32_16x16x32_bf16 v[84:87], a[44:47], a[148:151], v[84:87]
buffer_load_dwordx4 a[80:83], %[vY2], %[sWS], s55 offen offset:0 sc1
v_mfma_f32_16x16x32_bf16 v[52:55], a[48:51], a[144:147], v[52:55]
ds_read_b128 a[16:19], %[vXr2] offset:22528
v_mfma_f32_16x16x32_bf16 v[88:91], a[48:51], a[148:151], v[88:91]
ds_read_b128 a[20:23], %[vXr2] offset:23552
v_mfma_f32_16x16x32_bf16 v[56:59], a[52:55], a[144:147], v[56:59]
v_mfma_f32_16x16x32_bf16 v[92:95], a[52:55], a[148:151], v[92:95]
ds_read_b128 a[24:27], %[vXr2] offset:24576
v_mfma_f32_16x16x32_bf16 v[60:63], a[56:59], a[144:147], v[60:63]
ds_read_b128 a[28:31], %[vXr2] offset:25600
v_mfma_f32_16x16x32_bf16 v[96:99], a[56:59], a[148:151], v[96:99]
buffer_load_dwordx4 a[84:87], %[vY2], %[sWS], s55 offen offset:1024 sc1
v_mfma_f32_16x16x32_bf16 v[64:67], a[60:63], a[144:147], v[64:67]
s_add_u32 m0, %[sLx], 36864
ds_read_b128 a[32:35], %[vXr2] offset:26624
v_mfma_f32_16x16x32_bf16 v[100:103], a[60:63], a[148:151], v[100:103]
v_mfma_f32_16x16x32_bf16 v[68:71], a[64:67], a[144:147], v[68:71]
s_add_u32 s54, %[sHl], 5
s_and_b32 s54, s54, 7
s_mul_i32 s54, s54, 36864
s_add_u32 s48, s54, %[s2Xo]
buffer_load_dwordx4 %[vL16], %[sWS], s48 offen offset:0 sc1 lds
v_mfma_f32_16x16x32_bf16 v[104:107], a[64:67], a[148:151], v[104:107]
v_mfma_f32_16x16x32_bf16 v[72:75], a[68:71], a[144:147], v[72:75]
buffer_load_dwordx4 %[vL16], %[sWS], s48 offen offset:1024 sc1 lds
v_mfma_f32_16x16x32_bf16 v[108:111], a[68:71], a[148:151], v[108:111]
s_waitcnt vmcnt(15) lgkmcnt(0)
v_mfma_f32_16x16x32_bf16 v[40:43], a[0:3], a[152:155], v[40:43]
s_add_u32 s55, %[sHl15], 114688
ds_read_b128 a[36:39], %[vXr2] offset:27648
v_mfma_f32_16x16x32_bf16 v[76:79], a[0:3], a[156:159], v[76:79]
s_and_b32 s55, s55, 262143
ds_read_b128 a[40:43], %[vXr2] offset:28672
v_mfma_f32_16x16x32_bf16 v[44:47], a[4:7], a[152:155], v[44:47]
buffer_load_dwordx4 a[88:91], %[vY2], %[sWS], s55 offen offset:0 sc1
v_mfma_f32_16x16x32_bf16 v[80:83], a[4:7], a[156:159], v[80:83]
ds_read_b128 a[44:47], %[vXr2] offset:29696
v_mfma_f32_16x16x32_bf16 v[48:51], a[8:11], a[152:155], v[48:51]
ds_read_b128 a[48:51], %[vXr2] offset:30720
v_mfma_f32_16x16x32_bf16 v[84:87], a[8:11], a[156:159], v[84:87]
v_mfma_f32_16x16x32_bf16 v[52:55], a[12:15], a[152:155], v[52:55]
ds_read_b128 a[52:55], %[vXr2] offset:31744
v_mfma_f32_16x16x32_bf16 v[88:91], a[12:15], a[156:159], v[88:91]
ds_read_b128 a[56:59], %[vXr2] offset:32768
v_mfma_f32_16x16x32_bf16 v[56:59], a[16:19], a[152:155], v[56:59]
buffer_load_dwordx4 a[92:95], %[vY2], %[sWS], s55 offen offset:1024 sc1
v_mfma_f32_16x16x32_bf16 v[92:95], a[16:19], a[156:159], v[92:95]
ds_read_b128 a[60:63], %[vXr2] offset:33792
v_mfma_f32_16x16x32_bf16 v[60:63], a[20:23], a[152:155], v[60:63]
ds_read_b128 a[64:67], %[vXr2] offset:34816
v_mfma_f32_16x16x32_bf16 v[96:99], a[20:23], a[156:159], v[96:99]
v_mfma_f32_16x16x32_bf16 v[64:67], a[24:27], a[152:155], v[64:67]
ds_read_b128 a[68:71], %[vXr2] offset:35840
v_mfma_f32_16x16x32_bf16 v[100:103], a[24:27], a[156:159], v[100:103]
buffer_load_dwordx4 %[vL16], %[sWS], s48 offen offset:2048 sc1 lds
v_mfma_f32_16x16x32_bf16 v[68:71], a[28:31], a[152:155], v[68:71]
v_mfma_f32_16x16x32_bf16 v[104:107], a[28:31], a[156:159], v[104:107]
buffer_load_dwordx4 %[vL16], %[sWS], s48 offen offset:3072 sc1 lds
v_mfma_f32_16x16x32_bf16 v[72:75], a[32:35], a[152:155], v[72:75]
v_mfma_f32_16x16x32_bf16 v[108:111], a[32:35], a[156:159], v[108:111]
s_waitcnt vmcnt(15) lgkmcnt(0)
s_barrier
v_mfma_f32_16x16x32_bf16 v[40:43], a[36:39], a[160:163], v[40:43]
s_add_u32 s55, %[sHl15], 118784
ds_read_b128 a[0:3], %[vXr3] offset:0
v_mfma_f32_16x16x32_bf16 v[76:79], a[36:39], a[164:167], v[76:79]
s_and_b32 s55, s55, 262143
ds_read_b128 a[4:7], %[vXr3] offset:1024
v_mfma_f32_16x16x32_bf16 v[44:47], a[40:43], a[160:163], v[44:47]
buffer_load_dwordx4 a[96:99], %[vY2], %[sWS], s55 offen offset:0 sc1
v_mfma_f32_16x16x32_bf16 v[80:83], a[40:43], a[164:167], v[80:83]
ds_read_b128 a[8:11], %[vXr3] offset:2048
v_mfma_f32_16x16x32_bf16 v[48:51], a[44:47], a[160:163], v[48:51]
ds_read_b128 a[12:15], %[vXr3] offset:3072
v_mfma_f32_16x16x32_bf16 v[84:87], a[44:47], a[164:167], v[84:87]
v_mfma_f32_16x16x32_bf16 v[52:55], a[48:51], a[160:163], v[52:55]
ds_read_b128 a[16:19], %[vXr3] offset:4096
v_mfma_f32_16x16x32_bf16 v[88:91], a[48:51], a[164:167], v[88:91]
ds_read_b128 a[20:23], %[vXr3] offset:5120
v_mfma_f32_16x16x32_bf16 v[56:59], a[52:55], a[160:163], v[56:59]
buffer_load_dwordx4 a[100:103], %[vY2], %[sWS], s55 offen offset:1024 sc1
v_mfma_f32_16x16x32_bf16 v[92:95], a[52:55], a[164:167], v[92:95]
ds_read_b128 a[24:27], %[vXr3] offset:6144
v_mfma_f32_16x16x32_bf16 v[60:63], a[56:59], a[160:163], v[60:63]
ds_read_b128 a[28:31], %[vXr3] offset:7168
v_mfma_f32_16x16x32_bf16 v[96:99], a[56:59], a[164:167], v[96:99]
s_add_u32 m0, %[sLx], 40960
v_mfma_f32_16x16x32_bf16 v[64:67], a[60:63], a[160:163], v[64:67]
s_add_u32 s48, s48, 4096
ds_read_b128 a[32:35], %[vXr3] offset:8192
v_mfma_f32_16x16x32_bf16 v[100:103], a[60:63], a[164:167], v[100:103]
buffer_load_dwordx4 %[vL16], %[sWS], s48 offen offset:0 sc1 lds
v_mfma_f32_16x16x32_bf16 v[68:71], a[64:67], a[160:163], v[68:71]
v_mfma_f32_16x16x32_bf16 v[104:107], a[64:67], a[164:167], v[104:107]
buffer_load_dwordx4 %[vL16], %[sWS], s48 offen offset:1024 sc1 lds
v_mfma_f32_16x16x32_bf16 v[72:75], a[68:71], a[160:163], v[72:75]
v_mfma_f32_16x16x32_bf16 v[108:111], a[68:71], a[164:167], v[108:111]
s_waitcnt vmcnt(15) lgkmcnt(0)
v_mfma_f32_16x16x32_bf16 v[40:43], a[0:3], a[72:75], v[40:43]
s_add_u32 s55, %[sHl15], 131072
ds_read_b128 a[36:39], %[vXr3] offset:9216
v_mfma_f32_16x16x32_bf16 v[76:79], a[0:3], a[76:79], v[76:79]
s_and_b32 s55, s55, 262143
ds_read_b128 a[40:43], %[vXr3] offset:10240
v_mfma_f32_16x16x32_bf16 v[44:47], a[4:7], a[72:75], v[44:47]
buffer_load_dwordx4 a[104:107], %[vY2], %[sWS], s55 offen offset:0 sc1
v_mfma_f32_16x16x32_bf16 v[80:83], a[4:7], a[76:79], v[80:83]
ds_read_b128 a[44:47], %[vXr3] offset:11264
v_mfma_f32_16x16x32_bf16 v[48:51], a[8:11], a[72:75], v[48:51]
ds_read_b128 a[48:51], %[vXr3] offset:12288
v_mfma_f32_16x16x32_bf16 v[84:87], a[8:11], a[76:79], v[84:87]
v_mfma_f32_16x16x32_bf16 v[52:55], a[12:15], a[72:75], v[52:55]
ds_read_b128 a[52:55], %[vXr3] offset:13312
v_mfma_f32_16x16x32_bf16 v[88:91], a[12:15], a[76:79], v[88:91]
ds_read_b128 a[56:59], %[vXr3] offset:14336
v_mfma_f32_16x16x32_bf16 v[56:59], a[16:19], a[72:75], v[56:59]
buffer_load_dwordx4 a[108:111], %[vY2], %[sWS], s55 offen offset:1024 sc1
v_mfma_f32_16x16x32_bf16 v[92:95], a[16:19], a[76:79], v[92:95]
ds_read_b128 a[60:63], %[vXr3] offset:15360
v_mfma_f32_16x16x32_bf16 v[60:63], a[20:23], a[72:75], v[60:63]
ds_read_b128 a[64:67], %[vXr3] offset:16384
v_mfma_f32_16x16x32_bf16 v[96:99], a[20:23], a[76:79], v[96:99]
v_mfma_f32_16x16x32_bf16 v[64:67], a[24:27], a[72:75], v[64:67]
ds_read_b128 a[68:71], %[vXr3] offset:17408
v_mfma_f32_16x16x32_bf16 v[100:103], a[24:27], a[76:79], v[100:103]
buffer_load_dwordx4 %[vL16], %[sWS], s48 offen offset:2048 sc1 lds
v_mfma_f32_16x16x32_bf16 v[68:71], a[28:31], a[72:75], v[68:71]
v_mfma_f32_16x16x32_bf16 v[104:107], a[28:31], a[76:79], v[104:107]
buffer_load_dwordx4 %[vL16], %[sWS], s48 offen offset:3072 sc1 lds
v_mfma_f32_16x16x32_bf16 v[72:75], a[32:35], a[72:75], v[72:75]
v_mfma_f32_16x16x32_bf16 v[108:111], a[32:35], a[76:79], v[108:111]
s_waitcnt vmcnt(14) lgkmcnt(0)
v_mfma_f32_16x16x32_bf16 v[40:43], a[36:39], a[80:83], v[40:43]
s_add_u32 m0, %[sLx], 45056
ds_read_b128 a[0:3], %[vXr3] offset:18432
v_mfma_f32_16x16x32_bf16 v[76:79], a[36:39], a[84:87], v[76:79]
s_add_u32 s48, s48, 4096
ds_read_b128 a[4:7], %[vXr3] offset:19456
v_mfma_f32_16x16x32_bf16 v[44:47], a[40:43], a[80:83], v[44:47]
buffer_load_dwordx4 %[vL16], %[sWS], s48 offen offset:0 sc1 lds
v_mfma_f32_16x16x32_bf16 v[80:83], a[40:43], a[84:87], v[80:83]
s_add_u32 s55, %[sHl15], 135168
ds_read_b128 a[8:11], %[vXr3] offset:20480
v_mfma_f32_16x16x32_bf16 v[48:51], a[44:47], a[80:83], v[48:51]
s_and_b32 s55, s55, 262143
ds_read_b128 a[12:15], %[vXr3] offset:21504
v_mfma_f32_16x16x32_bf16 v[84:87], a[44:47], a[84:87], v[84:87]
buffer_load_dwordx4 a[112:115], %[vY2], %[sWS], s55 offen offset:0 sc1
v_mfma_f32_16x16x32_bf16 v[52:55], a[48:51], a[80:83], v[52:55]
ds_read_b128 a[16:19], %[vXr3] offset:22528
v_mfma_f32_16x16x32_bf16 v[88:91], a[48:51], a[84:87], v[88:91]
ds_read_b128 a[20:23], %[vXr3] offset:23552
v_mfma_f32_16x16x32_bf16 v[56:59], a[52:55], a[80:83], v[56:59]
v_mfma_f32_16x16x32_bf16 v[92:95], a[52:55], a[84:87], v[92:95]
ds_read_b128 a[24:27], %[vXr3] offset:24576
v_mfma_f32_16x16x32_bf16 v[60:63], a[56:59], a[80:83], v[60:63]
)"
R"(ds_read_b128 a[28:31], %[vXr3] offset:25600
v_mfma_f32_16x16x32_bf16 v[96:99], a[56:59], a[84:87], v[96:99]
buffer_load_dwordx4 a[116:119], %[vY2], %[sWS], s55 offen offset:1024 sc1
v_mfma_f32_16x16x32_bf16 v[64:67], a[60:63], a[80:83], v[64:67]
s_add_u32 m0, %[sLx], 73728
ds_read_b128 a[32:35], %[vXr3] offset:26624
v_mfma_f32_16x16x32_bf16 v[100:103], a[60:63], a[84:87], v[100:103]
v_mfma_f32_16x16x32_bf16 v[68:71], a[64:67], a[80:83], v[68:71]
s_add_u32 s54, %[sHl], 6
s_and_b32 s54, s54, 7
s_mul_i32 s54, s54, 36864
s_add_u32 s48, s54, %[s2Xo]
buffer_load_dwordx4 %[vL16], %[sWS], s48 offen offset:0 sc1 lds
v_mfma_f32_16x16x32_bf16 v[104:107], a[64:67], a[84:87], v[104:107]
v_mfma_f32_16x16x32_bf16 v[72:75], a[68:71], a[80:83], v[72:75]
buffer_load_dwordx4 %[vL16], %[sWS], s48 offen offset:1024 sc1 lds
v_mfma_f32_16x16x32_bf16 v[108:111], a[68:71], a[84:87], v[108:111]
s_waitcnt lgkmcnt(0)
s_waitcnt vmcnt(15) lgkmcnt(0)
v_mfma_f32_16x16x32_bf16 v[40:43], a[0:3], a[88:91], v[40:43]
s_xor_b32 s31, s31, s71
s_add_u32 s55, %[sHl15], 147456
ds_read_b128 a[36:39], %[vXr3] offset:27648
v_mfma_f32_16x16x32_bf16 v[76:79], a[0:3], a[92:95], v[76:79]
s_xor_b32 s32, s32, s71
s_and_b32 s55, s55, 262143
ds_read_b128 a[40:43], %[vXr3] offset:28672
v_mfma_f32_16x16x32_bf16 v[44:47], a[4:7], a[88:91], v[44:47]
s_xor_b32 s33, s33, s71
buffer_load_dwordx4 a[120:123], %[vY2], %[sWS], s55 offen offset:0 sc1
v_mfma_f32_16x16x32_bf16 v[80:83], a[4:7], a[92:95], v[80:83]
s_xor_b32 s34, s34, s71
ds_read_b128 a[44:47], %[vXr3] offset:29696
v_mfma_f32_16x16x32_bf16 v[48:51], a[8:11], a[88:91], v[48:51]
s_xor_b32 s35, s35, s71
ds_read_b128 a[48:51], %[vXr3] offset:30720
v_mfma_f32_16x16x32_bf16 v[84:87], a[8:11], a[92:95], v[84:87]
s_xor_b32 s36, s36, s71
buffer_load_dwordx4 a[124:127], %[vY2], %[sWS], s55 offen offset:1024 sc1
v_mfma_f32_16x16x32_bf16 v[52:55], a[12:15], a[88:91], v[52:55]
s_xor_b32 s37, s37, s71
ds_read_b128 a[52:55], %[vXr3] offset:31744
v_mfma_f32_16x16x32_bf16 v[88:91], a[12:15], a[92:95], v[88:91]
s_xor_b32 s38, s38, s71
ds_read_b128 a[56:59], %[vXr3] offset:32768
v_mfma_f32_16x16x32_bf16 v[56:59], a[16:19], a[88:91], v[56:59]
s_xor_b32 s39, s39, s71
v_mfma_f32_16x16x32_bf16 v[92:95], a[16:19], a[92:95], v[92:95]
s_xor_b32 s40, s40, s71
ds_read_b128 a[60:63], %[vXr3] offset:33792
v_mfma_f32_16x16x32_bf16 v[60:63], a[20:23], a[88:91], v[60:63]
s_xor_b32 s41, s41, s71
ds_read_b128 a[64:67], %[vXr3] offset:34816
v_mfma_f32_16x16x32_bf16 v[96:99], a[20:23], a[92:95], v[96:99]
s_xor_b32 s42, s42, s71
buffer_load_dwordx4 %[vL16], %[sWS], s48 offen offset:2048 sc1 lds
v_mfma_f32_16x16x32_bf16 v[64:67], a[24:27], a[88:91], v[64:67]
s_xor_b32 s43, s43, s71
ds_read_b128 a[68:71], %[vXr3] offset:35840
v_mfma_f32_16x16x32_bf16 v[100:103], a[24:27], a[92:95], v[100:103]
s_xor_b32 s44, s44, s71
v_mfma_f32_16x16x32_bf16 v[68:71], a[28:31], a[88:91], v[68:71]
s_xor_b32 s45, s45, s71
buffer_load_dwordx4 %[vL16], %[sWS], s48 offen offset:3072 sc1 lds
v_mfma_f32_16x16x32_bf16 v[104:107], a[28:31], a[92:95], v[104:107]
s_xor_b32 s46, s46, s71
v_mfma_f32_16x16x32_bf16 v[72:75], a[32:35], a[88:91], v[72:75]
s_xor_b32 s47, s47, s71
buffer_load_dwordx4 v[184:187], %[vR], %[sXin], 0 offen offset:0
v_mfma_f32_16x16x32_bf16 v[108:111], a[32:35], a[92:95], v[108:111]
s_waitcnt vmcnt(16) lgkmcnt(0)
s_barrier
v_mfma_f32_16x16x32_bf16 v[40:43], a[36:39], a[96:99], v[40:43]
s_add_u32 s55, %[sHl15], 151552
ds_read_b128 a[0:3], %[vXr0] offset:0
v_mfma_f32_16x16x32_bf16 v[76:79], a[36:39], a[100:103], v[76:79]
s_and_b32 s55, s55, 262143
ds_read_b128 a[4:7], %[vXr0] offset:1024
v_mfma_f32_16x16x32_bf16 v[44:47], a[40:43], a[96:99], v[44:47]
s_mov_b32 s49, s80
buffer_load_dwordx4 a[128:131], %[vY2], %[sWS], s55 offen offset:0 sc1
v_mfma_f32_16x16x32_bf16 v[80:83], a[40:43], a[100:103], v[80:83]
ds_read_b128 a[8:11], %[vXr0] offset:2048
v_mfma_f32_16x16x32_bf16 v[48:51], a[44:47], a[96:99], v[48:51]
ds_read_b128 a[12:15], %[vXr0] offset:3072
v_mfma_f32_16x16x32_bf16 v[84:87], a[44:47], a[100:103], v[84:87]
buffer_load_dwordx4 a[132:135], %[vY2], %[sWS], s55 offen offset:1024 sc1
v_mad_u32_u24 v236, %[vPk], s82, %[vPl]
v_mfma_f32_16x16x32_bf16 v[52:55], a[48:51], a[96:99], v[52:55]
ds_read_b128 a[16:19], %[vXr0] offset:4096
v_mfma_f32_16x16x32_bf16 v[88:91], a[48:51], a[100:103], v[88:91]
ds_read_b128 a[20:23], %[vXr0] offset:5120
v_mfma_f32_16x16x32_bf16 v[56:59], a[52:55], a[96:99], v[56:59]
v_mad_u32_u24 v237, %[vPk], s83, %[vPl]
v_mfma_f32_16x16x32_bf16 v[92:95], a[52:55], a[100:103], v[92:95]
s_add_u32 m0, %[sLx], 77824
ds_read_b128 a[24:27], %[vXr0] offset:6144
v_mfma_f32_16x16x32_bf16 v[60:63], a[56:59], a[96:99], v[60:63]
s_add_u32 s48, s48, 4096
ds_read_b128 a[28:31], %[vXr0] offset:7168
v_mfma_f32_16x16x32_bf16 v[96:99], a[56:59], a[100:103], v[96:99]
buffer_load_dwordx4 %[vL16], %[sWS], s48 offen offset:0 sc1 lds
v_mfma_f32_16x16x32_bf16 v[64:67], a[60:63], a[96:99], v[64:67]
ds_read_b128 a[32:35], %[vXr0] offset:8192
v_mfma_f32_16x16x32_bf16 v[100:103], a[60:63], a[100:103], v[100:103]
v_mfma_f32_16x16x32_bf16 v[68:71], a[64:67], a[96:99], v[68:71]
buffer_load_dwordx4 %[vL16], %[sWS], s48 offen offset:1024 sc1 lds
v_mfma_f32_16x16x32_bf16 v[104:107], a[64:67], a[100:103], v[104:107]
v_mfma_f32_16x16x32_bf16 v[72:75], a[68:71], a[96:99], v[72:75]
buffer_load_dwordx4 v[188:191], %[vR], %[sXin], 0 offen offset:64
v_mfma_f32_16x16x32_bf16 v[108:111], a[68:71], a[100:103], v[108:111]
s_waitcnt vmcnt(17) lgkmcnt(0)
v_mfma_f32_16x16x32_bf16 v[40:43], a[0:3], a[104:107], v[40:43]
s_add_u32 s55, %[sHl15], 163840
ds_read_b128 a[36:39], %[vXr0] offset:9216
v_mfma_f32_16x16x32_bf16 v[76:79], a[0:3], a[108:111], v[76:79]
s_and_b32 s55, s55, 262143
ds_read_b128 a[40:43], %[vXr0] offset:10240
v_mfma_f32_16x16x32_bf16 v[44:47], a[4:7], a[104:107], v[44:47]
buffer_load_dwordx4 a[136:139], %[vY2], %[sWS], s55 offen offset:0 sc1
v_mfma_f32_16x16x32_bf16 v[80:83], a[4:7], a[108:111], v[80:83]
ds_read_b128 a[44:47], %[vXr0] offset:11264
v_mfma_f32_16x16x32_bf16 v[48:51], a[8:11], a[104:107], v[48:51]
ds_read_b128 a[48:51], %[vXr0] offset:12288
v_mfma_f32_16x16x32_bf16 v[84:87], a[8:11], a[108:111], v[84:87]
buffer_load_dwordx4 a[140:143], %[vY2], %[sWS], s55 offen offset:1024 sc1
v_mfma_f32_16x16x32_bf16 v[52:55], a[12:15], a[104:107], v[52:55]
ds_read_b128 a[52:55], %[vXr0] offset:13312
v_mfma_f32_16x16x32_bf16 v[88:91], a[12:15], a[108:111], v[88:91]
ds_read_b128 a[56:59], %[vXr0] offset:14336
v_mfma_f32_16x16x32_bf16 v[56:59], a[16:19], a[104:107], v[56:59]
v_mfma_f32_16x16x32_bf16 v[92:95], a[16:19], a[108:111], v[92:95]
ds_read_b128 a[60:63], %[vXr0] offset:15360
v_mfma_f32_16x16x32_bf16 v[60:63], a[20:23], a[104:107], v[60:63]
ds_read_b128 a[64:67], %[vXr0] offset:16384
v_mfma_f32_16x16x32_bf16 v[96:99], a[20:23], a[108:111], v[96:99]
buffer_load_dwordx4 %[vL16], %[sWS], s48 offen offset:2048 sc1 lds
v_mfma_f32_16x16x32_bf16 v[64:67], a[24:27], a[104:107], v[64:67]
ds_read_b128 a[68:71], %[vXr0] offset:17408
v_mfma_f32_16x16x32_bf16 v[100:103], a[24:27], a[108:111], v[100:103]
v_mfma_f32_16x16x32_bf16 v[68:71], a[28:31], a[104:107], v[68:71]
buffer_load_dwordx4 %[vL16], %[sWS], s48 offen offset:3072 sc1 lds
v_mfma_f32_16x16x32_bf16 v[104:107], a[28:31], a[108:111], v[104:107]
v_mfma_f32_16x16x32_bf16 v[72:75], a[32:35], a[104:107], v[72:75]
buffer_load_dwordx2 v[192:193], %[vR8], %[sXin], 0 offen offset:0
v_mfma_f32_16x16x32_bf16 v[108:111], a[32:35], a[108:111], v[108:111]
s_waitcnt vmcnt(17) lgkmcnt(0)
v_mfma_f32_16x16x32_bf16 v[40:43], a[36:39], a[112:115], v[40:43]
s_add_u32 m0, %[sLx], 81920
ds_read_b128 a[0:3], %[vXr0] offset:18432
v_mfma_f32_16x16x32_bf16 v[76:79], a[36:39], a[116:119], v[76:79]
s_add_u32 s48, s48, 4096
ds_read_b128 a[4:7], %[vXr0] offset:19456
v_mfma_f32_16x16x32_bf16 v[44:47], a[40:43], a[112:115], v[44:47]
buffer_load_dwordx4 %[vL16], %[sWS], s48 offen offset:0 sc1 lds
v_mfma_f32_16x16x32_bf16 v[80:83], a[40:43], a[116:119], v[80:83]
s_add_u32 s55, %[sHl15], 167936
ds_read_b128 a[8:11], %[vXr0] offset:20480
v_mfma_f32_16x16x32_bf16 v[48:51], a[44:47], a[112:115], v[48:51]
s_and_b32 s55, s55, 262143
ds_read_b128 a[12:15], %[vXr0] offset:21504
v_mfma_f32_16x16x32_bf16 v[84:87], a[44:47], a[116:119], v[84:87]
buffer_load_dwordx4 a[144:147], %[vY2], %[sWS], s55 offen offset:0 sc1
v_mfma_f32_16x16x32_bf16 v[52:55], a[48:51], a[112:115], v[52:55]
ds_read_b128 a[16:19], %[vXr0] offset:22528
v_mfma_f32_16x16x32_bf16 v[88:91], a[48:51], a[116:119], v[88:91]
LPc_4:
s_cbranch_execz LRc
ds_read_b128 a[20:23], %[vXr0] offset:23552
v_mfma_f32_16x16x32_bf16 v[56:59], a[52:55], a[112:115], v[56:59]
v_mfma_f32_16x16x32_bf16 v[92:95], a[52:55], a[116:119], v[92:95]
ds_read_b128 a[24:27], %[vXr0] offset:24576
v_mfma_f32_16x16x32_bf16 v[60:63], a[56:59], a[112:115], v[60:63]
ds_read_b128 a[28:31], %[vXr0] offset:25600
v_mfma_f32_16x16x32_bf16 v[96:99], a[56:59], a[116:119], v[96:99]
buffer_load_dwordx4 a[148:151], %[vY2], %[sWS], s55 offen offset:1024 sc1
v_mfma_f32_16x16x32_bf16 v[64:67], a[60:63], a[112:115], v[64:67]
s_add_u32 m0, %[sLx], 110592
ds_read_b128 a[32:35], %[vXr0] offset:26624
v_mfma_f32_16x16x32_bf16 v[100:103], a[60:63], a[116:119], v[100:103]
s_add_u32 s54, %[sHl], 7
s_and_b32 s54, s54, 7
s_mul_i32 s54, s54, 36864
s_add_u32 s48, s54, %[s2Xo]
buffer_load_dwordx4 %[vL16], %[sWS], s48 offen offset:0 sc1 lds
v_mfma_f32_16x16x32_bf16 v[68:71], a[64:67], a[112:115], v[68:71]
v_mfma_f32_16x16x32_bf16 v[104:107], a[64:67], a[116:119], v[104:107]
buffer_load_dwordx4 %[vL16], %[sWS], s48 offen offset:1024 sc1 lds
v_mfma_f32_16x16x32_bf16 v[72:75], a[68:71], a[112:115], v[72:75]
buffer_load_dwordx4 v[194:197], %[vG], %[sWS], 0 offen offset:0 sc1
v_mfma_f32_16x16x32_bf16 v[108:111], a[68:71], a[116:119], v[108:111]
s_waitcnt vmcnt(19) lgkmcnt(0)
v_mfma_f32_16x16x32_bf16 v[40:43], a[0:3], a[120:123], v[40:43]
s_add_u32 s55, %[sHl15], 180224
ds_read_b128 a[36:39], %[vXr0] offset:27648
v_mfma_f32_16x16x32_bf16 v[76:79], a[0:3], a[124:127], v[76:79]
s_and_b32 s55, s55, 262143
ds_read_b128 a[40:43], %[vXr0] offset:28672
v_mfma_f32_16x16x32_bf16 v[44:47], a[4:7], a[120:123], v[44:47]
buffer_load_dwordx4 a[152:155], %[vY2], %[sWS], s55 offen offset:0 sc1
v_mfma_f32_16x16x32_bf16 v[80:83], a[4:7], a[124:127], v[80:83]
)"
R"(ds_read_b128 a[44:47], %[vXr0] offset:29696
v_mfma_f32_16x16x32_bf16 v[48:51], a[8:11], a[120:123], v[48:51]
ds_read_b128 a[48:51], %[vXr0] offset:30720
v_mfma_f32_16x16x32_bf16 v[84:87], a[8:11], a[124:127], v[84:87]
buffer_load_dwordx4 a[156:159], %[vY2], %[sWS], s55 offen offset:1024 sc1
v_mfma_f32_16x16x32_bf16 v[52:55], a[12:15], a[120:123], v[52:55]
ds_read_b128 a[52:55], %[vXr0] offset:31744
v_mfma_f32_16x16x32_bf16 v[88:91], a[12:15], a[124:127], v[88:91]
ds_read_b128 a[56:59], %[vXr0] offset:32768
v_mfma_f32_16x16x32_bf16 v[56:59], a[16:19], a[120:123], v[56:59]
v_mfma_f32_16x16x32_bf16 v[92:95], a[16:19], a[124:127], v[92:95]
ds_read_b128 a[60:63], %[vXr0] offset:33792
v_mfma_f32_16x16x32_bf16 v[60:63], a[20:23], a[120:123], v[60:63]
ds_read_b128 a[64:67], %[vXr0] offset:34816
v_mfma_f32_16x16x32_bf16 v[96:99], a[20:23], a[124:127], v[96:99]
buffer_load_dwordx4 %[vL16], %[sWS], s48 offen offset:2048 sc1 lds
v_mfma_f32_16x16x32_bf16 v[64:67], a[24:27], a[120:123], v[64:67]
ds_read_b128 a[68:71], %[vXr0] offset:35840
v_mfma_f32_16x16x32_bf16 v[100:103], a[24:27], a[124:127], v[100:103]
v_mfma_f32_16x16x32_bf16 v[68:71], a[28:31], a[120:123], v[68:71]
buffer_load_dwordx4 %[vL16], %[sWS], s48 offen offset:3072 sc1 lds
v_mfma_f32_16x16x32_bf16 v[104:107], a[28:31], a[124:127], v[104:107]
v_mfma_f32_16x16x32_bf16 v[72:75], a[32:35], a[120:123], v[72:75]
buffer_load_dwordx4 v[198:201], %[vG], %[sWS], 0 offen offset:16 sc1
v_mfma_f32_16x16x32_bf16 v[108:111], a[32:35], a[124:127], v[108:111]
s_waitcnt vmcnt(19) lgkmcnt(0)
s_barrier
v_mfma_f32_16x16x32_bf16 v[40:43], a[36:39], a[128:131], v[40:43]
s_add_u32 s55, %[sHl15], 184320
ds_read_b128 a[0:3], %[vXr1] offset:0
v_mfma_f32_16x16x32_bf16 v[76:79], a[36:39], a[132:135], v[76:79]
s_and_b32 s55, s55, 262143
ds_read_b128 a[4:7], %[vXr1] offset:1024
v_mfma_f32_16x16x32_bf16 v[44:47], a[40:43], a[128:131], v[44:47]
buffer_load_dwordx4 a[160:163], %[vY2], %[sWS], s55 offen offset:0 sc1
v_mfma_f32_16x16x32_bf16 v[80:83], a[40:43], a[132:135], v[80:83]
ds_read_b128 a[8:11], %[vXr1] offset:2048
v_mfma_f32_16x16x32_bf16 v[48:51], a[44:47], a[128:131], v[48:51]
ds_read_b128 a[12:15], %[vXr1] offset:3072
v_mfma_f32_16x16x32_bf16 v[84:87], a[44:47], a[132:135], v[84:87]
buffer_load_dwordx4 a[164:167], %[vY2], %[sWS], s55 offen offset:1024 sc1
v_mfma_f32_16x16x32_bf16 v[52:55], a[48:51], a[128:131], v[52:55]
ds_read_b128 a[16:19], %[vXr1] offset:4096
v_mfma_f32_16x16x32_bf16 v[88:91], a[48:51], a[132:135], v[88:91]
ds_read_b128 a[20:23], %[vXr1] offset:5120
v_mfma_f32_16x16x32_bf16 v[56:59], a[52:55], a[128:131], v[56:59]
v_mfma_f32_16x16x32_bf16 v[92:95], a[52:55], a[132:135], v[92:95]
s_add_u32 m0, %[sLx], 114688
ds_read_b128 a[24:27], %[vXr1] offset:6144
v_mfma_f32_16x16x32_bf16 v[60:63], a[56:59], a[128:131], v[60:63]
s_add_u32 s48, s48, 4096
ds_read_b128 a[28:31], %[vXr1] offset:7168
v_mfma_f32_16x16x32_bf16 v[96:99], a[56:59], a[132:135], v[96:99]
buffer_load_dwordx4 %[vL16], %[sWS], s48 offen offset:0 sc1 lds
v_mfma_f32_16x16x32_bf16 v[64:67], a[60:63], a[128:131], v[64:67]
ds_read_b128 a[32:35], %[vXr1] offset:8192
v_mfma_f32_16x16x32_bf16 v[100:103], a[60:63], a[132:135], v[100:103]
v_mfma_f32_16x16x32_bf16 v[68:71], a[64:67], a[128:131], v[68:71]
buffer_load_dwordx4 %[vL16], %[sWS], s48 offen offset:1024 sc1 lds
v_mfma_f32_16x16x32_bf16 v[104:107], a[64:67], a[132:135], v[104:107]
v_mfma_f32_16x16x32_bf16 v[72:75], a[68:71], a[128:131], v[72:75]
buffer_load_dwordx4 v[202:205], %[vG], %[sWS], 0 offen offset:128 sc1
v_mfma_f32_16x16x32_bf16 v[108:111], a[68:71], a[132:135], v[108:111]
s_waitcnt vmcnt(19) lgkmcnt(0)
v_mfma_f32_16x16x32_bf16 v[40:43], a[0:3], a[136:139], v[40:43]
s_add_u32 s55, %[sHl15], 196608
ds_read_b128 a[36:39], %[vXr1] offset:9216
v_mfma_f32_16x16x32_bf16 v[76:79], a[0:3], a[140:143], v[76:79]
s_and_b32 s55, s55, 262143
ds_read_b128 a[40:43], %[vXr1] offset:10240
v_mfma_f32_16x16x32_bf16 v[44:47], a[4:7], a[136:139], v[44:47]
buffer_load_dwordx4 a[72:75], %[vY2], %[sWS], s55 offen offset:0 sc1
v_mfma_f32_16x16x32_bf16 v[80:83], a[4:7], a[140:143], v[80:83]
ds_read_b128 a[44:47], %[vXr1] offset:11264
v_mfma_f32_16x16x32_bf16 v[48:51], a[8:11], a[136:139], v[48:51]
ds_read_b128 a[48:51], %[vXr1] offset:12288
v_mfma_f32_16x16x32_bf16 v[84:87], a[8:11], a[140:143], v[84:87]
buffer_load_dwordx4 a[76:79], %[vY2], %[sWS], s55 offen offset:1024 sc1
v_mfma_f32_16x16x32_bf16 v[52:55], a[12:15], a[136:139], v[52:55]
ds_read_b128 a[52:55], %[vXr1] offset:13312
v_mfma_f32_16x16x32_bf16 v[88:91], a[12:15], a[140:143], v[88:91]
ds_read_b128 a[56:59], %[vXr1] offset:14336
v_mfma_f32_16x16x32_bf16 v[56:59], a[16:19], a[136:139], v[56:59]
v_mfma_f32_16x16x32_bf16 v[92:95], a[16:19], a[140:143], v[92:95]
ds_read_b128 a[60:63], %[vXr1] offset:15360
v_mfma_f32_16x16x32_bf16 v[60:63], a[20:23], a[136:139], v[60:63]
ds_read_b128 a[64:67], %[vXr1] offset:16384
v_mfma_f32_16x16x32_bf16 v[96:99], a[20:23], a[140:143], v[96:99]
buffer_load_dwordx4 %[vL16], %[sWS], s48 offen offset:2048 sc1 lds
v_mfma_f32_16x16x32_bf16 v[64:67], a[24:27], a[136:139], v[64:67]
ds_read_b128 a[68:71], %[vXr1] offset:17408
v_mfma_f32_16x16x32_bf16 v[100:103], a[24:27], a[140:143], v[100:103]
v_mfma_f32_16x16x32_bf16 v[68:71], a[28:31], a[136:139], v[68:71]
buffer_load_dwordx4 %[vL16], %[sWS], s48 offen offset:3072 sc1 lds
v_mfma_f32_16x16x32_bf16 v[104:107], a[28:31], a[140:143], v[104:107]
v_mfma_f32_16x16x32_bf16 v[72:75], a[32:35], a[136:139], v[72:75]
buffer_load_dwordx4 v[206:209], %[vG], %[sWS], 0 offen offset:144 sc1
v_mfma_f32_16x16x32_bf16 v[108:111], a[32:35], a[140:143], v[108:111]
s_waitcnt vmcnt(18) lgkmcnt(0)
v_mfma_f32_16x16x32_bf16 v[40:43], a[36:39], a[144:147], v[40:43]
s_add_u32 m0, %[sLx], 118784
ds_read_b128 a[0:3], %[vXr1] offset:18432
v_mfma_f32_16x16x32_bf16 v[76:79], a[36:39], a[148:151], v[76:79]
s_add_u32 s48, s48, 4096
ds_read_b128 a[4:7], %[vXr1] offset:19456
v_mfma_f32_16x16x32_bf16 v[44:47], a[40:43], a[144:147], v[44:47]
buffer_load_dwordx4 %[vL16], %[sWS], s48 offen offset:0 sc1 lds
v_mfma_f32_16x16x32_bf16 v[80:83], a[40:43], a[148:151], v[80:83]
ds_read_b128 a[8:11], %[vXr1] offset:20480
v_mfma_f32_16x16x32_bf16 v[48:51], a[44:47], a[144:147], v[48:51]
ds_read_b128 a[12:15], %[vXr1] offset:21504
v_mfma_f32_16x16x32_bf16 v[84:87], a[44:47], a[148:151], v[84:87]
v_mfma_f32_16x16x32_bf16 v[52:55], a[48:51], a[144:147], v[52:55]
s_add_u32 s55, %[sHl15], 200704
ds_read_b128 a[16:19], %[vXr1] offset:22528
v_mfma_f32_16x16x32_bf16 v[88:91], a[48:51], a[148:151], v[88:91]
s_and_b32 s55, s55, 262143
ds_read_b128 a[20:23], %[vXr1] offset:23552
v_mfma_f32_16x16x32_bf16 v[56:59], a[52:55], a[144:147], v[56:59]
buffer_load_dwordx4 a[80:83], %[vY2], %[sWS], s55 offen offset:0 sc1
v_mfma_f32_16x16x32_bf16 v[92:95], a[52:55], a[148:151], v[92:95]
ds_read_b128 a[24:27], %[vXr1] offset:24576
v_mfma_f32_16x16x32_bf16 v[60:63], a[56:59], a[144:147], v[60:63]
ds_read_b128 a[28:31], %[vXr1] offset:25600
v_mfma_f32_16x16x32_bf16 v[96:99], a[56:59], a[148:151], v[96:99]
v_mfma_f32_16x16x32_bf16 v[64:67], a[60:63], a[144:147], v[64:67]
ds_read_b128 a[32:35], %[vXr1] offset:26624
v_mfma_f32_16x16x32_bf16 v[100:103], a[60:63], a[148:151], v[100:103]
buffer_load_dwordx4 a[84:87], %[vY2], %[sWS], s55 offen offset:1024 sc1
v_mfma_f32_16x16x32_bf16 v[68:71], a[64:67], a[144:147], v[68:71]
v_mfma_f32_16x16x32_bf16 v[104:107], a[64:67], a[148:151], v[104:107]
buffer_load_dwordx4 v[210:213], %[vG8], %[sWS], 0 offen offset:0 sc1
v_mfma_f32_16x16x32_bf16 v[72:75], a[68:71], a[144:147], v[72:75]
v_mfma_f32_16x16x32_bf16 v[108:111], a[68:71], a[148:151], v[108:111]
s_waitcnt vmcnt(17) lgkmcnt(0)
v_mfma_f32_16x16x32_bf16 v[40:43], a[0:3], a[152:155], v[40:43]
ds_read_b128 a[36:39], %[vXr1] offset:27648
v_mfma_f32_16x16x32_bf16 v[76:79], a[0:3], a[156:159], v[76:79]
ds_read_b128 a[40:43], %[vXr1] offset:28672
v_mfma_f32_16x16x32_bf16 v[44:47], a[4:7], a[152:155], v[44:47]
buffer_load_dwordx4 v[238:241], %[vFC], %[sWS], 0 offen offset:0 sc1
v_mfma_f32_16x16x32_bf16 v[80:83], a[4:7], a[156:159], v[80:83]
ds_read_b128 a[44:47], %[vXr1] offset:29696
v_mfma_f32_16x16x32_bf16 v[48:51], a[8:11], a[152:155], v[48:51]
ds_read_b128 a[48:51], %[vXr1] offset:30720
v_mfma_f32_16x16x32_bf16 v[84:87], a[8:11], a[156:159], v[84:87]
v_mfma_f32_16x16x32_bf16 v[52:55], a[12:15], a[152:155], v[52:55]
s_add_u32 s55, %[sHl15], 212992
ds_read_b128 a[52:55], %[vXr1] offset:31744
v_mfma_f32_16x16x32_bf16 v[88:91], a[12:15], a[156:159], v[88:91]
s_and_b32 s55, s55, 262143
ds_read_b128 a[56:59], %[vXr1] offset:32768
v_mfma_f32_16x16x32_bf16 v[56:59], a[16:19], a[152:155], v[56:59]
buffer_load_dwordx4 a[88:91], %[vY2], %[sWS], s55 offen offset:0 sc1
v_mfma_f32_16x16x32_bf16 v[92:95], a[16:19], a[156:159], v[92:95]
ds_read_b128 a[60:63], %[vXr1] offset:33792
v_mfma_f32_16x16x32_bf16 v[60:63], a[20:23], a[152:155], v[60:63]
ds_read_b128 a[64:67], %[vXr1] offset:34816
v_mfma_f32_16x16x32_bf16 v[96:99], a[20:23], a[156:159], v[96:99]
v_mfma_f32_16x16x32_bf16 v[64:67], a[24:27], a[152:155], v[64:67]
ds_read_b128 a[68:71], %[vXr1] offset:35840
v_mfma_f32_16x16x32_bf16 v[100:103], a[24:27], a[156:159], v[100:103]
buffer_load_dwordx4 a[92:95], %[vY2], %[sWS], s55 offen offset:1024 sc1
v_mfma_f32_16x16x32_bf16 v[68:71], a[28:31], a[152:155], v[68:71]
v_mfma_f32_16x16x32_bf16 v[104:107], a[28:31], a[156:159], v[104:107]
buffer_load_dwordx4 v[214:217], %[vG], %[sWS], s56 offen offset:0 sc1
v_mfma_f32_16x16x32_bf16 v[72:75], a[32:35], a[152:155], v[72:75]
v_mfma_f32_16x16x32_bf16 v[108:111], a[32:35], a[156:159], v[108:111]
s_waitcnt vmcnt(16) lgkmcnt(0)
s_barrier
v_mfma_f32_16x16x32_bf16 v[40:43], a[36:39], a[160:163], v[40:43]
s_add_u32 s55, %[sHl15], 217088
ds_read_b128 a[0:3], %[vXr2] offset:0
v_mfma_f32_16x16x32_bf16 v[76:79], a[36:39], a[164:167], v[76:79]
s_and_b32 s55, s55, 262143
ds_read_b128 a[4:7], %[vXr2] offset:1024
v_mfma_f32_16x16x32_bf16 v[44:47], a[40:43], a[160:163], v[44:47]
buffer_load_dwordx4 a[96:99], %[vY2], %[sWS], s55 offen offset:0 sc1
v_mfma_f32_16x16x32_bf16 v[80:83], a[40:43], a[164:167], v[80:83]
ds_read_b128 a[8:11], %[vXr2] offset:2048
v_mfma_f32_16x16x32_bf16 v[48:51], a[44:47], a[160:163], v[48:51]
ds_read_b128 a[12:15], %[vXr2] offset:3072
v_mfma_f32_16x16x32_bf16 v[84:87], a[44:47], a[164:167], v[84:87]
v_mfma_f32_16x16x32_bf16 v[52:55], a[48:51], a[160:163], v[52:55]
ds_read_b128 a[16:19], %[vXr2] offset:4096
v_mfma_f32_16x16x32_bf16 v[88:91], a[48:51], a[164:167], v[88:91]
ds_read_b128 a[20:23], %[vXr2] offset:5120
v_mfma_f32_16x16x32_bf16 v[56:59], a[52:55], a[160:163], v[56:59]
v_mfma_f32_16x16x32_bf16 v[92:95], a[52:55], a[164:167], v[92:95]
ds_read_b128 a[24:27], %[vXr2] offset:6144
v_mfma_f32_16x16x32_bf16 v[60:63], a[56:59], a[160:163], v[60:63]
ds_read_b128 a[28:31], %[vXr2] offset:7168
v_mfma_f32_16x16x32_bf16 v[96:99], a[56:59], a[164:167], v[96:99]
buffer_load_dwordx4 a[100:103], %[vY2], %[sWS], s55 offen offset:1024 sc1
v_mfma_f32_16x16x32_bf16 v[64:67], a[60:63], a[160:163], v[64:67]
ds_read_b128 a[32:35], %[vXr2] offset:8192
v_mfma_f32_16x16x32_bf16 v[100:103], a[60:63], a[164:167], v[100:103]
v_mfma_f32_16x16x32_bf16 v[68:71], a[64:67], a[160:163], v[68:71]
v_mfma_f32_16x16x32_bf16 v[104:107], a[64:67], a[164:167], v[104:107]
buffer_load_dwordx4 v[218:221], %[vG], %[sWS], s56 offen offset:16 sc1
)"
R"(v_mfma_f32_16x16x32_bf16 v[72:75], a[68:71], a[160:163], v[72:75]
v_mfma_f32_16x16x32_bf16 v[108:111], a[68:71], a[164:167], v[108:111]
s_waitcnt vmcnt(14) lgkmcnt(0)
v_mfma_f32_16x16x32_bf16 v[40:43], a[0:3], a[72:75], v[40:43]
s_add_u32 s55, %[sHl15], 229376
ds_read_b128 a[36:39], %[vXr2] offset:9216
v_mfma_f32_16x16x32_bf16 v[76:79], a[0:3], a[76:79], v[76:79]
s_and_b32 s55, s55, 262143
ds_read_b128 a[40:43], %[vXr2] offset:10240
v_mfma_f32_16x16x32_bf16 v[44:47], a[4:7], a[72:75], v[44:47]
buffer_load_dwordx4 a[104:107], %[vY2], %[sWS], s55 offen offset:0 sc1
v_mfma_f32_16x16x32_bf16 v[80:83], a[4:7], a[76:79], v[80:83]
ds_read_b128 a[44:47], %[vXr2] offset:11264
v_mfma_f32_16x16x32_bf16 v[48:51], a[8:11], a[72:75], v[48:51]
ds_read_b128 a[48:51], %[vXr2] offset:12288
v_mfma_f32_16x16x32_bf16 v[84:87], a[8:11], a[76:79], v[84:87]
v_mfma_f32_16x16x32_bf16 v[52:55], a[12:15], a[72:75], v[52:55]
ds_read_b128 a[52:55], %[vXr2] offset:13312
v_mfma_f32_16x16x32_bf16 v[88:91], a[12:15], a[76:79], v[88:91]
ds_read_b128 a[56:59], %[vXr2] offset:14336
v_mfma_f32_16x16x32_bf16 v[56:59], a[16:19], a[72:75], v[56:59]
v_mfma_f32_16x16x32_bf16 v[92:95], a[16:19], a[76:79], v[92:95]
ds_read_b128 a[60:63], %[vXr2] offset:15360
v_mfma_f32_16x16x32_bf16 v[60:63], a[20:23], a[72:75], v[60:63]
ds_read_b128 a[64:67], %[vXr2] offset:16384
v_mfma_f32_16x16x32_bf16 v[96:99], a[20:23], a[76:79], v[96:99]
buffer_load_dwordx4 a[108:111], %[vY2], %[sWS], s55 offen offset:1024 sc1
v_mfma_f32_16x16x32_bf16 v[64:67], a[24:27], a[72:75], v[64:67]
ds_read_b128 a[68:71], %[vXr2] offset:17408
v_mfma_f32_16x16x32_bf16 v[100:103], a[24:27], a[76:79], v[100:103]
v_mfma_f32_16x16x32_bf16 v[68:71], a[28:31], a[72:75], v[68:71]
v_mfma_f32_16x16x32_bf16 v[104:107], a[28:31], a[76:79], v[104:107]
buffer_load_dwordx4 v[222:225], %[vG], %[sWS], s56 offen offset:128 sc1
v_mfma_f32_16x16x32_bf16 v[72:75], a[32:35], a[72:75], v[72:75]
v_mfma_f32_16x16x32_bf16 v[108:111], a[32:35], a[76:79], v[108:111]
s_waitcnt vmcnt(11) lgkmcnt(0)
v_mfma_f32_16x16x32_bf16 v[40:43], a[36:39], a[80:83], v[40:43]
s_add_u32 s55, %[sHl15], 233472
ds_read_b128 a[0:3], %[vXr2] offset:18432
v_mfma_f32_16x16x32_bf16 v[76:79], a[36:39], a[84:87], v[76:79]
s_and_b32 s55, s55, 262143
ds_read_b128 a[4:7], %[vXr2] offset:19456
v_mfma_f32_16x16x32_bf16 v[44:47], a[40:43], a[80:83], v[44:47]
buffer_load_dwordx4 a[112:115], %[vY2], %[sWS], s55 offen offset:0 sc1
v_mfma_f32_16x16x32_bf16 v[80:83], a[40:43], a[84:87], v[80:83]
ds_read_b128 a[8:11], %[vXr2] offset:20480
v_mfma_f32_16x16x32_bf16 v[48:51], a[44:47], a[80:83], v[48:51]
ds_read_b128 a[12:15], %[vXr2] offset:21504
v_mfma_f32_16x16x32_bf16 v[84:87], a[44:47], a[84:87], v[84:87]
v_mfma_f32_16x16x32_bf16 v[52:55], a[48:51], a[80:83], v[52:55]
ds_read_b128 a[16:19], %[vXr2] offset:22528
v_mfma_f32_16x16x32_bf16 v[88:91], a[48:51], a[84:87], v[88:91]
ds_read_b128 a[20:23], %[vXr2] offset:23552
v_mfma_f32_16x16x32_bf16 v[56:59], a[52:55], a[80:83], v[56:59]
v_mfma_f32_16x16x32_bf16 v[92:95], a[52:55], a[84:87], v[92:95]
ds_read_b128 a[24:27], %[vXr2] offset:24576
v_mfma_f32_16x16x32_bf16 v[60:63], a[56:59], a[80:83], v[60:63]
ds_read_b128 a[28:31], %[vXr2] offset:25600
v_mfma_f32_16x16x32_bf16 v[96:99], a[56:59], a[84:87], v[96:99]
buffer_load_dwordx4 a[116:119], %[vY2], %[sWS], s55 offen offset:1024 sc1
v_mfma_f32_16x16x32_bf16 v[64:67], a[60:63], a[80:83], v[64:67]
ds_read_b128 a[32:35], %[vXr2] offset:26624
v_mfma_f32_16x16x32_bf16 v[100:103], a[60:63], a[84:87], v[100:103]
v_mfma_f32_16x16x32_bf16 v[68:71], a[64:67], a[80:83], v[68:71]
v_mfma_f32_16x16x32_bf16 v[104:107], a[64:67], a[84:87], v[104:107]
buffer_load_dwordx4 v[226:229], %[vG], %[sWS], s56 offen offset:144 sc1
v_mfma_f32_16x16x32_bf16 v[72:75], a[68:71], a[80:83], v[72:75]
v_mfma_f32_16x16x32_bf16 v[108:111], a[68:71], a[84:87], v[108:111]
s_waitcnt vmcnt(10) lgkmcnt(0)
v_mfma_f32_16x16x32_bf16 v[40:43], a[0:3], a[88:91], v[40:43]
s_add_u32 s55, %[sHl15], 245760
ds_read_b128 a[36:39], %[vXr2] offset:27648
v_mfma_f32_16x16x32_bf16 v[76:79], a[0:3], a[92:95], v[76:79]
s_and_b32 s55, s55, 262143
ds_read_b128 a[40:43], %[vXr2] offset:28672
v_mfma_f32_16x16x32_bf16 v[44:47], a[4:7], a[88:91], v[44:47]
buffer_load_dwordx4 a[120:123], %[vY2], %[sWS], s55 offen offset:0 sc1
v_mfma_f32_16x16x32_bf16 v[80:83], a[4:7], a[92:95], v[80:83]
ds_read_b128 a[44:47], %[vXr2] offset:29696
v_mfma_f32_16x16x32_bf16 v[48:51], a[8:11], a[88:91], v[48:51]
ds_read_b128 a[48:51], %[vXr2] offset:30720
v_mfma_f32_16x16x32_bf16 v[84:87], a[8:11], a[92:95], v[84:87]
v_mfma_f32_16x16x32_bf16 v[52:55], a[12:15], a[88:91], v[52:55]
ds_read_b128 a[52:55], %[vXr2] offset:31744
v_mfma_f32_16x16x32_bf16 v[88:91], a[12:15], a[92:95], v[88:91]
ds_read_b128 a[56:59], %[vXr2] offset:32768
v_mfma_f32_16x16x32_bf16 v[56:59], a[16:19], a[88:91], v[56:59]
v_mfma_f32_16x16x32_bf16 v[92:95], a[16:19], a[92:95], v[92:95]
ds_read_b128 a[60:63], %[vXr2] offset:33792
v_mfma_f32_16x16x32_bf16 v[60:63], a[20:23], a[88:91], v[60:63]
ds_read_b128 a[64:67], %[vXr2] offset:34816
v_mfma_f32_16x16x32_bf16 v[96:99], a[20:23], a[92:95], v[96:99]
buffer_load_dwordx4 a[124:127], %[vY2], %[sWS], s55 offen offset:1024 sc1
v_mfma_f32_16x16x32_bf16 v[64:67], a[24:27], a[88:91], v[64:67]
ds_read_b128 a[68:71], %[vXr2] offset:35840
v_mfma_f32_16x16x32_bf16 v[100:103], a[24:27], a[92:95], v[100:103]
v_mfma_f32_16x16x32_bf16 v[68:71], a[28:31], a[88:91], v[68:71]
v_min3_u32 v242, v238, v239, v240
v_min_u32_e64 v242, v242, v241
v_cmp_eq_u32 vcc, 0, v242
s_cbranch_vccnz L_sl1_%=
L_ok1_%=:
v_mfma_f32_16x16x32_bf16 v[104:107], a[28:31], a[92:95], v[104:107]
buffer_load_dwordx4 v[230:233], %[vG8], %[sWS], s56 offen offset:0 sc1
v_mfma_f32_16x16x32_bf16 v[72:75], a[32:35], a[88:91], v[72:75]
v_mfma_f32_16x16x32_bf16 v[108:111], a[32:35], a[92:95], v[108:111]
s_waitcnt vmcnt(10) lgkmcnt(0)
s_barrier
v_mfma_f32_16x16x32_bf16 v[40:43], a[36:39], a[96:99], v[40:43]
s_add_u32 s55, %[sHl15], 249856
ds_read_b128 a[0:3], %[vXr3] offset:0
v_mfma_f32_16x16x32_bf16 v[76:79], a[36:39], a[100:103], v[76:79]
s_and_b32 s55, s55, 262143
ds_read_b128 a[4:7], %[vXr3] offset:1024
v_mfma_f32_16x16x32_bf16 v[44:47], a[40:43], a[96:99], v[44:47]
buffer_load_dwordx4 a[128:131], %[vY2], %[sWS], s55 offen offset:0 sc1
v_mfma_f32_16x16x32_bf16 v[80:83], a[40:43], a[100:103], v[80:83]
ds_read_b128 a[8:11], %[vXr3] offset:2048
v_mfma_f32_16x16x32_bf16 v[48:51], a[44:47], a[96:99], v[48:51]
ds_read_b128 a[12:15], %[vXr3] offset:3072
v_mfma_f32_16x16x32_bf16 v[84:87], a[44:47], a[100:103], v[84:87]
buffer_load_dwordx4 a[132:135], %[vY2], %[sWS], s55 offen offset:1024 sc1
v_mfma_f32_16x16x32_bf16 v[52:55], a[48:51], a[96:99], v[52:55]
ds_read_b128 a[16:19], %[vXr3] offset:4096
v_mfma_f32_16x16x32_bf16 v[88:91], a[48:51], a[100:103], v[88:91]
ds_read_b128 a[20:23], %[vXr3] offset:5120
v_mfma_f32_16x16x32_bf16 v[56:59], a[52:55], a[96:99], v[56:59]
buffer_load_dword v235, v236, %[sWS], s80 offen offset:0 sc1
v_mfma_f32_16x16x32_bf16 v[92:95], a[52:55], a[100:103], v[92:95]
ds_read_b128 a[24:27], %[vXr3] offset:6144
v_mfma_f32_16x16x32_bf16 v[60:63], a[56:59], a[96:99], v[60:63]
ds_read_b128 a[28:31], %[vXr3] offset:7168
v_mfma_f32_16x16x32_bf16 v[96:99], a[56:59], a[100:103], v[96:99]
buffer_load_dword v235, v237, %[sWS], s81 offen offset:0 sc1
v_mfma_f32_16x16x32_bf16 v[64:67], a[60:63], a[96:99], v[64:67]
ds_read_b128 a[32:35], %[vXr3] offset:8192
v_mfma_f32_16x16x32_bf16 v[100:103], a[60:63], a[100:103], v[100:103]
global_load_dword v235, v234, s[98:99]
v_mfma_f32_16x16x32_bf16 v[68:71], a[64:67], a[96:99], v[68:71]
s_add_u32 m0, %[sLx], 0
s_add_u32 s48, s20, 0
buffer_load_dwordx4 %[vL16], %[sWS], s48 offen offset:0 sc1 lds
v_mfma_f32_16x16x32_bf16 v[104:107], a[64:67], a[100:103], v[104:107]
buffer_load_dwordx4 %[vL16], %[sWS], s48 offen offset:1024 sc1 lds
v_mfma_f32_16x16x32_bf16 v[72:75], a[68:71], a[96:99], v[72:75]
buffer_load_dwordx4 %[vL16], %[sWS], s48 offen offset:2048 sc1 lds
v_mfma_f32_16x16x32_bf16 v[108:111], a[68:71], a[100:103], v[108:111]
s_waitcnt vmcnt(15) lgkmcnt(0)
v_mfma_f32_16x16x32_bf16 v[40:43], a[0:3], a[104:107], v[40:43]
ds_read_b128 a[36:39], %[vXr3] offset:9216
v_mfma_f32_16x16x32_bf16 v[76:79], a[0:3], a[108:111], v[76:79]
ds_read_b128 a[40:43], %[vXr3] offset:10240
v_mfma_f32_16x16x32_bf16 v[44:47], a[4:7], a[104:107], v[44:47]
buffer_load_dwordx4 %[vL16], %[sWS], s48 offen offset:3072 sc1 lds
v_mfma_f32_16x16x32_bf16 v[80:83], a[4:7], a[108:111], v[80:83]
s_add_u32 m0, %[sLx], 4096
ds_read_b128 a[44:47], %[vXr3] offset:11264
v_mfma_f32_16x16x32_bf16 v[48:51], a[8:11], a[104:107], v[48:51]
s_add_u32 s48, s20, 4096
ds_read_b128 a[48:51], %[vXr3] offset:12288
v_mfma_f32_16x16x32_bf16 v[84:87], a[8:11], a[108:111], v[84:87]
buffer_load_dwordx4 %[vL16], %[sWS], s48 offen offset:0 sc1 lds
v_mfma_f32_16x16x32_bf16 v[52:55], a[12:15], a[104:107], v[52:55]
ds_read_b128 a[52:55], %[vXr3] offset:13312
v_mfma_f32_16x16x32_bf16 v[88:91], a[12:15], a[108:111], v[88:91]
ds_read_b128 a[56:59], %[vXr3] offset:14336
v_mfma_f32_16x16x32_bf16 v[56:59], a[16:19], a[104:107], v[56:59]
v_mfma_f32_16x16x32_bf16 v[92:95], a[16:19], a[108:111], v[92:95]
ds_read_b128 a[60:63], %[vXr3] offset:15360
v_mfma_f32_16x16x32_bf16 v[60:63], a[20:23], a[104:107], v[60:63]
ds_read_b128 a[64:67], %[vXr3] offset:16384
v_mfma_f32_16x16x32_bf16 v[96:99], a[20:23], a[108:111], v[96:99]
buffer_load_dwordx4 %[vL16], %[sWS], s48 offen offset:1024 sc1 lds
v_mfma_f32_16x16x32_bf16 v[64:67], a[24:27], a[104:107], v[64:67]
ds_read_b128 a[68:71], %[vXr3] offset:17408
v_mfma_f32_16x16x32_bf16 v[100:103], a[24:27], a[108:111], v[100:103]
buffer_load_dwordx4 %[vL16], %[sWS], s48 offen offset:2048 sc1 lds
v_mfma_f32_16x16x32_bf16 v[68:71], a[28:31], a[104:107], v[68:71]
v_mfma_f32_16x16x32_bf16 v[104:107], a[28:31], a[108:111], v[104:107]
buffer_load_dwordx4 %[vL16], %[sWS], s48 offen offset:3072 sc1 lds
v_mfma_f32_16x16x32_bf16 v[72:75], a[32:35], a[104:107], v[72:75]
s_add_u32 m0, %[sLx], 8192
s_add_u32 s48, s20, 8192
buffer_load_dwordx4 %[vL16], %[sWS], s48 offen offset:0 sc1 lds
v_mfma_f32_16x16x32_bf16 v[108:111], a[32:35], a[108:111], v[108:111]
s_waitcnt vmcnt(18) lgkmcnt(0)
v_mfma_f32_16x16x32_bf16 v[40:43], a[36:39], a[112:115], v[40:43]
ds_read_b128 a[0:3], %[vXr3] offset:18432
v_mfma_f32_16x16x32_bf16 v[76:79], a[36:39], a[116:119], v[76:79]
ds_read_b128 a[4:7], %[vXr3] offset:19456
v_mfma_f32_16x16x32_bf16 v[44:47], a[40:43], a[112:115], v[44:47]
v_mfma_f32_16x16x32_bf16 v[80:83], a[40:43], a[116:119], v[80:83]
ds_read_b128 a[8:11], %[vXr3] offset:20480
v_mfma_f32_16x16x32_bf16 v[48:51], a[44:47], a[112:115], v[48:51]
ds_read_b128 a[12:15], %[vXr3] offset:21504
v_mfma_f32_16x16x32_bf16 v[84:87], a[44:47], a[116:119], v[84:87]
v_mfma_f32_16x16x32_bf16 v[52:55], a[48:51], a[112:115], v[52:55]
ds_read_b128 a[16:19], %[vXr3] offset:22528
v_mfma_f32_16x16x32_bf16 v[88:91], a[48:51], a[116:119], v[88:91]
ds_read_b128 a[20:23], %[vXr3] offset:23552
)"
R"(v_mfma_f32_16x16x32_bf16 v[56:59], a[52:55], a[112:115], v[56:59]
v_mfma_f32_16x16x32_bf16 v[92:95], a[52:55], a[116:119], v[92:95]
ds_read_b128 a[24:27], %[vXr3] offset:24576
v_mfma_f32_16x16x32_bf16 v[60:63], a[56:59], a[112:115], v[60:63]
ds_read_b128 a[28:31], %[vXr3] offset:25600
v_mfma_f32_16x16x32_bf16 v[96:99], a[56:59], a[116:119], v[96:99]
v_mfma_f32_16x16x32_bf16 v[64:67], a[60:63], a[112:115], v[64:67]
ds_read_b128 a[32:35], %[vXr3] offset:26624
v_mfma_f32_16x16x32_bf16 v[100:103], a[60:63], a[116:119], v[100:103]
v_mfma_f32_16x16x32_bf16 v[68:71], a[64:67], a[112:115], v[68:71]
v_mfma_f32_16x16x32_bf16 v[104:107], a[64:67], a[116:119], v[104:107]
v_mfma_f32_16x16x32_bf16 v[72:75], a[68:71], a[112:115], v[72:75]
v_mfma_f32_16x16x32_bf16 v[108:111], a[68:71], a[116:119], v[108:111]
s_waitcnt vmcnt(15) lgkmcnt(0)
v_mfma_f32_16x16x32_bf16 v[40:43], a[0:3], a[120:123], v[40:43]
ds_read_b128 a[36:39], %[vXr3] offset:27648
v_mfma_f32_16x16x32_bf16 v[76:79], a[0:3], a[124:127], v[76:79]
ds_read_b128 a[40:43], %[vXr3] offset:28672
v_mfma_f32_16x16x32_bf16 v[44:47], a[4:7], a[120:123], v[44:47]
v_mfma_f32_16x16x32_bf16 v[80:83], a[4:7], a[124:127], v[80:83]
ds_read_b128 a[44:47], %[vXr3] offset:29696
v_mfma_f32_16x16x32_bf16 v[48:51], a[8:11], a[120:123], v[48:51]
ds_read_b128 a[48:51], %[vXr3] offset:30720
v_mfma_f32_16x16x32_bf16 v[84:87], a[8:11], a[124:127], v[84:87]
v_mfma_f32_16x16x32_bf16 v[52:55], a[12:15], a[120:123], v[52:55]
ds_read_b128 a[52:55], %[vXr3] offset:31744
v_mfma_f32_16x16x32_bf16 v[88:91], a[12:15], a[124:127], v[88:91]
ds_read_b128 a[56:59], %[vXr3] offset:32768
v_mfma_f32_16x16x32_bf16 v[56:59], a[16:19], a[120:123], v[56:59]
v_mfma_f32_16x16x32_bf16 v[92:95], a[16:19], a[124:127], v[92:95]
ds_read_b128 a[60:63], %[vXr3] offset:33792
v_mfma_f32_16x16x32_bf16 v[60:63], a[20:23], a[120:123], v[60:63]
ds_read_b128 a[64:67], %[vXr3] offset:34816
v_mfma_f32_16x16x32_bf16 v[96:99], a[20:23], a[124:127], v[96:99]
v_mfma_f32_16x16x32_bf16 v[64:67], a[24:27], a[120:123], v[64:67]
ds_read_b128 a[68:71], %[vXr3] offset:35840
v_mfma_f32_16x16x32_bf16 v[100:103], a[24:27], a[124:127], v[100:103]
v_mfma_f32_16x16x32_bf16 v[68:71], a[28:31], a[120:123], v[68:71]
v_mfma_f32_16x16x32_bf16 v[104:107], a[28:31], a[124:127], v[104:107]
s_cmp_eq_u32 %[sOwn], 0
v_mfma_f32_16x16x32_bf16 v[72:75], a[32:35], a[120:123], v[72:75]
v_mfma_f32_16x16x32_bf16 v[108:111], a[32:35], a[124:127], v[108:111]
s_waitcnt vmcnt(12) lgkmcnt(0)
s_cbranch_scc1 L_own2_%=
v_mfma_f32_16x16x32_bf16 v[40:43], a[36:39], a[128:131], v[40:43]
s_add_u32 s49, s34, 0
v_mfma_f32_16x16x32_bf16 v[76:79], a[36:39], a[132:135], v[76:79]
s_add_u32 s53, s49, 4096
v_mfma_f32_16x16x32_bf16 v[44:47], a[40:43], a[128:131], v[44:47]
s_add_u32 s55, s35, 0
v_mfma_f32_16x16x32_bf16 v[80:83], a[40:43], a[132:135], v[80:83]
s_nop 0
v_mfma_f32_16x16x32_bf16 v[48:51], a[44:47], a[128:131], v[48:51]
s_nop 0
v_mfma_f32_16x16x32_bf16 v[84:87], a[44:47], a[132:135], v[84:87]
v_cvt_pk_bf16_f32 v40, v40, v41
v_cvt_pk_bf16_f32 v41, v42, v43
v_max_u32 v40, 1, v40
v_max_u32 v41, 1, v41
v_mfma_f32_16x16x32_bf16 v[52:55], a[48:51], a[128:131], v[52:55]
v_cvt_pk_bf16_f32 v76, v76, v77
v_cvt_pk_bf16_f32 v77, v78, v79
v_max_u32 v76, 1, v76
v_max_u32 v77, 1, v77
v_mfma_f32_16x16x32_bf16 v[88:91], a[48:51], a[132:135], v[88:91]
v_cvt_pk_bf16_f32 v42, v44, v45
v_cvt_pk_bf16_f32 v43, v46, v47
v_max_u32 v42, 1, v42
v_max_u32 v43, 1, v43
v_mfma_f32_16x16x32_bf16 v[56:59], a[52:55], a[128:131], v[56:59]
buffer_store_dwordx4 v[40:43], %[vL16], %[sWS], s49 offen offset:0 sc1
v_cvt_pk_bf16_f32 v78, v80, v81
v_cvt_pk_bf16_f32 v79, v82, v83
v_max_u32 v78, 1, v78
v_max_u32 v79, 1, v79
v_mfma_f32_16x16x32_bf16 v[92:95], a[52:55], a[132:135], v[92:95]
buffer_store_dwordx4 v[76:79], %[vL16], %[sWS], s53 offen offset:0 sc1
v_cvt_pk_bf16_f32 v44, v48, v49
v_cvt_pk_bf16_f32 v45, v50, v51
v_max_u32 v44, 1, v44
v_max_u32 v45, 1, v45
v_mfma_f32_16x16x32_bf16 v[60:63], a[56:59], a[128:131], v[60:63]
v_cvt_pk_bf16_f32 v80, v84, v85
v_cvt_pk_bf16_f32 v81, v86, v87
v_max_u32 v80, 1, v80
v_max_u32 v81, 1, v81
v_mfma_f32_16x16x32_bf16 v[96:99], a[56:59], a[132:135], v[96:99]
v_cvt_pk_bf16_f32 v46, v52, v53
v_cvt_pk_bf16_f32 v47, v54, v55
v_max_u32 v46, 1, v46
v_max_u32 v47, 1, v47
v_mfma_f32_16x16x32_bf16 v[64:67], a[60:63], a[128:131], v[64:67]
buffer_store_dwordx4 v[44:47], %[vL16], %[sWS], s49 offen offset:1024 sc1
v_cvt_pk_bf16_f32 v82, v88, v89
v_cvt_pk_bf16_f32 v83, v90, v91
v_max_u32 v82, 1, v82
v_max_u32 v83, 1, v83
v_mfma_f32_16x16x32_bf16 v[100:103], a[60:63], a[132:135], v[100:103]
buffer_store_dwordx4 v[80:83], %[vL16], %[sWS], s53 offen offset:1024 sc1
v_cvt_pk_bf16_f32 v48, v56, v57
v_cvt_pk_bf16_f32 v49, v58, v59
v_max_u32_e64 v48, 1, v48
v_max_u32 v49, 1, v49
v_mfma_f32_16x16x32_bf16 v[68:71], a[64:67], a[128:131], v[68:71]
LPc_5:
s_cbranch_execz LRc
v_cvt_pk_bf16_f32 v84, v92, v93
v_cvt_pk_bf16_f32 v85, v94, v95
v_max_u32 v84, 1, v84
v_max_u32 v85, 1, v85
v_mfma_f32_16x16x32_bf16 v[104:107], a[64:67], a[132:135], v[104:107]
v_cvt_pk_bf16_f32 v50, v60, v61
v_cvt_pk_bf16_f32 v51, v62, v63
v_max_u32 v50, 1, v50
v_max_u32 v51, 1, v51
v_mfma_f32_16x16x32_bf16 v[72:75], a[68:71], a[128:131], v[72:75]
buffer_store_dwordx4 v[48:51], %[vL16], %[sWS], s49 offen offset:2048 sc1
v_cvt_pk_bf16_f32 v86, v96, v97
v_cvt_pk_bf16_f32 v87, v98, v99
v_max_u32 v86, 1, v86
v_max_u32 v87, 1, v87
v_mfma_f32_16x16x32_bf16 v[108:111], a[68:71], a[132:135], v[108:111]
buffer_store_dwordx4 v[84:87], %[vL16], %[sWS], s53 offen offset:2048 sc1
v_cvt_pk_bf16_f32 v52, v64, v65
v_cvt_pk_bf16_f32 v53, v66, v67
v_max_u32 v52, 1, v52
v_max_u32 v53, 1, v53
v_cvt_pk_bf16_f32 v88, v100, v101
v_cvt_pk_bf16_f32 v89, v102, v103
v_max_u32 v88, 1, v88
v_max_u32 v89, 1, v89
v_cvt_pk_bf16_f32 v54, v68, v69
v_cvt_pk_bf16_f32 v55, v70, v71
v_max_u32 v54, 1, v54
v_max_u32 v55, 1, v55
buffer_store_dwordx4 v[52:55], %[vL16], %[sWS], s49 offen offset:3072 sc1
v_cvt_pk_bf16_f32 v90, v104, v105
v_cvt_pk_bf16_f32 v91, v106, v107
v_max_u32 v90, 1, v90
v_max_u32 v91, 1, v91
buffer_store_dwordx4 v[88:91], %[vL16], %[sWS], s53 offen offset:3072 sc1
v_cvt_pk_bf16_f32 v56, v72, v73
v_cvt_pk_bf16_f32 v57, v74, v75
v_max_u32 v56, 1, v56
v_max_u32 v57, 1, v57
buffer_store_dwordx2 v[56:57], %[vL8], %[sWS], s55 offen offset:0 sc1
v_cvt_pk_bf16_f32 v92, v108, v109
v_cvt_pk_bf16_f32 v93, v110, v111
v_max_u32 v92, 1, v92
v_max_u32 v93, 1, v93
buffer_store_dwordx2 v[92:93], %[vL8], %[sWS], s55 offen offset:512 sc1
L_join2_%=:
s_barrier
s_cmp_eq_u32 %[sOwn], 0
s_cbranch_scc0 L_njb_%=
s_cmp_lg_u32 s83, 0
s_cbranch_scc0 L_nj2a_%=
s_mov_b32 s49, s81
buffer_load_dwordx4 a[0:3], %[vL16], %[sWS], s49 offen offset:0 sc1
s_add_u32 s49, s49, s83
buffer_load_dwordx4 a[4:7], %[vL16], %[sWS], s49 offen offset:0 sc1
s_add_u32 s49, s49, s83
buffer_load_dwordx4 a[8:11], %[vL16], %[sWS], s49 offen offset:0 sc1
s_add_u32 s49, s49, s83
buffer_load_dwordx4 a[12:15], %[vL16], %[sWS], s49 offen offset:0 sc1
s_add_u32 s49, s49, s83
buffer_load_dwordx4 a[16:19], %[vL16], %[sWS], s49 offen offset:0 sc1
s_add_u32 s49, s49, s83
buffer_load_dwordx4 a[20:23], %[vL16], %[sWS], s49 offen offset:0 sc1
s_add_u32 s49, s49, s83
buffer_load_dwordx4 a[24:27], %[vL16], %[sWS], s49 offen offset:0 sc1
s_add_u32 s49, s49, s83
buffer_load_dwordx4 a[28:31], %[vL16], %[sWS], s49 offen offset:0 sc1
s_add_u32 s49, s49, s83
buffer_load_dwordx4 a[32:35], %[vL16], %[sWS], s49 offen offset:0 sc1
s_add_u32 s49, s49, s83
buffer_load_dwordx4 a[36:39], %[vL16], %[sWS], s49 offen offset:0 sc1
s_add_u32 s49, s49, s83
buffer_load_dwordx4 a[40:43], %[vL16], %[sWS], s49 offen offset:0 sc1
s_add_u32 s49, s49, s83
buffer_load_dwordx4 a[44:47], %[vL16], %[sWS], s49 offen offset:0 sc1
s_add_u32 s49, s49, s83
buffer_load_dwordx4 a[48:51], %[vL16], %[sWS], s49 offen offset:0 sc1
s_add_u32 s49, s49, s83
buffer_load_dwordx4 a[52:55], %[vL16], %[sWS], s49 offen offset:0 sc1
s_add_u32 s49, s49, s83
buffer_load_dwordx4 a[56:59], %[vL16], %[sWS], s49 offen offset:0 sc1
s_add_u32 s49, s49, s83
buffer_load_dwordx4 a[60:63], %[vL16], %[sWS], s49 offen offset:0 sc1
s_add_u32 s49, s49, s83
buffer_load_dwordx4 a[64:67], %[vL16], %[sWS], s49 offen offset:0 sc1
s_add_u32 s49, s49, s83
buffer_load_dwordx4 a[68:71], %[vL16], %[sWS], s49 offen offset:0 sc1
s_add_u32 s49, s49, s83
L_nj2a_%=:
L_njb_%=:
s_branch L_end1_%=
L_sl0_%=:
s_cmp_eq_u32 s69, 0
s_cbranch_scc1 L_ok0_%=
s_sub_u32 s69, s69, 1
)"
R"(buffer_load_dword v117, v116, %[sWS], s73 offen offset:0 sc1
s_waitcnt vmcnt(0)
v_cmp_eq_u32 vcc, 0, v117
s_cbranch_vccnz L_sl0_%=
s_branch L_ok0_%=
L_sl1_%=:
s_cmp_eq_u32 s69, 0
s_cbranch_scc1 L_ok1_%=
s_sub_u32 s69, s69, 1
buffer_load_dwordx4 v[238:241], %[vFC], %[sWS], 0 offen offset:0 sc1
s_waitcnt vmcnt(0)
v_min3_u32 v242, v238, v239, v240
v_min_u32_e64 v242, v242, v241
v_cmp_eq_u32 vcc, 0, v242
s_cbranch_vccnz L_sl1_%=
s_branch L_ok1_%=
L_own2_%=:
LPc_6:
s_cbranch_execz LRc
v_mfma_f32_16x16x32_bf16 v[40:43], a[36:39], a[128:131], v[40:43]
s_nop 0
buffer_load_dwordx4 a[180:183], %[vL16], %[sWS], s49 offen offset:0 sc1
s_add_u32 s49, s49, s82
v_mfma_f32_16x16x32_bf16 v[76:79], a[36:39], a[132:135], v[76:79]
s_nop 0
buffer_load_dwordx4 a[184:187], %[vL16], %[sWS], s49 offen offset:0 sc1
s_add_u32 s49, s49, s82
v_mfma_f32_16x16x32_bf16 v[44:47], a[40:43], a[128:131], v[44:47]
s_nop 0
buffer_load_dwordx4 a[188:191], %[vL16], %[sWS], s49 offen offset:0 sc1
s_add_u32 s49, s49, s82
v_mfma_f32_16x16x32_bf16 v[80:83], a[40:43], a[132:135], v[80:83]
s_nop 0
buffer_load_dwordx4 a[192:195], %[vL16], %[sWS], s49 offen offset:0 sc1
s_add_u32 s49, s49, s82
v_mfma_f32_16x16x32_bf16 v[48:51], a[44:47], a[128:131], v[48:51]
s_nop 0
buffer_load_dwordx4 a[196:199], %[vL16], %[sWS], s49 offen offset:0 sc1
s_add_u32 s49, s49, s82
v_mfma_f32_16x16x32_bf16 v[84:87], a[44:47], a[132:135], v[84:87]
ds_write_b128 %[vXr2], v[40:43] offset:0
buffer_load_dwordx4 a[200:203], %[vL16], %[sWS], s49 offen offset:0 sc1
s_add_u32 s49, s49, s82
v_mfma_f32_16x16x32_bf16 v[52:55], a[48:51], a[128:131], v[52:55]
ds_write_b128 %[vXr2], v[76:79] offset:9216
buffer_load_dwordx4 a[204:207], %[vL16], %[sWS], s49 offen offset:0 sc1
s_add_u32 s49, s49, s82
v_mfma_f32_16x16x32_bf16 v[88:91], a[48:51], a[132:135], v[88:91]
ds_write_b128 %[vXr2], v[44:47] offset:1024
buffer_load_dwordx4 a[208:211], %[vL16], %[sWS], s49 offen offset:0 sc1
s_add_u32 s49, s49, s82
v_mfma_f32_16x16x32_bf16 v[56:59], a[52:55], a[128:131], v[56:59]
ds_write_b128 %[vXr2], v[80:83] offset:10240
buffer_load_dwordx4 a[212:215], %[vL16], %[sWS], s49 offen offset:0 sc1
s_add_u32 s49, s49, s82
v_mfma_f32_16x16x32_bf16 v[92:95], a[52:55], a[132:135], v[92:95]
ds_write_b128 %[vXr2], v[48:51] offset:2048
buffer_load_dwordx4 a[216:219], %[vL16], %[sWS], s49 offen offset:0 sc1
s_add_u32 s49, s49, s82
v_mfma_f32_16x16x32_bf16 v[60:63], a[56:59], a[128:131], v[60:63]
ds_write_b128 %[vXr2], v[84:87] offset:11264
buffer_load_dwordx4 a[220:223], %[vL16], %[sWS], s49 offen offset:0 sc1
s_add_u32 s49, s49, s82
v_mfma_f32_16x16x32_bf16 v[96:99], a[56:59], a[132:135], v[96:99]
ds_write_b128 %[vXr2], v[52:55] offset:3072
buffer_load_dwordx4 a[224:227], %[vL16], %[sWS], s49 offen offset:0 sc1
s_add_u32 s49, s49, s82
v_mfma_f32_16x16x32_bf16 v[64:67], a[60:63], a[128:131], v[64:67]
ds_write_b128 %[vXr2], v[88:91] offset:12288
buffer_load_dwordx4 a[228:231], %[vL16], %[sWS], s49 offen offset:0 sc1
s_add_u32 s49, s49, s82
v_mfma_f32_16x16x32_bf16 v[100:103], a[60:63], a[132:135], v[100:103]
ds_write_b128 %[vXr2], v[56:59] offset:4096
buffer_load_dwordx4 a[232:235], %[vL16], %[sWS], s49 offen offset:0 sc1
s_add_u32 s49, s49, s82
v_mfma_f32_16x16x32_bf16 v[68:71], a[64:67], a[128:131], v[68:71]
ds_write_b128 %[vXr2], v[92:95] offset:13312
buffer_load_dwordx4 a[236:239], %[vL16], %[sWS], s49 offen offset:0 sc1
s_add_u32 s49, s49, s82
v_mfma_f32_16x16x32_bf16 v[104:107], a[64:67], a[132:135], v[104:107]
ds_write_b128 %[vXr2], v[60:63] offset:5120
buffer_load_dwordx4 a[240:243], %[vL16], %[sWS], s49 offen offset:0 sc1
s_add_u32 s49, s49, s82
v_mfma_f32_16x16x32_bf16 v[72:75], a[68:71], a[128:131], v[72:75]
ds_write_b128 %[vXr2], v[96:99] offset:14336
buffer_load_dwordx4 a[244:247], %[vL16], %[sWS], s49 offen offset:0 sc1
s_add_u32 s49, s49, s82
v_mfma_f32_16x16x32_bf16 v[108:111], a[68:71], a[132:135], v[108:111]
ds_write_b128 %[vXr2], v[64:67] offset:6144
buffer_load_dwordx4 a[248:251], %[vL16], %[sWS], s49 offen offset:0 sc1
s_nop 2
ds_write_b128 %[vXr2], v[100:103] offset:15360
s_nop 2
ds_write_b128 %[vXr2], v[68:71] offset:7168
s_nop 2
ds_write_b128 %[vXr2], v[104:107] offset:16384
s_nop 2
ds_write_b128 %[vXr2], v[72:75] offset:8192
s_nop 2
ds_write_b128 %[vXr2], v[108:111] offset:17408
s_nop 2
s_nop 2
s_nop 2
s_nop 2
s_mov_b64 exec, s[94:95]
buffer_load_dwordx4 v[112:115], %[vT], %[sWS], 0 offen offset:0 sc1
buffer_load_dwordx4 v[116:119], %[vT], %[sWS], 0 offen offset:64 sc1
buffer_load_dwordx4 v[120:123], %[vT], %[sWS], 0 offen offset:128 sc1
buffer_load_dwordx4 v[124:127], %[vT], %[sWS], 0 offen offset:192 sc1
buffer_load_dwordx4 v[128:131], %[vT], %[sWS], 0 offen offset:256 sc1
buffer_load_dwordx4 v[132:135], %[vT], %[sWS], 0 offen offset:320 sc1
buffer_load_dwordx4 v[136:139], %[vT], %[sWS], 0 offen offset:384 sc1
buffer_load_dwordx4 v[140:143], %[vT], %[sWS], 0 offen offset:448 sc1
buffer_load_dwordx4 v[144:147], %[vT], %[sWS], 0 offen offset:512 sc1
buffer_load_dwordx4 v[148:151], %[vT], %[sWS], 0 offen offset:576 sc1
buffer_load_dwordx4 v[152:155], %[vT], %[sWS], 0 offen offset:640 sc1
buffer_load_dwordx4 v[156:159], %[vT], %[sWS], 0 offen offset:704 sc1
buffer_load_dwordx4 v[160:163], %[vT], %[sWS], 0 offen offset:768 sc1
buffer_load_dwordx4 v[164:167], %[vT], %[sWS], 0 offen offset:832 sc1
buffer_load_dwordx4 v[168:171], %[vT], %[sWS], 0 offen offset:896 sc1
buffer_load_dwordx4 v[172:175], %[vT], %[sWS], 0 offen offset:960 sc1
buffer_load_dwordx4 v[176:179], %[vT], %[sWS], 0 offen offset:1024 sc1
buffer_load_dwordx4 v[180:183], %[vT], %[sWS], 0 offen offset:1088 sc1
s_mov_b64 exec, -1
s_waitcnt lgkmcnt(0)
s_branch L_join2_%=
L_end1_%=:
LPc_7:
s_cbranch_execz LRc
L_pe2:
)"
    : "+{s69}"(r_s69), "+{s71}"(r_s71), "+{s73}"(r_s73), "+{s76}"(r_s76), "+{v[40:47]}"(r_v40), "+{v[48:55]}"(r_v48), "+{v[56:63]}"(r_v56), "+{v[64:95]}"(r_v64), "+{v[96:127]}"(r_v96), "=&{s16}"(r_s16), "=&{s17}"(r_s17), "=&{s18}"(r_s18), "=&{s19}"(r_s19), "=&{s20}"(r_s20), "=&{s21}"(r_s21), "=&{s22}"(r_s22), "=&{s23}"(r_s23), "=&{s24}"(r_s24), "=&{s25}"(r_s25), "=&{s26}"(r_s26), "=&{s27}"(r_s27), "=&{s28}"(r_s28), "=&{s29}"(r_s29), "=&{s30}"(r_s30), "=&{s31}"(r_s31), "=&{s32}"(r_s32), "=&{s33}"(r_s33), "=&{s34}"(r_s34), "=&{s35}"(r_s35), "=&{s36}"(r_s36), "=&{s37}"(r_s37), "=&{s38}"(r_s38), "=&{s39}"(r_s39), "=&{s40}"(r_s40), "=&{s41}"(r_s41), "=&{s42}"(r_s42), "=&{s43}"(r_s43), "=&{s44}"(r_s44), "=&{s45}"(r_s45), "=&{s46}"(r_s46), "=&{s47}"(r_s47), "=&{v[128:159]}"(r_v128), "=&{v[160:191]}"(r_v160), "=&{v[192:223]}"(r_v192), "=&{v[224:255]}"(r_v224), "+v"(tid), "+s"(bid)
    : [s2Xo] "s"(s2Xo), [sHl] "s"(sHl), [sHl15] "s"(sHl15), [sLx] "s"(sLx), [sOwn] "s"(sOwn), [sPo3] "s"(sPo3), [sPo4] "s"(sPo4), [sWS] "s"(sWS), [sXin] "s"(sXin), [vCp] "v"(vCp), [vFC] "v"(vFC), [vG] "v"(vG), [vG8] "v"(vG8), [vL16] "v"(vL16), [vL8] "v"(vL8), [vPk] "v"(vPk), [vPl] "v"(vPl), [vR] "v"(vR), [vR8] "v"(vR8), [vT] "v"(vT), [vXr0] "v"(vXr0), [vXr1] "v"(vXr1), [vXr2] "v"(vXr2), [vXr3] "v"(vXr3), [vY2] "v"(vY2)
    : "memory", "vcc", "scc", "m0","a0","a1","a2","a3","a4","a5","a6","a7","a8","a9","a10","a11","a12","a13","a14","a15","a16","a17","a18","a19","a20","a21","a22","a23","a24","a25","a26","a27","a28","a29","a30","a31","a32","a33","a34","a35","a36","a37","a38","a39","a40","a41","a42","a43","a44","a45","a46","a47","a48","a49","a50","a51","a52","a53","a54","a55","a56","a57","a58","a59","a60","a61","a62","a63","a64","a65","a66","a67","a68","a69","a70","a71","a72","a73","a74","a75","a76","a77","a78","a79","a80","a81","a82","a83","a84","a85","a86","a87","a88","a89","a90","a91","a92","a93","a94","a95","a96","a97","a98","a99","a100","a101","a102","a103","a104","a105","a106","a107","a108","a109","a110","a111","a112","a113","a114","a115","a116","a117","a118","a119","a120","a121","a122","a123","a124","a125","a126","a127","a128","a129","a130","a131","a132","a133","a134","a135","a136","a137","a138","a139","a140","a141","a142","a143","a144","a145","a146","a147","a148","a149","a150","a151","a152","a153","a154","a155","a156","a157","a158","a159","a160","a161","a162","a163","a164","a165","a166","a167","a168","a169","a170","a171","a172","a173","a174","a175","a176","a177","a178","a179","a180","a181","a182","a183","a184","a185","a186","a187","a188","a189","a190","a191","a192","a193","a194","a195","a196","a197","a198","a199","a200","a201","a202","a203","a204","a205","a206","a207","a208","a209","a210","a211","a212","a213","a214","a215","a216","a217","a218","a219","a220","a221","a222","a223","a224","a225","a226","a227","a228","a229","a230","a231","a232","a233","a234","a235","a236","a237","a238","a239","a240","a241","a242","a243","a244","a245","a246","a247","a248","a249","a250","a251","a252","a253","a254","a255","s48","s49","s53","s54","s55","s56","s80","s81","s82","s83","s94","s95","s98","s99");
  }
  bid = RFL(bid);
  {
  const unsigned hp = RFL(r_s71) >> 13, half = RFL(r_s76);
  const unsigned lane = tid & 63, w = RFL(tid >> 6), b = w & 1, hf = w >> 1;
  const unsigned xcd = bid & 7, jj = bid >> 3, kq = (xcd ^ KQX) & 3, rb = half * 4 + (jj >> 3), mt = 2 * rb + (jj & 1), nt = (jj >> 1) & 3, sHl = (jj & 7);
  const unsigned ldsb = 0, OOB = 0x7f000000u, vCp = CODE_OFF;
  const srd_t sWS = make_srd(ws, WS_END);
  const unsigned sLx = (ldsb + w * 9216), s2Xo = (WS_PK + (nt * 128 + kq * 32 + w) * 9216), sFBr = nr(WS_FB, hp) + (((2 * mt + hf) & 15) * 4 + kq) * 128;
  const unsigned r64 = ((sFBr) >> 9) & 15, gw4 = ((bid * 4 + w) * 4) & 4095;
  const unsigned vY2 = nr(WS_O + kq * 4 * MB + r64 * 262144, hp) + b * 2048 + lane * 16, sHl15 = sHl << 15;
  const unsigned ct = kq * 4 + nt, j = lane & 15, g = lane >> 4, sg = mt * 4 + w;
  const unsigned vL16 = (lane * 16), vL8 = lane * 8;
  const unsigned vXr0 = ldsb + vL16, vXr1 = vXr0 + 36864, vXr2 = vXr0 + 2 * 36864, vXr3 = vXr0 + 3 * 36864;
  const unsigned bLo = RFL(r_s16), bHi = RFL(r_s17), oLo = RFL(r_s18), oHi = RFL(r_s19), sPo3 = WS_PAR + 131072 + gw4 * 32, sPo4 = WS_PAR + 262144 + gw4 * 8;
  const srd_t sXin = make_srd(x, S_ * D_ * 2), sB = make_srd((void*)(bLo | (size_t)bHi << 32), S_ * 2), sOut = make_srd((void*)(oLo | (size_t)oHi << 32), S_ * D_ * 2);
  const unsigned vU1 = vXr2 + (b * 9 + hf * 4) * 1024, vV1 = vXr2 + (b * 9 + 8) * 1024, vX2 = ldsb + 4 * 36864 + 1024 + tid * 16;
  const unsigned s = half * 1024 + (r64 >> 1) * 128 + kq * 32 + rowperm(b, j), d0 = nt * 144 + 64 * hf + 8 * g, d8 = nt * 144 + 128 + 4 * g, m8 = (hf - 1) & OOB;
  const unsigned vR = (s * D_ + d0) * 2, vR8 = (s * D_ + d8) * 2 | m8;
  const unsigned vG = WS_MOD + (2 * D_ + d0) * 4, vG8 = WS_MOD + (2 * D_ + d8) * 4, vG58 = vG8 + 3 * D_ * 4, vRc = vL16 + hf * 2048;
  const unsigned vFC = WS_FB + (tid >> 1) * 128 + (tid & 1) * 16;
  const unsigned sOwn = w ^ kq, tl = ((mt & 7) * 4 + nt) * 4, sx = (kq - w - 1) & 3, sSnd = nr(WS_E2 + sx * 2 * MB + (tl + w) * 16384, hp), sRcv = nr(WS_E2 + (tl + kq) * 16384, hp) + b * 4096;
  const unsigned er = nr(WS_E2R, hp) + tl * 8192, sSnd2 = er + hf * 16384 + b * 4096 + sx * 1024, sRcv2 = RFL(r_s33), sRcv5 = sRcv2 - MB;
  const unsigned fc = bid, strip = fc / 9, fn = fc - strip * 9, fc2 = 256 + (fc & 31), strip2 = fc2 / 9, fn2 = fc2 - strip2 * 9, m2 = fc < 32 ? 0 : OOB;
  const unsigned sJob = RFL(win_stride(strip, fn) * 1024), sJob2 = RFL(fc < 32 ? win_stride(strip2, fn2) * 1024 : 0), wb = WS_PK + (unsigned)PK_WIN + win_base(strip, fn) * 1024, wb2 = WS_PK + (unsigned)PK_WIN + win_base(strip2, fn2) * 1024;
  const unsigned vW = wb + vL16, vW2 = wb2 + vL16, vT = WS_MOD + 13824 + (lane & 3) * 1152 + g * 16;
  const unsigned pk = ((sOwn - 1) & 3) * 8 + (lane >> 3), mp = pk < 18 ? 0 : OOB, vWp = (wb + pk * sJob + (lane & 7) * 128) | mp, vWp2 = (wb2 + pk * sJob2 + (lane & 7) * 128) | mp | m2;
  const unsigned vPk = pk, vPl = (lane & 7) * 128 | mp, sWb = wb, sWb2 = wb2 | m2;
  const unsigned mj = (j | sOwn) == 0 ? 0 : OOB;
  const unsigned yb = nr(WS_Y, hp) + ((mt & 7) * 4 + kq) * 16384, ybb = yb + b * 1024, k0 = (nt * 144 + 64 * hf) >> 5, dg = ((nt & 1) * 2 + g), yl = (j * 16 + (dg & 3) * 256), hAS = (g * 32 | mj), hSt = (rowperm(0, j) * 64 | (g == 0 ? 0 : OOB)), hF8 = (j * 16 + (g >> 1) * 256 + (g & 1) * 8);
  const unsigned aS1 = RFL(r_s28), aS2 = RFL(r_s29), ySb = RFL(r_s44);
  const unsigned Y0 = RFL(r_s40), Y1 = RFL(r_s41), Y2 = RFL(r_s42), Y8 = RFL(r_s43);
  unsigned yAS = aS1 | hAS, yAS2 = aS2 | hAS, ySt = ySb + hSt;
  unsigned yF = yl + (dg < 4 ? Y0 : Y1), yF2 = yl + (dg < 4 ? Y1 : Y2), yF8 = Y8 + hF8, yU1 = vU1, yV1 = vV1, yX2 = vX2, yRc = vRc, yL8 = vL8;
  const unsigned ck8 = 18 * (j >> 2) + 4 * (j & 2) + 4; unsigned yCan = yoff(ck8 >> 2) + (j & 1) * 1024 + (ck8 & 3) * 256;
  const unsigned long long sMsk = hf * 0x3f800000u; const unsigned sInv = RFL(__float_as_uint(hf ? 1.f / 80 : 1.f / 64));
  const unsigned tbl = ldsb + 4 * 36864 + w * 256, vOwn = vXr1 + w * 9216;
  const unsigned s3Yo = RFL(r_s31), vS = s3Yo + 24 * MB + 4096 + (lane & 31) * 64, ctb = RFL(r_s30), vA2 = ctb + 16 * 1152 + j * 8, vA1 = ctb + g * 32;
  const unsigned vTw = tbl + (lane & 31) * 4, vTr = tbl + g * 32, vT1 = tbl + rowperm(0, j) * 4;
  const unsigned ex = RFL(r_s45), hEw = ((ct >> 3) * 16384 + (ct & 7) * 1024 + g * 64 + (j >> 2) * 16), vEw4 = ex + hEw, hEr = ((lane >> 5) * 16384 + (lane & 31) * 8), vEr = ex + hEr;
  const unsigned sMt16 = RFL(r_s26), fz = RFL(r_s46), vFl = fz + (j & 7) * 4, vFs = lane == 0 ? fz + (sMt16 >> 14) : OOB;
  const unsigned vB = half * 2048 + (sMt16 >> 8) + w * 64 + 2 * rowperm(0, j), y4b = RFL(r_s47), vY4 = y4b + vL16;
  const unsigned s3Xo = WS_PK + (unsigned)PK_WIN + ct * 324 * 1024 + w * 9216, sXo4 = nr(WS_Z2 + (ct * 32 + w) * 16384, hp), sXr4 = nr(WS_Z2 + 8 * MB, hp) + (ct >> 1) * 16384 + (ct & 1) * 4096 + w * 1024;
  const unsigned sUo = nr(WS_U + ((sg & 31) * 4 + kq) * 65536, hp), sUr = RFL(r_s37), sNt = nt, vU8 = sUr + (nt >> 1) * 2048 + (nt & 1) * 512 + hF8;
  const unsigned vG5 = vG + 3 * D_ * 4;
  const unsigned hw = w < 3 ? 5120 * w : 13312, sLh = ldsb + hw, x5 = WS_PK + (unsigned)PK_WOUT + (nt * 72 + kq * 18) * 9216, s5X4 = x5 + 16 * 9216 + hw;
  const unsigned s5X0 = x5 + (4 * nt + w) * 9216, s5X1 = x5 + (4 * ((nt + 1 + hf) & 3) + b) * 9216, s5X2 = x5 + (4 * ((nt + 3 - 2 * hf) & 3) + w) * 9216, s5X3 = x5 + (4 * ((nt + 2 + hf) & 3) + 2 + b) * 9216;
  asm volatile(
R"(L_pb3:
s_nop 4
.p2align 3
ds_read_b128 v[40:43], %[yU1] offset:0
ds_read_b128 v[44:47], %[yU1] offset:1024
ds_read_b128 v[48:51], %[yU1] offset:2048
ds_read_b128 v[52:55], %[yU1] offset:3072
ds_read_b128 v[56:59], %[yV1] offset:0
s_add_u32 s53, s32, 0
buffer_load_dwordx4 v[80:83], %[yRc], %[sWS], s53 offen offset:0 sc1
buffer_load_dwordx4 v[84:87], %[yRc], %[sWS], s53 offen offset:1024 sc1
buffer_load_dwordx2 v[88:89], %[yL8], %[sWS], s33 offen offset:0 sc1
s_add_u32 s53, s32, 2097152
s_xor_b32 s53, s53, 8192
buffer_load_dwordx4 v[90:93], %[yRc], %[sWS], s53 offen offset:0 sc1
buffer_load_dwordx4 v[94:97], %[yRc], %[sWS], s53 offen offset:1024 sc1
buffer_load_dwordx2 v[98:99], %[yL8], %[sWS], s33 offen offset:1024 sc1
s_add_u32 s53, s32, 4194304
buffer_load_dwordx4 v[100:103], %[yRc], %[sWS], s53 offen offset:0 sc1
buffer_load_dwordx4 v[104:107], %[yRc], %[sWS], s53 offen offset:1024 sc1
buffer_load_dwordx2 v[108:109], %[yL8], %[sWS], s33 offen offset:2048 sc1
s_cmp_eq_u32 %[sOwn], 0
s_cbranch_scc0 L_nojob2_%=
s_waitcnt vmcnt(9)
v_mfma_f32_16x16x32_bf16 v[242:245], a[180:183], v[112:115], 0
v_mfma_f32_16x16x32_bf16 v[246:249], a[0:3], v[112:115], 0
v_mfma_f32_16x16x32_bf16 v[242:245], a[184:187], v[116:119], v[242:245]
v_mfma_f32_16x16x32_bf16 v[246:249], a[4:7], v[116:119], v[246:249]
v_mfma_f32_16x16x32_bf16 v[242:245], a[188:191], v[120:123], v[242:245]
v_mfma_f32_16x16x32_bf16 v[246:249], a[8:11], v[120:123], v[246:249]
v_mfma_f32_16x16x32_bf16 v[242:245], a[192:195], v[124:127], v[242:245]
v_mfma_f32_16x16x32_bf16 v[246:249], a[12:15], v[124:127], v[246:249]
v_mfma_f32_16x16x32_bf16 v[242:245], a[196:199], v[128:131], v[242:245]
v_mfma_f32_16x16x32_bf16 v[246:249], a[16:19], v[128:131], v[246:249]
v_mfma_f32_16x16x32_bf16 v[242:245], a[200:203], v[132:135], v[242:245]
v_mfma_f32_16x16x32_bf16 v[246:249], a[20:23], v[132:135], v[246:249]
v_mfma_f32_16x16x32_bf16 v[242:245], a[204:207], v[136:139], v[242:245]
v_mfma_f32_16x16x32_bf16 v[246:249], a[24:27], v[136:139], v[246:249]
v_mfma_f32_16x16x32_bf16 v[242:245], a[208:211], v[140:143], v[242:245]
v_mfma_f32_16x16x32_bf16 v[246:249], a[28:31], v[140:143], v[246:249]
v_mfma_f32_16x16x32_bf16 v[242:245], a[212:215], v[144:147], v[242:245]
v_mfma_f32_16x16x32_bf16 v[246:249], a[32:35], v[144:147], v[246:249]
v_mfma_f32_16x16x32_bf16 v[242:245], a[216:219], v[148:151], v[242:245]
v_mfma_f32_16x16x32_bf16 v[246:249], a[36:39], v[148:151], v[246:249]
v_mfma_f32_16x16x32_bf16 v[242:245], a[220:223], v[152:155], v[242:245]
v_mfma_f32_16x16x32_bf16 v[246:249], a[40:43], v[152:155], v[246:249]
v_mfma_f32_16x16x32_bf16 v[242:245], a[224:227], v[156:159], v[242:245]
v_mfma_f32_16x16x32_bf16 v[246:249], a[44:47], v[156:159], v[246:249]
v_mfma_f32_16x16x32_bf16 v[242:245], a[228:231], v[160:163], v[242:245]
v_mfma_f32_16x16x32_bf16 v[246:249], a[48:51], v[160:163], v[246:249]
v_mfma_f32_16x16x32_bf16 v[242:245], a[232:235], v[164:167], v[242:245]
v_mfma_f32_16x16x32_bf16 v[246:249], a[52:55], v[164:167], v[246:249]
v_mfma_f32_16x16x32_bf16 v[242:245], a[236:239], v[168:171], v[242:245]
v_mfma_f32_16x16x32_bf16 v[246:249], a[56:59], v[168:171], v[246:249]
v_mfma_f32_16x16x32_bf16 v[242:245], a[240:243], v[172:175], v[242:245]
v_mfma_f32_16x16x32_bf16 v[246:249], a[60:63], v[172:175], v[246:249]
v_mfma_f32_16x16x32_bf16 v[242:245], a[244:247], v[176:179], v[242:245]
v_mfma_f32_16x16x32_bf16 v[246:249], a[64:67], v[176:179], v[246:249]
v_mfma_f32_16x16x32_bf16 v[242:245], a[248:251], v[180:183], v[242:245]
v_mfma_f32_16x16x32_bf16 v[246:249], a[68:71], v[180:183], v[246:249]
L_nojob2_%=:
s_waitcnt vmcnt(9)
v_lshlrev_b32 v60, 16, v184
v_and_b32 v61, 4294901760, v184
v_lshlrev_b32_e64 v62, 16, v185
v_and_b32 v63, 4294901760, v185
v_lshlrev_b32_e64 v64, 16, v186
v_and_b32 v65, 4294901760, v186
v_lshlrev_b32_e64 v66, 16, v187
v_and_b32 v67, 4294901760, v187
v_lshlrev_b32_e64 v68, 16, v188
v_and_b32 v69, 4294901760, v188
v_lshlrev_b32_e64 v70, 16, v189
v_and_b32 v71, 4294901760, v189
v_lshlrev_b32_e64 v72, 16, v190
v_and_b32 v73, 4294901760, v190
v_lshlrev_b32_e64 v74, 16, v191
v_and_b32 v75, 4294901760, v191
v_lshlrev_b32_e64 v76, 16, v192
v_and_b32 v77, 4294901760, v192
v_lshlrev_b32 v78, 16, v193
v_and_b32 v79, 4294901760, v193
s_waitcnt lgkmcnt(0)
v_pk_fma_f32 v[40:41], v[40:41], v[194:195], v[60:61]
v_pk_fma_f32 v[42:43], v[42:43], v[196:197], v[62:63]
v_pk_fma_f32 v[44:45], v[44:45], v[198:199], v[64:65]
v_pk_fma_f32 v[46:47], v[46:47], v[200:201], v[66:67]
v_pk_fma_f32 v[48:49], v[48:49], v[202:203], v[68:69]
v_pk_fma_f32 v[50:51], v[50:51], v[204:205], v[70:71]
v_pk_fma_f32 v[52:53], v[52:53], v[206:207], v[72:73]
v_pk_fma_f32 v[54:55], v[54:55], v[208:209], v[74:75]
v_pk_fma_f32 v[56:57], v[56:57], v[210:211], v[76:77]
v_pk_fma_f32 v[58:59], v[58:59], v[212:213], v[78:79]
v_mov_b32 v250, 0xbeeff558
v_add_f32_dpp v136, v242, v242 quad_perm:[1,0,3,2] row_mask:0xf bank_mask:0xf
v_add_f32_dpp v138, v243, v243 quad_perm:[1,0,3,2] row_mask:0xf bank_mask:0xf
v_add_f32_dpp v140, v244, v244 quad_perm:[1,0,3,2] row_mask:0xf bank_mask:0xf
v_add_f32_dpp v142, v245, v245 quad_perm:[1,0,3,2] row_mask:0xf bank_mask:0xf
v_add_f32_dpp v144, v246, v246 quad_perm:[1,0,3,2] row_mask:0xf bank_mask:0xf
v_add_f32_dpp v146, v247, v247 quad_perm:[1,0,3,2] row_mask:0xf bank_mask:0xf
v_add_f32_dpp v148, v248, v248 quad_perm:[1,0,3,2] row_mask:0xf bank_mask:0xf
v_add_f32_dpp v150, v249, v249 quad_perm:[1,0,3,2] row_mask:0xf bank_mask:0xf
v_mul_f32_dpp v137, v136, v250 quad_perm:[2,3,0,1] row_mask:0xf bank_mask:0xf
v_mul_f32_dpp v139, v138, v250 quad_perm:[2,3,0,1] row_mask:0xf bank_mask:0xf
v_mul_f32_dpp v141, v140, v250 quad_perm:[2,3,0,1] row_mask:0xf bank_mask:0xf
v_mul_f32_dpp v143, v142, v250 quad_perm:[2,3,0,1] row_mask:0xf bank_mask:0xf
v_mul_f32_dpp v145, v144, v250 quad_perm:[2,3,0,1] row_mask:0xf bank_mask:0xf
v_mul_f32_dpp v147, v146, v250 quad_perm:[2,3,0,1] row_mask:0xf bank_mask:0xf
v_mul_f32_dpp v149, v148, v250 quad_perm:[2,3,0,1] row_mask:0xf bank_mask:0xf
v_mul_f32_dpp v151, v150, v250 quad_perm:[2,3,0,1] row_mask:0xf bank_mask:0xf
v_max_u32 v136, 1, v136
v_max_u32 v138, 1, v138
v_max_u32 v140, 1, v140
v_max_u32 v142, 1, v142
v_max_u32 v144, 1, v144
v_max_u32 v146, 1, v146
v_max_u32 v148, 1, v148
v_max_u32 v150, 1, v150
buffer_store_dwordx4 v[136:139], %[yAS], %[sWS], 0 offen offset:0 sc1
buffer_store_dwordx4 v[140:143], %[yAS], %[sWS], 0 offen offset:16 sc1
buffer_store_dwordx4 v[144:147], %[yAS2], %[sWS], 0 offen offset:0 sc1
buffer_store_dwordx4 v[148:151], %[yAS2], %[sWS], 0 offen offset:16 sc1
s_add_u32 m0, %[sLx], 36864
s_add_u32 s48, s20, 36864
buffer_load_dwordx4 %[vL16], %[sWS], s48 offen offset:0 sc1 lds
buffer_load_dwordx4 %[vL16], %[sWS], s48 offen offset:1024 sc1 lds
buffer_load_dwordx4 %[vL16], %[sWS], s48 offen offset:2048 sc1 lds
buffer_load_dwordx4 %[vL16], %[sWS], s48 offen offset:3072 sc1 lds
s_add_u32 m0, %[sLx], 40960
s_add_u32 s48, s20, 40960
buffer_load_dwordx4 %[vL16], %[sWS], s48 offen offset:0 sc1 lds
buffer_load_dwordx4 %[vL16], %[sWS], s48 offen offset:1024 sc1 lds
buffer_load_dwordx4 %[vL16], %[sWS], s48 offen offset:2048 sc1 lds
buffer_load_dwordx4 %[vL16], %[sWS], s48 offen offset:3072 sc1 lds
s_add_u32 m0, %[sLx], 45056
s_add_u32 s48, s20, 45056
buffer_load_dwordx4 %[vL16], %[sWS], s48 offen offset:0 sc1 lds
s_add_u32 m0, %[sLx], 110592
s_add_u32 s48, s20, 110592
buffer_load_dwordx4 %[vL16], %[sWS], s48 offen offset:0 sc1 lds
buffer_load_dwordx4 %[vL16], %[sWS], s48 offen offset:1024 sc1 lds
buffer_load_dwordx4 %[vL16], %[sWS], s48 offen offset:2048 sc1 lds
buffer_load_dwordx4 %[vL16], %[sWS], s48 offen offset:3072 sc1 lds
s_add_u32 m0, %[sLx], 114688
s_add_u32 s48, s20, 114688
buffer_load_dwordx4 %[vL16], %[sWS], s48 offen offset:0 sc1 lds
buffer_load_dwordx4 %[vL16], %[sWS], s48 offen offset:1024 sc1 lds
buffer_load_dwordx4 %[vL16], %[sWS], s48 offen offset:2048 sc1 lds
buffer_load_dwordx4 %[vL16], %[sWS], s48 offen offset:3072 sc1 lds
s_add_u32 m0, %[sLx], 118784
s_add_u32 s48, s20, 118784
buffer_load_dwordx4 %[vL16], %[sWS], s48 offen offset:0 sc1 lds
s_mov_b64 exec, 0
s_cmp_eq_u32 %[sOwn], 0
s_cbranch_scc1 LPb_0
s_cmp_eq_u32 %[sOwn], 1
s_cbranch_scc1 LPb_1
s_cmp_eq_u32 %[sOwn], 2
s_cbranch_scc1 LPb_2
s_branch LPb_3
LRb:
s_mov_b64 exec, -1
s_waitcnt vmcnt(22)
v_min3_u32 v234, v80, v81, v82
v_min3_u32 v234, v234, v83, v84
v_min3_u32 v234, v234, v85, v86
v_min3_u32 v234, v234, v87, v88
v_min_u32_e64 v234, v234, v89
v_min3_u32 v235, v90, v91, v92
v_min3_u32 v235, v235, v93, v94
v_min3_u32 v235, v235, v95, v96
v_min3_u32 v235, v235, v97, v98
v_min_u32_e64 v235, v235, v99
v_min3_u32 v236, v100, v101, v102
v_min3_u32 v236, v236, v103, v104
v_min3_u32 v236, v236, v105, v106
v_min3_u32 v236, v236, v107, v108
v_min_u32_e64 v236, v236, v109
v_min3_u32 v234, v234, v235, v236
v_cmp_eq_u32 vcc, 0, v234
s_cbranch_vccnz L_sl2_%=
L_ok2_%=:
v_lshlrev_b32_e64 v60, 16, v80
v_and_b32 v61, 4294901760, v80
v_lshlrev_b32_e64 v62, 16, v81
v_and_b32 v63, 4294901760, v81
v_lshlrev_b32_e64 v64, 16, v82
v_and_b32 v65, 4294901760, v82
v_lshlrev_b32_e64 v66, 16, v83
v_and_b32 v67, 4294901760, v83
v_lshlrev_b32_e64 v68, 16, v84
v_and_b32 v69, 4294901760, v84
v_lshlrev_b32_e64 v70, 16, v85
v_and_b32 v71, 4294901760, v85
v_lshlrev_b32_e64 v72, 16, v86
v_and_b32 v73, 4294901760, v86
v_lshlrev_b32_e64 v74, 16, v87
v_and_b32 v75, 4294901760, v87
v_lshlrev_b32_e64 v76, 16, v88
v_and_b32 v77, 4294901760, v88
)"
R"(v_lshlrev_b32_e64 v78, 16, v89
v_and_b32 v79, 4294901760, v89
v_lshlrev_b32_e64 v112, 16, v90
v_and_b32 v113, 4294901760, v90
v_lshlrev_b32_e64 v114, 16, v91
v_and_b32 v115, 4294901760, v91
v_lshlrev_b32_e64 v116, 16, v92
v_and_b32 v117, 4294901760, v92
v_lshlrev_b32_e64 v118, 16, v93
v_and_b32 v119, 4294901760, v93
v_lshlrev_b32_e64 v120, 16, v94
v_and_b32 v121, 4294901760, v94
v_lshlrev_b32_e64 v122, 16, v95
v_and_b32 v123, 4294901760, v95
v_lshlrev_b32_e64 v124, 16, v96
v_and_b32 v125, 4294901760, v96
v_lshlrev_b32_e64 v126, 16, v97
v_and_b32 v127, 4294901760, v97
v_lshlrev_b32_e64 v128, 16, v98
v_and_b32 v129, 4294901760, v98
v_lshlrev_b32_e64 v130, 16, v99
v_and_b32 v131, 4294901760, v99
v_pk_add_f32 v[60:61], v[60:61], v[112:113]
v_pk_add_f32 v[62:63], v[62:63], v[114:115]
v_pk_add_f32 v[64:65], v[64:65], v[116:117]
v_pk_add_f32 v[66:67], v[66:67], v[118:119]
v_pk_add_f32 v[68:69], v[68:69], v[120:121]
v_pk_add_f32 v[70:71], v[70:71], v[122:123]
v_pk_add_f32 v[72:73], v[72:73], v[124:125]
v_pk_add_f32 v[74:75], v[74:75], v[126:127]
v_pk_add_f32 v[76:77], v[76:77], v[128:129]
v_pk_add_f32 v[78:79], v[78:79], v[130:131]
v_lshlrev_b32_e64 v112, 16, v100
v_and_b32 v113, 4294901760, v100
v_lshlrev_b32_e64 v114, 16, v101
v_and_b32 v115, 4294901760, v101
v_lshlrev_b32_e64 v116, 16, v102
v_and_b32 v117, 4294901760, v102
v_lshlrev_b32_e64 v118, 16, v103
v_and_b32 v119, 4294901760, v103
v_lshlrev_b32_e64 v120, 16, v104
v_and_b32 v121, 4294901760, v104
v_lshlrev_b32_e64 v122, 16, v105
v_and_b32 v123, 4294901760, v105
v_lshlrev_b32_e64 v124, 16, v106
v_and_b32 v125, 4294901760, v106
v_lshlrev_b32_e64 v126, 16, v107
v_and_b32 v127, 4294901760, v107
v_lshlrev_b32_e64 v128, 16, v108
v_and_b32 v129, 4294901760, v108
v_lshlrev_b32_e64 v130, 16, v109
v_and_b32 v131, 4294901760, v109
v_pk_add_f32 v[60:61], v[60:61], v[112:113]
v_pk_add_f32 v[62:63], v[62:63], v[114:115]
v_pk_add_f32 v[64:65], v[64:65], v[116:117]
v_pk_add_f32 v[66:67], v[66:67], v[118:119]
v_pk_add_f32 v[68:69], v[68:69], v[120:121]
v_pk_add_f32 v[70:71], v[70:71], v[122:123]
v_pk_add_f32 v[72:73], v[72:73], v[124:125]
v_pk_add_f32 v[74:75], v[74:75], v[126:127]
v_pk_add_f32 v[76:77], v[76:77], v[128:129]
v_pk_add_f32 v[78:79], v[78:79], v[130:131]
v_pk_fma_f32 v[40:41], v[60:61], v[194:195], v[40:41]
v_pk_fma_f32 v[42:43], v[62:63], v[196:197], v[42:43]
v_pk_fma_f32 v[44:45], v[64:65], v[198:199], v[44:45]
v_pk_fma_f32 v[46:47], v[66:67], v[200:201], v[46:47]
v_pk_fma_f32 v[48:49], v[68:69], v[202:203], v[48:49]
v_pk_fma_f32 v[50:51], v[70:71], v[204:205], v[50:51]
LPb_0:
s_cbranch_execz LPb_4
v_pk_fma_f32 v[52:53], v[72:73], v[206:207], v[52:53]
v_pk_fma_f32 v[54:55], v[74:75], v[208:209], v[54:55]
v_pk_fma_f32 v[56:57], v[76:77], v[210:211], v[56:57]
v_pk_fma_f32 v[58:59], v[78:79], v[212:213], v[58:59]
v_cvt_pk_bf16_f32 v80, v40, v41
v_cvt_pk_bf16_f32 v81, v42, v43
v_cvt_pk_bf16_f32 v82, v44, v45
v_cvt_pk_bf16_f32 v83, v46, v47
v_cvt_pk_bf16_f32 v84, v48, v49
v_cvt_pk_bf16_f32 v85, v50, v51
v_cvt_pk_bf16_f32 v86, v52, v53
v_cvt_pk_bf16_f32 v87, v54, v55
v_cvt_pk_bf16_f32 v88, v56, v57
v_cvt_pk_bf16_f32 v89, v58, v59
v_lshlrev_b32 v40, 16, v80
v_and_b32 v41, 4294901760, v80
v_lshlrev_b32_e64 v42, 16, v81
v_and_b32 v43, 4294901760, v81
v_lshlrev_b32_e64 v44, 16, v82
v_and_b32 v45, 4294901760, v82
v_lshlrev_b32_e64 v46, 16, v83
v_and_b32 v47, 4294901760, v83
v_lshlrev_b32_e64 v48, 16, v84
v_and_b32 v49, 4294901760, v84
v_lshlrev_b32_e64 v50, 16, v85
v_and_b32 v51, 4294901760, v85
v_lshlrev_b32_e64 v52, 16, v86
v_and_b32 v53, 4294901760, v86
v_lshlrev_b32_e64 v54, 16, v87
v_and_b32 v55, 4294901760, v87
v_lshlrev_b32_e64 v56, 16, v88
v_and_b32 v57, 4294901760, v88
v_lshlrev_b32_e64 v58, 16, v89
v_and_b32 v59, 4294901760, v89
v_pk_fma_f32 v[60:61], v[40:41], v[214:215], v[40:41]
v_pk_fma_f32 v[62:63], v[42:43], v[216:217], v[42:43]
v_pk_fma_f32 v[64:65], v[44:45], v[218:219], v[44:45]
v_pk_fma_f32 v[66:67], v[46:47], v[220:221], v[46:47]
v_pk_fma_f32 v[68:69], v[48:49], v[222:223], v[48:49]
v_pk_fma_f32 v[70:71], v[50:51], v[224:225], v[50:51]
v_pk_fma_f32 v[72:73], v[52:53], v[226:227], v[52:53]
v_pk_fma_f32 v[74:75], v[54:55], v[228:229], v[54:55]
v_pk_fma_f32 v[76:77], v[56:57], v[230:231], v[56:57]
v_pk_fma_f32 v[78:79], v[58:59], v[232:233], v[58:59]
v_cvt_pk_bf16_f32 v90, v60, v61
v_cvt_pk_bf16_f32 v91, v62, v63
v_cvt_pk_bf16_f32 v92, v64, v65
v_cvt_pk_bf16_f32 v93, v66, v67
v_cvt_pk_bf16_f32 v94, v68, v69
v_cvt_pk_bf16_f32 v95, v70, v71
v_cvt_pk_bf16_f32 v96, v72, v73
v_cvt_pk_bf16_f32 v97, v74, v75
v_cvt_pk_bf16_f32 v98, v76, v77
v_cvt_pk_bf16_f32 v99, v78, v79
v_max_u32 v90, 1, v90
v_max_u32 v92, 1, v92
v_max_u32 v94, 1, v94
v_max_u32 v96, 1, v96
v_max_u32_e64 v98, 1, v98
buffer_store_dwordx4 v[90:93], %[yF], %[sWS], 0 offen offset:0 sc1
buffer_store_dwordx4 v[94:97], %[yF2], %[sWS], 0 offen offset:0 sc1
buffer_store_dwordx2 v[98:99], %[yF8], %[sWS], 0 offen offset:0 sc1
v_pk_add_f32 v[234:235], v[56:57], v[58:59]
v_pk_add_f32 v[236:237], v[40:41], v[42:43]
v_pk_mul_f32 v[234:235], v[234:235], %[sMsk] op_sel_hi:[1,0]
v_pk_add_f32 v[234:235], v[234:235], v[44:45]
v_pk_add_f32 v[236:237], v[236:237], v[46:47]
v_pk_add_f32 v[234:235], v[234:235], v[48:49]
v_pk_add_f32 v[236:237], v[236:237], v[50:51]
v_pk_add_f32 v[234:235], v[234:235], v[52:53]
v_pk_add_f32 v[236:237], v[236:237], v[54:55]
v_pk_add_f32 v[234:235], v[234:235], v[236:237]
v_add_f32 v234, v234, v235
v_mov_b32 v238, v234
s_nop 1
v_permlane32_swap_b32 v234, v238
s_nop 0
v_add_f32 v234, v234, v238
v_mov_b32 v238, v234
s_nop 1
v_permlane16_swap_b32 v234, v238
s_nop 0
v_add_f32 v234, v234, v238
v_mul_f32 v234, %[sInv], v234
v_pk_add_f32 v[40:41], v[40:41], v[234:235] op_sel_hi:[1,0] neg_lo:[0,1] neg_hi:[0,1]
v_pk_add_f32 v[42:43], v[42:43], v[234:235] op_sel_hi:[1,0] neg_lo:[0,1] neg_hi:[0,1]
v_pk_add_f32 v[44:45], v[44:45], v[234:235] op_sel_hi:[1,0] neg_lo:[0,1] neg_hi:[0,1]
v_pk_add_f32 v[46:47], v[46:47], v[234:235] op_sel_hi:[1,0] neg_lo:[0,1] neg_hi:[0,1]
v_pk_add_f32 v[48:49], v[48:49], v[234:235] op_sel_hi:[1,0] neg_lo:[0,1] neg_hi:[0,1]
v_pk_add_f32 v[50:51], v[50:51], v[234:235] op_sel_hi:[1,0] neg_lo:[0,1] neg_hi:[0,1]
v_pk_add_f32 v[52:53], v[52:53], v[234:235] op_sel_hi:[1,0] neg_lo:[0,1] neg_hi:[0,1]
v_pk_add_f32 v[54:55], v[54:55], v[234:235] op_sel_hi:[1,0] neg_lo:[0,1] neg_hi:[0,1]
v_pk_add_f32 v[56:57], v[56:57], v[234:235] op_sel_hi:[1,0] neg_lo:[0,1] neg_hi:[0,1]
v_pk_add_f32 v[58:59], v[58:59], v[234:235] op_sel_hi:[1,0] neg_lo:[0,1] neg_hi:[0,1]
v_pk_mul_f32 v[56:57], v[56:57], %[sMsk] op_sel_hi:[1,0]
v_pk_mul_f32 v[58:59], v[58:59], %[sMsk] op_sel_hi:[1,0]
v_pk_mul_f32 v[60:61], v[56:57], v[56:57]
v_pk_mul_f32 v[62:63], v[40:41], v[40:41]
v_pk_fma_f32 v[60:61], v[58:59], v[58:59], v[60:61]
v_pk_fma_f32 v[62:63], v[42:43], v[42:43], v[62:63]
v_pk_fma_f32 v[60:61], v[44:45], v[44:45], v[60:61]
v_pk_fma_f32 v[62:63], v[46:47], v[46:47], v[62:63]
v_pk_fma_f32 v[60:61], v[48:49], v[48:49], v[60:61]
v_pk_fma_f32 v[62:63], v[50:51], v[50:51], v[62:63]
v_pk_fma_f32 v[60:61], v[52:53], v[52:53], v[60:61]
v_pk_fma_f32 v[62:63], v[54:55], v[54:55], v[62:63]
v_pk_add_f32 v[60:61], v[60:61], v[62:63]
v_add_f32 v235, v60, v61
v_mov_b32 v238, v235
s_nop 1
v_permlane32_swap_b32 v235, v238
s_nop 0
v_add_f32 v235, v235, v238
v_mov_b32 v238, v235
s_nop 1
v_permlane16_swap_b32 v235, v238
s_nop 0
v_add_f32_e64 v235, v235, v238
v_or_b32 v235, 2147483648, v235
buffer_store_dwordx2 v[234:235], %[ySt], %[sWS], 0 offen offset:0 sc1
ds_write_b128 %[yX2], v[80:83] offset:0
ds_write_b128 %[yX2], v[84:87] offset:4096
ds_write_b64 %[yX2], v[88:89] offset:8192
L_g34:
s_mov_b32 s50, 0x409d30fc
s_mov_b32 s53, 4096
v_mov_b32_e64 v253, 1
s_xor_b32 s72, s38, 8192
s_xor_b32 s73, s39, 8192
s_lshl_b32 s74, s76, 19
s_xor_b32 s75, s74, 524288
)"
R"(v_mov_b32 v112, 0
v_mov_b32 v113, 0
v_mov_b32 v114, 0
v_mov_b32_e64 v115, 0
s_getpc_b64 s[98:99]
s_add_u32 s98, s98, L_g5@rel32@lo+4
s_addc_u32 s99, s99, L_g5@rel32@hi+12
v_min_u32 v254, code_end_lbl-L_g5-4, %[vCp]
s_barrier
ds_read_b128 a[144:147], %[vXr0] offset:0
ds_read_b128 a[148:151], %[vXr0] offset:1024
ds_read_b128 a[152:155], %[vXr0] offset:2048
ds_read_b128 a[156:159], %[vXr0] offset:3072
ds_read_b128 a[160:163], %[vXr0] offset:4096
ds_read_b128 a[164:167], %[vXr0] offset:5120
ds_read_b128 a[168:171], %[vXr0] offset:6144
ds_read_b128 a[172:175], %[vXr0] offset:7168
ds_read_b128 a[176:179], %[vXr0] offset:8192
s_add_u32 m0, %[sLx], 73728
s_add_u32 s48, s20, 73728
buffer_load_dwordx4 %[vL16], %[sWS], s48 offen offset:0 sc1 lds
buffer_load_dwordx4 %[vL16], %[sWS], s48 offen offset:1024 sc1 lds
buffer_load_dwordx4 %[vL16], %[sWS], s48 offen offset:2048 sc1 lds
buffer_load_dwordx4 %[vL16], %[sWS], s48 offen offset:3072 sc1 lds
s_add_u32 m0, %[sLx], 77824
s_add_u32 s48, s20, 77824
buffer_load_dwordx4 %[vL16], %[sWS], s48 offen offset:0 sc1 lds
buffer_load_dwordx4 %[vL16], %[sWS], s48 offen offset:1024 sc1 lds
buffer_load_dwordx4 %[vL16], %[sWS], s48 offen offset:2048 sc1 lds
buffer_load_dwordx4 %[vL16], %[sWS], s48 offen offset:3072 sc1 lds
s_add_u32 m0, %[sLx], 81920
s_add_u32 s48, s20, 81920
buffer_load_dwordx4 %[vL16], %[sWS], s48 offen offset:0 sc1 lds
buffer_load_dword v104, %[yCan], %[sWS], s31 offen offset:0 sc1
s_waitcnt vmcnt(0)
v_cmp_eq_u32 vcc, 0, v104
s_cbranch_vccnz L_sl8_%=
L_ok8_%=:
s_add_u32 s49, s31, 0
buffer_load_dwordx4 a[0:3], %[vL16], %[sWS], s49 offen offset:0 sc1
buffer_load_dwordx4 a[4:7], %[vL16], %[sWS], s49 offen offset:1024 sc1
s_add_u32 s49, s31, 2048
buffer_load_dwordx4 a[8:11], %[vL16], %[sWS], s49 offen offset:0 sc1
buffer_load_dwordx4 a[12:15], %[vL16], %[sWS], s49 offen offset:1024 sc1
s_add_u32 s49, s31, 4096
buffer_load_dwordx4 a[16:19], %[vL16], %[sWS], s49 offen offset:0 sc1
buffer_load_dwordx4 a[20:23], %[vL16], %[sWS], s49 offen offset:1024 sc1
s_add_u32 s49, s31, 6144
buffer_load_dwordx4 a[24:27], %[vL16], %[sWS], s49 offen offset:0 sc1
buffer_load_dwordx4 a[28:31], %[vL16], %[sWS], s49 offen offset:1024 sc1
s_add_u32 s49, s31, 524288
buffer_load_dwordx4 a[32:35], %[vL16], %[sWS], s49 offen offset:0 sc1
buffer_load_dwordx4 a[36:39], %[vL16], %[sWS], s49 offen offset:1024 sc1
s_add_u32 s49, s31, 526336
buffer_load_dwordx4 a[40:43], %[vL16], %[sWS], s49 offen offset:0 sc1
buffer_load_dwordx4 a[44:47], %[vL16], %[sWS], s49 offen offset:1024 sc1
s_add_u32 s49, s31, 528384
buffer_load_dwordx4 a[48:51], %[vL16], %[sWS], s49 offen offset:0 sc1
buffer_load_dwordx4 a[52:55], %[vL16], %[sWS], s49 offen offset:1024 sc1
s_add_u32 s49, s31, 530432
buffer_load_dwordx4 a[56:59], %[vL16], %[sWS], s49 offen offset:0 sc1
buffer_load_dwordx4 a[60:63], %[vL16], %[sWS], s49 offen offset:1024 sc1
s_add_u32 s49, s31, 1048576
buffer_load_dwordx4 a[64:67], %[vL16], %[sWS], s49 offen offset:0 sc1
buffer_load_dwordx4 a[68:71], %[vL16], %[sWS], s49 offen offset:1024 sc1
s_add_u32 s49, s31, 1050624
buffer_load_dwordx4 a[72:75], %[vL16], %[sWS], s49 offen offset:0 sc1
buffer_load_dwordx4 a[76:79], %[vL16], %[sWS], s49 offen offset:1024 sc1
s_add_u32 s49, s31, 1052672
buffer_load_dwordx4 a[80:83], %[vL16], %[sWS], s49 offen offset:0 sc1
buffer_load_dwordx4 a[84:87], %[vL16], %[sWS], s49 offen offset:1024 sc1
buffer_load_dwordx4 v[232:235], %[vS], %[sWS], 0 offen offset:0 sc1
buffer_load_dwordx4 v[236:239], %[vS], %[sWS], 0 offen offset:16 sc1
buffer_load_dwordx4 v[240:243], %[vS], %[sWS], 0 offen offset:32 sc1
buffer_load_dwordx4 v[244:247], %[vS], %[sWS], 0 offen offset:48 sc1
s_add_u32 s49, s31, 1054720
buffer_load_dwordx4 a[88:91], %[vL16], %[sWS], s49 offen offset:0 sc1
buffer_load_dwordx4 a[92:95], %[vL16], %[sWS], s49 offen offset:1024 sc1
s_waitcnt vmcnt(22)
v_accvgpr_read_b32 v104, a0
v_accvgpr_read_b32 v105, a4
v_accvgpr_read_b32 v106, a8
v_accvgpr_read_b32 v107, a12
v_accvgpr_read_b32 v108, a16
v_accvgpr_read_b32 v109, a20
v_min3_u32 v104, v104, v105, v106
v_min3_u32 v104, v104, v107, v108
v_min_u32 v104, v104, v109
v_cmp_eq_u32 vcc, 0, v104
s_cbranch_vccnz L_sl9_%=
L_ok9_%=:
s_waitcnt lgkmcnt(0)
v_mfma_f32_16x16x32_bf16 v[32:35], a[0:3], a[144:147], 0
ds_read_b128 a[180:183], %[vXr0] offset:9216
v_mfma_f32_16x16x32_bf16 v[36:39], a[4:7], a[144:147], 0
s_add_u32 s49, s31, 1572864
ds_read_b128 a[184:187], %[vXr0] offset:10240
v_mfma_f32_16x16x32_bf16 v[40:43], a[0:3], a[148:151], 0
buffer_load_dwordx4 a[96:99], %[vL16], %[sWS], s49 offen offset:0 sc1
v_mfma_f32_16x16x32_bf16 v[44:47], a[4:7], a[148:151], 0
ds_read_b128 a[188:191], %[vXr0] offset:11264
v_mfma_f32_16x16x32_bf16 v[48:51], a[0:3], a[152:155], 0
ds_read_b128 a[192:195], %[vXr0] offset:12288
v_mfma_f32_16x16x32_bf16 v[52:55], a[4:7], a[152:155], 0
buffer_load_dwordx4 a[100:103], %[vL16], %[sWS], s49 offen offset:1024 sc1
v_mfma_f32_16x16x32_bf16 v[32:35], a[8:11], a[156:159], v[32:35]
ds_read_b128 a[196:199], %[vXr0] offset:13312
v_mfma_f32_16x16x32_bf16 v[36:39], a[12:15], a[156:159], v[36:39]
ds_read_b128 a[200:203], %[vXr0] offset:14336
v_mfma_f32_16x16x32_bf16 v[40:43], a[8:11], a[160:163], v[40:43]
v_mfma_f32_16x16x32_bf16 v[44:47], a[12:15], a[160:163], v[44:47]
ds_read_b128 a[204:207], %[vXr0] offset:15360
s_waitcnt vmcnt(18)
v_mfma_f32_16x16x32_bf16 v[48:51], a[8:11], a[164:167], v[48:51]
s_add_u32 s49, s31, 1574912
ds_read_b128 a[208:211], %[vXr0] offset:16384
v_accvgpr_read_b32 v104, a24
v_accvgpr_read_b32 v105, a28
v_mfma_f32_16x16x32_bf16 v[52:55], a[12:15], a[164:167], v[52:55]
buffer_load_dwordx4 a[104:107], %[vL16], %[sWS], s49 offen offset:0 sc1
v_accvgpr_read_b32 v106, a32
v_accvgpr_read_b32 v107, a34
v_mfma_f32_16x16x32_bf16 v[32:35], a[16:19], a[168:171], v[32:35]
ds_read_b128 a[212:215], %[vXr0] offset:17408
v_accvgpr_read_b32 v108, a36
v_accvgpr_read_b32 v109, a38
v_mfma_f32_16x16x32_bf16 v[36:39], a[20:23], a[168:171], v[36:39]
buffer_load_dwordx4 a[108:111], %[vL16], %[sWS], s49 offen offset:1024 sc1
v_accvgpr_read_b32 v110, a40
v_accvgpr_read_b32 v111, a44
v_mfma_f32_16x16x32_bf16 v[40:43], a[16:19], a[172:175], v[40:43]
s_add_u32 s49, s31, 1576960
v_min3_u32 v104, v104, v105, v106
v_min3_u32 v104, v104, v107, v108
v_mfma_f32_16x16x32_bf16 v[44:47], a[20:23], a[172:175], v[44:47]
buffer_load_dwordx4 a[112:115], %[vL16], %[sWS], s49 offen offset:0 sc1
v_min3_u32 v104, v104, v109, v110
v_min_u32_e64 v104, v104, v111
v_mfma_f32_16x16x32_bf16 v[48:51], a[16:19], a[176:179], v[48:51]
buffer_load_dwordx4 a[116:119], %[vL16], %[sWS], s49 offen offset:1024 sc1
v_cmp_eq_u32 vcc, 0, v104
s_cbranch_vccnz L_sl10_%=
L_ok10_%=:
v_mfma_f32_16x16x32_bf16 v[52:55], a[20:23], a[176:179], v[52:55]
s_waitcnt lgkmcnt(0)
v_mfma_f32_16x16x32_bf16 v[32:35], a[24:27], a[180:183], v[32:35]
ds_read_b128 a[144:147], %[vXr0] offset:18432
v_mfma_f32_16x16x32_bf16 v[36:39], a[28:31], a[180:183], v[36:39]
s_add_u32 s49, s31, 1579008
ds_read_b128 a[148:151], %[vXr0] offset:19456
v_mfma_f32_16x16x32_bf16 v[40:43], a[24:27], a[184:187], v[40:43]
buffer_load_dwordx4 a[120:123], %[vL16], %[sWS], s49 offen offset:0 sc1
v_mfma_f32_16x16x32_bf16 v[44:47], a[28:31], a[184:187], v[44:47]
ds_read_b128 a[152:155], %[vXr0] offset:20480
v_mfma_f32_16x16x32_bf16 v[48:51], a[24:27], a[188:191], v[48:51]
ds_read_b128 a[156:159], %[vXr0] offset:21504
v_mfma_f32_16x16x32_bf16 v[52:55], a[28:31], a[188:191], v[52:55]
buffer_load_dwordx4 a[124:127], %[vL16], %[sWS], s49 offen offset:1024 sc1
v_mfma_f32_16x16x32_bf16 v[32:35], a[32:35], a[192:195], v[32:35]
ds_read_b128 a[160:163], %[vXr0] offset:22528
v_mfma_f32_16x16x32_bf16 v[36:39], a[36:39], a[192:195], v[36:39]
ds_read_b128 a[164:167], %[vXr0] offset:23552
v_mfma_f32_16x16x32_bf16 v[40:43], a[32:35], a[196:199], v[40:43]
v_mfma_f32_16x16x32_bf16 v[44:47], a[36:39], a[196:199], v[44:47]
ds_read_b128 a[168:171], %[vXr0] offset:24576
s_waitcnt vmcnt(18)
v_mfma_f32_16x16x32_bf16 v[48:51], a[32:35], a[200:203], v[48:51]
s_add_u32 s49, s31, 25165824
ds_read_b128 a[172:175], %[vXr0] offset:25600
v_accvgpr_read_b32 v104, a48
v_accvgpr_read_b32 v105, a52
v_mfma_f32_16x16x32_bf16 v[52:55], a[36:39], a[200:203], v[52:55]
buffer_load_dwordx4 a[128:131], %[vL16], %[sWS], s49 offen offset:0 sc1
v_accvgpr_read_b32 v106, a56
v_accvgpr_read_b32 v107, a60
v_mfma_f32_16x16x32_bf16 v[32:35], a[40:43], a[204:207], v[32:35]
ds_read_b128 a[176:179], %[vXr0] offset:26624
v_accvgpr_read_b32 v108, a64
v_accvgpr_read_b32 v109, a66
v_mfma_f32_16x16x32_bf16 v[36:39], a[44:47], a[204:207], v[36:39]
buffer_load_dwordx4 a[132:135], %[vL16], %[sWS], s49 offen offset:1024 sc1
v_accvgpr_read_b32 v110, a68
v_accvgpr_read_b32 v111, a70
v_mfma_f32_16x16x32_bf16 v[40:43], a[40:43], a[208:211], v[40:43]
s_add_u32 s49, s31, 25167872
v_min3_u32 v104, v104, v105, v106
v_min3_u32 v104, v104, v107, v108
v_mfma_f32_16x16x32_bf16 v[44:47], a[44:47], a[208:211], v[44:47]
buffer_load_dwordx4 a[136:139], %[vL16], %[sWS], s49 offen offset:0 sc1
v_min3_u32 v104, v104, v109, v110
v_min_u32_e64 v104, v104, v111
v_mfma_f32_16x16x32_bf16 v[48:51], a[40:43], a[212:215], v[48:51]
buffer_load_dwordx4 a[140:143], %[vL16], %[sWS], s49 offen offset:1024 sc1
v_cmp_eq_u32_e64 vcc, 0, v104
s_cbranch_vccnz L_sl11_%=
L_ok11_%=:
v_mfma_f32_16x16x32_bf16 v[52:55], a[44:47], a[212:215], v[52:55]
s_waitcnt lgkmcnt(0)
v_mfma_f32_16x16x32_bf16 v[32:35], a[48:51], a[144:147], v[32:35]
ds_read_b128 a[180:183], %[vXr0] offset:27648
)"
R"(v_mfma_f32_16x16x32_bf16 v[36:39], a[52:55], a[144:147], v[36:39]
ds_read_b128 a[184:187], %[vXr0] offset:28672
v_mfma_f32_16x16x32_bf16 v[40:43], a[48:51], a[148:151], v[40:43]
buffer_load_dwordx2 v[192:193], %[vA2], %[sWS], 0 offen offset:0 sc1
v_mfma_f32_16x16x32_bf16 v[44:47], a[52:55], a[148:151], v[44:47]
ds_read_b128 a[188:191], %[vXr0] offset:29696
v_mfma_f32_16x16x32_bf16 v[48:51], a[48:51], a[152:155], v[48:51]
ds_read_b128 a[192:195], %[vXr0] offset:30720
v_mfma_f32_16x16x32_bf16 v[52:55], a[52:55], a[152:155], v[52:55]
v_mfma_f32_16x16x32_bf16 v[32:35], a[56:59], a[156:159], v[32:35]
ds_read_b128 a[196:199], %[vXr0] offset:31744
v_mfma_f32_16x16x32_bf16 v[36:39], a[60:63], a[156:159], v[36:39]
ds_read_b128 a[200:203], %[vXr0] offset:32768
v_mfma_f32_16x16x32_bf16 v[40:43], a[56:59], a[160:163], v[40:43]
v_mfma_f32_16x16x32_bf16 v[44:47], a[60:63], a[160:163], v[44:47]
ds_read_b128 a[204:207], %[vXr0] offset:33792
v_mfma_f32_16x16x32_bf16 v[48:51], a[56:59], a[164:167], v[48:51]
ds_read_b128 a[208:211], %[vXr0] offset:34816
s_waitcnt vmcnt(13)
v_mfma_f32_16x16x32_bf16 v[52:55], a[60:63], a[164:167], v[52:55]
buffer_load_dwordx2 v[194:195], %[vA2], %[sWS], 0 offen offset:128 sc1
v_accvgpr_read_b32 v104, a72
v_accvgpr_read_b32 v105, a76
v_mfma_f32_16x16x32_bf16 v[32:35], a[64:67], a[168:171], v[32:35]
ds_read_b128 a[212:215], %[vXr0] offset:35840
v_accvgpr_read_b32 v106, a80
v_accvgpr_read_b32 v107, a84
v_mfma_f32_16x16x32_bf16 v[36:39], a[68:71], a[168:171], v[36:39]
v_accvgpr_read_b32 v108, a88
v_accvgpr_read_b32 v109, a92
v_mfma_f32_16x16x32_bf16 v[40:43], a[64:67], a[172:175], v[40:43]
v_min3_u32 v104, v104, v105, v106
v_min3_u32 v104, v104, v107, v108
v_mfma_f32_16x16x32_bf16 v[44:47], a[68:71], a[172:175], v[44:47]
buffer_load_dwordx2 v[196:197], %[vA2], %[sWS], 0 offen offset:256 sc1
v_min_u32 v104, v104, v109
v_mfma_f32_16x16x32_bf16 v[48:51], a[64:67], a[176:179], v[48:51]
v_cmp_eq_u32 vcc, 0, v104
s_cbranch_vccnz L_sl12_%=
L_ok12_%=:
v_mfma_f32_16x16x32_bf16 v[52:55], a[68:71], a[176:179], v[52:55]
s_waitcnt lgkmcnt(0)
s_barrier
v_mfma_f32_16x16x32_bf16 v[32:35], a[72:75], a[180:183], v[32:35]
ds_read_b128 a[144:147], %[vXr1] offset:0
v_mfma_f32_16x16x32_bf16 v[36:39], a[76:79], a[180:183], v[36:39]
ds_read_b128 a[148:151], %[vXr1] offset:1024
v_mfma_f32_16x16x32_bf16 v[40:43], a[72:75], a[184:187], v[40:43]
buffer_load_dwordx2 v[198:199], %[vA2], %[sWS], 0 offen offset:384 sc1
v_mfma_f32_16x16x32_bf16 v[44:47], a[76:79], a[184:187], v[44:47]
ds_read_b128 a[152:155], %[vXr1] offset:2048
v_and_b32 v248, v233, v235
v_and_b32 v249, v237, v239
v_and_b32_e64 v250, v241, v243
v_mfma_f32_16x16x32_bf16 v[48:51], a[72:75], a[188:191], v[48:51]
ds_read_b128 a[156:159], %[vXr1] offset:3072
v_and_b32 v251, v245, v247
v_and_b32 v248, v248, v249
v_and_b32_e64 v250, v250, v251
v_mfma_f32_16x16x32_bf16 v[52:55], a[76:79], a[188:191], v[52:55]
v_and_b32 v248, v248, v250
v_cmp_lt_i32 vcc, -1, v248
s_cbranch_vccnz L_sl3_%=
L_ok3_%=:
v_add_f32 v249, v232, v236
v_add_f32_e64 v250, v234, v238
v_mfma_f32_16x16x32_bf16 v[32:35], a[80:83], a[192:195], v[32:35]
ds_read_b128 a[160:163], %[vXr1] offset:4096
v_add_f32 v249, v249, v240
v_add_f32 v250, v250, v242
v_add_f32_e64 v249, v249, v244
v_mfma_f32_16x16x32_bf16 v[36:39], a[84:87], a[192:195], v[36:39]
ds_read_b128 a[164:167], %[vXr1] offset:5120
v_add_f32_e64 v250, v250, v246
v_mul_f32 v248, 0x3de38e39, v249
v_mfma_f32_16x16x32_bf16 v[40:43], a[80:83], a[196:199], v[40:43]
v_fmac_f32 v248, 0x3e0e38e4, v250
v_sub_f32 v232, v232, v248
v_sub_f32 v234, v234, v248
v_sub_f32_e64 v236, v236, v248
v_sub_f32 v238, v238, v248
v_mfma_f32_16x16x32_bf16 v[44:47], a[84:87], a[196:199], v[44:47]
ds_read_b128 a[168:171], %[vXr1] offset:6144
s_waitcnt vmcnt(10)
v_mfma_f32_16x16x32_bf16 v[48:51], a[80:83], a[200:203], v[48:51]
ds_read_b128 a[172:175], %[vXr1] offset:7168
v_accvgpr_read_b32 v104, a96
v_accvgpr_read_b32 v105, a100
v_mfma_f32_16x16x32_bf16 v[52:55], a[84:87], a[200:203], v[52:55]
buffer_load_dwordx2 v[200:201], %[vA2], %[sWS], 0 offen offset:512 sc1
v_accvgpr_read_b32 v106, a104
v_accvgpr_read_b32 v107, a106
v_mfma_f32_16x16x32_bf16 v[32:35], a[88:91], a[204:207], v[32:35]
ds_read_b128 a[176:179], %[vXr1] offset:8192
v_accvgpr_read_b32 v108, a108
v_accvgpr_read_b32 v109, a110
v_mfma_f32_16x16x32_bf16 v[36:39], a[92:95], a[204:207], v[36:39]
v_sub_f32_e64 v240, v240, v248
v_accvgpr_read_b32 v110, a112
v_accvgpr_read_b32 v111, a116
v_mfma_f32_16x16x32_bf16 v[40:43], a[88:91], a[208:211], v[40:43]
v_sub_f32_e64 v242, v242, v248
v_min3_u32 v104, v104, v105, v106
v_min3_u32 v104, v104, v107, v108
v_mfma_f32_16x16x32_bf16 v[44:47], a[92:95], a[208:211], v[44:47]
buffer_load_dwordx2 v[202:203], %[vA2], %[sWS], 0 offen offset:640 sc1
v_min3_u32 v104, v104, v109, v110
v_min_u32_e64 v104, v104, v111
v_mfma_f32_16x16x32_bf16 v[48:51], a[88:91], a[212:215], v[48:51]
v_sub_f32 v244, v244, v248
v_sub_f32 v246, v246, v248
v_mul_f32_e64 v232, v232, v232
v_cmp_eq_u32 vcc, 0, v104
s_cbranch_vccnz L_sl13_%=
L_ok13_%=:
v_mfma_f32_16x16x32_bf16 v[52:55], a[92:95], a[212:215], v[52:55]
s_waitcnt lgkmcnt(0)
v_mul_f32 v234, v234, v234
v_mul_f32 v236, v236, v236
v_mul_f32 v238, v238, v238
v_mul_f32 v240, v240, v240
v_mul_f32 v242, v242, v242
v_mfma_f32_16x16x32_bf16 v[32:35], a[96:99], a[144:147], v[32:35]
ds_read_b128 a[180:183], %[vXr1] offset:9216
v_mul_f32 v244, v244, v244
v_mul_f32 v246, v246, v246
v_mfma_f32_16x16x32_bf16 v[36:39], a[100:103], a[144:147], v[36:39]
ds_read_b128 a[184:187], %[vXr1] offset:10240
v_fmac_f32 v233, 0xc2800000, v232
v_fmac_f32 v235, 0xc2a00000, v234
v_mfma_f32_16x16x32_bf16 v[40:43], a[96:99], a[148:151], v[40:43]
buffer_load_dwordx2 v[204:205], %[vA2], %[sWS], 0 offen offset:768 sc1
v_fmac_f32 v237, 0xc2800000, v236
v_fmac_f32 v239, 0xc2a00000, v238
v_fmac_f32 v241, 0xc2800000, v240
v_mfma_f32_16x16x32_bf16 v[44:47], a[100:103], a[148:151], v[44:47]
ds_read_b128 a[188:191], %[vXr1] offset:11264
v_fmac_f32 v243, 0xc2a00000, v242
v_fmac_f32 v245, 0xc2800000, v244
v_mfma_f32_16x16x32_bf16 v[48:51], a[96:99], a[152:155], v[48:51]
ds_read_b128 a[192:195], %[vXr1] offset:12288
v_fmac_f32 v247, 0xc2a00000, v246
v_add_f32_e64 v233, v233, v235
v_mfma_f32_16x16x32_bf16 v[52:55], a[100:103], a[152:155], v[52:55]
v_add_f32 v237, v237, v239
v_add_f32 v241, v241, v243
v_add_f32 v245, v245, v247
v_add_f32 v233, v233, v237
v_mfma_f32_16x16x32_bf16 v[32:35], a[104:107], a[156:159], v[32:35]
ds_read_b128 a[196:199], %[vXr1] offset:13312
v_add_f32 v241, v241, v245
v_add_f32 v233, v233, v241
v_mov_b32 v249, 0x3727c5ac
v_mfma_f32_16x16x32_bf16 v[36:39], a[108:111], a[156:159], v[36:39]
ds_read_b128 a[200:203], %[vXr1] offset:14336
v_fmac_f32 v249, 0xbae38e39, v233
v_mfma_f32_16x16x32_bf16 v[40:43], a[104:107], a[160:163], v[40:43]
v_rsq_f32 v249, v249
v_mov_b32 v232, 0
v_mul_f32 v249, 0xbeeff558, v249
v_mul_f32 v250, v248, v249
v_mfma_f32_16x16x32_bf16 v[44:47], a[108:111], a[160:163], v[44:47]
ds_read_b128 a[204:207], %[vXr1] offset:15360
s_waitcnt vmcnt(7)
v_mfma_f32_16x16x32_bf16 v[48:51], a[104:107], a[164:167], v[48:51]
ds_read_b128 a[208:211], %[vXr1] offset:16384
v_accvgpr_read_b32 v104, a120
v_accvgpr_read_b32 v105, a124
v_mfma_f32_16x16x32_bf16 v[52:55], a[108:111], a[164:167], v[52:55]
buffer_load_dwordx2 v[206:207], %[vA2], %[sWS], 0 offen offset:896 sc1
v_accvgpr_read_b32 v106, a128
v_accvgpr_read_b32 v107, a132
v_mfma_f32_16x16x32_bf16 v[32:35], a[112:115], a[168:171], v[32:35]
ds_read_b128 a[212:215], %[vXr1] offset:17408
v_accvgpr_read_b32 v108, a136
v_accvgpr_read_b32 v109, a138
v_mfma_f32_16x16x32_bf16 v[36:39], a[116:119], a[168:171], v[36:39]
v_accvgpr_read_b32 v110, a140
v_accvgpr_read_b32 v111, a142
v_mfma_f32_16x16x32_bf16 v[40:43], a[112:115], a[172:175], v[40:43]
v_sub_f32_e64 v250, 0, v250
v_min3_u32 v104, v104, v105, v106
v_min3_u32 v104, v104, v107, v108
v_mfma_f32_16x16x32_bf16 v[44:47], a[116:119], a[172:175], v[44:47]
buffer_load_dwordx2 v[208:209], %[vA2], %[sWS], 0 offen offset:1024 sc1
v_min3_u32 v104, v104, v109, v110
v_min_u32_e64 v104, v104, v111
v_mfma_f32_16x16x32_bf16 v[48:51], a[112:115], a[176:179], v[48:51]
v_cmp_eq_u32_e64 vcc, 0, v104
s_cbranch_vccnz L_sl14_%=
L_ok14_%=:
v_mfma_f32_16x16x32_bf16 v[52:55], a[116:119], a[176:179], v[52:55]
s_waitcnt lgkmcnt(0)
ds_write_b32 %[vTw], v249 offset:0
ds_write_b32 %[vTw], v250 offset:128
v_mfma_f32_16x16x32_bf16 v[32:35], a[120:123], a[180:183], v[32:35]
ds_read_b128 a[144:147], %[vXr1] offset:18432
v_mfma_f32_16x16x32_bf16 v[36:39], a[124:127], a[180:183], v[36:39]
ds_read_b128 a[148:151], %[vXr1] offset:19456
v_mfma_f32_16x16x32_bf16 v[40:43], a[120:123], a[184:187], v[40:43]
)"
R"(ds_read_b128 v[176:179], %[vTr] offset:0
v_mfma_f32_16x16x32_bf16 v[44:47], a[124:127], a[184:187], v[44:47]
ds_read_b128 a[152:155], %[vXr1] offset:20480
v_mfma_f32_16x16x32_bf16 v[48:51], a[120:123], a[188:191], v[48:51]
ds_read_b128 a[156:159], %[vXr1] offset:21504
v_mfma_f32_16x16x32_bf16 v[52:55], a[124:127], a[188:191], v[52:55]
v_mfma_f32_16x16x32_bf16 v[32:35], a[128:131], a[192:195], v[32:35]
ds_read_b128 a[160:163], %[vXr1] offset:22528
v_mfma_f32_16x16x32_bf16 v[36:39], a[132:135], a[192:195], v[36:39]
ds_read_b128 a[164:167], %[vXr1] offset:23552
v_mfma_f32_16x16x32_bf16 v[40:43], a[128:131], a[196:199], v[40:43]
ds_read_b128 v[180:183], %[vTr] offset:16
v_mfma_f32_16x16x32_bf16 v[44:47], a[132:135], a[196:199], v[44:47]
ds_read_b128 a[168:171], %[vXr1] offset:24576
v_mfma_f32_16x16x32_bf16 v[48:51], a[128:131], a[200:203], v[48:51]
ds_read_b128 a[172:175], %[vXr1] offset:25600
v_mfma_f32_16x16x32_bf16 v[52:55], a[132:135], a[200:203], v[52:55]
v_mfma_f32_16x16x32_bf16 v[32:35], a[136:139], a[204:207], v[32:35]
ds_read_b128 a[176:179], %[vXr1] offset:26624
v_mfma_f32_16x16x32_bf16 v[36:39], a[140:143], a[204:207], v[36:39]
ds_read_b128 v[184:187], %[vTr] offset:128
v_mfma_f32_16x16x32_bf16 v[40:43], a[136:139], a[208:211], v[40:43]
v_mfma_f32_16x16x32_bf16 v[44:47], a[140:143], a[208:211], v[44:47]
ds_read_b128 v[188:191], %[vTr] offset:144
v_mfma_f32_16x16x32_bf16 v[48:51], a[136:139], a[212:215], v[48:51]
v_mfma_f32_16x16x32_bf16 v[52:55], a[140:143], a[212:215], v[52:55]
s_waitcnt vmcnt(0) lgkmcnt(0)
v_mfma_f32_16x16x32_bf16 v[56:59], a[0:3], a[144:147], 0
s_add_u32 m0, %[sLx], 0
ds_read_b128 a[180:183], %[vXr1] offset:27648
v_mfma_f32_16x16x32_bf16 v[60:63], a[4:7], a[144:147], 0
s_add_u32 s48, s20, 147456
ds_read_b128 a[184:187], %[vXr1] offset:28672
v_mfma_f32_16x16x32_bf16 v[64:67], a[0:3], a[148:151], 0
buffer_load_dwordx4 %[vL16], %[sWS], s48 offen offset:0 sc1 lds
v_mfma_f32_16x16x32_bf16 v[68:71], a[4:7], a[148:151], 0
ds_read_b128 a[188:191], %[vXr1] offset:29696
v_min3_u32 v232, v192, v194, v196
v_mfma_f32_16x16x32_bf16 v[72:75], a[0:3], a[152:155], 0
LPb_1:
s_cbranch_execz LPb_5
ds_read_b128 a[192:195], %[vXr1] offset:30720
v_min3_u32 v232, v232, v198, v200
v_mfma_f32_16x16x32_bf16 v[76:79], a[4:7], a[152:155], 0
v_min3_u32 v232, v232, v202, v204
v_min3_u32 v232, v232, v206, v208
v_mfma_f32_16x16x32_bf16 v[56:59], a[8:11], a[156:159], v[56:59]
ds_read_b128 a[196:199], %[vXr1] offset:31744
v_mfma_f32_16x16x32_bf16 v[60:63], a[12:15], a[156:159], v[60:63]
ds_read_b128 a[200:203], %[vXr1] offset:32768
v_cmp_eq_u32_e64 vcc, 0, v232
s_cbranch_vccnz L_sl4_%=
L_ok4_%=:
v_pk_fma_f32 v[152:153], v[184:185], v[192:193], v[192:193] op_sel:[0,0,1] op_sel_hi:[1,0,1]
v_pk_fma_f32 v[154:155], v[186:187], v[192:193], v[192:193] op_sel:[0,0,1] op_sel_hi:[1,0,1]
v_pk_fma_f32 v[156:157], v[188:189], v[192:193], v[192:193] op_sel:[0,0,1] op_sel_hi:[1,0,1]
v_pk_fma_f32 v[158:159], v[190:191], v[192:193], v[192:193] op_sel:[0,0,1] op_sel_hi:[1,0,1]
v_pk_fma_f32 v[160:161], v[184:185], v[194:195], v[194:195] op_sel:[0,0,1] op_sel_hi:[1,0,1]
v_pk_fma_f32 v[162:163], v[186:187], v[194:195], v[194:195] op_sel:[0,0,1] op_sel_hi:[1,0,1]
v_pk_fma_f32 v[164:165], v[188:189], v[194:195], v[194:195] op_sel:[0,0,1] op_sel_hi:[1,0,1]
v_pk_fma_f32 v[166:167], v[190:191], v[194:195], v[194:195] op_sel:[0,0,1] op_sel_hi:[1,0,1]
v_pk_fma_f32 v[168:169], v[184:185], v[196:197], v[196:197] op_sel:[0,0,1] op_sel_hi:[1,0,1]
v_pk_fma_f32 v[170:171], v[186:187], v[196:197], v[196:197] op_sel:[0,0,1] op_sel_hi:[1,0,1]
v_pk_fma_f32 v[172:173], v[188:189], v[196:197], v[196:197] op_sel:[0,0,1] op_sel_hi:[1,0,1]
v_pk_fma_f32 v[174:175], v[190:191], v[196:197], v[196:197] op_sel:[0,0,1] op_sel_hi:[1,0,1]
v_pk_fma_f32 v[32:33], v[32:33], v[176:177], v[152:153]
v_pk_fma_f32 v[34:35], v[34:35], v[178:179], v[154:155]
v_pk_fma_f32 v[36:37], v[36:37], v[180:181], v[156:157]
v_pk_fma_f32 v[38:39], v[38:39], v[182:183], v[158:159]
v_pk_fma_f32 v[40:41], v[40:41], v[176:177], v[160:161]
v_pk_fma_f32 v[42:43], v[42:43], v[178:179], v[162:163]
v_pk_fma_f32 v[44:45], v[44:45], v[180:181], v[164:165]
v_pk_fma_f32 v[46:47], v[46:47], v[182:183], v[166:167]
v_pk_fma_f32 v[48:49], v[48:49], v[176:177], v[168:169]
v_pk_fma_f32 v[50:51], v[50:51], v[178:179], v[170:171]
v_pk_fma_f32 v[52:53], v[52:53], v[180:181], v[172:173]
v_pk_fma_f32 v[54:55], v[54:55], v[182:183], v[174:175]
v_pk_fma_f32 v[152:153], v[32:33], v[32:33], s[50:51] op_sel_hi:[1,1,0]
v_pk_fma_f32 v[154:155], v[34:35], v[34:35], s[50:51] op_sel_hi:[1,1,0]
v_pk_fma_f32 v[156:157], v[36:37], v[36:37], s[50:51] op_sel_hi:[1,1,0]
v_pk_fma_f32 v[158:159], v[38:39], v[38:39], s[50:51] op_sel_hi:[1,1,0]
v_pk_fma_f32 v[160:161], v[40:41], v[40:41], s[50:51] op_sel_hi:[1,1,0]
v_pk_fma_f32 v[162:163], v[42:43], v[42:43], s[50:51] op_sel_hi:[1,1,0]
v_pk_fma_f32 v[164:165], v[44:45], v[44:45], s[50:51] op_sel_hi:[1,1,0]
v_pk_fma_f32 v[166:167], v[46:47], v[46:47], s[50:51] op_sel_hi:[1,1,0]
v_pk_fma_f32 v[168:169], v[48:49], v[48:49], s[50:51] op_sel_hi:[1,1,0]
v_pk_fma_f32 v[170:171], v[50:51], v[50:51], s[50:51] op_sel_hi:[1,1,0]
v_pk_fma_f32 v[172:173], v[52:53], v[52:53], s[50:51] op_sel_hi:[1,1,0]
v_pk_fma_f32 v[174:175], v[54:55], v[54:55], s[50:51] op_sel_hi:[1,1,0]
v_pk_mul_f32 v[152:153], v[152:153], v[32:33]
v_pk_mul_f32 v[154:155], v[154:155], v[34:35]
v_pk_mul_f32 v[156:157], v[156:157], v[36:37]
v_pk_mul_f32 v[158:159], v[158:159], v[38:39]
v_pk_mul_f32 v[160:161], v[160:161], v[40:41]
v_pk_mul_f32 v[162:163], v[162:163], v[42:43]
v_pk_mul_f32 v[164:165], v[164:165], v[44:45]
v_pk_mul_f32 v[166:167], v[166:167], v[46:47]
v_pk_mul_f32 v[168:169], v[168:169], v[48:49]
v_pk_mul_f32 v[170:171], v[170:171], v[50:51]
v_pk_mul_f32 v[172:173], v[172:173], v[52:53]
v_pk_mul_f32 v[174:175], v[174:175], v[54:55]
v_mfma_f32_16x16x32_bf16 v[64:67], a[8:11], a[160:163], v[64:67]
v_exp_f32_e64 v152, v152
v_mfma_f32_16x16x32_bf16 v[68:71], a[12:15], a[160:163], v[68:71]
ds_read_b128 a[204:207], %[vXr1] offset:33792
v_mfma_f32_16x16x32_bf16 v[72:75], a[8:11], a[164:167], v[72:75]
ds_read_b128 a[208:211], %[vXr1] offset:34816
v_exp_f32_e64 v153, v153
v_mfma_f32_16x16x32_bf16 v[76:79], a[12:15], a[164:167], v[76:79]
v_add_f32_e64 v152, 1.0, v152
v_mfma_f32_16x16x32_bf16 v[56:59], a[16:19], a[168:171], v[56:59]
ds_read_b128 a[212:215], %[vXr1] offset:35840
v_exp_f32_e64 v154, v154
v_mfma_f32_16x16x32_bf16 v[60:63], a[20:23], a[168:171], v[60:63]
buffer_load_dwordx4 %[vL16], %[sWS], s48 offen offset:1024 sc1 lds
v_add_f32_e64 v153, 1.0, v153
v_mfma_f32_16x16x32_bf16 v[64:67], a[16:19], a[172:175], v[64:67]
v_rcp_f32_e64 v152, v152
v_mfma_f32_16x16x32_bf16 v[68:71], a[20:23], a[172:175], v[68:71]
v_exp_f32_e64 v155, v155
v_mfma_f32_16x16x32_bf16 v[72:75], a[16:19], a[176:179], v[72:75]
v_add_f32_e64 v154, 1.0, v154
v_mfma_f32_16x16x32_bf16 v[76:79], a[20:23], a[176:179], v[76:79]
s_waitcnt lgkmcnt(0)
s_barrier
v_rcp_f32 v153, v153
v_exp_f32 v156, v156
v_mfma_f32_16x16x32_bf16 v[56:59], a[24:27], a[180:183], v[56:59]
ds_read_b128 a[144:147], %[vXr2] offset:0
v_add_f32_e64 v155, 1.0, v155
v_mfma_f32_16x16x32_bf16 v[60:63], a[28:31], a[180:183], v[60:63]
ds_read_b128 a[148:151], %[vXr2] offset:1024
v_mfma_f32_16x16x32_bf16 v[64:67], a[24:27], a[184:187], v[64:67]
ds_read_b32 v226, %[vT1] offset:0
v_rcp_f32_e64 v154, v154
v_mfma_f32_16x16x32_bf16 v[68:71], a[28:31], a[184:187], v[68:71]
ds_read_b128 a[152:155], %[vXr2] offset:2048
v_mfma_f32_16x16x32_bf16 v[72:75], a[24:27], a[188:191], v[72:75]
ds_read_b128 a[156:159], %[vXr2] offset:3072
v_exp_f32_e64 v157, v157
v_mfma_f32_16x16x32_bf16 v[76:79], a[28:31], a[188:191], v[76:79]
ds_read_b32 v227, %[vT1] offset:128
v_add_f32_e64 v156, 1.0, v156
v_mfma_f32_16x16x32_bf16 v[56:59], a[32:35], a[192:195], v[56:59]
ds_read_b128 a[160:163], %[vXr2] offset:4096
v_mfma_f32_16x16x32_bf16 v[60:63], a[36:39], a[192:195], v[60:63]
ds_read_b128 a[164:167], %[vXr2] offset:5120
v_mfma_f32_16x16x32_bf16 v[64:67], a[32:35], a[196:199], v[64:67]
v_rcp_f32 v155, v155
v_exp_f32 v158, v158
v_mfma_f32_16x16x32_bf16 v[68:71], a[36:39], a[196:199], v[68:71]
ds_read_b128 a[168:171], %[vXr2] offset:6144
v_add_f32_e64 v157, 1.0, v157
v_mfma_f32_16x16x32_bf16 v[72:75], a[32:35], a[200:203], v[72:75]
ds_read_b128 a[172:175], %[vXr2] offset:7168
v_mfma_f32_16x16x32_bf16 v[76:79], a[36:39], a[200:203], v[76:79]
ds_read_b32 v228, %[vT1] offset:16
v_rcp_f32_e64 v156, v156
v_mfma_f32_16x16x32_bf16 v[56:59], a[40:43], a[204:207], v[56:59]
ds_read_b128 a[176:179], %[vXr2] offset:8192
v_mfma_f32_16x16x32_bf16 v[60:63], a[44:47], a[204:207], v[60:63]
ds_read_b32 v229, %[vT1] offset:144
v_mfma_f32_16x16x32_bf16 v[64:67], a[40:43], a[208:211], v[64:67]
v_exp_f32 v159, v159
v_add_f32 v158, 1.0, v158
v_mfma_f32_16x16x32_bf16 v[68:71], a[44:47], a[208:211], v[68:71]
buffer_load_dwordx4 %[vL16], %[sWS], s48 offen offset:2048 sc1 lds
v_rcp_f32_e64 v157, v157
v_mfma_f32_16x16x32_bf16 v[72:75], a[40:43], a[212:215], v[72:75]
buffer_load_dwordx4 %[vL16], %[sWS], s48 offen offset:3072 sc1 lds
v_mfma_f32_16x16x32_bf16 v[76:79], a[44:47], a[212:215], v[76:79]
s_waitcnt lgkmcnt(1)
v_exp_f32 v160, v160
v_add_f32_e64 v159, 1.0, v159
v_mfma_f32_16x16x32_bf16 v[56:59], a[48:51], a[144:147], v[56:59]
ds_read_b128 a[180:183], %[vXr2] offset:9216
v_mfma_f32_16x16x32_bf16 v[60:63], a[52:55], a[144:147], v[60:63]
ds_read_b128 a[184:187], %[vXr2] offset:10240
v_rcp_f32_e64 v158, v158
v_mfma_f32_16x16x32_bf16 v[64:67], a[48:51], a[148:151], v[64:67]
buffer_load_dwordx4 a[216:219], %[vA1], %[sWS], 0 offen offset:512 sc1
v_mfma_f32_16x16x32_bf16 v[68:71], a[52:55], a[148:151], v[68:71]
ds_read_b128 a[188:191], %[vXr2] offset:11264
v_mfma_f32_16x16x32_bf16 v[72:75], a[48:51], a[152:155], v[72:75]
ds_read_b128 a[192:195], %[vXr2] offset:12288
v_exp_f32_e64 v161, v161
v_mfma_f32_16x16x32_bf16 v[76:79], a[52:55], a[152:155], v[76:79]
v_add_f32_e64 v160, 1.0, v160
v_mfma_f32_16x16x32_bf16 v[56:59], a[56:59], a[156:159], v[56:59]
ds_read_b128 a[196:199], %[vXr2] offset:13312
v_rcp_f32_e64 v159, v159
v_mfma_f32_16x16x32_bf16 v[60:63], a[60:63], a[156:159], v[60:63]
ds_read_b128 a[200:203], %[vXr2] offset:14336
v_mfma_f32_16x16x32_bf16 v[64:67], a[56:59], a[160:163], v[64:67]
buffer_load_dwordx4 a[220:223], %[vA1], %[sWS], 0 offen offset:528 sc1
v_exp_f32_e64 v162, v162
v_mfma_f32_16x16x32_bf16 v[68:71], a[60:63], a[160:163], v[68:71]
ds_read_b128 a[204:207], %[vXr2] offset:15360
v_mfma_f32_16x16x32_bf16 v[72:75], a[56:59], a[164:167], v[72:75]
ds_read_b128 a[208:211], %[vXr2] offset:16384
v_add_f32_e64 v161, 1.0, v161
)"
R"(v_mfma_f32_16x16x32_bf16 v[76:79], a[60:63], a[164:167], v[76:79]
s_add_u32 m0, %[sLx], 4096
v_rcp_f32_e64 v160, v160
v_mfma_f32_16x16x32_bf16 v[56:59], a[64:67], a[168:171], v[56:59]
s_add_u32 s48, s20, 151552
ds_read_b128 a[212:215], %[vXr2] offset:17408
v_mfma_f32_16x16x32_bf16 v[60:63], a[68:71], a[168:171], v[60:63]
buffer_load_dwordx4 %[vL16], %[sWS], s48 offen offset:0 sc1 lds
v_exp_f32_e64 v163, v163
v_mfma_f32_16x16x32_bf16 v[64:67], a[64:67], a[172:175], v[64:67]
v_add_f32_e64 v162, 1.0, v162
v_mfma_f32_16x16x32_bf16 v[68:71], a[68:71], a[172:175], v[68:71]
buffer_load_dwordx4 %[vL16], %[sWS], s48 offen offset:1024 sc1 lds
v_rcp_f32_e64 v161, v161
v_mfma_f32_16x16x32_bf16 v[72:75], a[64:67], a[176:179], v[72:75]
v_exp_f32_e64 v164, v164
v_mfma_f32_16x16x32_bf16 v[76:79], a[68:71], a[176:179], v[76:79]
s_waitcnt lgkmcnt(0)
v_add_f32 v163, 1.0, v163
v_rcp_f32_e64 v162, v162
v_mfma_f32_16x16x32_bf16 v[56:59], a[72:75], a[180:183], v[56:59]
ds_read_b128 a[144:147], %[vXr2] offset:18432
v_mfma_f32_16x16x32_bf16 v[60:63], a[76:79], a[180:183], v[60:63]
ds_read_b128 a[148:151], %[vXr2] offset:19456
v_exp_f32_e64 v165, v165
v_mfma_f32_16x16x32_bf16 v[64:67], a[72:75], a[184:187], v[64:67]
buffer_load_dwordx4 a[224:227], %[vA1], %[sWS], 0 offen offset:640 sc1
v_mfma_f32_16x16x32_bf16 v[68:71], a[76:79], a[184:187], v[68:71]
ds_read_b128 a[152:155], %[vXr2] offset:20480
v_add_f32_e64 v164, 1.0, v164
v_mfma_f32_16x16x32_bf16 v[72:75], a[72:75], a[188:191], v[72:75]
ds_read_b128 a[156:159], %[vXr2] offset:21504
v_mfma_f32_16x16x32_bf16 v[76:79], a[76:79], a[188:191], v[76:79]
v_rcp_f32_e64 v163, v163
v_mfma_f32_16x16x32_bf16 v[56:59], a[80:83], a[192:195], v[56:59]
ds_read_b128 a[160:163], %[vXr2] offset:22528
v_exp_f32_e64 v166, v166
v_mfma_f32_16x16x32_bf16 v[60:63], a[84:87], a[192:195], v[60:63]
ds_read_b128 a[164:167], %[vXr2] offset:23552
v_add_f32_e64 v165, 1.0, v165
v_mfma_f32_16x16x32_bf16 v[64:67], a[80:83], a[196:199], v[64:67]
buffer_load_dwordx4 a[228:231], %[vA1], %[sWS], 0 offen offset:656 sc1
v_mfma_f32_16x16x32_bf16 v[68:71], a[84:87], a[196:199], v[68:71]
ds_read_b128 a[168:171], %[vXr2] offset:24576
v_mfma_f32_16x16x32_bf16 v[72:75], a[80:83], a[200:203], v[72:75]
ds_read_b128 a[172:175], %[vXr2] offset:25600
v_rcp_f32_e64 v164, v164
v_mfma_f32_16x16x32_bf16 v[76:79], a[84:87], a[200:203], v[76:79]
v_exp_f32_e64 v167, v167
v_mfma_f32_16x16x32_bf16 v[56:59], a[88:91], a[204:207], v[56:59]
ds_read_b128 a[176:179], %[vXr2] offset:26624
v_add_f32_e64 v166, 1.0, v166
v_mfma_f32_16x16x32_bf16 v[60:63], a[92:95], a[204:207], v[60:63]
buffer_load_dwordx4 %[vL16], %[sWS], s48 offen offset:2048 sc1 lds
v_mfma_f32_16x16x32_bf16 v[64:67], a[88:91], a[208:211], v[64:67]
v_rcp_f32_e64 v165, v165
v_mfma_f32_16x16x32_bf16 v[68:71], a[92:95], a[208:211], v[68:71]
buffer_load_dwordx4 %[vL16], %[sWS], s48 offen offset:3072 sc1 lds
v_exp_f32_e64 v168, v168
v_mfma_f32_16x16x32_bf16 v[72:75], a[88:91], a[212:215], v[72:75]
v_add_f32_e64 v167, 1.0, v167
v_mfma_f32_16x16x32_bf16 v[76:79], a[92:95], a[212:215], v[76:79]
s_waitcnt lgkmcnt(0)
v_rcp_f32 v166, v166
v_exp_f32_e64 v169, v169
v_mfma_f32_16x16x32_bf16 v[56:59], a[96:99], a[144:147], v[56:59]
ds_read_b128 a[180:183], %[vXr2] offset:27648
v_add_f32_e64 v168, 1.0, v168
v_mfma_f32_16x16x32_bf16 v[60:63], a[100:103], a[144:147], v[60:63]
ds_read_b128 a[184:187], %[vXr2] offset:28672
v_mfma_f32_16x16x32_bf16 v[64:67], a[96:99], a[148:151], v[64:67]
buffer_load_dwordx4 a[232:235], %[vA1], %[sWS], 0 offen offset:768 sc1
v_mfma_f32_16x16x32_bf16 v[68:71], a[100:103], a[148:151], v[68:71]
ds_read_b128 a[188:191], %[vXr2] offset:29696
v_rcp_f32_e64 v167, v167
v_mfma_f32_16x16x32_bf16 v[72:75], a[96:99], a[152:155], v[72:75]
ds_read_b128 a[192:195], %[vXr2] offset:30720
v_mfma_f32_16x16x32_bf16 v[76:79], a[100:103], a[152:155], v[76:79]
buffer_load_dwordx4 a[236:239], %[vA1], %[sWS], 0 offen offset:784 sc1
v_exp_f32_e64 v170, v170
v_mfma_f32_16x16x32_bf16 v[56:59], a[104:107], a[156:159], v[56:59]
ds_read_b128 a[196:199], %[vXr2] offset:31744
v_add_f32_e64 v169, 1.0, v169
v_mfma_f32_16x16x32_bf16 v[60:63], a[108:111], a[156:159], v[60:63]
ds_read_b128 a[200:203], %[vXr2] offset:32768
v_mfma_f32_16x16x32_bf16 v[64:67], a[104:107], a[160:163], v[64:67]
v_rcp_f32_e64 v168, v168
v_mfma_f32_16x16x32_bf16 v[68:71], a[108:111], a[160:163], v[68:71]
s_add_u32 m0, %[sLx], 8192
ds_read_b128 a[204:207], %[vXr2] offset:33792
v_mfma_f32_16x16x32_bf16 v[72:75], a[104:107], a[164:167], v[72:75]
s_add_u32 s48, s20, 155648
ds_read_b128 a[208:211], %[vXr2] offset:34816
v_exp_f32_e64 v171, v171
v_mfma_f32_16x16x32_bf16 v[76:79], a[108:111], a[164:167], v[76:79]
buffer_load_dwordx4 %[vL16], %[sWS], s48 offen offset:0 sc1 lds
v_add_f32_e64 v170, 1.0, v170
v_mfma_f32_16x16x32_bf16 v[56:59], a[112:115], a[168:171], v[56:59]
s_add_u32 m0, %[sLx], 36864
ds_read_b128 a[212:215], %[vXr2] offset:35840
v_mfma_f32_16x16x32_bf16 v[60:63], a[116:119], a[168:171], v[60:63]
s_add_u32 s48, s20, 184320
v_rcp_f32_e64 v169, v169
v_mfma_f32_16x16x32_bf16 v[64:67], a[112:115], a[172:175], v[64:67]
buffer_load_dwordx4 %[vL16], %[sWS], s48 offen offset:0 sc1 lds
v_exp_f32_e64 v172, v172
v_mfma_f32_16x16x32_bf16 v[68:71], a[116:119], a[172:175], v[68:71]
v_add_f32_e64 v171, 1.0, v171
v_mfma_f32_16x16x32_bf16 v[72:75], a[112:115], a[176:179], v[72:75]
buffer_load_dwordx4 %[vL16], %[sWS], s48 offen offset:1024 sc1 lds
v_rcp_f32_e64 v170, v170
v_mfma_f32_16x16x32_bf16 v[76:79], a[116:119], a[176:179], v[76:79]
s_waitcnt lgkmcnt(0)
s_barrier
v_exp_f32_e64 v173, v173
v_mfma_f32_16x16x32_bf16 v[56:59], a[120:123], a[180:183], v[56:59]
ds_read_b128 a[144:147], %[vXr3] offset:0
v_add_f32_e64 v172, 1.0, v172
v_mfma_f32_16x16x32_bf16 v[60:63], a[124:127], a[180:183], v[60:63]
ds_read_b128 a[148:151], %[vXr3] offset:1024
v_mfma_f32_16x16x32_bf16 v[64:67], a[120:123], a[184:187], v[64:67]
buffer_load_dwordx4 a[240:243], %[vA1], %[sWS], 0 offen offset:896 sc1
v_mfma_f32_16x16x32_bf16 v[68:71], a[124:127], a[184:187], v[68:71]
ds_read_b128 a[152:155], %[vXr3] offset:2048
v_rcp_f32_e64 v171, v171
v_mfma_f32_16x16x32_bf16 v[72:75], a[120:123], a[188:191], v[72:75]
ds_read_b128 a[156:159], %[vXr3] offset:3072
v_mfma_f32_16x16x32_bf16 v[76:79], a[124:127], a[188:191], v[76:79]
v_exp_f32_e64 v174, v174
v_mfma_f32_16x16x32_bf16 v[56:59], a[128:131], a[192:195], v[56:59]
ds_read_b128 a[160:163], %[vXr3] offset:4096
v_add_f32_e64 v173, 1.0, v173
v_mfma_f32_16x16x32_bf16 v[60:63], a[132:135], a[192:195], v[60:63]
ds_read_b128 a[164:167], %[vXr3] offset:5120
v_mfma_f32_16x16x32_bf16 v[64:67], a[128:131], a[196:199], v[64:67]
buffer_load_dwordx4 a[244:247], %[vA1], %[sWS], 0 offen offset:912 sc1
v_rcp_f32_e64 v172, v172
v_mfma_f32_16x16x32_bf16 v[68:71], a[132:135], a[196:199], v[68:71]
ds_read_b128 a[168:171], %[vXr3] offset:6144
v_mfma_f32_16x16x32_bf16 v[72:75], a[128:131], a[200:203], v[72:75]
ds_read_b128 a[172:175], %[vXr3] offset:7168
v_mfma_f32_16x16x32_bf16 v[76:79], a[132:135], a[200:203], v[76:79]
v_exp_f32 v175, v175
v_add_f32 v174, 1.0, v174
v_mfma_f32_16x16x32_bf16 v[56:59], a[136:139], a[204:207], v[56:59]
ds_read_b128 a[176:179], %[vXr3] offset:8192
v_mfma_f32_16x16x32_bf16 v[60:63], a[140:143], a[204:207], v[60:63]
buffer_load_dwordx4 %[vL16], %[sWS], s48 offen offset:2048 sc1 lds
v_mfma_f32_16x16x32_bf16 v[64:67], a[136:139], a[208:211], v[64:67]
v_rcp_f32 v173, v173
v_add_f32 v175, 1.0, v175
v_mfma_f32_16x16x32_bf16 v[68:71], a[140:143], a[208:211], v[68:71]
buffer_load_dwordx4 %[vL16], %[sWS], s48 offen offset:3072 sc1 lds
v_mfma_f32_16x16x32_bf16 v[72:75], a[136:139], a[212:215], v[72:75]
v_rcp_f32 v174, v174
v_mfma_f32_16x16x32_bf16 v[76:79], a[140:143], a[212:215], v[76:79]
s_waitcnt lgkmcnt(0)
v_rcp_f32 v175, v175
s_nop 0
v_pk_mul_f32 v[32:33], v[32:33], v[152:153]
v_pk_mul_f32 v[34:35], v[34:35], v[154:155]
v_pk_mul_f32 v[36:37], v[36:37], v[156:157]
v_pk_mul_f32 v[38:39], v[38:39], v[158:159]
v_pk_mul_f32 v[40:41], v[40:41], v[160:161]
v_pk_mul_f32 v[42:43], v[42:43], v[162:163]
v_pk_mul_f32 v[44:45], v[44:45], v[164:165]
v_pk_mul_f32 v[46:47], v[46:47], v[166:167]
v_pk_mul_f32 v[48:49], v[48:49], v[168:169]
v_pk_mul_f32 v[50:51], v[50:51], v[170:171]
v_pk_mul_f32 v[52:53], v[52:53], v[172:173]
v_pk_mul_f32 v[54:55], v[54:55], v[174:175]
v_pk_mul_f32 v[218:219], v[32:33], v[32:33]
v_pk_mul_f32 v[220:221], v[34:35], v[34:35]
v_pk_mul_f32 v[222:223], v[36:37], v[36:37]
v_pk_mul_f32 v[224:225], v[38:39], v[38:39]
v_pk_fma_f32 v[218:219], v[40:41], v[40:41], v[218:219]
v_pk_add_f32 v[210:211], v[40:41], v[32:33]
v_pk_fma_f32 v[220:221], v[42:43], v[42:43], v[220:221]
v_pk_add_f32 v[212:213], v[42:43], v[34:35]
v_pk_fma_f32 v[222:223], v[44:45], v[44:45], v[222:223]
v_pk_add_f32 v[214:215], v[44:45], v[36:37]
v_pk_fma_f32 v[224:225], v[46:47], v[46:47], v[224:225]
v_pk_add_f32 v[216:217], v[46:47], v[38:39]
v_pk_fma_f32 v[218:219], v[48:49], v[48:49], v[218:219]
v_pk_add_f32 v[210:211], v[48:49], v[210:211]
v_pk_fma_f32 v[220:221], v[50:51], v[50:51], v[220:221]
v_pk_add_f32 v[212:213], v[50:51], v[212:213]
v_pk_fma_f32 v[222:223], v[52:53], v[52:53], v[222:223]
v_pk_add_f32 v[214:215], v[52:53], v[214:215]
v_pk_fma_f32 v[224:225], v[54:55], v[54:55], v[224:225]
v_pk_add_f32 v[216:217], v[54:55], v[216:217]
v_mfma_f32_16x16x32_bf16 v[80:83], a[0:3], a[144:147], 0
ds_read_b128 a[180:183], %[vXr3] offset:9216
v_mfma_f32_16x16x32_bf16 v[84:87], a[4:7], a[144:147], 0
ds_read_b128 a[184:187], %[vXr3] offset:10240
v_mfma_f32_16x16x32_bf16 v[88:91], a[0:3], a[148:151], 0
buffer_load_dwordx4 a[248:251], %[vA1], %[sWS], 0 offen offset:1024 sc1
v_mfma_f32_16x16x32_bf16 v[92:95], a[4:7], a[148:151], 0
ds_read_b128 a[188:191], %[vXr3] offset:11264
v_pk_fma_f32 v[152:153], v[184:185], v[198:199], v[198:199] op_sel:[0,0,1] op_sel_hi:[1,0,1]
)"
R"(v_pk_fma_f32 v[154:155], v[186:187], v[198:199], v[198:199] op_sel:[0,0,1] op_sel_hi:[1,0,1]
v_pk_fma_f32 v[156:157], v[188:189], v[198:199], v[198:199] op_sel:[0,0,1] op_sel_hi:[1,0,1]
v_pk_fma_f32 v[158:159], v[190:191], v[198:199], v[198:199] op_sel:[0,0,1] op_sel_hi:[1,0,1]
v_pk_fma_f32 v[160:161], v[184:185], v[200:201], v[200:201] op_sel:[0,0,1] op_sel_hi:[1,0,1]
v_pk_fma_f32 v[162:163], v[186:187], v[200:201], v[200:201] op_sel:[0,0,1] op_sel_hi:[1,0,1]
v_pk_fma_f32 v[164:165], v[188:189], v[200:201], v[200:201] op_sel:[0,0,1] op_sel_hi:[1,0,1]
v_pk_fma_f32 v[166:167], v[190:191], v[200:201], v[200:201] op_sel:[0,0,1] op_sel_hi:[1,0,1]
v_pk_fma_f32 v[168:169], v[184:185], v[202:203], v[202:203] op_sel:[0,0,1] op_sel_hi:[1,0,1]
v_pk_fma_f32 v[170:171], v[186:187], v[202:203], v[202:203] op_sel:[0,0,1] op_sel_hi:[1,0,1]
v_pk_fma_f32 v[172:173], v[188:189], v[202:203], v[202:203] op_sel:[0,0,1] op_sel_hi:[1,0,1]
v_pk_fma_f32 v[174:175], v[190:191], v[202:203], v[202:203] op_sel:[0,0,1] op_sel_hi:[1,0,1]
v_pk_fma_f32 v[56:57], v[56:57], v[176:177], v[152:153]
v_pk_fma_f32 v[58:59], v[58:59], v[178:179], v[154:155]
v_pk_fma_f32 v[60:61], v[60:61], v[180:181], v[156:157]
v_pk_fma_f32 v[62:63], v[62:63], v[182:183], v[158:159]
v_pk_fma_f32 v[64:65], v[64:65], v[176:177], v[160:161]
v_pk_fma_f32 v[66:67], v[66:67], v[178:179], v[162:163]
v_pk_fma_f32 v[68:69], v[68:69], v[180:181], v[164:165]
v_pk_fma_f32 v[70:71], v[70:71], v[182:183], v[166:167]
v_pk_fma_f32 v[72:73], v[72:73], v[176:177], v[168:169]
v_pk_fma_f32 v[74:75], v[74:75], v[178:179], v[170:171]
v_pk_fma_f32 v[76:77], v[76:77], v[180:181], v[172:173]
v_pk_fma_f32 v[78:79], v[78:79], v[182:183], v[174:175]
v_pk_fma_f32 v[152:153], v[56:57], v[56:57], s[50:51] op_sel_hi:[1,1,0]
v_pk_fma_f32 v[154:155], v[58:59], v[58:59], s[50:51] op_sel_hi:[1,1,0]
v_pk_fma_f32 v[156:157], v[60:61], v[60:61], s[50:51] op_sel_hi:[1,1,0]
v_pk_fma_f32 v[158:159], v[62:63], v[62:63], s[50:51] op_sel_hi:[1,1,0]
v_pk_fma_f32 v[160:161], v[64:65], v[64:65], s[50:51] op_sel_hi:[1,1,0]
v_pk_fma_f32 v[162:163], v[66:67], v[66:67], s[50:51] op_sel_hi:[1,1,0]
v_pk_fma_f32 v[164:165], v[68:69], v[68:69], s[50:51] op_sel_hi:[1,1,0]
v_pk_fma_f32 v[166:167], v[70:71], v[70:71], s[50:51] op_sel_hi:[1,1,0]
v_pk_fma_f32 v[168:169], v[72:73], v[72:73], s[50:51] op_sel_hi:[1,1,0]
v_pk_fma_f32 v[170:171], v[74:75], v[74:75], s[50:51] op_sel_hi:[1,1,0]
v_pk_fma_f32 v[172:173], v[76:77], v[76:77], s[50:51] op_sel_hi:[1,1,0]
v_pk_fma_f32 v[174:175], v[78:79], v[78:79], s[50:51] op_sel_hi:[1,1,0]
v_pk_mul_f32 v[152:153], v[152:153], v[56:57]
v_pk_mul_f32 v[154:155], v[154:155], v[58:59]
v_pk_mul_f32 v[156:157], v[156:157], v[60:61]
v_pk_mul_f32 v[158:159], v[158:159], v[62:63]
v_pk_mul_f32 v[160:161], v[160:161], v[64:65]
v_pk_mul_f32 v[162:163], v[162:163], v[66:67]
v_pk_mul_f32 v[164:165], v[164:165], v[68:69]
v_pk_mul_f32 v[166:167], v[166:167], v[70:71]
v_pk_mul_f32 v[168:169], v[168:169], v[72:73]
v_pk_mul_f32 v[170:171], v[170:171], v[74:75]
v_pk_mul_f32 v[172:173], v[172:173], v[76:77]
v_pk_mul_f32 v[174:175], v[174:175], v[78:79]
v_mfma_f32_16x16x32_bf16 v[96:99], a[0:3], a[152:155], 0
ds_read_b128 a[192:195], %[vXr3] offset:12288
v_exp_f32_e64 v152, v152
v_mfma_f32_16x16x32_bf16 v[100:103], a[4:7], a[152:155], 0
v_exp_f32_e64 v153, v153
v_mfma_f32_16x16x32_bf16 v[80:83], a[8:11], a[156:159], v[80:83]
ds_read_b128 a[196:199], %[vXr3] offset:13312
v_add_f32_e64 v152, 1.0, v152
v_mfma_f32_16x16x32_bf16 v[84:87], a[12:15], a[156:159], v[84:87]
ds_read_b128 a[200:203], %[vXr3] offset:14336
v_mfma_f32_16x16x32_bf16 v[88:91], a[8:11], a[160:163], v[88:91]
buffer_load_dwordx4 a[252:255], %[vA1], %[sWS], 0 offen offset:1040 sc1
v_exp_f32_e64 v154, v154
v_mfma_f32_16x16x32_bf16 v[92:95], a[12:15], a[160:163], v[92:95]
ds_read_b128 a[204:207], %[vXr3] offset:15360
v_add_f32_e64 v153, 1.0, v153
v_mfma_f32_16x16x32_bf16 v[96:99], a[8:11], a[164:167], v[96:99]
ds_read_b128 a[208:211], %[vXr3] offset:16384
v_mfma_f32_16x16x32_bf16 v[100:103], a[12:15], a[164:167], v[100:103]
s_add_u32 m0, %[sLx], 40960
v_rcp_f32_e64 v152, v152
v_mfma_f32_16x16x32_bf16 v[80:83], a[16:19], a[168:171], v[80:83]
s_add_u32 s48, s20, 188416
ds_read_b128 a[212:215], %[vXr3] offset:17408
v_exp_f32_e64 v155, v155
v_mfma_f32_16x16x32_bf16 v[84:87], a[20:23], a[168:171], v[84:87]
buffer_load_dwordx4 %[vL16], %[sWS], s48 offen offset:0 sc1 lds
v_add_f32_e64 v154, 1.0, v154
v_mfma_f32_16x16x32_bf16 v[88:91], a[16:19], a[172:175], v[88:91]
v_rcp_f32_e64 v153, v153
v_mfma_f32_16x16x32_bf16 v[92:95], a[20:23], a[172:175], v[92:95]
buffer_load_dwordx4 %[vL16], %[sWS], s48 offen offset:1024 sc1 lds
v_exp_f32_e64 v156, v156
v_mfma_f32_16x16x32_bf16 v[96:99], a[16:19], a[176:179], v[96:99]
v_add_f32_e64 v155, 1.0, v155
v_mfma_f32_16x16x32_bf16 v[100:103], a[20:23], a[176:179], v[100:103]
s_waitcnt lgkmcnt(0)
v_rcp_f32 v154, v154
v_exp_f32_e64 v157, v157
v_mfma_f32_16x16x32_bf16 v[80:83], a[24:27], a[180:183], v[80:83]
ds_read_b128 a[144:147], %[vXr3] offset:18432
v_add_f32_e64 v156, 1.0, v156
v_mfma_f32_16x16x32_bf16 v[84:87], a[28:31], a[180:183], v[84:87]
ds_read_b128 a[148:151], %[vXr3] offset:19456
v_mfma_f32_16x16x32_bf16 v[88:91], a[24:27], a[184:187], v[88:91]
buffer_load_ushort v28, %[vB], %[sB], 0 offen offset:0
v_mfma_f32_16x16x32_bf16 v[92:95], a[28:31], a[184:187], v[92:95]
ds_read_b128 a[152:155], %[vXr3] offset:20480
v_rcp_f32_e64 v155, v155
v_mfma_f32_16x16x32_bf16 v[96:99], a[24:27], a[188:191], v[96:99]
ds_read_b128 a[156:159], %[vXr3] offset:21504
v_mfma_f32_16x16x32_bf16 v[100:103], a[28:31], a[188:191], v[100:103]
v_exp_f32 v158, v158
v_add_f32 v157, 1.0, v157
v_mfma_f32_16x16x32_bf16 v[80:83], a[32:35], a[192:195], v[80:83]
ds_read_b128 a[160:163], %[vXr3] offset:22528
v_mfma_f32_16x16x32_bf16 v[84:87], a[36:39], a[192:195], v[84:87]
ds_read_b128 a[164:167], %[vXr3] offset:23552
v_mfma_f32_16x16x32_bf16 v[88:91], a[32:35], a[196:199], v[88:91]
buffer_load_ushort v30, %[vB], %[sB], 0 offen offset:8
v_rcp_f32_e64 v156, v156
v_mfma_f32_16x16x32_bf16 v[92:95], a[36:39], a[196:199], v[92:95]
ds_read_b128 a[168:171], %[vXr3] offset:24576
v_mfma_f32_16x16x32_bf16 v[96:99], a[32:35], a[200:203], v[96:99]
ds_read_b128 a[172:175], %[vXr3] offset:25600
v_exp_f32_e64 v159, v159
v_mfma_f32_16x16x32_bf16 v[100:103], a[36:39], a[200:203], v[100:103]
v_add_f32_e64 v158, 1.0, v158
v_mfma_f32_16x16x32_bf16 v[80:83], a[40:43], a[204:207], v[80:83]
ds_read_b128 a[176:179], %[vXr3] offset:26624
v_mfma_f32_16x16x32_bf16 v[84:87], a[44:47], a[204:207], v[84:87]
buffer_load_dwordx4 %[vL16], %[sWS], s48 offen offset:2048 sc1 lds
v_rcp_f32_e64 v157, v157
v_mfma_f32_16x16x32_bf16 v[88:91], a[40:43], a[208:211], v[88:91]
v_exp_f32_e64 v160, v160
v_mfma_f32_16x16x32_bf16 v[92:95], a[44:47], a[208:211], v[92:95]
buffer_load_dwordx4 %[vL16], %[sWS], s48 offen offset:3072 sc1 lds
v_add_f32_e64 v159, 1.0, v159
v_mfma_f32_16x16x32_bf16 v[96:99], a[40:43], a[212:215], v[96:99]
v_rcp_f32_e64 v158, v158
v_mfma_f32_16x16x32_bf16 v[100:103], a[44:47], a[212:215], v[100:103]
s_waitcnt lgkmcnt(0)
v_exp_f32 v161, v161
v_mfma_f32_16x16x32_bf16 v[80:83], a[48:51], a[144:147], v[80:83]
ds_read_b128 a[180:183], %[vXr3] offset:27648
v_add_f32 v160, 1.0, v160
v_mfma_f32_16x16x32_bf16 v[84:87], a[52:55], a[144:147], v[84:87]
s_add_u32 s57, s26, s38
ds_read_b128 a[184:187], %[vXr3] offset:28672
v_mfma_f32_16x16x32_bf16 v[88:91], a[48:51], a[148:151], v[88:91]
buffer_load_dword v255, off, %[sWS], s57 sc1
v_mfma_f32_16x16x32_bf16 v[92:95], a[52:55], a[148:151], v[92:95]
ds_read_b128 a[188:191], %[vXr3] offset:29696
v_rcp_f32 v159, v159
v_mfma_f32_16x16x32_bf16 v[96:99], a[48:51], a[152:155], v[96:99]
s_lshl1_add_u32 s57, s26, s39
ds_read_b128 a[192:195], %[vXr3] offset:30720
v_mfma_f32_16x16x32_bf16 v[100:103], a[52:55], a[152:155], v[100:103]
buffer_load_dword v255, off, %[sWS], s57 sc1
v_exp_f32_e64 v162, v162
v_mfma_f32_16x16x32_bf16 v[80:83], a[56:59], a[156:159], v[80:83]
ds_read_b128 a[196:199], %[vXr3] offset:31744
v_add_f32_e64 v161, 1.0, v161
v_mfma_f32_16x16x32_bf16 v[84:87], a[60:63], a[156:159], v[84:87]
ds_read_b128 a[200:203], %[vXr3] offset:32768
v_mfma_f32_16x16x32_bf16 v[88:91], a[56:59], a[160:163], v[88:91]
v_rcp_f32_e64 v160, v160
v_mfma_f32_16x16x32_bf16 v[92:95], a[60:63], a[160:163], v[92:95]
s_add_u32 m0, %[sLx], 45056
ds_read_b128 a[204:207], %[vXr3] offset:33792
v_mfma_f32_16x16x32_bf16 v[96:99], a[56:59], a[164:167], v[96:99]
s_add_u32 s48, s20, 192512
ds_read_b128 a[208:211], %[vXr3] offset:34816
v_exp_f32_e64 v163, v163
v_mfma_f32_16x16x32_bf16 v[100:103], a[60:63], a[164:167], v[100:103]
buffer_load_dwordx4 %[vL16], %[sWS], s48 offen offset:0 sc1 lds
v_add_f32_e64 v162, 1.0, v162
v_mfma_f32_16x16x32_bf16 v[80:83], a[64:67], a[168:171], v[80:83]
s_add_u32 m0, %[sLx], 73728
ds_read_b128 a[212:215], %[vXr3] offset:35840
v_mfma_f32_16x16x32_bf16 v[84:87], a[68:71], a[168:171], v[84:87]
LPb_2:
s_cbranch_execz LPb_6
s_add_u32 s48, s20, 221184
v_rcp_f32 v161, v161
v_mfma_f32_16x16x32_bf16 v[88:91], a[64:67], a[172:175], v[88:91]
buffer_load_dwordx4 %[vL16], %[sWS], s48 offen offset:0 sc1 lds
v_exp_f32_e64 v164, v164
v_mfma_f32_16x16x32_bf16 v[92:95], a[68:71], a[172:175], v[92:95]
v_add_f32_e64 v163, 1.0, v163
v_mfma_f32_16x16x32_bf16 v[96:99], a[64:67], a[176:179], v[96:99]
buffer_load_dwordx4 %[vL16], %[sWS], s48 offen offset:1024 sc1 lds
v_rcp_f32_e64 v162, v162
v_mfma_f32_16x16x32_bf16 v[100:103], a[68:71], a[176:179], v[100:103]
s_waitcnt vmcnt(19) lgkmcnt(0)
s_barrier
v_exp_f32_e64 v165, v165
v_mfma_f32_16x16x32_bf16 v[80:83], a[72:75], a[180:183], v[80:83]
ds_read_b128 a[144:147], %[vXr0] offset:0
v_add_f32_e64 v164, 1.0, v164
v_mfma_f32_16x16x32_bf16 v[84:87], a[76:79], a[180:183], v[84:87]
ds_read_b128 a[148:151], %[vXr0] offset:1024
v_mfma_f32_16x16x32_bf16 v[88:91], a[72:75], a[184:187], v[88:91]
buffer_load_dwordx4 %[vL16], %[sWS], s48 offen offset:2048 sc1 lds
v_mfma_f32_16x16x32_bf16 v[92:95], a[76:79], a[184:187], v[92:95]
ds_read_b128 a[152:155], %[vXr0] offset:2048
v_rcp_f32_e64 v163, v163
v_mfma_f32_16x16x32_bf16 v[96:99], a[72:75], a[188:191], v[96:99]
ds_read_b128 a[156:159], %[vXr0] offset:3072
v_mfma_f32_16x16x32_bf16 v[100:103], a[76:79], a[188:191], v[100:103]
v_exp_f32_e64 v166, v166
v_mfma_f32_16x16x32_bf16 v[80:83], a[80:83], a[192:195], v[80:83]
ds_read_b128 a[160:163], %[vXr0] offset:4096
v_add_f32_e64 v165, 1.0, v165
)"
R"(v_mfma_f32_16x16x32_bf16 v[84:87], a[84:87], a[192:195], v[84:87]
ds_read_b128 a[164:167], %[vXr0] offset:5120
v_mfma_f32_16x16x32_bf16 v[88:91], a[80:83], a[196:199], v[88:91]
v_rcp_f32_e64 v164, v164
v_mfma_f32_16x16x32_bf16 v[92:95], a[84:87], a[196:199], v[92:95]
ds_read_b128 a[168:171], %[vXr0] offset:6144
v_mfma_f32_16x16x32_bf16 v[96:99], a[80:83], a[200:203], v[96:99]
ds_read_b128 a[172:175], %[vXr0] offset:7168
v_exp_f32_e64 v167, v167
v_mfma_f32_16x16x32_bf16 v[100:103], a[84:87], a[200:203], v[100:103]
v_add_f32_e64 v166, 1.0, v166
v_mfma_f32_16x16x32_bf16 v[80:83], a[88:91], a[204:207], v[80:83]
ds_read_b128 a[176:179], %[vXr0] offset:8192
v_mfma_f32_16x16x32_bf16 v[84:87], a[92:95], a[204:207], v[84:87]
buffer_load_dwordx4 %[vL16], %[sWS], s48 offen offset:3072 sc1 lds
v_rcp_f32_e64 v165, v165
v_mfma_f32_16x16x32_bf16 v[88:91], a[88:91], a[208:211], v[88:91]
v_exp_f32_e64 v168, v168
v_mfma_f32_16x16x32_bf16 v[92:95], a[92:95], a[208:211], v[92:95]
v_add_f32_e64 v167, 1.0, v167
v_mfma_f32_16x16x32_bf16 v[96:99], a[88:91], a[212:215], v[96:99]
v_rcp_f32_e64 v166, v166
v_mfma_f32_16x16x32_bf16 v[100:103], a[92:95], a[212:215], v[100:103]
s_waitcnt lgkmcnt(0)
v_exp_f32 v169, v169
v_mfma_f32_16x16x32_bf16 v[80:83], a[96:99], a[144:147], v[80:83]
s_add_u32 m0, %[sLx], 77824
ds_read_b128 a[180:183], %[vXr0] offset:9216
v_add_f32_e64 v168, 1.0, v168
v_mfma_f32_16x16x32_bf16 v[84:87], a[100:103], a[144:147], v[84:87]
s_add_u32 s48, s20, 225280
ds_read_b128 a[184:187], %[vXr0] offset:10240
v_mfma_f32_16x16x32_bf16 v[88:91], a[96:99], a[148:151], v[88:91]
buffer_load_dwordx4 %[vL16], %[sWS], s48 offen offset:0 sc1 lds
v_mfma_f32_16x16x32_bf16 v[92:95], a[100:103], a[148:151], v[92:95]
ds_read_b128 a[188:191], %[vXr0] offset:11264
v_rcp_f32_e64 v167, v167
v_mfma_f32_16x16x32_bf16 v[96:99], a[96:99], a[152:155], v[96:99]
ds_read_b128 a[192:195], %[vXr0] offset:12288
v_mfma_f32_16x16x32_bf16 v[100:103], a[100:103], a[152:155], v[100:103]
v_exp_f32_e64 v170, v170
v_mfma_f32_16x16x32_bf16 v[80:83], a[104:107], a[156:159], v[80:83]
ds_read_b128 a[196:199], %[vXr0] offset:13312
v_add_f32_e64 v169, 1.0, v169
v_mfma_f32_16x16x32_bf16 v[84:87], a[108:111], a[156:159], v[84:87]
ds_read_b128 a[200:203], %[vXr0] offset:14336
v_mfma_f32_16x16x32_bf16 v[88:91], a[104:107], a[160:163], v[88:91]
v_rcp_f32_e64 v168, v168
v_mfma_f32_16x16x32_bf16 v[92:95], a[108:111], a[160:163], v[92:95]
ds_read_b128 a[204:207], %[vXr0] offset:15360
v_mfma_f32_16x16x32_bf16 v[96:99], a[104:107], a[164:167], v[96:99]
ds_read_b128 a[208:211], %[vXr0] offset:16384
v_exp_f32_e64 v171, v171
v_mfma_f32_16x16x32_bf16 v[100:103], a[108:111], a[164:167], v[100:103]
v_add_f32_e64 v170, 1.0, v170
v_mfma_f32_16x16x32_bf16 v[80:83], a[112:115], a[168:171], v[80:83]
ds_read_b128 a[212:215], %[vXr0] offset:17408
v_mfma_f32_16x16x32_bf16 v[84:87], a[116:119], a[168:171], v[84:87]
buffer_load_dwordx4 %[vL16], %[sWS], s48 offen offset:1024 sc1 lds
v_rcp_f32_e64 v169, v169
v_mfma_f32_16x16x32_bf16 v[88:91], a[112:115], a[172:175], v[88:91]
v_exp_f32_e64 v172, v172
v_mfma_f32_16x16x32_bf16 v[92:95], a[116:119], a[172:175], v[92:95]
v_add_f32_e64 v171, 1.0, v171
v_mfma_f32_16x16x32_bf16 v[96:99], a[112:115], a[176:179], v[96:99]
v_rcp_f32_e64 v170, v170
v_mfma_f32_16x16x32_bf16 v[100:103], a[116:119], a[176:179], v[100:103]
s_waitcnt lgkmcnt(0)
v_exp_f32 v173, v173
v_mfma_f32_16x16x32_bf16 v[80:83], a[120:123], a[180:183], v[80:83]
ds_read_b128 a[144:147], %[vXr0] offset:18432
v_add_f32_e64 v172, 1.0, v172
v_mfma_f32_16x16x32_bf16 v[84:87], a[124:127], a[180:183], v[84:87]
ds_read_b128 a[148:151], %[vXr0] offset:19456
v_mfma_f32_16x16x32_bf16 v[88:91], a[120:123], a[184:187], v[88:91]
buffer_load_dwordx4 %[vL16], %[sWS], s48 offen offset:2048 sc1 lds
v_mfma_f32_16x16x32_bf16 v[92:95], a[124:127], a[184:187], v[92:95]
ds_read_b128 a[152:155], %[vXr0] offset:20480
v_mfma_f32_16x16x32_bf16 v[96:99], a[120:123], a[188:191], v[96:99]
ds_read_b128 a[156:159], %[vXr0] offset:21504
v_rcp_f32_e64 v171, v171
v_mfma_f32_16x16x32_bf16 v[100:103], a[124:127], a[188:191], v[100:103]
v_exp_f32_e64 v174, v174
v_mfma_f32_16x16x32_bf16 v[80:83], a[128:131], a[192:195], v[80:83]
ds_read_b128 a[160:163], %[vXr0] offset:22528
v_mfma_f32_16x16x32_bf16 v[84:87], a[132:135], a[192:195], v[84:87]
ds_read_b128 a[164:167], %[vXr0] offset:23552
v_add_f32_e64 v173, 1.0, v173
v_mfma_f32_16x16x32_bf16 v[88:91], a[128:131], a[196:199], v[88:91]
v_rcp_f32_e64 v172, v172
v_mfma_f32_16x16x32_bf16 v[92:95], a[132:135], a[196:199], v[92:95]
ds_read_b128 a[168:171], %[vXr0] offset:24576
v_mfma_f32_16x16x32_bf16 v[96:99], a[128:131], a[200:203], v[96:99]
ds_read_b128 a[172:175], %[vXr0] offset:25600
v_mfma_f32_16x16x32_bf16 v[100:103], a[132:135], a[200:203], v[100:103]
v_exp_f32 v175, v175
v_add_f32 v174, 1.0, v174
v_mfma_f32_16x16x32_bf16 v[80:83], a[136:139], a[204:207], v[80:83]
ds_read_b128 a[176:179], %[vXr0] offset:26624
v_mfma_f32_16x16x32_bf16 v[84:87], a[140:143], a[204:207], v[84:87]
buffer_load_dwordx4 %[vL16], %[sWS], s48 offen offset:3072 sc1 lds
v_mfma_f32_16x16x32_bf16 v[88:91], a[136:139], a[208:211], v[88:91]
v_rcp_f32 v173, v173
v_add_f32 v175, 1.0, v175
v_mfma_f32_16x16x32_bf16 v[92:95], a[140:143], a[208:211], v[92:95]
v_rcp_f32_e64 v174, v174
v_mfma_f32_16x16x32_bf16 v[96:99], a[136:139], a[212:215], v[96:99]
v_mfma_f32_16x16x32_bf16 v[100:103], a[140:143], a[212:215], v[100:103]
s_waitcnt lgkmcnt(0)
v_rcp_f32_e64 v175, v175
s_nop 0
v_pk_mul_f32 v[56:57], v[56:57], v[152:153]
v_pk_mul_f32 v[58:59], v[58:59], v[154:155]
v_pk_mul_f32 v[60:61], v[60:61], v[156:157]
v_pk_mul_f32 v[62:63], v[62:63], v[158:159]
v_pk_mul_f32 v[64:65], v[64:65], v[160:161]
v_pk_mul_f32 v[66:67], v[66:67], v[162:163]
v_pk_mul_f32 v[68:69], v[68:69], v[164:165]
v_pk_mul_f32 v[70:71], v[70:71], v[166:167]
v_pk_mul_f32 v[72:73], v[72:73], v[168:169]
v_pk_mul_f32 v[74:75], v[74:75], v[170:171]
v_pk_mul_f32 v[76:77], v[76:77], v[172:173]
v_pk_mul_f32 v[78:79], v[78:79], v[174:175]
v_pk_fma_f32 v[218:219], v[56:57], v[56:57], v[218:219]
v_pk_add_f32 v[210:211], v[56:57], v[210:211]
v_pk_fma_f32 v[220:221], v[58:59], v[58:59], v[220:221]
v_pk_add_f32 v[212:213], v[58:59], v[212:213]
v_pk_fma_f32 v[222:223], v[60:61], v[60:61], v[222:223]
v_pk_add_f32 v[214:215], v[60:61], v[214:215]
v_pk_fma_f32 v[224:225], v[62:63], v[62:63], v[224:225]
v_pk_add_f32 v[216:217], v[62:63], v[216:217]
v_pk_fma_f32 v[218:219], v[64:65], v[64:65], v[218:219]
v_pk_add_f32 v[210:211], v[64:65], v[210:211]
v_pk_fma_f32 v[220:221], v[66:67], v[66:67], v[220:221]
v_pk_add_f32 v[212:213], v[66:67], v[212:213]
v_pk_fma_f32 v[222:223], v[68:69], v[68:69], v[222:223]
v_pk_add_f32 v[214:215], v[68:69], v[214:215]
v_pk_fma_f32 v[224:225], v[70:71], v[70:71], v[224:225]
v_pk_add_f32 v[216:217], v[70:71], v[216:217]
v_pk_fma_f32 v[218:219], v[72:73], v[72:73], v[218:219]
v_pk_add_f32 v[210:211], v[72:73], v[210:211]
v_pk_fma_f32 v[220:221], v[74:75], v[74:75], v[220:221]
v_pk_add_f32 v[212:213], v[74:75], v[212:213]
v_pk_fma_f32 v[222:223], v[76:77], v[76:77], v[222:223]
v_pk_add_f32 v[214:215], v[76:77], v[214:215]
v_pk_fma_f32 v[224:225], v[78:79], v[78:79], v[224:225]
v_pk_add_f32 v[216:217], v[78:79], v[216:217]
v_mfma_f32_16x16x32_bf16 v[104:107], a[144:147], a[0:3], 0
s_add_u32 m0, %[sLx], 81920
ds_read_b128 a[180:183], %[vXr0] offset:27648
v_mfma_f32_16x16x32_bf16 v[108:111], a[144:147], a[4:7], 0
s_add_u32 s48, s20, 229376
ds_read_b128 a[184:187], %[vXr0] offset:28672
v_mfma_f32_16x16x32_bf16 v[112:115], a[148:151], a[0:3], 0
buffer_load_dwordx4 %[vL16], %[sWS], s48 offen offset:0 sc1 lds
v_mfma_f32_16x16x32_bf16 v[116:119], a[148:151], a[4:7], 0
ds_read_b128 a[188:191], %[vXr0] offset:29696
v_pk_fma_f32 v[152:153], v[184:185], v[204:205], v[204:205] op_sel:[0,0,1] op_sel_hi:[1,0,1]
v_pk_fma_f32 v[154:155], v[186:187], v[204:205], v[204:205] op_sel:[0,0,1] op_sel_hi:[1,0,1]
v_pk_fma_f32 v[156:157], v[188:189], v[204:205], v[204:205] op_sel:[0,0,1] op_sel_hi:[1,0,1]
v_pk_fma_f32 v[158:159], v[190:191], v[204:205], v[204:205] op_sel:[0,0,1] op_sel_hi:[1,0,1]
v_pk_fma_f32 v[160:161], v[184:185], v[206:207], v[206:207] op_sel:[0,0,1] op_sel_hi:[1,0,1]
v_pk_fma_f32 v[162:163], v[186:187], v[206:207], v[206:207] op_sel:[0,0,1] op_sel_hi:[1,0,1]
v_pk_fma_f32 v[164:165], v[188:189], v[206:207], v[206:207] op_sel:[0,0,1] op_sel_hi:[1,0,1]
v_pk_fma_f32 v[166:167], v[190:191], v[206:207], v[206:207] op_sel:[0,0,1] op_sel_hi:[1,0,1]
v_pk_fma_f32 v[168:169], v[184:185], v[208:209], v[208:209] op_sel:[0,0,1] op_sel_hi:[1,0,1]
v_pk_fma_f32 v[170:171], v[186:187], v[208:209], v[208:209] op_sel:[0,0,1] op_sel_hi:[1,0,1]
v_pk_fma_f32 v[172:173], v[188:189], v[208:209], v[208:209] op_sel:[0,0,1] op_sel_hi:[1,0,1]
v_pk_fma_f32 v[174:175], v[190:191], v[208:209], v[208:209] op_sel:[0,0,1] op_sel_hi:[1,0,1]
v_pk_fma_f32 v[80:81], v[80:81], v[176:177], v[152:153]
v_pk_fma_f32 v[82:83], v[82:83], v[178:179], v[154:155]
v_pk_fma_f32 v[84:85], v[84:85], v[180:181], v[156:157]
v_pk_fma_f32 v[86:87], v[86:87], v[182:183], v[158:159]
v_pk_fma_f32 v[88:89], v[88:89], v[176:177], v[160:161]
v_pk_fma_f32 v[90:91], v[90:91], v[178:179], v[162:163]
v_pk_fma_f32 v[92:93], v[92:93], v[180:181], v[164:165]
v_pk_fma_f32 v[94:95], v[94:95], v[182:183], v[166:167]
v_pk_fma_f32 v[96:97], v[96:97], v[176:177], v[168:169]
v_pk_fma_f32 v[98:99], v[98:99], v[178:179], v[170:171]
v_pk_fma_f32 v[100:101], v[100:101], v[180:181], v[172:173]
v_pk_fma_f32 v[102:103], v[102:103], v[182:183], v[174:175]
v_pk_fma_f32 v[152:153], v[80:81], v[80:81], s[50:51] op_sel_hi:[1,1,0]
v_pk_fma_f32 v[154:155], v[82:83], v[82:83], s[50:51] op_sel_hi:[1,1,0]
v_pk_fma_f32 v[156:157], v[84:85], v[84:85], s[50:51] op_sel_hi:[1,1,0]
v_pk_fma_f32 v[158:159], v[86:87], v[86:87], s[50:51] op_sel_hi:[1,1,0]
v_pk_fma_f32 v[160:161], v[88:89], v[88:89], s[50:51] op_sel_hi:[1,1,0]
v_pk_fma_f32 v[162:163], v[90:91], v[90:91], s[50:51] op_sel_hi:[1,1,0]
v_pk_fma_f32 v[164:165], v[92:93], v[92:93], s[50:51] op_sel_hi:[1,1,0]
v_pk_fma_f32 v[166:167], v[94:95], v[94:95], s[50:51] op_sel_hi:[1,1,0]
v_pk_fma_f32 v[168:169], v[96:97], v[96:97], s[50:51] op_sel_hi:[1,1,0]
v_pk_fma_f32 v[170:171], v[98:99], v[98:99], s[50:51] op_sel_hi:[1,1,0]
v_pk_fma_f32 v[172:173], v[100:101], v[100:101], s[50:51] op_sel_hi:[1,1,0]
v_pk_fma_f32 v[174:175], v[102:103], v[102:103], s[50:51] op_sel_hi:[1,1,0]
v_pk_mul_f32 v[152:153], v[152:153], v[80:81]
v_pk_mul_f32 v[154:155], v[154:155], v[82:83]
v_pk_mul_f32 v[156:157], v[156:157], v[84:85]
v_pk_mul_f32 v[158:159], v[158:159], v[86:87]
v_pk_mul_f32 v[160:161], v[160:161], v[88:89]
v_pk_mul_f32 v[162:163], v[162:163], v[90:91]
v_pk_mul_f32 v[164:165], v[164:165], v[92:93]
)"
R"(v_pk_mul_f32 v[166:167], v[166:167], v[94:95]
v_pk_mul_f32 v[168:169], v[168:169], v[96:97]
v_pk_mul_f32 v[170:171], v[170:171], v[98:99]
v_pk_mul_f32 v[172:173], v[172:173], v[100:101]
v_pk_mul_f32 v[174:175], v[174:175], v[102:103]
v_exp_f32 v152, v152
v_exp_f32 v153, v153
v_add_f32_e64 v152, 1.0, v152
v_mfma_f32_16x16x32_bf16 v[120:123], a[152:155], a[0:3], 0
ds_read_b128 a[192:195], %[vXr0] offset:30720
v_exp_f32 v154, v154
v_add_f32 v153, 1.0, v153
v_rcp_f32_e64 v152, v152
v_mfma_f32_16x16x32_bf16 v[124:127], a[152:155], a[4:7], 0
v_exp_f32 v155, v155
v_add_f32 v154, 1.0, v154
v_rcp_f32_e64 v153, v153
v_mfma_f32_16x16x32_bf16 v[128:131], a[156:159], a[0:3], 0
ds_read_b128 a[196:199], %[vXr0] offset:31744
v_exp_f32 v156, v156
v_add_f32 v155, 1.0, v155
v_rcp_f32_e64 v154, v154
v_mfma_f32_16x16x32_bf16 v[132:135], a[156:159], a[4:7], 0
ds_read_b128 a[200:203], %[vXr0] offset:32768
v_exp_f32 v157, v157
v_add_f32 v156, 1.0, v156
v_rcp_f32_e64 v155, v155
v_mfma_f32_16x16x32_bf16 v[104:107], a[160:163], a[8:11], v[104:107]
v_exp_f32 v158, v158
v_add_f32 v157, 1.0, v157
v_rcp_f32 v156, v156
v_exp_f32 v159, v159
v_mfma_f32_16x16x32_bf16 v[108:111], a[160:163], a[12:15], v[108:111]
s_add_u32 m0, %[sLx], 110592
ds_read_b128 a[204:207], %[vXr0] offset:33792
v_add_f32 v158, 1.0, v158
v_rcp_f32 v157, v157
v_exp_f32_e64 v160, v160
v_mfma_f32_16x16x32_bf16 v[112:115], a[164:167], a[8:11], v[112:115]
s_add_u32 s48, s20, 258048
ds_read_b128 a[208:211], %[vXr0] offset:34816
v_add_f32 v159, 1.0, v159
v_rcp_f32 v158, v158
v_mfma_f32_16x16x32_bf16 v[116:119], a[164:167], a[12:15], v[116:119]
buffer_load_dwordx4 %[vL16], %[sWS], s48 offen offset:0 sc1 lds
v_exp_f32 v161, v161
v_add_f32 v160, 1.0, v160
v_rcp_f32_e64 v159, v159
v_mfma_f32_16x16x32_bf16 v[120:123], a[168:171], a[8:11], v[120:123]
ds_read_b128 a[212:215], %[vXr0] offset:35840
v_exp_f32 v162, v162
v_add_f32 v161, 1.0, v161
v_rcp_f32_e64 v160, v160
v_mfma_f32_16x16x32_bf16 v[124:127], a[168:171], a[12:15], v[124:127]
v_exp_f32 v163, v163
v_add_f32 v162, 1.0, v162
v_rcp_f32 v161, v161
v_exp_f32 v164, v164
v_mfma_f32_16x16x32_bf16 v[128:131], a[172:175], a[8:11], v[128:131]
v_add_f32 v163, 1.0, v163
v_rcp_f32 v162, v162
v_exp_f32 v165, v165
v_add_f32 v164, 1.0, v164
v_mfma_f32_16x16x32_bf16 v[132:135], a[172:175], a[12:15], v[132:135]
buffer_load_dwordx4 %[vL16], %[sWS], s48 offen offset:1024 sc1 lds
v_rcp_f32 v163, v163
v_exp_f32 v166, v166
v_add_f32_e64 v165, 1.0, v165
v_mfma_f32_16x16x32_bf16 v[104:107], a[176:179], a[16:19], v[104:107]
v_rcp_f32 v164, v164
v_exp_f32 v167, v167
v_add_f32_e64 v166, 1.0, v166
v_mfma_f32_16x16x32_bf16 v[108:111], a[176:179], a[20:23], v[108:111]
s_waitcnt vmcnt(11) lgkmcnt(0)
s_barrier
v_rcp_f32 v165, v165
v_exp_f32 v168, v168
v_add_f32 v167, 1.0, v167
v_rcp_f32 v166, v166
v_mfma_f32_16x16x32_bf16 v[112:115], a[180:183], a[16:19], v[112:115]
ds_read_b128 a[144:147], %[vXr1] offset:0
v_exp_f32 v169, v169
v_add_f32 v168, 1.0, v168
v_mfma_f32_16x16x32_bf16 v[116:119], a[180:183], a[20:23], v[116:119]
ds_read_b128 a[148:151], %[vXr1] offset:1024
v_rcp_f32 v167, v167
v_exp_f32 v170, v170
v_add_f32_e64 v169, 1.0, v169
v_mfma_f32_16x16x32_bf16 v[120:123], a[184:187], a[16:19], v[120:123]
buffer_load_dwordx4 %[vL16], %[sWS], s48 offen offset:2048 sc1 lds
v_rcp_f32 v168, v168
v_exp_f32 v171, v171
v_mfma_f32_16x16x32_bf16 v[124:127], a[184:187], a[20:23], v[124:127]
ds_read_b128 a[152:155], %[vXr1] offset:2048
v_add_f32 v170, 1.0, v170
v_rcp_f32 v169, v169
v_mfma_f32_16x16x32_bf16 v[128:131], a[188:191], a[16:19], v[128:131]
ds_read_b128 a[156:159], %[vXr1] offset:3072
v_exp_f32 v172, v172
v_add_f32 v171, 1.0, v171
v_rcp_f32_e64 v170, v170
v_mfma_f32_16x16x32_bf16 v[132:135], a[188:191], a[20:23], v[132:135]
v_exp_f32 v173, v173
v_add_f32 v172, 1.0, v172
v_rcp_f32_e64 v171, v171
v_mfma_f32_16x16x32_bf16 v[104:107], a[192:195], a[24:27], v[104:107]
ds_read_b128 a[160:163], %[vXr1] offset:4096
v_exp_f32 v174, v174
v_add_f32 v173, 1.0, v173
v_mfma_f32_16x16x32_bf16 v[108:111], a[192:195], a[28:31], v[108:111]
ds_read_b128 a[164:167], %[vXr1] offset:5120
v_rcp_f32 v172, v172
v_exp_f32 v175, v175
v_add_f32_e64 v174, 1.0, v174
v_mfma_f32_16x16x32_bf16 v[112:115], a[196:199], a[24:27], v[112:115]
v_rcp_f32 v173, v173
v_add_f32 v175, 1.0, v175
v_rcp_f32_e64 v174, v174
v_mfma_f32_16x16x32_bf16 v[116:119], a[196:199], a[28:31], v[116:119]
ds_read_b128 a[168:171], %[vXr1] offset:6144
v_rcp_f32 v175, v175
s_nop 0
v_pk_mul_f32 v[80:81], v[80:81], v[152:153]
v_pk_mul_f32 v[82:83], v[82:83], v[154:155]
v_pk_mul_f32 v[84:85], v[84:85], v[156:157]
v_pk_mul_f32 v[86:87], v[86:87], v[158:159]
v_pk_mul_f32 v[88:89], v[88:89], v[160:161]
v_pk_mul_f32 v[90:91], v[90:91], v[162:163]
v_pk_mul_f32 v[92:93], v[92:93], v[164:165]
v_pk_mul_f32 v[94:95], v[94:95], v[166:167]
v_pk_mul_f32 v[96:97], v[96:97], v[168:169]
v_pk_mul_f32 v[98:99], v[98:99], v[170:171]
v_pk_mul_f32 v[100:101], v[100:101], v[172:173]
v_pk_mul_f32 v[102:103], v[102:103], v[174:175]
v_pk_fma_f32 v[218:219], v[80:81], v[80:81], v[218:219]
v_pk_add_f32 v[210:211], v[80:81], v[210:211]
v_pk_fma_f32 v[220:221], v[82:83], v[82:83], v[220:221]
v_pk_add_f32 v[212:213], v[82:83], v[212:213]
v_pk_fma_f32 v[222:223], v[84:85], v[84:85], v[222:223]
v_pk_add_f32 v[214:215], v[84:85], v[214:215]
v_pk_fma_f32 v[224:225], v[86:87], v[86:87], v[224:225]
v_pk_add_f32 v[216:217], v[86:87], v[216:217]
v_pk_fma_f32 v[218:219], v[88:89], v[88:89], v[218:219]
v_pk_add_f32 v[210:211], v[88:89], v[210:211]
v_pk_fma_f32 v[220:221], v[90:91], v[90:91], v[220:221]
v_pk_add_f32 v[212:213], v[90:91], v[212:213]
v_pk_fma_f32 v[222:223], v[92:93], v[92:93], v[222:223]
v_pk_add_f32 v[214:215], v[92:93], v[214:215]
v_pk_fma_f32 v[224:225], v[94:95], v[94:95], v[224:225]
v_pk_add_f32 v[216:217], v[94:95], v[216:217]
v_pk_fma_f32 v[218:219], v[96:97], v[96:97], v[218:219]
v_pk_add_f32 v[210:211], v[96:97], v[210:211]
v_pk_fma_f32 v[220:221], v[98:99], v[98:99], v[220:221]
v_pk_add_f32 v[212:213], v[98:99], v[212:213]
v_pk_fma_f32 v[222:223], v[100:101], v[100:101], v[222:223]
v_pk_add_f32 v[214:215], v[100:101], v[214:215]
v_pk_fma_f32 v[224:225], v[102:103], v[102:103], v[224:225]
v_pk_add_f32 v[216:217], v[102:103], v[216:217]
s_nop 0
v_mfma_f32_16x16x32_bf16 v[120:123], a[200:203], a[24:27], v[120:123]
ds_read_b128 a[172:175], %[vXr1] offset:7168
v_add_f32_dpp v210, v210, v210 row_ror:8 row_mask:0xf bank_mask:0x3
v_add_f32_dpp v218, v218, v218 row_ror:8 row_mask:0xf bank_mask:0x3
v_add_f32_dpp v211, v211, v211 row_ror:8 row_mask:0xf bank_mask:0x3
v_add_f32_dpp v219, v219, v219 row_ror:8 row_mask:0xf bank_mask:0x3
v_mfma_f32_16x16x32_bf16 v[124:127], a[200:203], a[28:31], v[124:127]
v_add_f32_dpp v212, v212, v212 row_ror:8 row_mask:0xf bank_mask:0x3
v_add_f32_dpp v220, v220, v220 row_ror:8 row_mask:0xf bank_mask:0x3
v_add_f32_dpp v213, v213, v213 row_ror:8 row_mask:0xf bank_mask:0x3
v_add_f32_dpp v221, v221, v221 row_ror:8 row_mask:0xf bank_mask:0x3
v_add_f32_dpp v210, v214, v214 row_ror:8 row_mask:0xf bank_mask:0xc
v_mfma_f32_16x16x32_bf16 v[128:131], a[204:207], a[24:27], v[128:131]
ds_read_b128 a[176:179], %[vXr1] offset:8192
v_add_f32_dpp v218, v222, v222 row_ror:8 row_mask:0xf bank_mask:0xc
v_add_f32_dpp v211, v215, v215 row_ror:8 row_mask:0xf bank_mask:0xc
v_add_f32_dpp v219, v223, v223 row_ror:8 row_mask:0xf bank_mask:0xc
v_add_f32_dpp v212, v216, v216 row_ror:8 row_mask:0xf bank_mask:0xc
v_mfma_f32_16x16x32_bf16 v[132:135], a[204:207], a[28:31], v[132:135]
buffer_load_dwordx4 %[vL16], %[sWS], s48 offen offset:3072 sc1 lds
v_add_f32_dpp v220, v224, v224 row_ror:8 row_mask:0xf bank_mask:0xc
v_add_f32_dpp v213, v217, v217 row_ror:8 row_mask:0xf bank_mask:0xc
v_add_f32_dpp v221, v225, v225 row_ror:8 row_mask:0xf bank_mask:0xc
v_add_f32_dpp v152, v210, v210 row_ror:12 row_mask:0xf bank_mask:0x5
v_mfma_f32_16x16x32_bf16 v[104:107], a[208:211], a[32:35], v[104:107]
v_add_f32_dpp v153, v218, v218 row_ror:12 row_mask:0xf bank_mask:0x5
v_add_f32_dpp v154, v211, v211 row_ror:12 row_mask:0xf bank_mask:0x5
v_add_f32_dpp v155, v219, v219 row_ror:12 row_mask:0xf bank_mask:0x5
v_add_f32_dpp v152, v212, v212 row_ror:4 row_mask:0xf bank_mask:0xa
v_add_f32_dpp v153, v220, v220 row_ror:4 row_mask:0xf bank_mask:0xa
v_mfma_f32_16x16x32_bf16 v[108:111], a[208:211], a[36:39], v[108:111]
v_add_f32_dpp v154, v213, v213 row_ror:4 row_mask:0xf bank_mask:0xa
v_add_f32_dpp v155, v221, v221 row_ror:4 row_mask:0xf bank_mask:0xa
v_add_f32_dpp v152, v152, v152 quad_perm:[1,0,3,2] row_mask:0xf bank_mask:0xf
v_add_f32_dpp v153, v153, v153 quad_perm:[1,0,3,2] row_mask:0xf bank_mask:0xf
v_add_f32_dpp v154, v154, v154 quad_perm:[1,0,3,2] row_mask:0xf bank_mask:0xf
v_mfma_f32_16x16x32_bf16 v[112:115], a[212:215], a[32:35], v[112:115]
v_add_f32_dpp v155, v155, v155 quad_perm:[1,0,3,2] row_mask:0xf bank_mask:0xf
v_add_f32_dpp v152, v152, v152 quad_perm:[2,3,0,1] row_mask:0xf bank_mask:0xf
v_add_f32_dpp v153, v153, v153 quad_perm:[2,3,0,1] row_mask:0xf bank_mask:0xf
v_add_f32_dpp v154, v154, v154 quad_perm:[2,3,0,1] row_mask:0xf bank_mask:0xf
)"
R"(v_add_f32_dpp v155, v155, v155 quad_perm:[2,3,0,1] row_mask:0xf bank_mask:0xf
v_mfma_f32_16x16x32_bf16 v[116:119], a[212:215], a[36:39], v[116:119]
s_waitcnt lgkmcnt(0)
v_or_b32 v153, 2147483648, v153
v_or_b32 v155, 2147483648, v155
buffer_store_dwordx4 v[152:155], %[vEw4], %[sWS], 0 offen offset:0 sc1
v_mfma_f32_16x16x32_bf16 v[120:123], a[144:147], a[32:35], v[120:123]
ds_read_b128 a[180:183], %[vXr1] offset:9216
v_mfma_f32_16x16x32_bf16 v[124:127], a[144:147], a[36:39], v[124:127]
ds_read_b128 a[184:187], %[vXr1] offset:10240
v_mfma_f32_16x16x32_bf16 v[128:131], a[148:151], a[32:35], v[128:131]
buffer_load_dwordx4 v[192:195], %[vA1], %[sWS], 0 offen offset:0 sc1
v_mfma_f32_16x16x32_bf16 v[132:135], a[148:151], a[36:39], v[132:135]
ds_read_b128 a[188:191], %[vXr1] offset:11264
v_mfma_f32_16x16x32_bf16 v[104:107], a[152:155], a[40:43], v[104:107]
ds_read_b128 a[192:195], %[vXr1] offset:12288
v_mfma_f32_16x16x32_bf16 v[108:111], a[152:155], a[44:47], v[108:111]
buffer_load_dwordx4 v[196:199], %[vA1], %[sWS], 0 offen offset:16 sc1
v_mfma_f32_16x16x32_bf16 v[112:115], a[156:159], a[40:43], v[112:115]
ds_read_b128 a[196:199], %[vXr1] offset:13312
v_mfma_f32_16x16x32_bf16 v[116:119], a[156:159], a[44:47], v[116:119]
ds_read_b128 a[200:203], %[vXr1] offset:14336
v_mfma_f32_16x16x32_bf16 v[120:123], a[160:163], a[40:43], v[120:123]
v_mfma_f32_16x16x32_bf16 v[124:127], a[160:163], a[44:47], v[124:127]
ds_read_b128 a[204:207], %[vXr1] offset:15360
v_mfma_f32_16x16x32_bf16 v[128:131], a[164:167], a[40:43], v[128:131]
ds_read_b128 a[208:211], %[vXr1] offset:16384
v_mfma_f32_16x16x32_bf16 v[132:135], a[164:167], a[44:47], v[132:135]
buffer_load_dwordx4 v[200:203], %[vA1], %[sWS], 0 offen offset:128 sc1
v_mfma_f32_16x16x32_bf16 v[104:107], a[168:171], a[48:51], v[104:107]
ds_read_b128 a[212:215], %[vXr1] offset:17408
v_mfma_f32_16x16x32_bf16 v[108:111], a[168:171], a[52:55], v[108:111]
buffer_load_dwordx4 v[204:207], %[vA1], %[sWS], 0 offen offset:144 sc1
v_mfma_f32_16x16x32_bf16 v[112:115], a[172:175], a[48:51], v[112:115]
s_add_u32 m0, %[sLx], 114688
v_mfma_f32_16x16x32_bf16 v[116:119], a[172:175], a[52:55], v[116:119]
s_add_u32 s48, s20, 262144
buffer_load_dwordx4 %[vL16], %[sWS], s48 offen offset:0 sc1 lds
v_mfma_f32_16x16x32_bf16 v[120:123], a[176:179], a[48:51], v[120:123]
buffer_load_dwordx4 %[vL16], %[sWS], s48 offen offset:1024 sc1 lds
v_mfma_f32_16x16x32_bf16 v[124:127], a[176:179], a[52:55], v[124:127]
s_waitcnt lgkmcnt(0)
v_mfma_f32_16x16x32_bf16 v[128:131], a[180:183], a[48:51], v[128:131]
ds_read_b128 a[144:147], %[vXr1] offset:18432
v_mfma_f32_16x16x32_bf16 v[132:135], a[180:183], a[52:55], v[132:135]
ds_read_b128 a[148:151], %[vXr1] offset:19456
v_mfma_f32_16x16x32_bf16 v[104:107], a[184:187], a[56:59], v[104:107]
buffer_load_dwordx4 v[208:211], %[vA1], %[sWS], 0 offen offset:256 sc1
v_mfma_f32_16x16x32_bf16 v[108:111], a[184:187], a[60:63], v[108:111]
ds_read_b128 a[152:155], %[vXr1] offset:20480
v_mfma_f32_16x16x32_bf16 v[112:115], a[188:191], a[56:59], v[112:115]
ds_read_b128 a[156:159], %[vXr1] offset:21504
v_mfma_f32_16x16x32_bf16 v[116:119], a[188:191], a[60:63], v[116:119]
buffer_load_dwordx4 v[212:215], %[vA1], %[sWS], 0 offen offset:272 sc1
v_mfma_f32_16x16x32_bf16 v[120:123], a[192:195], a[56:59], v[120:123]
ds_read_b128 a[160:163], %[vXr1] offset:22528
v_mfma_f32_16x16x32_bf16 v[124:127], a[192:195], a[60:63], v[124:127]
ds_read_b128 a[164:167], %[vXr1] offset:23552
v_mfma_f32_16x16x32_bf16 v[128:131], a[196:199], a[56:59], v[128:131]
v_mfma_f32_16x16x32_bf16 v[132:135], a[196:199], a[60:63], v[132:135]
ds_read_b128 a[168:171], %[vXr1] offset:24576
v_mfma_f32_16x16x32_bf16 v[104:107], a[200:203], a[64:67], v[104:107]
ds_read_b128 a[172:175], %[vXr1] offset:25600
v_mfma_f32_16x16x32_bf16 v[108:111], a[200:203], a[68:71], v[108:111]
buffer_load_dwordx4 v[216:219], %[vA1], %[sWS], 0 offen offset:384 sc1
v_mfma_f32_16x16x32_bf16 v[112:115], a[204:207], a[64:67], v[112:115]
ds_read_b128 a[176:179], %[vXr1] offset:26624
v_mfma_f32_16x16x32_bf16 v[116:119], a[204:207], a[68:71], v[116:119]
buffer_load_dwordx4 v[220:223], %[vA1], %[sWS], 0 offen offset:400 sc1
v_mfma_f32_16x16x32_bf16 v[120:123], a[208:211], a[64:67], v[120:123]
v_mfma_f32_16x16x32_bf16 v[124:127], a[208:211], a[68:71], v[124:127]
buffer_load_dwordx4 %[vL16], %[sWS], s48 offen offset:2048 sc1 lds
v_mfma_f32_16x16x32_bf16 v[128:131], a[212:215], a[64:67], v[128:131]
buffer_load_dwordx4 %[vL16], %[sWS], s48 offen offset:3072 sc1 lds
v_mfma_f32_16x16x32_bf16 v[132:135], a[212:215], a[68:71], v[132:135]
s_waitcnt lgkmcnt(0)
v_mfma_f32_16x16x32_bf16 v[104:107], a[144:147], a[72:75], v[104:107]
ds_read_b128 a[180:183], %[vXr1] offset:27648
v_mfma_f32_16x16x32_bf16 v[108:111], a[144:147], a[76:79], v[108:111]
ds_read_b128 a[184:187], %[vXr1] offset:28672
v_mfma_f32_16x16x32_bf16 v[112:115], a[148:151], a[72:75], v[112:115]
buffer_load_dwordx2 v[232:233], %[vEr], %[sWS], 0 offen offset:0 sc1
v_mfma_f32_16x16x32_bf16 v[116:119], a[148:151], a[76:79], v[116:119]
ds_read_b128 a[188:191], %[vXr1] offset:29696
v_mfma_f32_16x16x32_bf16 v[120:123], a[152:155], a[72:75], v[120:123]
ds_read_b128 a[192:195], %[vXr1] offset:30720
v_mfma_f32_16x16x32_bf16 v[124:127], a[152:155], a[76:79], v[124:127]
buffer_load_dwordx2 v[234:235], %[vEr], %[sWS], 0 offen offset:1024 sc1
v_mfma_f32_16x16x32_bf16 v[128:131], a[156:159], a[72:75], v[128:131]
ds_read_b128 a[196:199], %[vXr1] offset:31744
v_mfma_f32_16x16x32_bf16 v[132:135], a[156:159], a[76:79], v[132:135]
ds_read_b128 a[200:203], %[vXr1] offset:32768
v_mfma_f32_16x16x32_bf16 v[104:107], a[160:163], a[80:83], v[104:107]
buffer_load_dwordx2 v[236:237], %[vEr], %[sWS], 0 offen offset:2048 sc1
v_mfma_f32_16x16x32_bf16 v[108:111], a[160:163], a[84:87], v[108:111]
ds_read_b128 a[204:207], %[vXr1] offset:33792
v_mfma_f32_16x16x32_bf16 v[112:115], a[164:167], a[80:83], v[112:115]
ds_read_b128 a[208:211], %[vXr1] offset:34816
v_mfma_f32_16x16x32_bf16 v[116:119], a[164:167], a[84:87], v[116:119]
buffer_load_dwordx2 v[238:239], %[vEr], %[sWS], 0 offen offset:3072 sc1
v_mfma_f32_16x16x32_bf16 v[120:123], a[168:171], a[80:83], v[120:123]
ds_read_b128 a[212:215], %[vXr1] offset:35840
v_mfma_f32_16x16x32_bf16 v[124:127], a[168:171], a[84:87], v[124:127]
buffer_load_dwordx2 v[240:241], %[vEr], %[sWS], s53 offen offset:0 sc1
v_mfma_f32_16x16x32_bf16 v[128:131], a[172:175], a[80:83], v[128:131]
buffer_load_dwordx2 v[242:243], %[vEr], %[sWS], s53 offen offset:1024 sc1
v_mfma_f32_16x16x32_bf16 v[132:135], a[172:175], a[84:87], v[132:135]
buffer_load_dwordx2 v[244:245], %[vEr], %[sWS], s53 offen offset:2048 sc1
v_mfma_f32_16x16x32_bf16 v[104:107], a[176:179], a[88:91], v[104:107]
buffer_load_dwordx2 v[246:247], %[vEr], %[sWS], s53 offen offset:3072 sc1
v_mfma_f32_16x16x32_bf16 v[108:111], a[176:179], a[92:95], v[108:111]
s_waitcnt vmcnt(25) lgkmcnt(0)
s_barrier
s_add_u32 m0, %[sLx], 118784
s_add_u32 s48, s20, 266240
buffer_load_dwordx4 %[vL16], %[sWS], s48 offen offset:0 sc1 lds
v_mfma_f32_16x16x32_bf16 v[112:115], a[180:183], a[88:91], v[112:115]
s_add_u32 m0, %[sLx], 0
ds_read_b128 a[144:147], %[vXr2] offset:0
v_mfma_f32_16x16x32_bf16 v[116:119], a[180:183], a[92:95], v[116:119]
s_add_u32 s48, s20, 294912
ds_read_b128 a[148:151], %[vXr2] offset:1024
v_mfma_f32_16x16x32_bf16 v[120:123], a[184:187], a[88:91], v[120:123]
LPb_3:
s_cbranch_execz LPb_7
buffer_load_dwordx4 %[vL16], %[sWS], s48 offen offset:0 sc1 lds
v_mfma_f32_16x16x32_bf16 v[124:127], a[184:187], a[92:95], v[124:127]
ds_read_b128 a[152:155], %[vXr2] offset:2048
v_mfma_f32_16x16x32_bf16 v[128:131], a[188:191], a[88:91], v[128:131]
ds_read_b128 a[156:159], %[vXr2] offset:3072
v_mfma_f32_16x16x32_bf16 v[132:135], a[188:191], a[92:95], v[132:135]
v_mfma_f32_16x16x32_bf16 v[104:107], a[192:195], a[96:99], v[104:107]
ds_read_b128 a[160:163], %[vXr2] offset:4096
v_mfma_f32_16x16x32_bf16 v[108:111], a[192:195], a[100:103], v[108:111]
ds_read_b128 a[164:167], %[vXr2] offset:5120
v_mfma_f32_16x16x32_bf16 v[112:115], a[196:199], a[96:99], v[112:115]
buffer_load_dwordx4 %[vL16], %[sWS], s48 offen offset:1024 sc1 lds
v_mfma_f32_16x16x32_bf16 v[116:119], a[196:199], a[100:103], v[116:119]
ds_read_b128 a[168:171], %[vXr2] offset:6144
v_mfma_f32_16x16x32_bf16 v[120:123], a[200:203], a[96:99], v[120:123]
ds_read_b128 a[172:175], %[vXr2] offset:7168
v_mfma_f32_16x16x32_bf16 v[124:127], a[200:203], a[100:103], v[124:127]
v_mfma_f32_16x16x32_bf16 v[128:131], a[204:207], a[96:99], v[128:131]
ds_read_b128 a[176:179], %[vXr2] offset:8192
v_mfma_f32_16x16x32_bf16 v[132:135], a[204:207], a[100:103], v[132:135]
buffer_load_dwordx4 %[vL16], %[sWS], s48 offen offset:2048 sc1 lds
v_mfma_f32_16x16x32_bf16 v[104:107], a[208:211], a[104:107], v[104:107]
v_mfma_f32_16x16x32_bf16 v[108:111], a[208:211], a[108:111], v[108:111]
buffer_load_dwordx4 %[vL16], %[sWS], s48 offen offset:3072 sc1 lds
v_mfma_f32_16x16x32_bf16 v[112:115], a[212:215], a[104:107], v[112:115]
v_mfma_f32_16x16x32_bf16 v[116:119], a[212:215], a[108:111], v[116:119]
s_waitcnt lgkmcnt(0)
v_mfma_f32_16x16x32_bf16 v[120:123], a[144:147], a[104:107], v[120:123]
s_add_u32 m0, %[sLx], 4096
ds_read_b128 a[180:183], %[vXr2] offset:9216
v_mfma_f32_16x16x32_bf16 v[124:127], a[144:147], a[108:111], v[124:127]
s_add_u32 s48, s20, 299008
ds_read_b128 a[184:187], %[vXr2] offset:10240
v_mfma_f32_16x16x32_bf16 v[128:131], a[148:151], a[104:107], v[128:131]
buffer_load_dwordx4 %[vL16], %[sWS], s48 offen offset:0 sc1 lds
v_mfma_f32_16x16x32_bf16 v[132:135], a[148:151], a[108:111], v[132:135]
ds_read_b128 a[188:191], %[vXr2] offset:11264
v_mfma_f32_16x16x32_bf16 v[104:107], a[152:155], a[112:115], v[104:107]
ds_read_b128 a[192:195], %[vXr2] offset:12288
v_mfma_f32_16x16x32_bf16 v[108:111], a[152:155], a[116:119], v[108:111]
v_mfma_f32_16x16x32_bf16 v[112:115], a[156:159], a[112:115], v[112:115]
ds_read_b128 a[196:199], %[vXr2] offset:13312
v_mfma_f32_16x16x32_bf16 v[116:119], a[156:159], a[116:119], v[116:119]
ds_read_b128 a[200:203], %[vXr2] offset:14336
v_mfma_f32_16x16x32_bf16 v[120:123], a[160:163], a[112:115], v[120:123]
v_mfma_f32_16x16x32_bf16 v[124:127], a[160:163], a[116:119], v[124:127]
ds_read_b128 a[204:207], %[vXr2] offset:15360
v_mfma_f32_16x16x32_bf16 v[128:131], a[164:167], a[112:115], v[128:131]
ds_read_b128 a[208:211], %[vXr2] offset:16384
v_mfma_f32_16x16x32_bf16 v[132:135], a[164:167], a[116:119], v[132:135]
v_mfma_f32_16x16x32_bf16 v[104:107], a[168:171], a[120:123], v[104:107]
ds_read_b128 a[212:215], %[vXr2] offset:17408
v_mfma_f32_16x16x32_bf16 v[108:111], a[168:171], a[124:127], v[108:111]
buffer_load_dwordx4 %[vL16], %[sWS], s48 offen offset:1024 sc1 lds
v_mfma_f32_16x16x32_bf16 v[112:115], a[172:175], a[120:123], v[112:115]
v_mfma_f32_16x16x32_bf16 v[116:119], a[172:175], a[124:127], v[116:119]
v_mfma_f32_16x16x32_bf16 v[120:123], a[176:179], a[120:123], v[120:123]
v_mfma_f32_16x16x32_bf16 v[124:127], a[176:179], a[124:127], v[124:127]
s_waitcnt vmcnt(7) lgkmcnt(0)
v_mfma_f32_16x16x32_bf16 v[128:131], a[180:183], a[120:123], v[128:131]
ds_read_b128 a[144:147], %[vXr2] offset:18432
v_mfma_f32_16x16x32_bf16 v[132:135], a[180:183], a[124:127], v[132:135]
ds_read_b128 a[148:151], %[vXr2] offset:19456
v_mfma_f32_16x16x32_bf16 v[104:107], a[184:187], a[128:131], v[104:107]
buffer_load_dwordx4 %[vL16], %[sWS], s48 offen offset:2048 sc1 lds
v_mfma_f32_16x16x32_bf16 v[108:111], a[184:187], a[132:135], v[108:111]
ds_read_b128 a[152:155], %[vXr2] offset:20480
v_and_b32 v248, v233, v235
v_and_b32 v249, v237, v239
v_and_b32 v250, v241, v243
v_and_b32 v251, v245, v247
v_mfma_f32_16x16x32_bf16 v[112:115], a[188:191], a[128:131], v[112:115]
ds_read_b128 a[156:159], %[vXr2] offset:21504
v_and_b32 v248, v248, v249
v_and_b32 v250, v250, v251
v_and_b32_e64 v248, v248, v250
)"
R"(v_mfma_f32_16x16x32_bf16 v[116:119], a[188:191], a[132:135], v[116:119]
v_cmp_lt_i32 vcc, -1, v248
s_cbranch_vccnz L_sl5_%=
L_ok5_%=:
v_and_b32 v233, 2147483647, v233
v_and_b32 v235, 2147483647, v235
v_and_b32 v237, 2147483647, v237
v_mfma_f32_16x16x32_bf16 v[120:123], a[192:195], a[128:131], v[120:123]
ds_read_b128 a[160:163], %[vXr2] offset:22528
v_and_b32 v239, 2147483647, v239
v_and_b32 v241, 2147483647, v241
v_and_b32 v243, 2147483647, v243
v_mfma_f32_16x16x32_bf16 v[124:127], a[192:195], a[132:135], v[124:127]
ds_read_b128 a[164:167], %[vXr2] offset:23552
v_and_b32 v245, 2147483647, v245
v_and_b32 v247, 2147483647, v247
v_add_f32 v232, v232, v240
v_add_f32 v233, v233, v241
v_mfma_f32_16x16x32_bf16 v[128:131], a[196:199], a[128:131], v[128:131]
v_add_f32 v234, v234, v242
v_add_f32 v235, v235, v243
v_add_f32 v236, v236, v244
v_add_f32 v237, v237, v245
v_add_f32_e64 v238, v238, v246
v_mfma_f32_16x16x32_bf16 v[132:135], a[196:199], a[132:135], v[132:135]
ds_read_b128 a[168:171], %[vXr2] offset:24576
v_add_f32 v239, v239, v247
v_add_f32 v232, v232, v236
v_add_f32_e64 v233, v233, v237
v_mfma_f32_16x16x32_bf16 v[104:107], a[200:203], a[136:139], v[104:107]
ds_read_b128 a[172:175], %[vXr2] offset:25600
v_add_f32 v234, v234, v238
v_add_f32 v235, v235, v239
v_add_f32 v232, v232, v234
v_add_f32 v233, v233, v235
v_mfma_f32_16x16x32_bf16 v[108:111], a[200:203], a[140:143], v[108:111]
v_mov_b32 v234, v232
v_mov_b32 v235, v233
s_nop 1
v_permlane32_swap_b32 v232, v234
v_mfma_f32_16x16x32_bf16 v[112:115], a[204:207], a[136:139], v[112:115]
ds_read_b128 a[176:179], %[vXr2] offset:26624
v_permlane32_swap_b32 v233, v235
s_nop 0
v_add_f32 v232, v232, v234
v_add_f32 v233, v233, v235
v_mfma_f32_16x16x32_bf16 v[116:119], a[204:207], a[140:143], v[116:119]
buffer_load_dwordx4 %[vL16], %[sWS], s48 offen offset:3072 sc1 lds
v_mul_f32 v248, 0x39e38e39, v232
v_mul_f32_e64 v250, v248, v248
v_mul_f32 v249, 0x39e38e39, v233
v_sub_f32_e64 v249, v249, v250
v_mfma_f32_16x16x32_bf16 v[120:123], a[208:211], a[136:139], v[120:123]
v_max_f32_e64 v249, 0, v249
v_add_f32 v249, 0x361367a4, v249
v_mul_f32 v249, 0x3e60ec05, v249
v_mfma_f32_16x16x32_bf16 v[124:127], a[208:211], a[140:143], v[124:127]
v_rsq_f32 v249, v249
s_nop 0
v_mul_f32_e64 v250, v248, v249
v_sub_f32 v250, 0, v250
v_mfma_f32_16x16x32_bf16 v[128:131], a[212:215], a[136:139], v[128:131]
ds_write_b32 %[vTw], v249 offset:0
v_mfma_f32_16x16x32_bf16 v[132:135], a[212:215], a[140:143], v[132:135]
s_waitcnt lgkmcnt(1)
ds_write_b32 %[vTw], v250 offset:128
ds_read_b128 v[176:179], %[vTr] offset:0
ds_read_b128 v[180:183], %[vTr] offset:16
ds_read_b128 v[184:187], %[vTr] offset:128
ds_read_b128 v[188:191], %[vTr] offset:144
v_mfma_f32_16x16x32_bf16 v[136:139], a[144:147], a[0:3], 0
s_add_u32 m0, %[sLx], 8192
ds_read_b128 a[180:183], %[vXr2] offset:27648
v_mfma_f32_16x16x32_bf16 v[140:143], a[144:147], a[4:7], 0
s_add_u32 s48, s20, 303104
ds_read_b128 a[184:187], %[vXr2] offset:28672
v_mfma_f32_16x16x32_bf16 v[144:147], a[148:151], a[0:3], 0
buffer_load_dwordx4 %[vL16], %[sWS], s48 offen offset:0 sc1 lds
v_mfma_f32_16x16x32_bf16 v[148:151], a[148:151], a[4:7], 0
ds_read_b128 a[188:191], %[vXr2] offset:29696
s_waitcnt lgkmcnt(3)
v_pk_fma_f32 v[32:33], v[32:33], v[176:177], v[184:185]
v_pk_fma_f32 v[34:35], v[34:35], v[178:179], v[186:187]
v_pk_fma_f32 v[36:37], v[36:37], v[180:181], v[188:189]
v_pk_fma_f32 v[38:39], v[38:39], v[182:183], v[190:191]
v_pk_fma_f32 v[40:41], v[40:41], v[176:177], v[184:185]
v_pk_fma_f32 v[42:43], v[42:43], v[178:179], v[186:187]
v_pk_fma_f32 v[44:45], v[44:45], v[180:181], v[188:189]
v_pk_fma_f32 v[46:47], v[46:47], v[182:183], v[190:191]
v_pk_fma_f32 v[48:49], v[48:49], v[176:177], v[184:185]
v_pk_fma_f32 v[50:51], v[50:51], v[178:179], v[186:187]
v_pk_fma_f32 v[52:53], v[52:53], v[180:181], v[188:189]
v_pk_fma_f32 v[54:55], v[54:55], v[182:183], v[190:191]
v_pk_fma_f32 v[56:57], v[56:57], v[176:177], v[184:185]
v_pk_fma_f32 v[58:59], v[58:59], v[178:179], v[186:187]
v_pk_fma_f32 v[60:61], v[60:61], v[180:181], v[188:189]
v_pk_fma_f32 v[62:63], v[62:63], v[182:183], v[190:191]
v_pk_fma_f32 v[64:65], v[64:65], v[176:177], v[184:185]
v_pk_fma_f32 v[66:67], v[66:67], v[178:179], v[186:187]
v_pk_fma_f32 v[68:69], v[68:69], v[180:181], v[188:189]
v_pk_fma_f32 v[70:71], v[70:71], v[182:183], v[190:191]
v_pk_fma_f32 v[72:73], v[72:73], v[176:177], v[184:185]
v_pk_fma_f32 v[74:75], v[74:75], v[178:179], v[186:187]
v_pk_fma_f32 v[76:77], v[76:77], v[180:181], v[188:189]
v_pk_fma_f32 v[78:79], v[78:79], v[182:183], v[190:191]
v_pk_fma_f32 v[80:81], v[80:81], v[176:177], v[184:185]
v_pk_fma_f32 v[82:83], v[82:83], v[178:179], v[186:187]
v_pk_fma_f32 v[84:85], v[84:85], v[180:181], v[188:189]
v_pk_fma_f32 v[86:87], v[86:87], v[182:183], v[190:191]
v_pk_fma_f32 v[88:89], v[88:89], v[176:177], v[184:185]
v_pk_fma_f32 v[90:91], v[90:91], v[178:179], v[186:187]
v_pk_fma_f32 v[92:93], v[92:93], v[180:181], v[188:189]
v_pk_fma_f32 v[94:95], v[94:95], v[182:183], v[190:191]
v_pk_fma_f32 v[96:97], v[96:97], v[176:177], v[184:185]
v_pk_fma_f32 v[98:99], v[98:99], v[178:179], v[186:187]
v_pk_fma_f32 v[100:101], v[100:101], v[180:181], v[188:189]
v_pk_fma_f32 v[102:103], v[102:103], v[182:183], v[190:191]
v_cvt_pk_bf16_f32 v32, v32, v33
v_cvt_pk_bf16_f32 v33, v34, v35
v_cvt_pk_bf16_f32 v34, v36, v37
v_cvt_pk_bf16_f32 v35, v38, v39
v_mfma_f32_16x16x32_bf16 v[136:139], a[152:155], a[8:11], v[136:139]
ds_read_b128 a[192:195], %[vXr2] offset:30720
s_add_u32 s49, s26, s38
buffer_store_dwordx4 v[32:35], %[vL16], %[sWS], s49 offen offset:0 sc1
v_mfma_f32_16x16x32_bf16 v[140:143], a[152:155], a[12:15], v[140:143]
v_cvt_pk_bf16_f32 v40, v40, v41
v_cvt_pk_bf16_f32 v41, v42, v43
v_cvt_pk_bf16_f32 v42, v44, v45
v_cvt_pk_bf16_f32 v43, v46, v47
v_mfma_f32_16x16x32_bf16 v[144:147], a[156:159], a[8:11], v[144:147]
ds_read_b128 a[196:199], %[vXr2] offset:31744
buffer_store_dwordx4 v[40:43], %[vL16], %[sWS], s49 offen offset:1024 sc1
v_cvt_pk_bf16_f32 v48, v48, v49
v_mfma_f32_16x16x32_bf16 v[148:151], a[156:159], a[12:15], v[148:151]
ds_read_b128 a[200:203], %[vXr2] offset:32768
v_cvt_pk_bf16_f32 v49, v50, v51
v_cvt_pk_bf16_f32 v50, v52, v53
v_cvt_pk_bf16_f32 v51, v54, v55
v_mfma_f32_16x16x32_bf16 v[136:139], a[160:163], a[16:19], v[136:139]
buffer_store_dwordx4 v[48:51], %[vL16], %[sWS], s49 offen offset:2048 sc1
v_cvt_pk_bf16_f32 v56, v56, v57
v_cvt_pk_bf16_f32 v57, v58, v59
v_cvt_pk_bf16_f32 v58, v60, v61
v_mfma_f32_16x16x32_bf16 v[140:143], a[160:163], a[20:23], v[140:143]
ds_read_b128 a[204:207], %[vXr2] offset:33792
v_cvt_pk_bf16_f32 v59, v62, v63
buffer_store_dwordx4 v[56:59], %[vL16], %[sWS], s49 offen offset:3072 sc1
v_mfma_f32_16x16x32_bf16 v[144:147], a[164:167], a[16:19], v[144:147]
ds_read_b128 a[208:211], %[vXr2] offset:34816
v_cvt_pk_bf16_f32 v64, v64, v65
v_cvt_pk_bf16_f32 v65, v66, v67
v_cvt_pk_bf16_f32 v66, v68, v69
v_cvt_pk_bf16_f32 v67, v70, v71
v_mfma_f32_16x16x32_bf16 v[148:151], a[164:167], a[20:23], v[148:151]
s_add_u32 s49, s49, 4096
buffer_store_dwordx4 v[64:67], %[vL16], %[sWS], s49 offen offset:0 sc1
v_cvt_pk_bf16_f32 v72, v72, v73
v_mfma_f32_16x16x32_bf16 v[136:139], a[168:171], a[24:27], v[136:139]
ds_read_b128 a[212:215], %[vXr2] offset:35840
v_cvt_pk_bf16_f32 v73, v74, v75
v_cvt_pk_bf16_f32 v74, v76, v77
v_cvt_pk_bf16_f32 v75, v78, v79
v_mfma_f32_16x16x32_bf16 v[140:143], a[168:171], a[28:31], v[140:143]
buffer_store_dwordx4 v[72:75], %[vL16], %[sWS], s49 offen offset:1024 sc1
v_cvt_pk_bf16_f32 v80, v80, v81
v_cvt_pk_bf16_f32 v81, v82, v83
v_cvt_pk_bf16_f32 v82, v84, v85
v_mfma_f32_16x16x32_bf16 v[144:147], a[172:175], a[24:27], v[144:147]
v_cvt_pk_bf16_f32 v83, v86, v87
buffer_store_dwordx4 v[80:83], %[vL16], %[sWS], s49 offen offset:2048 sc1
v_mfma_f32_16x16x32_bf16 v[148:151], a[172:175], a[28:31], v[148:151]
v_cvt_pk_bf16_f32 v88, v88, v89
v_cvt_pk_bf16_f32 v89, v90, v91
v_cvt_pk_bf16_f32 v90, v92, v93
v_cvt_pk_bf16_f32 v91, v94, v95
v_mfma_f32_16x16x32_bf16 v[136:139], a[176:179], a[32:35], v[136:139]
buffer_store_dwordx4 v[88:91], %[vL16], %[sWS], s49 offen offset:3072 sc1
v_cvt_pk_bf16_f32 v96, v96, v97
v_cvt_pk_bf16_f32 v97, v98, v99
v_cvt_pk_bf16_f32 v98, v100, v101
v_mfma_f32_16x16x32_bf16 v[140:143], a[176:179], a[36:39], v[140:143]
s_waitcnt vmcnt(17) lgkmcnt(0)
s_barrier
v_cvt_pk_bf16_f32 v99, v102, v103
s_lshl1_add_u32 s49, s26, s39
buffer_store_dwordx4 v[96:99], %[vL16], %[sWS], s49 offen offset:0 sc1
v_mfma_f32_16x16x32_bf16 v[144:147], a[180:183], a[32:35], v[144:147]
ds_read_b128 a[144:147], %[vXr3] offset:0
v_mfma_f32_16x16x32_bf16 v[148:151], a[180:183], a[36:39], v[148:151]
ds_read_b128 a[148:151], %[vXr3] offset:1024
v_mfma_f32_16x16x32_bf16 v[136:139], a[184:187], a[40:43], v[136:139]
v_mfma_f32_16x16x32_bf16 v[140:143], a[184:187], a[44:47], v[140:143]
ds_read_b128 a[152:155], %[vXr3] offset:2048
v_mfma_f32_16x16x32_bf16 v[144:147], a[188:191], a[40:43], v[144:147]
ds_read_b128 a[156:159], %[vXr3] offset:3072
v_mfma_f32_16x16x32_bf16 v[148:151], a[188:191], a[44:47], v[148:151]
v_lshlrev_b32 v28, 16, v28
v_mfma_f32_16x16x32_bf16 v[136:139], a[192:195], a[48:51], v[136:139]
ds_read_b128 a[160:163], %[vXr3] offset:4096
)"
R"(v_lshlrev_b32_e64 v30, 16, v30
ds_write_b128 %[vOwn], v[32:35] offset:0
v_mfma_f32_16x16x32_bf16 v[140:143], a[192:195], a[52:55], v[140:143]
ds_read_b128 a[164:167], %[vXr3] offset:5120
v_mfma_f32_16x16x32_bf16 v[144:147], a[196:199], a[48:51], v[144:147]
v_mul_f32 v232, 0xc0088e99, v28
v_mfma_f32_16x16x32_bf16 v[148:151], a[196:199], a[52:55], v[148:151]
ds_read_b128 a[168:171], %[vXr3] offset:6144
v_mfma_f32_16x16x32_bf16 v[136:139], a[200:203], a[56:59], v[136:139]
ds_read_b128 a[172:175], %[vXr3] offset:7168
v_mfma_f32_16x16x32_bf16 v[140:143], a[200:203], a[60:63], v[140:143]
v_mul_f32 v233, 0xc0088e99, v28
ds_write_b128 %[vOwn], v[40:43] offset:1024
v_mfma_f32_16x16x32_bf16 v[144:147], a[204:207], a[56:59], v[144:147]
ds_read_b128 a[176:179], %[vXr3] offset:8192
v_mfma_f32_16x16x32_bf16 v[148:151], a[204:207], a[60:63], v[148:151]
v_mul_f32 v234, 0xc0088e99, v28
v_mul_f32 v235, 0xc0088e99, v28
v_mfma_f32_16x16x32_bf16 v[136:139], a[208:211], a[64:67], v[136:139]
v_mul_f32 v236, 0xc0088e99, v30
ds_write_b128 %[vOwn], v[48:51] offset:2048
v_mfma_f32_16x16x32_bf16 v[140:143], a[208:211], a[68:71], v[140:143]
v_mul_f32 v237, 0xc0088e99, v30
v_mfma_f32_16x16x32_bf16 v[144:147], a[212:215], a[64:67], v[144:147]
v_mul_f32 v238, 0xc0088e99, v30
ds_write_b128 %[vOwn], v[56:59] offset:3072
v_mfma_f32_16x16x32_bf16 v[148:151], a[212:215], a[68:71], v[148:151]
s_waitcnt lgkmcnt(2)
v_mul_f32 v239, 0xc0088e99, v30
ds_write_b128 %[vOwn], v[64:67] offset:4096
v_mfma_f32_16x16x32_bf16 v[136:139], a[144:147], a[72:75], v[136:139]
ds_read_b128 a[180:183], %[vXr3] offset:9216
v_mfma_f32_16x16x32_bf16 v[140:143], a[144:147], a[76:79], v[140:143]
ds_read_b128 a[184:187], %[vXr3] offset:10240
v_mfma_f32_16x16x32_bf16 v[144:147], a[148:151], a[72:75], v[144:147]
v_mfma_f32_16x16x32_bf16 v[148:151], a[148:151], a[76:79], v[148:151]
ds_read_b128 a[188:191], %[vXr3] offset:11264
v_min3_u32 v230, v192, v196, v200
v_mfma_f32_16x16x32_bf16 v[136:139], a[152:155], a[80:83], v[136:139]
ds_read_b128 a[192:195], %[vXr3] offset:12288
v_min3_u32 v230, v230, v204, v208
v_min3_u32 v230, v230, v212, v216
v_mfma_f32_16x16x32_bf16 v[140:143], a[152:155], a[84:87], v[140:143]
v_min_u32 v230, v230, v220
ds_write_b128 %[vOwn], v[72:75] offset:5120
v_mfma_f32_16x16x32_bf16 v[144:147], a[156:159], a[80:83], v[144:147]
ds_read_b128 a[196:199], %[vXr3] offset:13312
v_cmp_eq_u32 vcc, 0, v230
s_cbranch_vccnz L_sl6_%=
L_ok6_%=:
v_mfma_f32_16x16x32_bf16 v[148:151], a[156:159], a[84:87], v[148:151]
ds_read_b128 a[200:203], %[vXr3] offset:14336
v_fma_f32 v230, v227, v192, v193
v_fma_f32 v192, v229, v192, v193
v_mfma_f32_16x16x32_bf16 v[136:139], a[160:163], a[88:91], v[136:139]
v_fma_f32 v104, v104, v226, v230
v_fma_f32 v108, v108, v228, v192
v_mfma_f32_16x16x32_bf16 v[140:143], a[160:163], a[92:95], v[140:143]
ds_read_b128 a[204:207], %[vXr3] offset:15360
v_fma_f32 v231, v227, v194, v195
v_mfma_f32_16x16x32_bf16 v[144:147], a[164:167], a[88:91], v[144:147]
ds_read_b128 a[208:211], %[vXr3] offset:16384
v_fma_f32 v194, v229, v194, v195
v_fma_f32 v105, v105, v226, v231
v_mfma_f32_16x16x32_bf16 v[148:151], a[164:167], a[92:95], v[148:151]
v_fma_f32 v109, v109, v228, v194
v_fma_f32 v230, v227, v196, v197
ds_write_b128 %[vOwn], v[80:83] offset:6144
v_mfma_f32_16x16x32_bf16 v[136:139], a[168:171], a[96:99], v[136:139]
ds_read_b128 a[212:215], %[vXr3] offset:17408
v_fma_f32 v196, v229, v196, v197
v_mfma_f32_16x16x32_bf16 v[140:143], a[168:171], a[100:103], v[140:143]
v_fma_f32 v106, v106, v226, v230
v_fma_f32 v110, v110, v228, v196
v_fma_f32 v231, v227, v198, v199
v_mfma_f32_16x16x32_bf16 v[144:147], a[172:175], a[96:99], v[144:147]
v_fma_f32 v198, v229, v198, v199
v_fma_f32 v107, v107, v226, v231
ds_write_b128 %[vOwn], v[88:91] offset:7168
v_mfma_f32_16x16x32_bf16 v[148:151], a[172:175], a[100:103], v[148:151]
v_fma_f32 v111, v111, v228, v198
v_fma_f32 v24, v104, v104, s50
v_mfma_f32_16x16x32_bf16 v[136:139], a[176:179], a[104:107], v[136:139]
v_fma_f32 v25, v105, v105, s50
v_mul_f32 v24, v24, v104
v_mul_f32 v25, v25, v105
v_mfma_f32_16x16x32_bf16 v[140:143], a[176:179], a[108:111], v[140:143]
s_waitcnt lgkmcnt(1)
v_exp_f32 v24, v24
ds_write_b128 %[vOwn], v[96:99] offset:8192
v_mfma_f32_16x16x32_bf16 v[144:147], a[180:183], a[104:107], v[144:147]
ds_read_b128 a[144:147], %[vXr3] offset:18432
v_mfma_f32_16x16x32_bf16 v[148:151], a[180:183], a[108:111], v[148:151]
s_waitcnt vmcnt(0)
ds_read_b128 a[148:151], %[vXr3] offset:19456
v_exp_f32 v25, v25
v_mfma_f32_16x16x32_bf16 v[136:139], a[184:187], a[112:115], v[136:139]
buffer_store_dword v253, %[vFs], %[sWS], 0 offen offset:0 sc1
v_add_f32_e64 v24, 1.0, v24
v_mfma_f32_16x16x32_bf16 v[140:143], a[184:187], a[116:119], v[140:143]
ds_read_b128 a[152:155], %[vXr3] offset:20480
v_add_f32_e64 v25, 1.0, v25
v_mfma_f32_16x16x32_bf16 v[144:147], a[188:191], a[112:115], v[144:147]
ds_read_b128 a[156:159], %[vXr3] offset:21504
v_mfma_f32_16x16x32_bf16 v[148:151], a[188:191], a[116:119], v[148:151]
v_rcp_f32 v24, v24
v_rcp_f32 v25, v25
v_mfma_f32_16x16x32_bf16 v[136:139], a[192:195], a[120:123], v[136:139]
ds_read_b128 a[160:163], %[vXr3] offset:22528
v_mul_f32_e64 v104, v104, v24
v_mfma_f32_16x16x32_bf16 v[140:143], a[192:195], a[124:127], v[140:143]
ds_read_b128 a[164:167], %[vXr3] offset:23552
v_mul_f32_e64 v105, v105, v25
v_mfma_f32_16x16x32_bf16 v[144:147], a[196:199], a[120:123], v[144:147]
v_fma_f32 v26, v106, v106, s50
v_fma_f32 v27, v107, v107, s50
v_mfma_f32_16x16x32_bf16 v[148:151], a[196:199], a[124:127], v[148:151]
ds_read_b128 a[168:171], %[vXr3] offset:24576
v_mul_f32_e64 v26, v26, v106
v_mfma_f32_16x16x32_bf16 v[136:139], a[200:203], a[128:131], v[136:139]
ds_read_b128 a[172:175], %[vXr3] offset:25600
v_mul_f32_e64 v27, v27, v107
v_mfma_f32_16x16x32_bf16 v[140:143], a[200:203], a[132:135], v[140:143]
v_exp_f32_e64 v26, v26
v_mfma_f32_16x16x32_bf16 v[144:147], a[204:207], a[128:131], v[144:147]
ds_read_b128 a[176:179], %[vXr3] offset:26624
v_mfma_f32_16x16x32_bf16 v[148:151], a[204:207], a[132:135], v[148:151]
v_exp_f32 v27, v27
v_add_f32 v26, 1.0, v26
v_mfma_f32_16x16x32_bf16 v[136:139], a[208:211], a[136:139], v[136:139]
v_add_f32 v27, 1.0, v27
v_rcp_f32 v26, v26
v_mfma_f32_16x16x32_bf16 v[140:143], a[208:211], a[140:143], v[140:143]
v_rcp_f32_e64 v27, v27
v_mfma_f32_16x16x32_bf16 v[144:147], a[212:215], a[136:139], v[144:147]
v_mul_f32_e64 v106, v106, v26
v_mul_f32 v107, v107, v27
v_mfma_f32_16x16x32_bf16 v[148:151], a[212:215], a[140:143], v[148:151]
s_waitcnt lgkmcnt(0)
v_fma_f32 v24, v108, v108, s50
v_fma_f32 v25, v109, v109, s50
v_mfma_f32_16x16x32_bf16 v[152:155], a[144:147], a[0:3], 0
ds_read_b128 a[180:183], %[vXr3] offset:27648
v_mul_f32_e64 v24, v24, v108
v_mfma_f32_16x16x32_bf16 v[156:159], a[144:147], a[4:7], 0
ds_read_b128 a[184:187], %[vXr3] offset:28672
v_mul_f32_e64 v25, v25, v109
v_mfma_f32_16x16x32_bf16 v[160:163], a[148:151], a[0:3], 0
v_exp_f32_e64 v24, v24
v_mfma_f32_16x16x32_bf16 v[164:167], a[148:151], a[4:7], 0
ds_read_b128 a[188:191], %[vXr3] offset:29696
v_mfma_f32_16x16x32_bf16 v[168:171], a[152:155], a[0:3], 0
ds_read_b128 a[192:195], %[vXr3] offset:30720
v_exp_f32_e64 v25, v25
v_mfma_f32_16x16x32_bf16 v[172:175], a[152:155], a[4:7], 0
v_add_f32 v24, 1.0, v24
v_add_f32 v25, 1.0, v25
v_mfma_f32_16x16x32_bf16 v[152:155], a[156:159], a[8:11], v[152:155]
ds_read_b128 a[196:199], %[vXr3] offset:31744
v_mfma_f32_16x16x32_bf16 v[156:159], a[156:159], a[12:15], v[156:159]
ds_read_b128 a[200:203], %[vXr3] offset:32768
v_rcp_f32_e64 v24, v24
v_mfma_f32_16x16x32_bf16 v[160:163], a[160:163], a[8:11], v[160:163]
v_rcp_f32_e64 v25, v25
v_mfma_f32_16x16x32_bf16 v[164:167], a[160:163], a[12:15], v[164:167]
ds_read_b128 a[204:207], %[vXr3] offset:33792
v_mul_f32_e64 v108, v108, v24
v_mfma_f32_16x16x32_bf16 v[168:171], a[164:167], a[8:11], v[168:171]
ds_read_b128 a[208:211], %[vXr3] offset:34816
v_mul_f32_e64 v109, v109, v25
v_mfma_f32_16x16x32_bf16 v[172:175], a[164:167], a[12:15], v[172:175]
v_fma_f32 v26, v110, v110, s50
v_fma_f32 v27, v111, v111, s50
v_mfma_f32_16x16x32_bf16 v[152:155], a[168:171], a[16:19], v[152:155]
ds_read_b128 a[212:215], %[vXr3] offset:35840
v_mul_f32_e64 v26, v26, v110
v_mfma_f32_16x16x32_bf16 v[156:159], a[168:171], a[20:23], v[156:159]
v_mul_f32_e64 v27, v27, v111
v_mfma_f32_16x16x32_bf16 v[160:163], a[172:175], a[16:19], v[160:163]
v_exp_f32_e64 v26, v26
v_mfma_f32_16x16x32_bf16 v[164:167], a[172:175], a[20:23], v[164:167]
v_exp_f32 v27, v27
v_add_f32 v26, 1.0, v26
v_mfma_f32_16x16x32_bf16 v[168:171], a[176:179], a[16:19], v[168:171]
v_add_f32_e64 v27, 1.0, v27
v_mfma_f32_16x16x32_bf16 v[172:175], a[176:179], a[20:23], v[172:175]
s_waitcnt lgkmcnt(0)
s_barrier
v_rcp_f32 v26, v26
v_rcp_f32 v27, v27
v_mfma_f32_16x16x32_bf16 v[152:155], a[180:183], a[24:27], v[152:155]
ds_read_b128 a[144:147], %[vXr0] offset:0
v_mul_f32_e64 v110, v110, v26
v_mfma_f32_16x16x32_bf16 v[156:159], a[180:183], a[28:31], v[156:159]
ds_read_b128 a[148:151], %[vXr0] offset:1024
v_mul_f32_e64 v111, v111, v27
v_mfma_f32_16x16x32_bf16 v[160:163], a[184:187], a[24:27], v[160:163]
v_fma_f32 v230, v227, v200, v201
v_fma_f32 v200, v229, v200, v201
v_mfma_f32_16x16x32_bf16 v[164:167], a[184:187], a[28:31], v[164:167]
)"
R"(ds_read_b128 a[152:155], %[vXr0] offset:2048
v_fma_f32 v112, v112, v226, v230
v_mfma_f32_16x16x32_bf16 v[168:171], a[188:191], a[24:27], v[168:171]
ds_read_b128 a[156:159], %[vXr0] offset:3072
v_fma_f32 v116, v116, v228, v200
v_mfma_f32_16x16x32_bf16 v[172:175], a[188:191], a[28:31], v[172:175]
v_fma_f32 v231, v227, v202, v203
v_fma_f32 v202, v229, v202, v203
v_mfma_f32_16x16x32_bf16 v[152:155], a[192:195], a[32:35], v[152:155]
ds_read_b128 a[160:163], %[vXr0] offset:4096
v_fma_f32 v113, v113, v226, v231
v_mfma_f32_16x16x32_bf16 v[156:159], a[192:195], a[36:39], v[156:159]
ds_read_b128 a[164:167], %[vXr0] offset:5120
v_fma_f32 v117, v117, v228, v202
v_mfma_f32_16x16x32_bf16 v[160:163], a[196:199], a[32:35], v[160:163]
v_fma_f32 v230, v227, v204, v205
v_fma_f32 v204, v229, v204, v205
v_mfma_f32_16x16x32_bf16 v[164:167], a[196:199], a[36:39], v[164:167]
ds_read_b128 a[168:171], %[vXr0] offset:6144
v_fma_f32 v114, v114, v226, v230
v_mfma_f32_16x16x32_bf16 v[168:171], a[200:203], a[32:35], v[168:171]
ds_read_b128 a[172:175], %[vXr0] offset:7168
v_fma_f32 v118, v118, v228, v204
v_mfma_f32_16x16x32_bf16 v[172:175], a[200:203], a[36:39], v[172:175]
v_fma_f32 v231, v227, v206, v207
v_fma_f32 v206, v229, v206, v207
v_mfma_f32_16x16x32_bf16 v[152:155], a[204:207], a[40:43], v[152:155]
ds_read_b128 a[176:179], %[vXr0] offset:8192
v_fma_f32 v115, v115, v226, v231
v_mfma_f32_16x16x32_bf16 v[156:159], a[204:207], a[44:47], v[156:159]
v_fma_f32 v119, v119, v228, v206
v_fma_f32 v24, v112, v112, s50
v_mfma_f32_16x16x32_bf16 v[160:163], a[208:211], a[40:43], v[160:163]
v_fma_f32 v25, v113, v113, s50
v_mul_f32_e64 v24, v24, v112
v_mfma_f32_16x16x32_bf16 v[164:167], a[208:211], a[44:47], v[164:167]
v_mul_f32_e64 v25, v25, v113
v_mfma_f32_16x16x32_bf16 v[168:171], a[212:215], a[40:43], v[168:171]
v_exp_f32_e64 v24, v24
v_mfma_f32_16x16x32_bf16 v[172:175], a[212:215], a[44:47], v[172:175]
s_waitcnt lgkmcnt(0)
v_exp_f32 v25, v25
v_add_f32_e64 v24, 1.0, v24
v_mfma_f32_16x16x32_bf16 v[152:155], a[144:147], a[48:51], v[152:155]
ds_read_b128 a[180:183], %[vXr0] offset:9216
v_add_f32_e64 v25, 1.0, v25
v_mfma_f32_16x16x32_bf16 v[156:159], a[144:147], a[52:55], v[156:159]
ds_read_b128 a[184:187], %[vXr0] offset:10240
v_mfma_f32_16x16x32_bf16 v[160:163], a[148:151], a[48:51], v[160:163]
buffer_load_dword v252, %[vFl], %[sWS], 0 offen offset:0 sc1
v_rcp_f32_e64 v24, v24
v_mfma_f32_16x16x32_bf16 v[164:167], a[148:151], a[52:55], v[164:167]
ds_read_b128 a[188:191], %[vXr0] offset:11264
v_rcp_f32_e64 v25, v25
v_mfma_f32_16x16x32_bf16 v[168:171], a[152:155], a[48:51], v[168:171]
ds_read_b128 a[192:195], %[vXr0] offset:12288
v_mul_f32_e64 v112, v112, v24
v_mfma_f32_16x16x32_bf16 v[172:175], a[152:155], a[52:55], v[172:175]
v_mul_f32_e64 v113, v113, v25
v_fma_f32 v26, v114, v114, s50
v_mfma_f32_16x16x32_bf16 v[152:155], a[156:159], a[56:59], v[152:155]
ds_read_b128 a[196:199], %[vXr0] offset:13312
v_fma_f32 v27, v115, v115, s50
v_mfma_f32_16x16x32_bf16 v[156:159], a[156:159], a[60:63], v[156:159]
ds_read_b128 a[200:203], %[vXr0] offset:14336
v_mul_f32 v26, v26, v114
v_mfma_f32_16x16x32_bf16 v[160:163], a[160:163], a[56:59], v[160:163]
s_add_u32 s55, s26, 0
v_mul_f32 v27, v27, v115
v_mfma_f32_16x16x32_bf16 v[164:167], a[160:163], a[60:63], v[164:167]
LPb_4:
s_cbranch_execz LPb_8
s_and_b32 s55, s55, 524287
ds_read_b128 a[204:207], %[vXr0] offset:15360
v_exp_f32 v26, v26
v_mfma_f32_16x16x32_bf16 v[168:171], a[164:167], a[56:59], v[168:171]
s_or_b32 s55, s55, s74
ds_read_b128 a[208:211], %[vXr0] offset:16384
v_exp_f32_e64 v27, v27
v_mfma_f32_16x16x32_bf16 v[172:175], a[164:167], a[60:63], v[172:175]
buffer_load_dwordx4 a[0:3], %[vY4], %[sWS], s55 offen offset:0 sc1
v_add_f32_e64 v26, 1.0, v26
v_mfma_f32_16x16x32_bf16 v[152:155], a[168:171], a[64:67], v[152:155]
ds_read_b128 a[212:215], %[vXr0] offset:17408
v_add_f32_e64 v27, 1.0, v27
v_mfma_f32_16x16x32_bf16 v[156:159], a[168:171], a[68:71], v[156:159]
v_rcp_f32_e64 v26, v26
v_mfma_f32_16x16x32_bf16 v[160:163], a[172:175], a[64:67], v[160:163]
v_rcp_f32_e64 v27, v27
v_mfma_f32_16x16x32_bf16 v[164:167], a[172:175], a[68:71], v[164:167]
buffer_load_dwordx4 a[4:7], %[vY4], %[sWS], s55 offen offset:1024 sc1
v_mul_f32 v114, v114, v26
v_mul_f32 v115, v115, v27
v_mfma_f32_16x16x32_bf16 v[168:171], a[176:179], a[64:67], v[168:171]
v_fma_f32 v24, v116, v116, s50
v_fma_f32 v25, v117, v117, s50
v_mfma_f32_16x16x32_bf16 v[172:175], a[176:179], a[68:71], v[172:175]
s_waitcnt lgkmcnt(0)
v_mul_f32 v24, v24, v116
v_mul_f32_e64 v25, v25, v117
v_mfma_f32_16x16x32_bf16 v[152:155], a[180:183], a[72:75], v[152:155]
s_add_u32 s55, s26, 16384
ds_read_b128 a[144:147], %[vXr0] offset:18432
v_mfma_f32_16x16x32_bf16 v[156:159], a[180:183], a[76:79], v[156:159]
ds_read_b128 a[148:151], %[vXr0] offset:19456
v_exp_f32 v24, v24
v_mfma_f32_16x16x32_bf16 v[160:163], a[184:187], a[72:75], v[160:163]
s_and_b32 s55, s55, 524287
s_or_b32 s55, s55, s74
buffer_load_dwordx4 a[8:11], %[vY4], %[sWS], s55 offen offset:0 sc1
v_mfma_f32_16x16x32_bf16 v[164:167], a[184:187], a[76:79], v[164:167]
ds_read_b128 a[152:155], %[vXr0] offset:20480
v_exp_f32_e64 v25, v25
v_mfma_f32_16x16x32_bf16 v[168:171], a[188:191], a[72:75], v[168:171]
ds_read_b128 a[156:159], %[vXr0] offset:21504
v_add_f32_e64 v24, 1.0, v24
v_mfma_f32_16x16x32_bf16 v[172:175], a[188:191], a[76:79], v[172:175]
v_add_f32 v25, 1.0, v25
v_rcp_f32 v24, v24
v_mfma_f32_16x16x32_bf16 v[152:155], a[192:195], a[80:83], v[152:155]
ds_read_b128 a[160:163], %[vXr0] offset:22528
v_mfma_f32_16x16x32_bf16 v[156:159], a[192:195], a[84:87], v[156:159]
ds_read_b128 a[164:167], %[vXr0] offset:23552
v_rcp_f32_e64 v25, v25
v_mfma_f32_16x16x32_bf16 v[160:163], a[196:199], a[80:83], v[160:163]
v_mul_f32 v116, v116, v24
v_mul_f32 v117, v117, v25
v_mfma_f32_16x16x32_bf16 v[164:167], a[196:199], a[84:87], v[164:167]
ds_read_b128 a[168:171], %[vXr0] offset:24576
v_fma_f32 v26, v118, v118, s50
v_mfma_f32_16x16x32_bf16 v[168:171], a[200:203], a[80:83], v[168:171]
ds_read_b128 a[172:175], %[vXr0] offset:25600
v_fma_f32 v27, v119, v119, s50
v_mfma_f32_16x16x32_bf16 v[172:175], a[200:203], a[84:87], v[172:175]
v_mul_f32 v26, v26, v118
v_mul_f32 v27, v27, v119
v_mfma_f32_16x16x32_bf16 v[152:155], a[204:207], a[88:91], v[152:155]
ds_read_b128 a[176:179], %[vXr0] offset:26624
v_mfma_f32_16x16x32_bf16 v[156:159], a[204:207], a[92:95], v[156:159]
buffer_load_dwordx4 a[12:15], %[vY4], %[sWS], s55 offen offset:1024 sc1
v_exp_f32_e64 v26, v26
v_mfma_f32_16x16x32_bf16 v[160:163], a[208:211], a[88:91], v[160:163]
v_exp_f32 v27, v27
v_add_f32 v26, 1.0, v26
v_mfma_f32_16x16x32_bf16 v[164:167], a[208:211], a[92:95], v[164:167]
v_add_f32_e64 v27, 1.0, v27
v_mfma_f32_16x16x32_bf16 v[168:171], a[212:215], a[88:91], v[168:171]
v_rcp_f32_e64 v26, v26
v_mfma_f32_16x16x32_bf16 v[172:175], a[212:215], a[92:95], v[172:175]
s_waitcnt lgkmcnt(0)
v_rcp_f32 v27, v27
v_mul_f32_e64 v118, v118, v26
v_mfma_f32_16x16x32_bf16 v[152:155], a[144:147], a[96:99], v[152:155]
ds_read_b128 a[180:183], %[vXr0] offset:27648
v_mul_f32_e64 v119, v119, v27
v_mfma_f32_16x16x32_bf16 v[156:159], a[144:147], a[100:103], v[156:159]
ds_read_b128 a[184:187], %[vXr0] offset:28672
s_waitcnt vmcnt(4)
v_cmp_eq_u32_e64 vcc, 0, v252
s_cbranch_vccnz L_sl15_%=
L_ok15_%=:
v_fma_f32 v230, v227, v208, v209
v_fma_f32 v208, v229, v208, v209
v_mfma_f32_16x16x32_bf16 v[160:163], a[148:151], a[96:99], v[160:163]
s_add_u32 m0, %[sLx], 73728
v_fma_f32 v120, v120, v226, v230
v_fma_f32 v124, v124, v228, v208
v_fma_f32 v231, v227, v210, v211
v_mfma_f32_16x16x32_bf16 v[164:167], a[148:151], a[100:103], v[164:167]
ds_read_b128 a[188:191], %[vXr0] offset:29696
v_fma_f32 v210, v229, v210, v211
v_mfma_f32_16x16x32_bf16 v[168:171], a[152:155], a[96:99], v[168:171]
ds_read_b128 a[192:195], %[vXr0] offset:30720
v_fma_f32 v121, v121, v226, v231
v_fma_f32 v125, v125, v228, v210
v_mfma_f32_16x16x32_bf16 v[172:175], a[152:155], a[100:103], v[172:175]
s_add_u32 s54, s26, 65536
s_and_b32 s54, s54, 458752
s_add_u32 s48, s54, s38
buffer_load_dwordx4 %[vL16], %[sWS], s48 offen offset:0 sc1 lds
v_fma_f32 v230, v227, v212, v213
v_mfma_f32_16x16x32_bf16 v[152:155], a[156:159], a[104:107], v[152:155]
s_add_u32 s55, s26, 32768
ds_read_b128 a[196:199], %[vXr0] offset:31744
v_fma_f32 v212, v229, v212, v213
v_mfma_f32_16x16x32_bf16 v[156:159], a[156:159], a[108:111], v[156:159]
ds_read_b128 a[200:203], %[vXr0] offset:32768
v_fma_f32 v122, v122, v226, v230
v_fma_f32 v126, v126, v228, v212
v_mfma_f32_16x16x32_bf16 v[160:163], a[160:163], a[104:107], v[160:163]
s_and_b32 s55, s55, 524287
s_or_b32 s55, s55, s74
buffer_load_dwordx4 a[16:19], %[vY4], %[sWS], s55 offen offset:0 sc1
v_fma_f32 v231, v227, v214, v215
v_mfma_f32_16x16x32_bf16 v[164:167], a[160:163], a[108:111], v[164:167]
ds_read_b128 a[204:207], %[vXr0] offset:33792
v_fma_f32 v214, v229, v214, v215
v_fma_f32 v123, v123, v226, v231
v_mfma_f32_16x16x32_bf16 v[168:171], a[164:167], a[104:107], v[168:171]
ds_read_b128 a[208:211], %[vXr0] offset:34816
v_fma_f32 v127, v127, v228, v214
)"
R"(v_mfma_f32_16x16x32_bf16 v[172:175], a[164:167], a[108:111], v[172:175]
buffer_load_dwordx4 a[20:23], %[vY4], %[sWS], s55 offen offset:1024 sc1
v_fma_f32 v24, v120, v120, s50
v_mfma_f32_16x16x32_bf16 v[152:155], a[168:171], a[112:115], v[152:155]
ds_read_b128 a[212:215], %[vXr0] offset:35840
v_fma_f32 v25, v121, v121, s50
v_mul_f32_e64 v24, v24, v120
v_mfma_f32_16x16x32_bf16 v[156:159], a[168:171], a[116:119], v[156:159]
buffer_load_dwordx4 %[vL16], %[sWS], s48 offen offset:1024 sc1 lds
v_mul_f32_e64 v25, v25, v121
v_mfma_f32_16x16x32_bf16 v[160:163], a[172:175], a[112:115], v[160:163]
buffer_load_dwordx4 %[vL16], %[sWS], s48 offen offset:2048 sc1 lds
v_exp_f32_e64 v24, v24
v_mfma_f32_16x16x32_bf16 v[164:167], a[172:175], a[116:119], v[164:167]
buffer_load_dwordx4 %[vL16], %[sWS], s48 offen offset:3072 sc1 lds
v_mfma_f32_16x16x32_bf16 v[168:171], a[176:179], a[112:115], v[168:171]
s_add_u32 m0, %[sLx], 77824
s_add_u32 s48, s48, 4096
buffer_load_dwordx4 %[vL16], %[sWS], s48 offen offset:0 sc1 lds
v_exp_f32 v25, v25
v_add_f32 v24, 1.0, v24
v_mfma_f32_16x16x32_bf16 v[172:175], a[176:179], a[116:119], v[172:175]
s_waitcnt lgkmcnt(0)
s_barrier
buffer_load_dwordx4 %[vL16], %[sWS], s48 offen offset:1024 sc1 lds
v_add_f32_e64 v25, 1.0, v25
v_mfma_f32_16x16x32_bf16 v[152:155], a[180:183], a[120:123], v[152:155]
s_add_u32 s55, s26, 49152
ds_read_b128 a[144:147], %[vXr1] offset:0
v_mfma_f32_16x16x32_bf16 v[156:159], a[180:183], a[124:127], v[156:159]
ds_read_b128 a[148:151], %[vXr1] offset:1024
v_rcp_f32_e64 v24, v24
v_mfma_f32_16x16x32_bf16 v[160:163], a[184:187], a[120:123], v[160:163]
s_and_b32 s55, s55, 524287
s_or_b32 s55, s55, s74
buffer_load_dwordx4 a[24:27], %[vY4], %[sWS], s55 offen offset:0 sc1
v_rcp_f32 v25, v25
v_mfma_f32_16x16x32_bf16 v[164:167], a[184:187], a[124:127], v[164:167]
ds_read_b128 a[152:155], %[vXr1] offset:2048
v_mul_f32_e64 v120, v120, v24
v_mfma_f32_16x16x32_bf16 v[168:171], a[188:191], a[120:123], v[168:171]
ds_read_b128 a[156:159], %[vXr1] offset:3072
v_mul_f32_e64 v121, v121, v25
v_mfma_f32_16x16x32_bf16 v[172:175], a[188:191], a[124:127], v[172:175]
v_fma_f32 v26, v122, v122, s50
v_fma_f32 v27, v123, v123, s50
v_mfma_f32_16x16x32_bf16 v[152:155], a[192:195], a[128:131], v[152:155]
ds_read_b128 a[160:163], %[vXr1] offset:4096
v_mul_f32_e64 v26, v26, v122
v_mfma_f32_16x16x32_bf16 v[156:159], a[192:195], a[132:135], v[156:159]
ds_read_b128 a[164:167], %[vXr1] offset:5120
v_mul_f32_e64 v27, v27, v123
v_mfma_f32_16x16x32_bf16 v[160:163], a[196:199], a[128:131], v[160:163]
buffer_load_dwordx4 a[28:31], %[vY4], %[sWS], s55 offen offset:1024 sc1
v_exp_f32_e64 v26, v26
v_mfma_f32_16x16x32_bf16 v[164:167], a[196:199], a[132:135], v[164:167]
ds_read_b128 a[168:171], %[vXr1] offset:6144
v_mfma_f32_16x16x32_bf16 v[168:171], a[200:203], a[128:131], v[168:171]
ds_read_b128 a[172:175], %[vXr1] offset:7168
v_exp_f32_e64 v27, v27
v_mfma_f32_16x16x32_bf16 v[172:175], a[200:203], a[132:135], v[172:175]
v_add_f32 v26, 1.0, v26
v_add_f32 v27, 1.0, v27
v_mfma_f32_16x16x32_bf16 v[152:155], a[204:207], a[136:139], v[152:155]
ds_read_b128 a[176:179], %[vXr1] offset:8192
v_rcp_f32_e64 v26, v26
v_mfma_f32_16x16x32_bf16 v[156:159], a[204:207], a[140:143], v[156:159]
buffer_load_dwordx4 %[vL16], %[sWS], s48 offen offset:2048 sc1 lds
v_mfma_f32_16x16x32_bf16 v[160:163], a[208:211], a[136:139], v[160:163]
v_rcp_f32 v27, v27
v_mul_f32 v122, v122, v26
v_mfma_f32_16x16x32_bf16 v[164:167], a[208:211], a[140:143], v[164:167]
buffer_load_dwordx4 %[vL16], %[sWS], s48 offen offset:3072 sc1 lds
v_mul_f32_e64 v123, v123, v27
v_mfma_f32_16x16x32_bf16 v[168:171], a[212:215], a[136:139], v[168:171]
v_fma_f32 v24, v124, v124, s50
v_fma_f32 v25, v125, v125, s50
v_mul_f32_e64 v24, v24, v124
v_mfma_f32_16x16x32_bf16 v[172:175], a[212:215], a[140:143], v[172:175]
s_waitcnt vmcnt(14) lgkmcnt(0)
v_mul_f32 v25, v25, v125
v_exp_f32_e64 v24, v24
v_mfma_f32_16x16x32_bf16 v[32:35], a[144:147], a[0:3], v[232:235]
s_add_u32 m0, %[sLx], 81920
ds_read_b128 a[180:183], %[vXr1] offset:9216
v_mfma_f32_16x16x32_bf16 v[36:39], a[144:147], a[4:7], v[236:239]
s_lshl1_add_u32 s48, s54, s39
ds_read_b128 a[184:187], %[vXr1] offset:10240
v_exp_f32 v25, v25
v_mfma_f32_16x16x32_bf16 v[40:43], a[148:151], a[0:3], v[232:235]
buffer_load_dwordx4 %[vL16], %[sWS], s48 offen offset:0 sc1 lds
v_add_f32 v24, 1.0, v24
v_add_f32 v25, 1.0, v25
v_mfma_f32_16x16x32_bf16 v[44:47], a[148:151], a[4:7], v[236:239]
s_add_u32 s55, s26, 65536
ds_read_b128 a[188:191], %[vXr1] offset:11264
v_mfma_f32_16x16x32_bf16 v[48:51], a[152:155], a[0:3], v[232:235]
ds_read_b128 a[192:195], %[vXr1] offset:12288
v_rcp_f32 v24, v24
v_mfma_f32_16x16x32_bf16 v[52:55], a[152:155], a[4:7], v[236:239]
s_and_b32 s55, s55, 524287
s_or_b32 s55, s55, s74
buffer_load_dwordx4 a[32:35], %[vY4], %[sWS], s55 offen offset:0 sc1
v_mfma_f32_16x16x32_bf16 v[56:59], a[156:159], a[0:3], v[232:235]
ds_read_b128 a[196:199], %[vXr1] offset:13312
v_rcp_f32 v25, v25
v_mul_f32 v124, v124, v24
v_mfma_f32_16x16x32_bf16 v[60:63], a[156:159], a[4:7], v[236:239]
ds_read_b128 a[200:203], %[vXr1] offset:14336
v_mul_f32_e64 v125, v125, v25
v_mfma_f32_16x16x32_bf16 v[64:67], a[160:163], a[0:3], v[232:235]
v_fma_f32 v26, v126, v126, s50
v_fma_f32 v27, v127, v127, s50
v_mfma_f32_16x16x32_bf16 v[68:71], a[160:163], a[4:7], v[236:239]
ds_read_b128 a[204:207], %[vXr1] offset:15360
v_mul_f32 v26, v26, v126
v_mul_f32 v27, v27, v127
v_mfma_f32_16x16x32_bf16 v[72:75], a[164:167], a[0:3], v[232:235]
ds_read_b128 a[208:211], %[vXr1] offset:16384
v_mfma_f32_16x16x32_bf16 v[76:79], a[164:167], a[4:7], v[236:239]
buffer_load_dwordx4 a[36:39], %[vY4], %[sWS], s55 offen offset:1024 sc1
v_exp_f32_e64 v26, v26
v_mfma_f32_16x16x32_bf16 v[80:83], a[168:171], a[0:3], v[232:235]
s_add_u32 m0, %[sLx], 110592
ds_read_b128 a[212:215], %[vXr1] offset:17408
v_mfma_f32_16x16x32_bf16 v[84:87], a[168:171], a[4:7], v[236:239]
v_exp_f32 v27, v27
v_add_f32 v26, 1.0, v26
v_add_f32_e64 v27, 1.0, v27
v_mfma_f32_16x16x32_bf16 v[88:91], a[172:175], a[0:3], v[232:235]
s_add_u32 s54, s26, 131072
s_and_b32 s54, s54, 458752
s_add_u32 s48, s54, s38
buffer_load_dwordx4 %[vL16], %[sWS], s48 offen offset:0 sc1 lds
v_mfma_f32_16x16x32_bf16 v[92:95], a[172:175], a[4:7], v[236:239]
v_rcp_f32 v26, v26
v_mfma_f32_16x16x32_bf16 v[96:99], a[176:179], a[0:3], v[232:235]
buffer_load_dwordx4 %[vL16], %[sWS], s48 offen offset:1024 sc1 lds
v_rcp_f32_e64 v27, v27
v_mul_f32 v126, v126, v26
v_mfma_f32_16x16x32_bf16 v[100:103], a[176:179], a[4:7], v[236:239]
s_waitcnt vmcnt(17) lgkmcnt(0)
v_mul_f32 v127, v127, v27
v_fma_f32 v230, v227, v216, v217
v_mfma_f32_16x16x32_bf16 v[32:35], a[180:183], a[8:11], v[32:35]
s_add_u32 s55, s26, 81920
ds_read_b128 a[144:147], %[vXr1] offset:18432
v_fma_f32 v216, v229, v216, v217
v_mfma_f32_16x16x32_bf16 v[36:39], a[180:183], a[12:15], v[36:39]
ds_read_b128 a[148:151], %[vXr1] offset:19456
v_fma_f32 v128, v128, v226, v230
v_mfma_f32_16x16x32_bf16 v[40:43], a[184:187], a[8:11], v[40:43]
s_and_b32 s55, s55, 524287
s_or_b32 s55, s55, s74
buffer_load_dwordx4 a[40:43], %[vY4], %[sWS], s55 offen offset:0 sc1
v_fma_f32 v132, v132, v228, v216
v_fma_f32 v231, v227, v218, v219
v_mfma_f32_16x16x32_bf16 v[44:47], a[184:187], a[12:15], v[44:47]
ds_read_b128 a[152:155], %[vXr1] offset:20480
v_fma_f32 v218, v229, v218, v219
v_mfma_f32_16x16x32_bf16 v[48:51], a[188:191], a[8:11], v[48:51]
ds_read_b128 a[156:159], %[vXr1] offset:21504
v_fma_f32 v129, v129, v226, v231
v_mfma_f32_16x16x32_bf16 v[52:55], a[188:191], a[12:15], v[52:55]
v_fma_f32 v133, v133, v228, v218
v_fma_f32 v230, v227, v220, v221
v_mfma_f32_16x16x32_bf16 v[56:59], a[192:195], a[8:11], v[56:59]
ds_read_b128 a[160:163], %[vXr1] offset:22528
v_fma_f32 v220, v229, v220, v221
v_mfma_f32_16x16x32_bf16 v[60:63], a[192:195], a[12:15], v[60:63]
ds_read_b128 a[164:167], %[vXr1] offset:23552
v_fma_f32 v130, v130, v226, v230
v_fma_f32 v134, v134, v228, v220
v_mfma_f32_16x16x32_bf16 v[64:67], a[196:199], a[8:11], v[64:67]
buffer_load_dwordx4 a[44:47], %[vY4], %[sWS], s55 offen offset:1024 sc1
v_fma_f32 v231, v227, v222, v223
v_mfma_f32_16x16x32_bf16 v[68:71], a[196:199], a[12:15], v[68:71]
ds_read_b128 a[168:171], %[vXr1] offset:24576
v_fma_f32 v222, v229, v222, v223
v_mfma_f32_16x16x32_bf16 v[72:75], a[200:203], a[8:11], v[72:75]
ds_read_b128 a[172:175], %[vXr1] offset:25600
v_fma_f32 v131, v131, v226, v231
v_mfma_f32_16x16x32_bf16 v[76:79], a[200:203], a[12:15], v[76:79]
v_fma_f32 v135, v135, v228, v222
v_fma_f32 v24, v128, v128, s50
v_mfma_f32_16x16x32_bf16 v[80:83], a[204:207], a[8:11], v[80:83]
ds_read_b128 a[176:179], %[vXr1] offset:26624
v_fma_f32 v25, v129, v129, s50
v_mul_f32_e64 v24, v24, v128
v_mfma_f32_16x16x32_bf16 v[84:87], a[204:207], a[12:15], v[84:87]
buffer_load_dwordx4 %[vL16], %[sWS], s48 offen offset:2048 sc1 lds
v_mul_f32_e64 v25, v25, v129
v_mfma_f32_16x16x32_bf16 v[88:91], a[208:211], a[8:11], v[88:91]
v_exp_f32_e64 v24, v24
v_mfma_f32_16x16x32_bf16 v[92:95], a[208:211], a[12:15], v[92:95]
buffer_load_dwordx4 %[vL16], %[sWS], s48 offen offset:3072 sc1 lds
v_mfma_f32_16x16x32_bf16 v[96:99], a[212:215], a[8:11], v[96:99]
v_exp_f32 v25, v25
v_add_f32 v24, 1.0, v24
v_add_f32_e64 v25, 1.0, v25
)"
R"(v_mfma_f32_16x16x32_bf16 v[100:103], a[212:215], a[12:15], v[100:103]
s_waitcnt vmcnt(18) lgkmcnt(0)
v_rcp_f32 v24, v24
v_mfma_f32_16x16x32_bf16 v[32:35], a[144:147], a[16:19], v[32:35]
s_add_u32 s55, s26, 98304
ds_read_b128 a[180:183], %[vXr1] offset:27648
v_mfma_f32_16x16x32_bf16 v[36:39], a[144:147], a[20:23], v[36:39]
ds_read_b128 a[184:187], %[vXr1] offset:28672
v_rcp_f32_e64 v25, v25
v_mfma_f32_16x16x32_bf16 v[40:43], a[148:151], a[16:19], v[40:43]
s_and_b32 s55, s55, 524287
s_or_b32 s55, s55, s74
buffer_load_dwordx4 a[48:51], %[vY4], %[sWS], s55 offen offset:0 sc1
v_mul_f32 v128, v128, v24
v_mfma_f32_16x16x32_bf16 v[44:47], a[148:151], a[20:23], v[44:47]
ds_read_b128 a[188:191], %[vXr1] offset:29696
v_mul_f32_e64 v129, v129, v25
v_mfma_f32_16x16x32_bf16 v[48:51], a[152:155], a[16:19], v[48:51]
ds_read_b128 a[192:195], %[vXr1] offset:30720
v_fma_f32 v26, v130, v130, s50
v_fma_f32 v27, v131, v131, s50
v_mfma_f32_16x16x32_bf16 v[52:55], a[152:155], a[20:23], v[52:55]
v_mul_f32 v26, v26, v130
v_mul_f32 v27, v27, v131
v_mfma_f32_16x16x32_bf16 v[56:59], a[156:159], a[16:19], v[56:59]
ds_read_b128 a[196:199], %[vXr1] offset:31744
v_mfma_f32_16x16x32_bf16 v[60:63], a[156:159], a[20:23], v[60:63]
ds_read_b128 a[200:203], %[vXr1] offset:32768
v_exp_f32_e64 v26, v26
v_mfma_f32_16x16x32_bf16 v[64:67], a[160:163], a[16:19], v[64:67]
buffer_load_dwordx4 a[52:55], %[vY4], %[sWS], s55 offen offset:1024 sc1
v_mfma_f32_16x16x32_bf16 v[68:71], a[160:163], a[20:23], v[68:71]
ds_read_b128 a[204:207], %[vXr1] offset:33792
v_exp_f32_e64 v27, v27
v_mfma_f32_16x16x32_bf16 v[72:75], a[164:167], a[16:19], v[72:75]
ds_read_b128 a[208:211], %[vXr1] offset:34816
v_add_f32_e64 v26, 1.0, v26
v_mfma_f32_16x16x32_bf16 v[76:79], a[164:167], a[20:23], v[76:79]
s_add_u32 m0, %[sLx], 114688
v_add_f32 v27, 1.0, v27
v_rcp_f32 v26, v26
v_mfma_f32_16x16x32_bf16 v[80:83], a[168:171], a[16:19], v[80:83]
s_add_u32 s48, s48, 4096
ds_read_b128 a[212:215], %[vXr1] offset:35840
v_mfma_f32_16x16x32_bf16 v[84:87], a[168:171], a[20:23], v[84:87]
buffer_load_dwordx4 %[vL16], %[sWS], s48 offen offset:0 sc1 lds
v_rcp_f32_e64 v27, v27
v_mfma_f32_16x16x32_bf16 v[88:91], a[172:175], a[16:19], v[88:91]
v_mul_f32 v130, v130, v26
v_mul_f32 v131, v131, v27
v_mfma_f32_16x16x32_bf16 v[92:95], a[172:175], a[20:23], v[92:95]
buffer_load_dwordx4 %[vL16], %[sWS], s48 offen offset:1024 sc1 lds
v_fma_f32 v24, v132, v132, s50
v_mfma_f32_16x16x32_bf16 v[96:99], a[176:179], a[16:19], v[96:99]
v_fma_f32 v25, v133, v133, s50
v_mul_f32 v24, v24, v132
v_mul_f32 v25, v25, v133
v_mfma_f32_16x16x32_bf16 v[100:103], a[176:179], a[20:23], v[100:103]
s_waitcnt vmcnt(12) lgkmcnt(0)
s_barrier
v_exp_f32_e64 v24, v24
v_mfma_f32_16x16x32_bf16 v[32:35], a[180:183], a[24:27], v[32:35]
ds_read_b128 a[144:147], %[vXr2] offset:0
v_mfma_f32_16x16x32_bf16 v[36:39], a[180:183], a[28:31], v[36:39]
v_xor_b32 v251, 8192, %[vFl]
ds_read_b128 a[148:151], %[vXr2] offset:1024
v_exp_f32_e64 v25, v25
v_mfma_f32_16x16x32_bf16 v[40:43], a[184:187], a[24:27], v[40:43]
buffer_load_dword v252, v251, %[sWS], 0 offen offset:0 sc1
v_add_f32 v24, 1.0, v24
v_add_f32 v25, 1.0, v25
v_mfma_f32_16x16x32_bf16 v[44:47], a[184:187], a[28:31], v[44:47]
s_add_u32 s55, s26, 114688
ds_read_b128 a[152:155], %[vXr2] offset:2048
v_mfma_f32_16x16x32_bf16 v[48:51], a[188:191], a[24:27], v[48:51]
ds_read_b128 a[156:159], %[vXr2] offset:3072
v_rcp_f32 v24, v24
v_mfma_f32_16x16x32_bf16 v[52:55], a[188:191], a[28:31], v[52:55]
s_and_b32 s55, s55, 524287
s_or_b32 s55, s55, s74
buffer_load_dwordx4 a[56:59], %[vY4], %[sWS], s55 offen offset:0 sc1
v_mfma_f32_16x16x32_bf16 v[56:59], a[192:195], a[24:27], v[56:59]
ds_read_b128 a[160:163], %[vXr2] offset:4096
v_rcp_f32_e64 v25, v25
v_mfma_f32_16x16x32_bf16 v[60:63], a[192:195], a[28:31], v[60:63]
ds_read_b128 a[164:167], %[vXr2] offset:5120
v_mul_f32_e64 v132, v132, v24
v_mfma_f32_16x16x32_bf16 v[64:67], a[196:199], a[24:27], v[64:67]
v_mul_f32_e64 v133, v133, v25
v_fma_f32 v26, v134, v134, s50
v_fma_f32 v27, v135, v135, s50
v_mfma_f32_16x16x32_bf16 v[68:71], a[196:199], a[28:31], v[68:71]
ds_read_b128 a[168:171], %[vXr2] offset:6144
v_mul_f32_e64 v26, v26, v134
v_mfma_f32_16x16x32_bf16 v[72:75], a[200:203], a[24:27], v[72:75]
ds_read_b128 a[172:175], %[vXr2] offset:7168
v_mul_f32_e64 v27, v27, v135
v_mfma_f32_16x16x32_bf16 v[76:79], a[200:203], a[28:31], v[76:79]
buffer_load_dwordx4 a[60:63], %[vY4], %[sWS], s55 offen offset:1024 sc1
v_mfma_f32_16x16x32_bf16 v[80:83], a[204:207], a[24:27], v[80:83]
ds_read_b128 a[176:179], %[vXr2] offset:8192
v_exp_f32_e64 v26, v26
v_mfma_f32_16x16x32_bf16 v[84:87], a[204:207], a[28:31], v[84:87]
v_exp_f32 v27, v27
v_add_f32 v26, 1.0, v26
v_mfma_f32_16x16x32_bf16 v[88:91], a[208:211], a[24:27], v[88:91]
buffer_load_dwordx4 %[vL16], %[sWS], s48 offen offset:2048 sc1 lds
v_add_f32_e64 v27, 1.0, v27
v_mfma_f32_16x16x32_bf16 v[92:95], a[208:211], a[28:31], v[92:95]
v_rcp_f32_e64 v26, v26
v_mfma_f32_16x16x32_bf16 v[96:99], a[212:215], a[24:27], v[96:99]
buffer_load_dwordx4 %[vL16], %[sWS], s48 offen offset:3072 sc1 lds
v_rcp_f32_e64 v27, v27
v_mfma_f32_16x16x32_bf16 v[100:103], a[212:215], a[28:31], v[100:103]
s_waitcnt vmcnt(15) lgkmcnt(0)
v_mul_f32 v134, v134, v26
v_mul_f32_e64 v135, v135, v27
v_mfma_f32_16x16x32_bf16 v[32:35], a[144:147], a[32:35], v[32:35]
s_add_u32 m0, %[sLx], 118784
ds_read_b128 a[180:183], %[vXr2] offset:9216
v_mfma_f32_16x16x32_bf16 v[36:39], a[144:147], a[36:39], v[36:39]
s_lshl1_add_u32 s48, s54, s39
ds_read_b128 a[184:187], %[vXr2] offset:10240
v_mfma_f32_16x16x32_bf16 v[40:43], a[148:151], a[32:35], v[40:43]
buffer_load_dwordx4 %[vL16], %[sWS], s48 offen offset:0 sc1 lds
v_mfma_f32_16x16x32_bf16 v[44:47], a[148:151], a[36:39], v[44:47]
s_add_u32 s55, s26, 131072
ds_read_b128 a[188:191], %[vXr2] offset:11264
v_accvgpr_read_b32 v192, a216
v_mfma_f32_16x16x32_bf16 v[48:51], a[152:155], a[32:35], v[48:51]
ds_read_b128 a[192:195], %[vXr2] offset:12288
v_mfma_f32_16x16x32_bf16 v[52:55], a[152:155], a[36:39], v[52:55]
s_and_b32 s55, s55, 524287
s_or_b32 s55, s55, s74
buffer_load_dwordx4 a[64:67], %[vY4], %[sWS], s55 offen offset:0 sc1
v_accvgpr_read_b32 v193, a220
v_mfma_f32_16x16x32_bf16 v[56:59], a[156:159], a[32:35], v[56:59]
ds_read_b128 a[196:199], %[vXr2] offset:13312
v_accvgpr_read_b32 v194, a224
v_mfma_f32_16x16x32_bf16 v[60:63], a[156:159], a[36:39], v[60:63]
ds_read_b128 a[200:203], %[vXr2] offset:14336
v_mfma_f32_16x16x32_bf16 v[64:67], a[160:163], a[32:35], v[64:67]
v_accvgpr_read_b32 v195, a228
v_accvgpr_read_b32 v196, a232
v_mfma_f32_16x16x32_bf16 v[68:71], a[160:163], a[36:39], v[68:71]
ds_read_b128 a[204:207], %[vXr2] offset:15360
v_accvgpr_read_b32 v197, a236
v_mfma_f32_16x16x32_bf16 v[72:75], a[164:167], a[32:35], v[72:75]
ds_read_b128 a[208:211], %[vXr2] offset:16384
v_accvgpr_read_b32 v198, a240
v_mfma_f32_16x16x32_bf16 v[76:79], a[164:167], a[36:39], v[76:79]
buffer_load_dwordx4 a[68:71], %[vY4], %[sWS], s55 offen offset:1024 sc1
v_mfma_f32_16x16x32_bf16 v[80:83], a[168:171], a[32:35], v[80:83]
s_add_u32 m0, %[sLx], 0
ds_read_b128 a[212:215], %[vXr2] offset:17408
v_accvgpr_read_b32 v199, a244
v_mfma_f32_16x16x32_bf16 v[84:87], a[168:171], a[36:39], v[84:87]
v_accvgpr_read_b32 v200, a248
v_accvgpr_read_b32 v201, a252
v_mfma_f32_16x16x32_bf16 v[88:91], a[172:175], a[32:35], v[88:91]
s_add_u32 s54, s26, 196608
s_and_b32 s54, s54, 458752
s_add_u32 s48, s54, s38
buffer_load_dwordx4 %[vL16], %[sWS], s48 offen offset:0 sc1 lds
v_mfma_f32_16x16x32_bf16 v[92:95], a[172:175], a[36:39], v[92:95]
v_min3_u32 v230, v192, v193, v194
v_min3_u32 v230, v230, v195, v196
v_mfma_f32_16x16x32_bf16 v[96:99], a[176:179], a[32:35], v[96:99]
buffer_load_dwordx4 %[vL16], %[sWS], s48 offen offset:1024 sc1 lds
v_min3_u32 v230, v230, v197, v198
v_mfma_f32_16x16x32_bf16 v[100:103], a[176:179], a[36:39], v[100:103]
s_waitcnt vmcnt(16) lgkmcnt(0)
v_min3_u32 v230, v230, v199, v200
v_min_u32 v230, v230, v201
v_mfma_f32_16x16x32_bf16 v[32:35], a[180:183], a[40:43], v[32:35]
s_add_u32 s55, s26, 147456
ds_read_b128 a[144:147], %[vXr2] offset:18432
v_mfma_f32_16x16x32_bf16 v[36:39], a[180:183], a[44:47], v[36:39]
ds_read_b128 a[148:151], %[vXr2] offset:19456
v_mfma_f32_16x16x32_bf16 v[40:43], a[184:187], a[40:43], v[40:43]
s_and_b32 s55, s55, 524287
s_or_b32 s55, s55, s74
buffer_load_dwordx4 a[72:75], %[vY4], %[sWS], s55 offen offset:0 sc1
v_mfma_f32_16x16x32_bf16 v[44:47], a[184:187], a[44:47], v[44:47]
ds_read_b128 a[152:155], %[vXr2] offset:20480
v_cmp_eq_u32 vcc, 0, v230
s_cbranch_vccnz L_sl7_%=
L_ok7_%=:
v_mfma_f32_16x16x32_bf16 v[48:51], a[188:191], a[40:43], v[48:51]
ds_read_b128 a[156:159], %[vXr2] offset:21504
v_mfma_f32_16x16x32_bf16 v[52:55], a[188:191], a[44:47], v[52:55]
v_accvgpr_read_b32 v192, a216
v_accvgpr_read_b32 v193, a217
v_mfma_f32_16x16x32_bf16 v[56:59], a[192:195], a[40:43], v[56:59]
ds_read_b128 a[160:163], %[vXr2] offset:22528
v_accvgpr_read_b32 v194, a218
v_mfma_f32_16x16x32_bf16 v[60:63], a[192:195], a[44:47], v[60:63]
ds_read_b128 a[164:167], %[vXr2] offset:23552
v_mfma_f32_16x16x32_bf16 v[64:67], a[196:199], a[40:43], v[64:67]
buffer_load_dwordx4 a[76:79], %[vY4], %[sWS], s55 offen offset:1024 sc1
)"
R"(v_mfma_f32_16x16x32_bf16 v[68:71], a[196:199], a[44:47], v[68:71]
ds_read_b128 a[168:171], %[vXr2] offset:24576
v_accvgpr_read_b32 v195, a219
v_mfma_f32_16x16x32_bf16 v[72:75], a[200:203], a[40:43], v[72:75]
ds_read_b128 a[172:175], %[vXr2] offset:25600
v_mfma_f32_16x16x32_bf16 v[76:79], a[200:203], a[44:47], v[76:79]
v_accvgpr_read_b32 v196, a220
v_accvgpr_read_b32 v197, a221
v_mfma_f32_16x16x32_bf16 v[80:83], a[204:207], a[40:43], v[80:83]
ds_read_b128 a[176:179], %[vXr2] offset:26624
v_accvgpr_read_b32 v198, a222
v_mfma_f32_16x16x32_bf16 v[84:87], a[204:207], a[44:47], v[84:87]
buffer_load_dwordx4 %[vL16], %[sWS], s48 offen offset:2048 sc1 lds
v_mfma_f32_16x16x32_bf16 v[88:91], a[208:211], a[40:43], v[88:91]
v_accvgpr_read_b32 v199, a223
v_fma_f32 v230, v227, v192, v193
v_mfma_f32_16x16x32_bf16 v[92:95], a[208:211], a[44:47], v[92:95]
buffer_load_dwordx4 %[vL16], %[sWS], s48 offen offset:3072 sc1 lds
v_mfma_f32_16x16x32_bf16 v[96:99], a[212:215], a[40:43], v[96:99]
v_fma_f32 v192, v229, v192, v193
v_fma_f32 v136, v136, v226, v230
v_mfma_f32_16x16x32_bf16 v[100:103], a[212:215], a[44:47], v[100:103]
s_waitcnt vmcnt(16) lgkmcnt(0)
v_fma_f32 v140, v140, v228, v192
v_fma_f32 v231, v227, v194, v195
v_mfma_f32_16x16x32_bf16 v[32:35], a[144:147], a[48:51], v[32:35]
s_add_u32 s55, s26, 163840
ds_read_b128 a[180:183], %[vXr2] offset:27648
v_mfma_f32_16x16x32_bf16 v[36:39], a[144:147], a[52:55], v[36:39]
LPb_5:
s_cbranch_execz LPb_9
ds_read_b128 a[184:187], %[vXr2] offset:28672
v_fma_f32 v194, v229, v194, v195
v_mfma_f32_16x16x32_bf16 v[40:43], a[148:151], a[48:51], v[40:43]
s_and_b32 s55, s55, 524287
s_or_b32 s55, s55, s74
buffer_load_dwordx4 a[80:83], %[vY4], %[sWS], s55 offen offset:0 sc1
v_mfma_f32_16x16x32_bf16 v[44:47], a[148:151], a[52:55], v[44:47]
ds_read_b128 a[188:191], %[vXr2] offset:29696
v_fma_f32 v137, v137, v226, v231
v_mfma_f32_16x16x32_bf16 v[48:51], a[152:155], a[48:51], v[48:51]
ds_read_b128 a[192:195], %[vXr2] offset:30720
v_mfma_f32_16x16x32_bf16 v[52:55], a[152:155], a[52:55], v[52:55]
v_fma_f32 v141, v141, v228, v194
v_fma_f32 v230, v227, v196, v197
v_mfma_f32_16x16x32_bf16 v[56:59], a[156:159], a[48:51], v[56:59]
ds_read_b128 a[196:199], %[vXr2] offset:31744
v_fma_f32 v196, v229, v196, v197
v_mfma_f32_16x16x32_bf16 v[60:63], a[156:159], a[52:55], v[60:63]
ds_read_b128 a[200:203], %[vXr2] offset:32768
v_mfma_f32_16x16x32_bf16 v[64:67], a[160:163], a[48:51], v[64:67]
buffer_load_dwordx4 a[84:87], %[vY4], %[sWS], s55 offen offset:1024 sc1
v_mfma_f32_16x16x32_bf16 v[68:71], a[160:163], a[52:55], v[68:71]
ds_read_b128 a[204:207], %[vXr2] offset:33792
v_fma_f32 v138, v138, v226, v230
v_mfma_f32_16x16x32_bf16 v[72:75], a[164:167], a[48:51], v[72:75]
ds_read_b128 a[208:211], %[vXr2] offset:34816
v_mfma_f32_16x16x32_bf16 v[76:79], a[164:167], a[52:55], v[76:79]
s_add_u32 m0, %[sLx], 4096
v_fma_f32 v142, v142, v228, v196
v_fma_f32 v231, v227, v198, v199
v_mfma_f32_16x16x32_bf16 v[80:83], a[168:171], a[48:51], v[80:83]
s_add_u32 s48, s48, 4096
ds_read_b128 a[212:215], %[vXr2] offset:35840
v_fma_f32 v198, v229, v198, v199
v_mfma_f32_16x16x32_bf16 v[84:87], a[168:171], a[52:55], v[84:87]
buffer_load_dwordx4 %[vL16], %[sWS], s48 offen offset:0 sc1 lds
v_mfma_f32_16x16x32_bf16 v[88:91], a[172:175], a[48:51], v[88:91]
v_fma_f32 v139, v139, v226, v231
v_fma_f32 v143, v143, v228, v198
v_mfma_f32_16x16x32_bf16 v[92:95], a[172:175], a[52:55], v[92:95]
buffer_load_dwordx4 %[vL16], %[sWS], s48 offen offset:1024 sc1 lds
v_mfma_f32_16x16x32_bf16 v[96:99], a[176:179], a[48:51], v[96:99]
s_waitcnt vmcnt(17)
v_cmp_eq_u32_e64 vcc, 0, v252
s_cbranch_vccnz L_sl16_%=
L_ok16_%=:
v_fma_f32 v24, v136, v136, s50
v_fma_f32 v25, v137, v137, s50
v_mfma_f32_16x16x32_bf16 v[100:103], a[176:179], a[52:55], v[100:103]
s_waitcnt vmcnt(12) lgkmcnt(0)
s_barrier
v_mul_f32 v24, v24, v136
v_mul_f32 v25, v25, v137
v_mfma_f32_16x16x32_bf16 v[32:35], a[180:183], a[56:59], v[32:35]
v_lshlrev_b32 v251, 3, %[vL16]
ds_read_b128 a[144:147], %[vXr3] offset:0
v_mfma_f32_16x16x32_bf16 v[36:39], a[180:183], a[60:63], v[36:39]
ds_read_b128 a[148:151], %[vXr3] offset:1024
v_mfma_f32_16x16x32_bf16 v[40:43], a[184:187], a[56:59], v[40:43]
v_min_u32 v251, 896, v251
s_add_u32 s57, s26, s72
buffer_load_dword v255, v251, %[sWS], s57 offen offset:0 sc1
v_mfma_f32_16x16x32_bf16 v[44:47], a[184:187], a[60:63], v[44:47]
s_add_u32 s55, s26, 180224
ds_read_b128 a[152:155], %[vXr3] offset:2048
v_exp_f32 v24, v24
v_mfma_f32_16x16x32_bf16 v[48:51], a[188:191], a[56:59], v[48:51]
ds_read_b128 a[156:159], %[vXr3] offset:3072
v_mfma_f32_16x16x32_bf16 v[52:55], a[188:191], a[60:63], v[52:55]
s_and_b32 s55, s55, 524287
s_or_b32 s55, s55, s74
buffer_load_dwordx4 a[88:91], %[vY4], %[sWS], s55 offen offset:0 sc1
v_mfma_f32_16x16x32_bf16 v[56:59], a[192:195], a[56:59], v[56:59]
ds_read_b128 a[160:163], %[vXr3] offset:4096
v_mfma_f32_16x16x32_bf16 v[60:63], a[192:195], a[60:63], v[60:63]
ds_read_b128 a[164:167], %[vXr3] offset:5120
v_exp_f32_e64 v25, v25
v_mfma_f32_16x16x32_bf16 v[64:67], a[196:199], a[56:59], v[64:67]
v_add_f32 v24, 1.0, v24
v_add_f32 v25, 1.0, v25
v_mfma_f32_16x16x32_bf16 v[68:71], a[196:199], a[60:63], v[68:71]
ds_read_b128 a[168:171], %[vXr3] offset:6144
v_mfma_f32_16x16x32_bf16 v[72:75], a[200:203], a[56:59], v[72:75]
ds_read_b128 a[172:175], %[vXr3] offset:7168
v_mfma_f32_16x16x32_bf16 v[76:79], a[200:203], a[60:63], v[76:79]
buffer_load_dwordx4 a[92:95], %[vY4], %[sWS], s55 offen offset:1024 sc1
v_mfma_f32_16x16x32_bf16 v[80:83], a[204:207], a[56:59], v[80:83]
ds_read_b128 a[176:179], %[vXr3] offset:8192
v_rcp_f32_e64 v24, v24
v_mfma_f32_16x16x32_bf16 v[84:87], a[204:207], a[60:63], v[84:87]
v_rcp_f32_e64 v25, v25
v_mfma_f32_16x16x32_bf16 v[88:91], a[208:211], a[56:59], v[88:91]
buffer_load_dwordx4 %[vL16], %[sWS], s48 offen offset:2048 sc1 lds
v_mfma_f32_16x16x32_bf16 v[92:95], a[208:211], a[60:63], v[92:95]
v_mul_f32 v136, v136, v24
v_mul_f32 v137, v137, v25
v_mfma_f32_16x16x32_bf16 v[96:99], a[212:215], a[56:59], v[96:99]
buffer_load_dwordx4 %[vL16], %[sWS], s48 offen offset:3072 sc1 lds
v_fma_f32 v26, v138, v138, s50
v_mfma_f32_16x16x32_bf16 v[100:103], a[212:215], a[60:63], v[100:103]
s_waitcnt vmcnt(15) lgkmcnt(0)
v_fma_f32 v27, v139, v139, s50
v_mul_f32 v26, v26, v138
v_mfma_f32_16x16x32_bf16 v[32:35], a[144:147], a[64:67], v[32:35]
s_add_u32 m0, %[sLx], 8192
ds_read_b128 a[180:183], %[vXr3] offset:9216
v_mfma_f32_16x16x32_bf16 v[36:39], a[144:147], a[68:71], v[36:39]
s_lshl1_add_u32 s48, s54, s39
ds_read_b128 a[184:187], %[vXr3] offset:10240
v_mul_f32 v27, v27, v139
v_mfma_f32_16x16x32_bf16 v[40:43], a[148:151], a[64:67], v[40:43]
buffer_load_dwordx4 %[vL16], %[sWS], s48 offen offset:0 sc1 lds
v_mfma_f32_16x16x32_bf16 v[44:47], a[148:151], a[68:71], v[44:47]
s_add_u32 s55, s26, 196608
ds_read_b128 a[188:191], %[vXr3] offset:11264
v_mfma_f32_16x16x32_bf16 v[48:51], a[152:155], a[64:67], v[48:51]
ds_read_b128 a[192:195], %[vXr3] offset:12288
v_mfma_f32_16x16x32_bf16 v[52:55], a[152:155], a[68:71], v[52:55]
s_and_b32 s55, s55, 524287
s_or_b32 s55, s55, s74
buffer_load_dwordx4 a[96:99], %[vY4], %[sWS], s55 offen offset:0 sc1
v_exp_f32 v26, v26
v_mfma_f32_16x16x32_bf16 v[56:59], a[156:159], a[64:67], v[56:59]
ds_read_b128 a[196:199], %[vXr3] offset:13312
v_mfma_f32_16x16x32_bf16 v[60:63], a[156:159], a[68:71], v[60:63]
ds_read_b128 a[200:203], %[vXr3] offset:14336
v_mfma_f32_16x16x32_bf16 v[64:67], a[160:163], a[64:67], v[64:67]
v_exp_f32 v27, v27
v_add_f32 v26, 1.0, v26
v_mfma_f32_16x16x32_bf16 v[68:71], a[160:163], a[68:71], v[68:71]
ds_read_b128 a[204:207], %[vXr3] offset:15360
v_mfma_f32_16x16x32_bf16 v[72:75], a[164:167], a[64:67], v[72:75]
ds_read_b128 a[208:211], %[vXr3] offset:16384
v_add_f32_e64 v27, 1.0, v27
v_mfma_f32_16x16x32_bf16 v[76:79], a[164:167], a[68:71], v[76:79]
buffer_load_dwordx4 a[100:103], %[vY4], %[sWS], s55 offen offset:1024 sc1
v_mfma_f32_16x16x32_bf16 v[80:83], a[168:171], a[64:67], v[80:83]
s_add_u32 m0, %[sLx], 36864
ds_read_b128 a[212:215], %[vXr3] offset:17408
v_mfma_f32_16x16x32_bf16 v[84:87], a[168:171], a[68:71], v[84:87]
v_rcp_f32_e64 v26, v26
v_mfma_f32_16x16x32_bf16 v[88:91], a[172:175], a[64:67], v[88:91]
s_add_u32 s54, s26, 262144
s_and_b32 s54, s54, 458752
s_add_u32 s48, s54, s38
buffer_load_dwordx4 %[vL16], %[sWS], s48 offen offset:0 sc1 lds
v_mfma_f32_16x16x32_bf16 v[92:95], a[172:175], a[68:71], v[92:95]
v_rcp_f32 v27, v27
v_mul_f32_e64 v138, v138, v26
v_mfma_f32_16x16x32_bf16 v[96:99], a[176:179], a[64:67], v[96:99]
buffer_load_dwordx4 %[vL16], %[sWS], s48 offen offset:1024 sc1 lds
v_mul_f32 v139, v139, v27
v_mfma_f32_16x16x32_bf16 v[100:103], a[176:179], a[68:71], v[100:103]
s_waitcnt vmcnt(16) lgkmcnt(0)
v_fma_f32 v24, v140, v140, s50
v_fma_f32 v25, v141, v141, s50
v_mfma_f32_16x16x32_bf16 v[32:35], a[180:183], a[72:75], v[32:35]
ds_read_b128 a[144:147], %[vXr3] offset:18432
v_mfma_f32_16x16x32_bf16 v[36:39], a[180:183], a[76:79], v[36:39]
s_add_u32 s57, s57, 1024
ds_read_b128 a[148:151], %[vXr3] offset:19456
v_mul_f32_e64 v24, v24, v140
v_mfma_f32_16x16x32_bf16 v[40:43], a[184:187], a[72:75], v[40:43]
buffer_load_dword v255, v251, %[sWS], s57 offen offset:0 sc1
v_mfma_f32_16x16x32_bf16 v[44:47], a[184:187], a[76:79], v[44:47]
s_add_u32 s55, s26, 212992
ds_read_b128 a[152:155], %[vXr3] offset:20480
v_mul_f32 v25, v25, v141
v_mfma_f32_16x16x32_bf16 v[48:51], a[188:191], a[72:75], v[48:51]
ds_read_b128 a[156:159], %[vXr3] offset:21504
)"
R"(v_mfma_f32_16x16x32_bf16 v[52:55], a[188:191], a[76:79], v[52:55]
s_and_b32 s55, s55, 524287
s_or_b32 s55, s55, s74
buffer_load_dwordx4 a[104:107], %[vY4], %[sWS], s55 offen offset:0 sc1
v_mfma_f32_16x16x32_bf16 v[56:59], a[192:195], a[72:75], v[56:59]
ds_read_b128 a[160:163], %[vXr3] offset:22528
v_mfma_f32_16x16x32_bf16 v[60:63], a[192:195], a[76:79], v[60:63]
ds_read_b128 a[164:167], %[vXr3] offset:23552
v_exp_f32_e64 v24, v24
v_mfma_f32_16x16x32_bf16 v[64:67], a[196:199], a[72:75], v[64:67]
v_exp_f32_e64 v25, v25
v_mfma_f32_16x16x32_bf16 v[68:71], a[196:199], a[76:79], v[68:71]
ds_read_b128 a[168:171], %[vXr3] offset:24576
v_mfma_f32_16x16x32_bf16 v[72:75], a[200:203], a[72:75], v[72:75]
ds_read_b128 a[172:175], %[vXr3] offset:25600
v_add_f32_e64 v24, 1.0, v24
v_mfma_f32_16x16x32_bf16 v[76:79], a[200:203], a[76:79], v[76:79]
buffer_load_dwordx4 a[108:111], %[vY4], %[sWS], s55 offen offset:1024 sc1
v_mfma_f32_16x16x32_bf16 v[80:83], a[204:207], a[72:75], v[80:83]
ds_read_b128 a[176:179], %[vXr3] offset:26624
v_add_f32_e64 v25, 1.0, v25
v_mfma_f32_16x16x32_bf16 v[84:87], a[204:207], a[76:79], v[84:87]
v_rcp_f32_e64 v24, v24
v_mfma_f32_16x16x32_bf16 v[88:91], a[208:211], a[72:75], v[88:91]
buffer_load_dwordx4 %[vL16], %[sWS], s48 offen offset:2048 sc1 lds
v_mfma_f32_16x16x32_bf16 v[92:95], a[208:211], a[76:79], v[92:95]
v_rcp_f32_e64 v25, v25
v_mfma_f32_16x16x32_bf16 v[96:99], a[212:215], a[72:75], v[96:99]
buffer_load_dwordx4 %[vL16], %[sWS], s48 offen offset:3072 sc1 lds
v_mul_f32_e64 v140, v140, v24
v_mfma_f32_16x16x32_bf16 v[100:103], a[212:215], a[76:79], v[100:103]
s_waitcnt vmcnt(17) lgkmcnt(0)
v_mul_f32 v141, v141, v25
v_fma_f32 v26, v142, v142, s50
v_mfma_f32_16x16x32_bf16 v[32:35], a[144:147], a[80:83], v[32:35]
s_add_u32 s55, s26, 229376
ds_read_b128 a[180:183], %[vXr3] offset:27648
v_mfma_f32_16x16x32_bf16 v[36:39], a[144:147], a[84:87], v[36:39]
ds_read_b128 a[184:187], %[vXr3] offset:28672
v_fma_f32 v27, v143, v143, s50
v_mfma_f32_16x16x32_bf16 v[40:43], a[148:151], a[80:83], v[40:43]
s_and_b32 s55, s55, 524287
s_or_b32 s55, s55, s74
buffer_load_dwordx4 a[112:115], %[vY4], %[sWS], s55 offen offset:0 sc1
v_mfma_f32_16x16x32_bf16 v[44:47], a[148:151], a[84:87], v[44:47]
ds_read_b128 a[188:191], %[vXr3] offset:29696
v_mul_f32 v26, v26, v142
v_mfma_f32_16x16x32_bf16 v[48:51], a[152:155], a[80:83], v[48:51]
ds_read_b128 a[192:195], %[vXr3] offset:30720
v_mfma_f32_16x16x32_bf16 v[52:55], a[152:155], a[84:87], v[52:55]
v_mul_f32_e64 v27, v27, v143
v_mfma_f32_16x16x32_bf16 v[56:59], a[156:159], a[80:83], v[56:59]
ds_read_b128 a[196:199], %[vXr3] offset:31744
v_exp_f32_e64 v26, v26
v_mfma_f32_16x16x32_bf16 v[60:63], a[156:159], a[84:87], v[60:63]
ds_read_b128 a[200:203], %[vXr3] offset:32768
v_mfma_f32_16x16x32_bf16 v[64:67], a[160:163], a[80:83], v[64:67]
buffer_load_dwordx4 a[116:119], %[vY4], %[sWS], s55 offen offset:1024 sc1
v_mfma_f32_16x16x32_bf16 v[68:71], a[160:163], a[84:87], v[68:71]
ds_read_b128 a[204:207], %[vXr3] offset:33792
v_mfma_f32_16x16x32_bf16 v[72:75], a[164:167], a[80:83], v[72:75]
ds_read_b128 a[208:211], %[vXr3] offset:34816
v_mfma_f32_16x16x32_bf16 v[76:79], a[164:167], a[84:87], v[76:79]
s_add_u32 m0, %[sLx], 40960
v_exp_f32 v27, v27
v_add_f32 v26, 1.0, v26
v_mfma_f32_16x16x32_bf16 v[80:83], a[168:171], a[80:83], v[80:83]
s_add_u32 s48, s48, 4096
ds_read_b128 a[212:215], %[vXr3] offset:35840
v_add_f32_e64 v27, 1.0, v27
v_mfma_f32_16x16x32_bf16 v[84:87], a[168:171], a[84:87], v[84:87]
buffer_load_dwordx4 %[vL16], %[sWS], s48 offen offset:0 sc1 lds
v_mfma_f32_16x16x32_bf16 v[88:91], a[172:175], a[80:83], v[88:91]
v_rcp_f32_e64 v26, v26
v_mfma_f32_16x16x32_bf16 v[92:95], a[172:175], a[84:87], v[92:95]
buffer_load_dwordx4 %[vL16], %[sWS], s48 offen offset:1024 sc1 lds
v_mfma_f32_16x16x32_bf16 v[96:99], a[176:179], a[80:83], v[96:99]
v_rcp_f32_e64 v27, v27
v_mfma_f32_16x16x32_bf16 v[100:103], a[176:179], a[84:87], v[100:103]
s_waitcnt vmcnt(13) lgkmcnt(0)
s_barrier
v_mul_f32 v142, v142, v26
v_mul_f32 v143, v143, v27
v_mfma_f32_16x16x32_bf16 v[32:35], a[180:183], a[88:91], v[32:35]
ds_read_b128 a[144:147], %[vXr0] offset:0
v_mfma_f32_16x16x32_bf16 v[36:39], a[180:183], a[92:95], v[36:39]
s_add_u32 s57, s57, 1024
ds_read_b128 a[148:151], %[vXr0] offset:1024
v_accvgpr_read_b32 v200, a224
v_mfma_f32_16x16x32_bf16 v[40:43], a[184:187], a[88:91], v[40:43]
buffer_load_dword v255, v251, %[sWS], s57 offen offset:0 sc1
v_mfma_f32_16x16x32_bf16 v[44:47], a[184:187], a[92:95], v[44:47]
s_add_u32 s55, s26, 245760
ds_read_b128 a[152:155], %[vXr0] offset:2048
v_accvgpr_read_b32 v201, a225
v_mfma_f32_16x16x32_bf16 v[48:51], a[188:191], a[88:91], v[48:51]
ds_read_b128 a[156:159], %[vXr0] offset:3072
v_mfma_f32_16x16x32_bf16 v[52:55], a[188:191], a[92:95], v[52:55]
s_and_b32 s55, s55, 524287
s_or_b32 s55, s55, s74
buffer_load_dwordx4 a[120:123], %[vY4], %[sWS], s55 offen offset:0 sc1
v_accvgpr_read_b32 v202, a226
v_mfma_f32_16x16x32_bf16 v[56:59], a[192:195], a[88:91], v[56:59]
ds_read_b128 a[160:163], %[vXr0] offset:4096
v_mfma_f32_16x16x32_bf16 v[60:63], a[192:195], a[92:95], v[60:63]
ds_read_b128 a[164:167], %[vXr0] offset:5120
v_accvgpr_read_b32 v203, a227
v_mfma_f32_16x16x32_bf16 v[64:67], a[196:199], a[88:91], v[64:67]
v_accvgpr_read_b32 v204, a228
v_accvgpr_read_b32 v205, a229
v_mfma_f32_16x16x32_bf16 v[68:71], a[196:199], a[92:95], v[68:71]
ds_read_b128 a[168:171], %[vXr0] offset:6144
v_mfma_f32_16x16x32_bf16 v[72:75], a[200:203], a[88:91], v[72:75]
ds_read_b128 a[172:175], %[vXr0] offset:7168
v_accvgpr_read_b32 v206, a230
v_mfma_f32_16x16x32_bf16 v[76:79], a[200:203], a[92:95], v[76:79]
buffer_load_dwordx4 a[124:127], %[vY4], %[sWS], s55 offen offset:1024 sc1
v_mfma_f32_16x16x32_bf16 v[80:83], a[204:207], a[88:91], v[80:83]
ds_read_b128 a[176:179], %[vXr0] offset:8192
v_accvgpr_read_b32 v207, a231
v_mfma_f32_16x16x32_bf16 v[84:87], a[204:207], a[92:95], v[84:87]
v_fma_f32 v230, v227, v200, v201
v_fma_f32 v200, v229, v200, v201
v_mfma_f32_16x16x32_bf16 v[88:91], a[208:211], a[88:91], v[88:91]
buffer_load_dwordx4 %[vL16], %[sWS], s48 offen offset:2048 sc1 lds
v_mfma_f32_16x16x32_bf16 v[92:95], a[208:211], a[92:95], v[92:95]
v_fma_f32 v144, v144, v226, v230
v_fma_f32 v148, v148, v228, v200
v_mfma_f32_16x16x32_bf16 v[96:99], a[212:215], a[88:91], v[96:99]
buffer_load_dwordx4 %[vL16], %[sWS], s48 offen offset:3072 sc1 lds
v_fma_f32 v231, v227, v202, v203
v_mfma_f32_16x16x32_bf16 v[100:103], a[212:215], a[92:95], v[100:103]
s_waitcnt vmcnt(16) lgkmcnt(0)
v_fma_f32 v202, v229, v202, v203
v_fma_f32 v145, v145, v226, v231
v_mfma_f32_16x16x32_bf16 v[32:35], a[144:147], a[96:99], v[32:35]
s_add_u32 m0, %[sLx], 45056
ds_read_b128 a[180:183], %[vXr0] offset:9216
v_mfma_f32_16x16x32_bf16 v[36:39], a[144:147], a[100:103], v[36:39]
s_lshl1_add_u32 s48, s54, s39
ds_read_b128 a[184:187], %[vXr0] offset:10240
v_fma_f32 v149, v149, v228, v202
v_mfma_f32_16x16x32_bf16 v[40:43], a[148:151], a[96:99], v[40:43]
buffer_load_dwordx4 %[vL16], %[sWS], s48 offen offset:0 sc1 lds
v_mfma_f32_16x16x32_bf16 v[44:47], a[148:151], a[100:103], v[44:47]
s_add_u32 s55, s26, 262144
ds_read_b128 a[188:191], %[vXr0] offset:11264
v_fma_f32 v230, v227, v204, v205
v_mfma_f32_16x16x32_bf16 v[48:51], a[152:155], a[96:99], v[48:51]
ds_read_b128 a[192:195], %[vXr0] offset:12288
v_mfma_f32_16x16x32_bf16 v[52:55], a[152:155], a[100:103], v[52:55]
s_and_b32 s55, s55, 524287
s_or_b32 s55, s55, s74
buffer_load_dwordx4 a[0:3], %[vY4], %[sWS], s55 offen offset:0 sc1
v_fma_f32 v204, v229, v204, v205
v_mfma_f32_16x16x32_bf16 v[56:59], a[156:159], a[96:99], v[56:59]
ds_read_b128 a[196:199], %[vXr0] offset:13312
v_mfma_f32_16x16x32_bf16 v[60:63], a[156:159], a[100:103], v[60:63]
ds_read_b128 a[200:203], %[vXr0] offset:14336
v_fma_f32 v146, v146, v226, v230
v_mfma_f32_16x16x32_bf16 v[64:67], a[160:163], a[96:99], v[64:67]
v_fma_f32 v150, v150, v228, v204
v_fma_f32 v231, v227, v206, v207
v_mfma_f32_16x16x32_bf16 v[68:71], a[160:163], a[100:103], v[68:71]
ds_read_b128 a[204:207], %[vXr0] offset:15360
v_mfma_f32_16x16x32_bf16 v[72:75], a[164:167], a[96:99], v[72:75]
ds_read_b128 a[208:211], %[vXr0] offset:16384
v_fma_f32 v206, v229, v206, v207
v_mfma_f32_16x16x32_bf16 v[76:79], a[164:167], a[100:103], v[76:79]
buffer_load_dwordx4 a[4:7], %[vY4], %[sWS], s55 offen offset:1024 sc1
v_mfma_f32_16x16x32_bf16 v[80:83], a[168:171], a[96:99], v[80:83]
s_add_u32 m0, %[sLx], 73728
ds_read_b128 a[212:215], %[vXr0] offset:17408
v_fma_f32 v147, v147, v226, v231
v_mfma_f32_16x16x32_bf16 v[84:87], a[168:171], a[100:103], v[84:87]
v_fma_f32 v151, v151, v228, v206
v_fma_f32 v24, v144, v144, s50
v_mfma_f32_16x16x32_bf16 v[88:91], a[172:175], a[96:99], v[88:91]
s_add_u32 s54, s26, 327680
s_and_b32 s54, s54, 458752
s_add_u32 s48, s54, s38
buffer_load_dwordx4 %[vL16], %[sWS], s48 offen offset:0 sc1 lds
v_mfma_f32_16x16x32_bf16 v[92:95], a[172:175], a[100:103], v[92:95]
v_fma_f32 v25, v145, v145, s50
v_mul_f32 v24, v24, v144
v_mfma_f32_16x16x32_bf16 v[96:99], a[176:179], a[96:99], v[96:99]
buffer_load_dwordx4 %[vL16], %[sWS], s48 offen offset:1024 sc1 lds
v_mul_f32_e64 v25, v25, v145
v_mfma_f32_16x16x32_bf16 v[100:103], a[176:179], a[100:103], v[100:103]
s_waitcnt vmcnt(16) lgkmcnt(0)
v_exp_f32 v24, v24
v_mfma_f32_16x16x32_bf16 v[32:35], a[180:183], a[104:107], v[32:35]
ds_read_b128 a[144:147], %[vXr0] offset:18432
v_mfma_f32_16x16x32_bf16 v[36:39], a[180:183], a[108:111], v[36:39]
s_add_u32 s57, s57, 1024
ds_read_b128 a[148:151], %[vXr0] offset:19456
v_mfma_f32_16x16x32_bf16 v[40:43], a[184:187], a[104:107], v[40:43]
buffer_load_dword v255, v251, %[sWS], s57 offen offset:0 sc1
v_mfma_f32_16x16x32_bf16 v[44:47], a[184:187], a[108:111], v[44:47]
s_add_u32 s55, s26, 278528
)"
R"(ds_read_b128 a[152:155], %[vXr0] offset:20480
v_exp_f32_e64 v25, v25
v_mfma_f32_16x16x32_bf16 v[48:51], a[188:191], a[104:107], v[48:51]
ds_read_b128 a[156:159], %[vXr0] offset:21504
v_mfma_f32_16x16x32_bf16 v[52:55], a[188:191], a[108:111], v[52:55]
s_and_b32 s55, s55, 524287
s_or_b32 s55, s55, s74
buffer_load_dwordx4 a[8:11], %[vY4], %[sWS], s55 offen offset:0 sc1
v_add_f32 v24, 1.0, v24
v_mfma_f32_16x16x32_bf16 v[56:59], a[192:195], a[104:107], v[56:59]
ds_read_b128 a[160:163], %[vXr0] offset:22528
v_mfma_f32_16x16x32_bf16 v[60:63], a[192:195], a[108:111], v[60:63]
ds_read_b128 a[164:167], %[vXr0] offset:23552
v_add_f32_e64 v25, 1.0, v25
v_mfma_f32_16x16x32_bf16 v[64:67], a[196:199], a[104:107], v[64:67]
v_rcp_f32_e64 v24, v24
v_mfma_f32_16x16x32_bf16 v[68:71], a[196:199], a[108:111], v[68:71]
ds_read_b128 a[168:171], %[vXr0] offset:24576
v_mfma_f32_16x16x32_bf16 v[72:75], a[200:203], a[104:107], v[72:75]
ds_read_b128 a[172:175], %[vXr0] offset:25600
v_mfma_f32_16x16x32_bf16 v[76:79], a[200:203], a[108:111], v[76:79]
buffer_load_dwordx4 a[12:15], %[vY4], %[sWS], s55 offen offset:1024 sc1
v_mfma_f32_16x16x32_bf16 v[80:83], a[204:207], a[104:107], v[80:83]
ds_read_b128 a[176:179], %[vXr0] offset:26624
v_rcp_f32_e64 v25, v25
v_mfma_f32_16x16x32_bf16 v[84:87], a[204:207], a[108:111], v[84:87]
v_mul_f32 v144, v144, v24
v_mul_f32 v145, v145, v25
v_mfma_f32_16x16x32_bf16 v[88:91], a[208:211], a[104:107], v[88:91]
buffer_load_dwordx4 %[vL16], %[sWS], s48 offen offset:2048 sc1 lds
v_mfma_f32_16x16x32_bf16 v[92:95], a[208:211], a[108:111], v[92:95]
v_fma_f32 v26, v146, v146, s50
v_fma_f32 v27, v147, v147, s50
v_mfma_f32_16x16x32_bf16 v[96:99], a[212:215], a[104:107], v[96:99]
buffer_load_dwordx4 %[vL16], %[sWS], s48 offen offset:3072 sc1 lds
v_mul_f32_e64 v26, v26, v146
v_mfma_f32_16x16x32_bf16 v[100:103], a[212:215], a[108:111], v[100:103]
s_waitcnt vmcnt(17) lgkmcnt(0)
v_mul_f32 v27, v27, v147
v_exp_f32_e64 v26, v26
v_mfma_f32_16x16x32_bf16 v[32:35], a[144:147], a[112:115], v[32:35]
s_add_u32 s55, s26, 294912
ds_read_b128 a[180:183], %[vXr0] offset:27648
v_mfma_f32_16x16x32_bf16 v[36:39], a[144:147], a[116:119], v[36:39]
ds_read_b128 a[184:187], %[vXr0] offset:28672
v_mfma_f32_16x16x32_bf16 v[40:43], a[148:151], a[112:115], v[40:43]
s_and_b32 s55, s55, 524287
s_or_b32 s55, s55, s74
buffer_load_dwordx4 a[16:19], %[vY4], %[sWS], s55 offen offset:0 sc1
v_mfma_f32_16x16x32_bf16 v[44:47], a[148:151], a[116:119], v[44:47]
ds_read_b128 a[188:191], %[vXr0] offset:29696
v_exp_f32 v27, v27
v_mfma_f32_16x16x32_bf16 v[48:51], a[152:155], a[112:115], v[48:51]
ds_read_b128 a[192:195], %[vXr0] offset:30720
v_mfma_f32_16x16x32_bf16 v[52:55], a[152:155], a[116:119], v[52:55]
v_add_f32 v26, 1.0, v26
v_add_f32 v27, 1.0, v27
v_mfma_f32_16x16x32_bf16 v[56:59], a[156:159], a[112:115], v[56:59]
ds_read_b128 a[196:199], %[vXr0] offset:31744
v_mfma_f32_16x16x32_bf16 v[60:63], a[156:159], a[116:119], v[60:63]
ds_read_b128 a[200:203], %[vXr0] offset:32768
v_mfma_f32_16x16x32_bf16 v[64:67], a[160:163], a[112:115], v[64:67]
buffer_load_dwordx4 a[20:23], %[vY4], %[sWS], s55 offen offset:1024 sc1
v_mfma_f32_16x16x32_bf16 v[68:71], a[160:163], a[116:119], v[68:71]
ds_read_b128 a[204:207], %[vXr0] offset:33792
v_mfma_f32_16x16x32_bf16 v[72:75], a[164:167], a[112:115], v[72:75]
ds_read_b128 a[208:211], %[vXr0] offset:34816
v_rcp_f32_e64 v26, v26
v_mfma_f32_16x16x32_bf16 v[76:79], a[164:167], a[116:119], v[76:79]
s_add_u32 m0, %[sLx], 77824
v_mfma_f32_16x16x32_bf16 v[80:83], a[168:171], a[112:115], v[80:83]
s_add_u32 s48, s48, 4096
ds_read_b128 a[212:215], %[vXr0] offset:35840
v_rcp_f32_e64 v27, v27
v_mfma_f32_16x16x32_bf16 v[84:87], a[168:171], a[116:119], v[84:87]
buffer_load_dwordx4 %[vL16], %[sWS], s48 offen offset:0 sc1 lds
v_mfma_f32_16x16x32_bf16 v[88:91], a[172:175], a[112:115], v[88:91]
v_mul_f32 v146, v146, v26
v_mul_f32 v147, v147, v27
v_mfma_f32_16x16x32_bf16 v[92:95], a[172:175], a[116:119], v[92:95]
buffer_load_dwordx4 %[vL16], %[sWS], s48 offen offset:1024 sc1 lds
v_mfma_f32_16x16x32_bf16 v[96:99], a[176:179], a[112:115], v[96:99]
v_fma_f32 v24, v148, v148, s50
v_fma_f32 v25, v149, v149, s50
v_mfma_f32_16x16x32_bf16 v[100:103], a[176:179], a[116:119], v[100:103]
s_waitcnt vmcnt(13) lgkmcnt(0)
s_barrier
v_mul_f32 v24, v24, v148
v_mul_f32 v25, v25, v149
v_mfma_f32_16x16x32_bf16 v[32:35], a[180:183], a[120:123], v[32:35]
ds_read_b128 a[144:147], %[vXr1] offset:0
v_mfma_f32_16x16x32_bf16 v[36:39], a[180:183], a[124:127], v[36:39]
s_add_u32 s57, s57, 1024
ds_read_b128 a[148:151], %[vXr1] offset:1024
v_mfma_f32_16x16x32_bf16 v[40:43], a[184:187], a[120:123], v[40:43]
buffer_load_dword v255, v251, %[sWS], s57 offen offset:0 sc1
v_mfma_f32_16x16x32_bf16 v[44:47], a[184:187], a[124:127], v[44:47]
s_add_u32 s55, s26, 311296
ds_read_b128 a[152:155], %[vXr1] offset:2048
v_exp_f32 v24, v24
v_mfma_f32_16x16x32_bf16 v[48:51], a[188:191], a[120:123], v[48:51]
ds_read_b128 a[156:159], %[vXr1] offset:3072
v_mfma_f32_16x16x32_bf16 v[52:55], a[188:191], a[124:127], v[52:55]
s_and_b32 s55, s55, 524287
s_or_b32 s55, s55, s74
buffer_load_dwordx4 a[24:27], %[vY4], %[sWS], s55 offen offset:0 sc1
v_mfma_f32_16x16x32_bf16 v[56:59], a[192:195], a[120:123], v[56:59]
ds_read_b128 a[160:163], %[vXr1] offset:4096
v_mfma_f32_16x16x32_bf16 v[60:63], a[192:195], a[124:127], v[60:63]
ds_read_b128 a[164:167], %[vXr1] offset:5120
v_mfma_f32_16x16x32_bf16 v[64:67], a[196:199], a[120:123], v[64:67]
v_exp_f32 v25, v25
v_add_f32 v24, 1.0, v24
v_mfma_f32_16x16x32_bf16 v[68:71], a[196:199], a[124:127], v[68:71]
ds_read_b128 a[168:171], %[vXr1] offset:6144
v_add_f32_e64 v25, 1.0, v25
v_mfma_f32_16x16x32_bf16 v[72:75], a[200:203], a[120:123], v[72:75]
ds_read_b128 a[172:175], %[vXr1] offset:7168
v_mfma_f32_16x16x32_bf16 v[76:79], a[200:203], a[124:127], v[76:79]
buffer_load_dwordx4 a[28:31], %[vY4], %[sWS], s55 offen offset:1024 sc1
v_mfma_f32_16x16x32_bf16 v[80:83], a[204:207], a[120:123], v[80:83]
ds_read_b128 a[176:179], %[vXr1] offset:8192
v_mfma_f32_16x16x32_bf16 v[84:87], a[204:207], a[124:127], v[84:87]
v_rcp_f32_e64 v24, v24
v_mfma_f32_16x16x32_bf16 v[88:91], a[208:211], a[120:123], v[88:91]
buffer_load_dwordx4 %[vL16], %[sWS], s48 offen offset:2048 sc1 lds
v_rcp_f32_e64 v25, v25
v_mfma_f32_16x16x32_bf16 v[92:95], a[208:211], a[124:127], v[92:95]
v_mul_f32_e64 v148, v148, v24
v_mul_f32 v149, v149, v25
v_mfma_f32_16x16x32_bf16 v[96:99], a[212:215], a[120:123], v[96:99]
buffer_load_dwordx4 %[vL16], %[sWS], s48 offen offset:3072 sc1 lds
v_mfma_f32_16x16x32_bf16 v[100:103], a[212:215], a[124:127], v[100:103]
s_waitcnt vmcnt(16) lgkmcnt(0)
v_fma_f32 v26, v150, v150, s50
v_fma_f32 v27, v151, v151, s50
v_mfma_f32_16x16x32_bf16 v[32:35], a[144:147], a[0:3], v[32:35]
s_add_u32 m0, %[sLx], 81920
ds_read_b128 a[180:183], %[vXr1] offset:9216
v_mfma_f32_16x16x32_bf16 v[36:39], a[144:147], a[4:7], v[36:39]
s_lshl1_add_u32 s48, s54, s39
ds_read_b128 a[184:187], %[vXr1] offset:10240
v_mul_f32 v26, v26, v150
v_mfma_f32_16x16x32_bf16 v[40:43], a[148:151], a[0:3], v[40:43]
buffer_load_dwordx4 %[vL16], %[sWS], s48 offen offset:0 sc1 lds
v_mfma_f32_16x16x32_bf16 v[44:47], a[148:151], a[4:7], v[44:47]
s_add_u32 s55, s26, 327680
ds_read_b128 a[188:191], %[vXr1] offset:11264
v_mul_f32 v27, v27, v151
v_mfma_f32_16x16x32_bf16 v[48:51], a[152:155], a[0:3], v[48:51]
ds_read_b128 a[192:195], %[vXr1] offset:12288
v_mfma_f32_16x16x32_bf16 v[52:55], a[152:155], a[4:7], v[52:55]
s_and_b32 s55, s55, 524287
s_or_b32 s55, s55, s74
buffer_load_dwordx4 a[32:35], %[vY4], %[sWS], s55 offen offset:0 sc1
v_mfma_f32_16x16x32_bf16 v[56:59], a[156:159], a[0:3], v[56:59]
ds_read_b128 a[196:199], %[vXr1] offset:13312
v_mfma_f32_16x16x32_bf16 v[60:63], a[156:159], a[4:7], v[60:63]
ds_read_b128 a[200:203], %[vXr1] offset:14336
v_mfma_f32_16x16x32_bf16 v[64:67], a[160:163], a[0:3], v[64:67]
v_exp_f32_e64 v26, v26
v_mfma_f32_16x16x32_bf16 v[68:71], a[160:163], a[4:7], v[68:71]
ds_read_b128 a[204:207], %[vXr1] offset:15360
v_exp_f32_e64 v27, v27
v_mfma_f32_16x16x32_bf16 v[72:75], a[164:167], a[0:3], v[72:75]
ds_read_b128 a[208:211], %[vXr1] offset:16384
v_mfma_f32_16x16x32_bf16 v[76:79], a[164:167], a[4:7], v[76:79]
buffer_load_dwordx4 a[36:39], %[vY4], %[sWS], s55 offen offset:1024 sc1
v_add_f32_e64 v26, 1.0, v26
v_mfma_f32_16x16x32_bf16 v[80:83], a[168:171], a[0:3], v[80:83]
s_add_u32 m0, %[sLx], 110592
ds_read_b128 a[212:215], %[vXr1] offset:17408
v_mfma_f32_16x16x32_bf16 v[84:87], a[168:171], a[4:7], v[84:87]
v_add_f32_e64 v27, 1.0, v27
v_mfma_f32_16x16x32_bf16 v[88:91], a[172:175], a[0:3], v[88:91]
s_add_u32 s54, s26, 393216
s_and_b32 s54, s54, 458752
s_add_u32 s48, s54, s38
buffer_load_dwordx4 %[vL16], %[sWS], s48 offen offset:0 sc1 lds
v_rcp_f32 v26, v26
v_mfma_f32_16x16x32_bf16 v[92:95], a[172:175], a[4:7], v[92:95]
LPb_6:
s_cbranch_execz LPb_10
v_rcp_f32 v27, v27
v_mfma_f32_16x16x32_bf16 v[96:99], a[176:179], a[0:3], v[96:99]
buffer_load_dwordx4 %[vL16], %[sWS], s48 offen offset:1024 sc1 lds
v_mfma_f32_16x16x32_bf16 v[100:103], a[176:179], a[4:7], v[100:103]
s_waitcnt vmcnt(16) lgkmcnt(0)
v_mul_f32 v150, v150, v26
v_mul_f32 v151, v151, v27
v_mfma_f32_16x16x32_bf16 v[32:35], a[180:183], a[8:11], v[32:35]
ds_read_b128 a[144:147], %[vXr1] offset:18432
v_mfma_f32_16x16x32_bf16 v[36:39], a[180:183], a[12:15], v[36:39]
s_add_u32 s57, s57, 1024
ds_read_b128 a[148:151], %[vXr1] offset:19456
v_mfma_f32_16x16x32_bf16 v[40:43], a[184:187], a[8:11], v[40:43]
buffer_load_dword v255, v251, %[sWS], s57 offen offset:0 sc1
v_mfma_f32_16x16x32_bf16 v[44:47], a[184:187], a[12:15], v[44:47]
s_add_u32 s55, s26, 344064
ds_read_b128 a[152:155], %[vXr1] offset:20480
v_accvgpr_read_b32 v192, a232
)"
R"(v_mfma_f32_16x16x32_bf16 v[48:51], a[188:191], a[8:11], v[48:51]
ds_read_b128 a[156:159], %[vXr1] offset:21504
v_mfma_f32_16x16x32_bf16 v[52:55], a[188:191], a[12:15], v[52:55]
s_and_b32 s55, s55, 524287
s_or_b32 s55, s55, s74
buffer_load_dwordx4 a[40:43], %[vY4], %[sWS], s55 offen offset:0 sc1
v_accvgpr_read_b32 v193, a233
v_mfma_f32_16x16x32_bf16 v[56:59], a[192:195], a[8:11], v[56:59]
ds_read_b128 a[160:163], %[vXr1] offset:22528
v_mfma_f32_16x16x32_bf16 v[60:63], a[192:195], a[12:15], v[60:63]
ds_read_b128 a[164:167], %[vXr1] offset:23552
v_accvgpr_read_b32 v194, a234
v_mfma_f32_16x16x32_bf16 v[64:67], a[196:199], a[8:11], v[64:67]
v_accvgpr_read_b32 v195, a235
v_accvgpr_read_b32 v196, a236
v_mfma_f32_16x16x32_bf16 v[68:71], a[196:199], a[12:15], v[68:71]
ds_read_b128 a[168:171], %[vXr1] offset:24576
v_mfma_f32_16x16x32_bf16 v[72:75], a[200:203], a[8:11], v[72:75]
ds_read_b128 a[172:175], %[vXr1] offset:25600
v_accvgpr_read_b32 v197, a237
v_mfma_f32_16x16x32_bf16 v[76:79], a[200:203], a[12:15], v[76:79]
buffer_load_dwordx4 a[44:47], %[vY4], %[sWS], s55 offen offset:1024 sc1
v_accvgpr_read_b32 v198, a238
v_mfma_f32_16x16x32_bf16 v[80:83], a[204:207], a[8:11], v[80:83]
ds_read_b128 a[176:179], %[vXr1] offset:26624
v_mfma_f32_16x16x32_bf16 v[84:87], a[204:207], a[12:15], v[84:87]
v_accvgpr_read_b32 v199, a239
v_fma_f32 v230, v227, v192, v193
v_mfma_f32_16x16x32_bf16 v[88:91], a[208:211], a[8:11], v[88:91]
buffer_load_dwordx4 %[vL16], %[sWS], s48 offen offset:2048 sc1 lds
v_fma_f32 v192, v229, v192, v193
v_mfma_f32_16x16x32_bf16 v[92:95], a[208:211], a[12:15], v[92:95]
v_fma_f32 v152, v152, v226, v230
v_fma_f32 v156, v156, v228, v192
v_mfma_f32_16x16x32_bf16 v[96:99], a[212:215], a[8:11], v[96:99]
buffer_load_dwordx4 %[vL16], %[sWS], s48 offen offset:3072 sc1 lds
v_mfma_f32_16x16x32_bf16 v[100:103], a[212:215], a[12:15], v[100:103]
s_waitcnt vmcnt(17) lgkmcnt(0)
v_fma_f32 v231, v227, v194, v195
v_fma_f32 v194, v229, v194, v195
v_mfma_f32_16x16x32_bf16 v[32:35], a[144:147], a[16:19], v[32:35]
s_add_u32 s55, s26, 360448
ds_read_b128 a[180:183], %[vXr1] offset:27648
v_mfma_f32_16x16x32_bf16 v[36:39], a[144:147], a[20:23], v[36:39]
ds_read_b128 a[184:187], %[vXr1] offset:28672
v_fma_f32 v153, v153, v226, v231
v_mfma_f32_16x16x32_bf16 v[40:43], a[148:151], a[16:19], v[40:43]
s_and_b32 s55, s55, 524287
s_or_b32 s55, s55, s74
buffer_load_dwordx4 a[48:51], %[vY4], %[sWS], s55 offen offset:0 sc1
v_mfma_f32_16x16x32_bf16 v[44:47], a[148:151], a[20:23], v[44:47]
ds_read_b128 a[188:191], %[vXr1] offset:29696
v_fma_f32 v157, v157, v228, v194
v_mfma_f32_16x16x32_bf16 v[48:51], a[152:155], a[16:19], v[48:51]
ds_read_b128 a[192:195], %[vXr1] offset:30720
v_mfma_f32_16x16x32_bf16 v[52:55], a[152:155], a[20:23], v[52:55]
v_fma_f32 v230, v227, v196, v197
v_fma_f32 v196, v229, v196, v197
v_mfma_f32_16x16x32_bf16 v[56:59], a[156:159], a[16:19], v[56:59]
ds_read_b128 a[196:199], %[vXr1] offset:31744
v_mfma_f32_16x16x32_bf16 v[60:63], a[156:159], a[20:23], v[60:63]
ds_read_b128 a[200:203], %[vXr1] offset:32768
v_fma_f32 v154, v154, v226, v230
v_mfma_f32_16x16x32_bf16 v[64:67], a[160:163], a[16:19], v[64:67]
buffer_load_dwordx4 a[52:55], %[vY4], %[sWS], s55 offen offset:1024 sc1
v_mfma_f32_16x16x32_bf16 v[68:71], a[160:163], a[20:23], v[68:71]
ds_read_b128 a[204:207], %[vXr1] offset:33792
v_mfma_f32_16x16x32_bf16 v[72:75], a[164:167], a[16:19], v[72:75]
ds_read_b128 a[208:211], %[vXr1] offset:34816
v_fma_f32 v158, v158, v228, v196
v_mfma_f32_16x16x32_bf16 v[76:79], a[164:167], a[20:23], v[76:79]
s_add_u32 m0, %[sLx], 114688
v_fma_f32 v231, v227, v198, v199
v_mfma_f32_16x16x32_bf16 v[80:83], a[168:171], a[16:19], v[80:83]
s_add_u32 s48, s48, 4096
ds_read_b128 a[212:215], %[vXr1] offset:35840
v_fma_f32 v198, v229, v198, v199
v_mfma_f32_16x16x32_bf16 v[84:87], a[168:171], a[20:23], v[84:87]
buffer_load_dwordx4 %[vL16], %[sWS], s48 offen offset:0 sc1 lds
v_mfma_f32_16x16x32_bf16 v[88:91], a[172:175], a[16:19], v[88:91]
v_fma_f32 v155, v155, v226, v231
v_fma_f32 v159, v159, v228, v198
v_mfma_f32_16x16x32_bf16 v[92:95], a[172:175], a[20:23], v[92:95]
buffer_load_dwordx4 %[vL16], %[sWS], s48 offen offset:1024 sc1 lds
v_mfma_f32_16x16x32_bf16 v[96:99], a[176:179], a[16:19], v[96:99]
v_fma_f32 v24, v152, v152, s50
v_fma_f32 v25, v153, v153, s50
v_mfma_f32_16x16x32_bf16 v[100:103], a[176:179], a[20:23], v[100:103]
s_waitcnt vmcnt(13) lgkmcnt(0)
s_barrier
v_mul_f32 v24, v24, v152
v_mul_f32 v25, v25, v153
v_mfma_f32_16x16x32_bf16 v[32:35], a[180:183], a[24:27], v[32:35]
ds_read_b128 a[144:147], %[vXr2] offset:0
v_mfma_f32_16x16x32_bf16 v[36:39], a[180:183], a[28:31], v[36:39]
s_add_u32 s57, s57, 1024
ds_read_b128 a[148:151], %[vXr2] offset:1024
v_mfma_f32_16x16x32_bf16 v[40:43], a[184:187], a[24:27], v[40:43]
buffer_load_dword v255, v251, %[sWS], s57 offen offset:0 sc1
v_mfma_f32_16x16x32_bf16 v[44:47], a[184:187], a[28:31], v[44:47]
s_add_u32 s55, s26, 376832
ds_read_b128 a[152:155], %[vXr2] offset:2048
v_exp_f32 v24, v24
v_mfma_f32_16x16x32_bf16 v[48:51], a[188:191], a[24:27], v[48:51]
ds_read_b128 a[156:159], %[vXr2] offset:3072
v_mfma_f32_16x16x32_bf16 v[52:55], a[188:191], a[28:31], v[52:55]
s_and_b32 s55, s55, 524287
s_or_b32 s55, s55, s74
buffer_load_dwordx4 a[56:59], %[vY4], %[sWS], s55 offen offset:0 sc1
v_mfma_f32_16x16x32_bf16 v[56:59], a[192:195], a[24:27], v[56:59]
ds_read_b128 a[160:163], %[vXr2] offset:4096
v_mfma_f32_16x16x32_bf16 v[60:63], a[192:195], a[28:31], v[60:63]
ds_read_b128 a[164:167], %[vXr2] offset:5120
v_mfma_f32_16x16x32_bf16 v[64:67], a[196:199], a[24:27], v[64:67]
v_exp_f32 v25, v25
v_add_f32 v24, 1.0, v24
v_mfma_f32_16x16x32_bf16 v[68:71], a[196:199], a[28:31], v[68:71]
ds_read_b128 a[168:171], %[vXr2] offset:6144
v_add_f32_e64 v25, 1.0, v25
v_mfma_f32_16x16x32_bf16 v[72:75], a[200:203], a[24:27], v[72:75]
ds_read_b128 a[172:175], %[vXr2] offset:7168
v_mfma_f32_16x16x32_bf16 v[76:79], a[200:203], a[28:31], v[76:79]
buffer_load_dwordx4 a[60:63], %[vY4], %[sWS], s55 offen offset:1024 sc1
v_mfma_f32_16x16x32_bf16 v[80:83], a[204:207], a[24:27], v[80:83]
ds_read_b128 a[176:179], %[vXr2] offset:8192
v_mfma_f32_16x16x32_bf16 v[84:87], a[204:207], a[28:31], v[84:87]
v_rcp_f32_e64 v24, v24
v_mfma_f32_16x16x32_bf16 v[88:91], a[208:211], a[24:27], v[88:91]
buffer_load_dwordx4 %[vL16], %[sWS], s48 offen offset:2048 sc1 lds
v_rcp_f32_e64 v25, v25
v_mfma_f32_16x16x32_bf16 v[92:95], a[208:211], a[28:31], v[92:95]
v_mul_f32_e64 v152, v152, v24
v_mul_f32 v153, v153, v25
v_mfma_f32_16x16x32_bf16 v[96:99], a[212:215], a[24:27], v[96:99]
buffer_load_dwordx4 %[vL16], %[sWS], s48 offen offset:3072 sc1 lds
v_mfma_f32_16x16x32_bf16 v[100:103], a[212:215], a[28:31], v[100:103]
s_waitcnt vmcnt(16) lgkmcnt(0)
v_fma_f32 v26, v154, v154, s50
v_fma_f32 v27, v155, v155, s50
v_mfma_f32_16x16x32_bf16 v[32:35], a[144:147], a[32:35], v[32:35]
s_add_u32 m0, %[sLx], 118784
ds_read_b128 a[180:183], %[vXr2] offset:9216
v_mfma_f32_16x16x32_bf16 v[36:39], a[144:147], a[36:39], v[36:39]
s_lshl1_add_u32 s48, s54, s39
ds_read_b128 a[184:187], %[vXr2] offset:10240
v_mul_f32 v26, v26, v154
v_mfma_f32_16x16x32_bf16 v[40:43], a[148:151], a[32:35], v[40:43]
buffer_load_dwordx4 %[vL16], %[sWS], s48 offen offset:0 sc1 lds
v_mfma_f32_16x16x32_bf16 v[44:47], a[148:151], a[36:39], v[44:47]
s_add_u32 s55, s26, 393216
ds_read_b128 a[188:191], %[vXr2] offset:11264
v_mul_f32 v27, v27, v155
v_mfma_f32_16x16x32_bf16 v[48:51], a[152:155], a[32:35], v[48:51]
ds_read_b128 a[192:195], %[vXr2] offset:12288
v_mfma_f32_16x16x32_bf16 v[52:55], a[152:155], a[36:39], v[52:55]
s_and_b32 s55, s55, 524287
s_or_b32 s55, s55, s74
buffer_load_dwordx4 a[64:67], %[vY4], %[sWS], s55 offen offset:0 sc1
v_mfma_f32_16x16x32_bf16 v[56:59], a[156:159], a[32:35], v[56:59]
ds_read_b128 a[196:199], %[vXr2] offset:13312
v_mfma_f32_16x16x32_bf16 v[60:63], a[156:159], a[36:39], v[60:63]
ds_read_b128 a[200:203], %[vXr2] offset:14336
v_mfma_f32_16x16x32_bf16 v[64:67], a[160:163], a[32:35], v[64:67]
v_exp_f32_e64 v26, v26
v_mfma_f32_16x16x32_bf16 v[68:71], a[160:163], a[36:39], v[68:71]
ds_read_b128 a[204:207], %[vXr2] offset:15360
v_exp_f32_e64 v27, v27
v_mfma_f32_16x16x32_bf16 v[72:75], a[164:167], a[32:35], v[72:75]
ds_read_b128 a[208:211], %[vXr2] offset:16384
v_mfma_f32_16x16x32_bf16 v[76:79], a[164:167], a[36:39], v[76:79]
buffer_load_dwordx4 a[68:71], %[vY4], %[sWS], s55 offen offset:1024 sc1
v_add_f32 v26, 1.0, v26
v_mfma_f32_16x16x32_bf16 v[80:83], a[168:171], a[32:35], v[80:83]
s_add_u32 m0, %[sLx], 0
ds_read_b128 a[212:215], %[vXr2] offset:17408
v_mfma_f32_16x16x32_bf16 v[84:87], a[168:171], a[36:39], v[84:87]
v_add_f32_e64 v27, 1.0, v27
v_mfma_f32_16x16x32_bf16 v[88:91], a[172:175], a[32:35], v[88:91]
s_add_u32 s54, s26, 458752
s_and_b32 s54, s54, 458752
s_add_u32 s48, s54, s38
buffer_load_dwordx4 %[vL16], %[sWS], s48 offen offset:0 sc1 lds
v_rcp_f32 v26, v26
v_mfma_f32_16x16x32_bf16 v[92:95], a[172:175], a[36:39], v[92:95]
v_rcp_f32_e64 v27, v27
v_mfma_f32_16x16x32_bf16 v[96:99], a[176:179], a[32:35], v[96:99]
buffer_load_dwordx4 %[vL16], %[sWS], s48 offen offset:1024 sc1 lds
v_mfma_f32_16x16x32_bf16 v[100:103], a[176:179], a[36:39], v[100:103]
s_waitcnt vmcnt(16) lgkmcnt(0)
v_mul_f32 v154, v154, v26
v_mul_f32_e64 v155, v155, v27
v_mfma_f32_16x16x32_bf16 v[32:35], a[180:183], a[40:43], v[32:35]
ds_read_b128 a[144:147], %[vXr2] offset:18432
v_mfma_f32_16x16x32_bf16 v[36:39], a[180:183], a[44:47], v[36:39]
s_add_u32 s57, s57, 1024
ds_read_b128 a[148:151], %[vXr2] offset:19456
v_fma_f32 v24, v156, v156, s50
v_mfma_f32_16x16x32_bf16 v[40:43], a[184:187], a[40:43], v[40:43]
buffer_load_dword v255, v251, %[sWS], s57 offen offset:0 sc1
v_mfma_f32_16x16x32_bf16 v[44:47], a[184:187], a[44:47], v[44:47]
)"
R"(s_add_u32 s55, s26, 409600
ds_read_b128 a[152:155], %[vXr2] offset:20480
v_mfma_f32_16x16x32_bf16 v[48:51], a[188:191], a[40:43], v[48:51]
ds_read_b128 a[156:159], %[vXr2] offset:21504
v_fma_f32 v25, v157, v157, s50
v_mfma_f32_16x16x32_bf16 v[52:55], a[188:191], a[44:47], v[52:55]
s_and_b32 s55, s55, 524287
s_or_b32 s55, s55, s74
buffer_load_dwordx4 a[72:75], %[vY4], %[sWS], s55 offen offset:0 sc1
v_mfma_f32_16x16x32_bf16 v[56:59], a[192:195], a[40:43], v[56:59]
ds_read_b128 a[160:163], %[vXr2] offset:22528
v_mul_f32 v24, v24, v156
v_mfma_f32_16x16x32_bf16 v[60:63], a[192:195], a[44:47], v[60:63]
ds_read_b128 a[164:167], %[vXr2] offset:23552
v_mfma_f32_16x16x32_bf16 v[64:67], a[196:199], a[40:43], v[64:67]
v_mul_f32_e64 v25, v25, v157
v_mfma_f32_16x16x32_bf16 v[68:71], a[196:199], a[44:47], v[68:71]
ds_read_b128 a[168:171], %[vXr2] offset:24576
v_mfma_f32_16x16x32_bf16 v[72:75], a[200:203], a[40:43], v[72:75]
ds_read_b128 a[172:175], %[vXr2] offset:25600
v_exp_f32_e64 v24, v24
v_mfma_f32_16x16x32_bf16 v[76:79], a[200:203], a[44:47], v[76:79]
buffer_load_dwordx4 a[76:79], %[vY4], %[sWS], s55 offen offset:1024 sc1
v_mfma_f32_16x16x32_bf16 v[80:83], a[204:207], a[40:43], v[80:83]
ds_read_b128 a[176:179], %[vXr2] offset:26624
v_mfma_f32_16x16x32_bf16 v[84:87], a[204:207], a[44:47], v[84:87]
v_exp_f32_e64 v25, v25
v_mfma_f32_16x16x32_bf16 v[88:91], a[208:211], a[40:43], v[88:91]
buffer_load_dwordx4 %[vL16], %[sWS], s48 offen offset:2048 sc1 lds
v_add_f32_e64 v24, 1.0, v24
v_mfma_f32_16x16x32_bf16 v[92:95], a[208:211], a[44:47], v[92:95]
v_add_f32_e64 v25, 1.0, v25
v_mfma_f32_16x16x32_bf16 v[96:99], a[212:215], a[40:43], v[96:99]
buffer_load_dwordx4 %[vL16], %[sWS], s48 offen offset:3072 sc1 lds
v_mfma_f32_16x16x32_bf16 v[100:103], a[212:215], a[44:47], v[100:103]
s_waitcnt vmcnt(17) lgkmcnt(0)
v_rcp_f32 v24, v24
v_rcp_f32_e64 v25, v25
v_mfma_f32_16x16x32_bf16 v[32:35], a[144:147], a[48:51], v[32:35]
s_add_u32 s55, s26, 425984
ds_read_b128 a[180:183], %[vXr2] offset:27648
v_mfma_f32_16x16x32_bf16 v[36:39], a[144:147], a[52:55], v[36:39]
ds_read_b128 a[184:187], %[vXr2] offset:28672
v_mul_f32 v156, v156, v24
v_mfma_f32_16x16x32_bf16 v[40:43], a[148:151], a[48:51], v[40:43]
s_and_b32 s55, s55, 524287
s_or_b32 s55, s55, s74
buffer_load_dwordx4 a[80:83], %[vY4], %[sWS], s55 offen offset:0 sc1
v_mfma_f32_16x16x32_bf16 v[44:47], a[148:151], a[52:55], v[44:47]
ds_read_b128 a[188:191], %[vXr2] offset:29696
v_mfma_f32_16x16x32_bf16 v[48:51], a[152:155], a[48:51], v[48:51]
ds_read_b128 a[192:195], %[vXr2] offset:30720
v_mul_f32_e64 v157, v157, v25
v_mfma_f32_16x16x32_bf16 v[52:55], a[152:155], a[52:55], v[52:55]
v_fma_f32 v26, v158, v158, s50
v_mfma_f32_16x16x32_bf16 v[56:59], a[156:159], a[48:51], v[56:59]
ds_read_b128 a[196:199], %[vXr2] offset:31744
v_fma_f32 v27, v159, v159, s50
v_mfma_f32_16x16x32_bf16 v[60:63], a[156:159], a[52:55], v[60:63]
ds_read_b128 a[200:203], %[vXr2] offset:32768
v_mfma_f32_16x16x32_bf16 v[64:67], a[160:163], a[48:51], v[64:67]
buffer_load_dwordx4 a[84:87], %[vY4], %[sWS], s55 offen offset:1024 sc1
v_mfma_f32_16x16x32_bf16 v[68:71], a[160:163], a[52:55], v[68:71]
ds_read_b128 a[204:207], %[vXr2] offset:33792
v_mul_f32_e64 v26, v26, v158
v_mfma_f32_16x16x32_bf16 v[72:75], a[164:167], a[48:51], v[72:75]
ds_read_b128 a[208:211], %[vXr2] offset:34816
v_mfma_f32_16x16x32_bf16 v[76:79], a[164:167], a[52:55], v[76:79]
s_add_u32 m0, %[sLx], 4096
v_mul_f32_e64 v27, v27, v159
v_mfma_f32_16x16x32_bf16 v[80:83], a[168:171], a[48:51], v[80:83]
s_add_u32 s48, s48, 4096
ds_read_b128 a[212:215], %[vXr2] offset:35840
v_mfma_f32_16x16x32_bf16 v[84:87], a[168:171], a[52:55], v[84:87]
buffer_load_dwordx4 %[vL16], %[sWS], s48 offen offset:0 sc1 lds
v_mfma_f32_16x16x32_bf16 v[88:91], a[172:175], a[48:51], v[88:91]
v_exp_f32_e64 v26, v26
v_mfma_f32_16x16x32_bf16 v[92:95], a[172:175], a[52:55], v[92:95]
buffer_load_dwordx4 %[vL16], %[sWS], s48 offen offset:1024 sc1 lds
v_mfma_f32_16x16x32_bf16 v[96:99], a[176:179], a[48:51], v[96:99]
v_exp_f32 v27, v27
v_add_f32 v26, 1.0, v26
v_mfma_f32_16x16x32_bf16 v[100:103], a[176:179], a[52:55], v[100:103]
s_waitcnt vmcnt(13) lgkmcnt(0)
s_barrier
v_add_f32_e64 v27, 1.0, v27
v_rcp_f32 v26, v26
v_mfma_f32_16x16x32_bf16 v[32:35], a[180:183], a[56:59], v[32:35]
v_min_u32 v251, 896, v251
ds_read_b128 a[144:147], %[vXr3] offset:0
v_mfma_f32_16x16x32_bf16 v[36:39], a[180:183], a[60:63], v[36:39]
s_lshl1_add_u32 s57, s26, s73
ds_read_b128 a[148:151], %[vXr3] offset:1024
v_mfma_f32_16x16x32_bf16 v[40:43], a[184:187], a[56:59], v[40:43]
buffer_load_dword v255, v251, %[sWS], s57 offen offset:0 sc1
v_mfma_f32_16x16x32_bf16 v[44:47], a[184:187], a[60:63], v[44:47]
s_add_u32 s55, s26, 442368
ds_read_b128 a[152:155], %[vXr3] offset:2048
v_mfma_f32_16x16x32_bf16 v[48:51], a[188:191], a[56:59], v[48:51]
ds_read_b128 a[156:159], %[vXr3] offset:3072
v_rcp_f32 v27, v27
v_mfma_f32_16x16x32_bf16 v[52:55], a[188:191], a[60:63], v[52:55]
s_and_b32 s55, s55, 524287
s_or_b32 s55, s55, s74
buffer_load_dwordx4 a[88:91], %[vY4], %[sWS], s55 offen offset:0 sc1
v_mfma_f32_16x16x32_bf16 v[56:59], a[192:195], a[56:59], v[56:59]
ds_read_b128 a[160:163], %[vXr3] offset:4096
v_mul_f32_e64 v158, v158, v26
v_mfma_f32_16x16x32_bf16 v[60:63], a[192:195], a[60:63], v[60:63]
ds_read_b128 a[164:167], %[vXr3] offset:5120
v_mfma_f32_16x16x32_bf16 v[64:67], a[196:199], a[56:59], v[64:67]
v_mul_f32 v159, v159, v27
v_accvgpr_read_b32 v200, a240
v_mfma_f32_16x16x32_bf16 v[68:71], a[196:199], a[60:63], v[68:71]
ds_read_b128 a[168:171], %[vXr3] offset:6144
v_mfma_f32_16x16x32_bf16 v[72:75], a[200:203], a[56:59], v[72:75]
ds_read_b128 a[172:175], %[vXr3] offset:7168
v_accvgpr_read_b32 v201, a241
v_mfma_f32_16x16x32_bf16 v[76:79], a[200:203], a[60:63], v[76:79]
buffer_load_dwordx4 a[92:95], %[vY4], %[sWS], s55 offen offset:1024 sc1
v_mfma_f32_16x16x32_bf16 v[80:83], a[204:207], a[56:59], v[80:83]
ds_read_b128 a[176:179], %[vXr3] offset:8192
v_mfma_f32_16x16x32_bf16 v[84:87], a[204:207], a[60:63], v[84:87]
v_accvgpr_read_b32 v202, a242
v_accvgpr_read_b32 v203, a243
v_mfma_f32_16x16x32_bf16 v[88:91], a[208:211], a[56:59], v[88:91]
buffer_load_dwordx4 %[vL16], %[sWS], s48 offen offset:2048 sc1 lds
v_accvgpr_read_b32 v204, a244
v_mfma_f32_16x16x32_bf16 v[92:95], a[208:211], a[60:63], v[92:95]
v_accvgpr_read_b32 v205, a245
v_accvgpr_read_b32 v206, a246
v_mfma_f32_16x16x32_bf16 v[96:99], a[212:215], a[56:59], v[96:99]
buffer_load_dwordx4 %[vL16], %[sWS], s48 offen offset:3072 sc1 lds
v_mfma_f32_16x16x32_bf16 v[100:103], a[212:215], a[60:63], v[100:103]
s_waitcnt vmcnt(16) lgkmcnt(0)
v_accvgpr_read_b32 v207, a247
v_fma_f32 v230, v227, v200, v201
v_mfma_f32_16x16x32_bf16 v[32:35], a[144:147], a[64:67], v[32:35]
s_add_u32 m0, %[sLx], 8192
ds_read_b128 a[180:183], %[vXr3] offset:9216
v_mfma_f32_16x16x32_bf16 v[36:39], a[144:147], a[68:71], v[36:39]
s_lshl1_add_u32 s48, s54, s39
ds_read_b128 a[184:187], %[vXr3] offset:10240
v_fma_f32 v200, v229, v200, v201
v_mfma_f32_16x16x32_bf16 v[40:43], a[148:151], a[64:67], v[40:43]
buffer_load_dwordx4 %[vL16], %[sWS], s48 offen offset:0 sc1 lds
v_mfma_f32_16x16x32_bf16 v[44:47], a[148:151], a[68:71], v[44:47]
s_add_u32 s55, s26, 458752
ds_read_b128 a[188:191], %[vXr3] offset:11264
v_mfma_f32_16x16x32_bf16 v[48:51], a[152:155], a[64:67], v[48:51]
ds_read_b128 a[192:195], %[vXr3] offset:12288
v_fma_f32 v160, v160, v226, v230
v_mfma_f32_16x16x32_bf16 v[52:55], a[152:155], a[68:71], v[52:55]
s_and_b32 s55, s55, 524287
s_or_b32 s55, s55, s74
buffer_load_dwordx4 a[96:99], %[vY4], %[sWS], s55 offen offset:0 sc1
v_mfma_f32_16x16x32_bf16 v[56:59], a[156:159], a[64:67], v[56:59]
ds_read_b128 a[196:199], %[vXr3] offset:13312
v_fma_f32 v164, v164, v228, v200
v_mfma_f32_16x16x32_bf16 v[60:63], a[156:159], a[68:71], v[60:63]
ds_read_b128 a[200:203], %[vXr3] offset:14336
v_mfma_f32_16x16x32_bf16 v[64:67], a[160:163], a[64:67], v[64:67]
v_fma_f32 v231, v227, v202, v203
v_fma_f32 v202, v229, v202, v203
v_mfma_f32_16x16x32_bf16 v[68:71], a[160:163], a[68:71], v[68:71]
ds_read_b128 a[204:207], %[vXr3] offset:15360
v_mfma_f32_16x16x32_bf16 v[72:75], a[164:167], a[64:67], v[72:75]
ds_read_b128 a[208:211], %[vXr3] offset:16384
v_fma_f32 v161, v161, v226, v231
v_mfma_f32_16x16x32_bf16 v[76:79], a[164:167], a[68:71], v[76:79]
buffer_load_dwordx4 a[100:103], %[vY4], %[sWS], s55 offen offset:1024 sc1
v_mfma_f32_16x16x32_bf16 v[80:83], a[168:171], a[64:67], v[80:83]
s_add_u32 m0, %[sLx], 36864
ds_read_b128 a[212:215], %[vXr3] offset:17408
v_mfma_f32_16x16x32_bf16 v[84:87], a[168:171], a[68:71], v[84:87]
v_fma_f32 v165, v165, v228, v202
v_fma_f32 v230, v227, v204, v205
v_mfma_f32_16x16x32_bf16 v[88:91], a[172:175], a[64:67], v[88:91]
s_add_u32 s54, s26, 0
s_and_b32 s54, s54, 458752
s_add_u32 s48, s54, s72
buffer_load_dwordx4 %[vL16], %[sWS], s48 offen offset:0 sc1 lds
v_fma_f32 v204, v229, v204, v205
v_mfma_f32_16x16x32_bf16 v[92:95], a[172:175], a[68:71], v[92:95]
v_fma_f32 v162, v162, v226, v230
v_fma_f32 v166, v166, v228, v204
v_mfma_f32_16x16x32_bf16 v[96:99], a[176:179], a[64:67], v[96:99]
buffer_load_dwordx4 %[vL16], %[sWS], s48 offen offset:1024 sc1 lds
v_mfma_f32_16x16x32_bf16 v[100:103], a[176:179], a[68:71], v[100:103]
s_waitcnt vmcnt(16) lgkmcnt(0)
v_fma_f32 v231, v227, v206, v207
v_fma_f32 v206, v229, v206, v207
v_mfma_f32_16x16x32_bf16 v[32:35], a[180:183], a[72:75], v[32:35]
s_add_u32 s55, s26, 475136
ds_read_b128 a[144:147], %[vXr3] offset:18432
v_mfma_f32_16x16x32_bf16 v[36:39], a[180:183], a[76:79], v[36:39]
ds_read_b128 a[148:151], %[vXr3] offset:19456
v_fma_f32 v163, v163, v226, v231
v_mfma_f32_16x16x32_bf16 v[40:43], a[184:187], a[72:75], v[40:43]
s_and_b32 s55, s55, 524287
)"
R"(s_or_b32 s55, s55, s74
buffer_load_dwordx4 a[104:107], %[vY4], %[sWS], s55 offen offset:0 sc1
v_mfma_f32_16x16x32_bf16 v[44:47], a[184:187], a[76:79], v[44:47]
ds_read_b128 a[152:155], %[vXr3] offset:20480
v_mfma_f32_16x16x32_bf16 v[48:51], a[188:191], a[72:75], v[48:51]
ds_read_b128 a[156:159], %[vXr3] offset:21504
v_fma_f32 v167, v167, v228, v206
v_mfma_f32_16x16x32_bf16 v[52:55], a[188:191], a[76:79], v[52:55]
v_fma_f32 v24, v160, v160, s50
v_mfma_f32_16x16x32_bf16 v[56:59], a[192:195], a[72:75], v[56:59]
ds_read_b128 a[160:163], %[vXr3] offset:22528
v_fma_f32 v25, v161, v161, s50
v_mfma_f32_16x16x32_bf16 v[60:63], a[192:195], a[76:79], v[60:63]
ds_read_b128 a[164:167], %[vXr3] offset:23552
v_mfma_f32_16x16x32_bf16 v[64:67], a[196:199], a[72:75], v[64:67]
buffer_load_dwordx4 a[108:111], %[vY4], %[sWS], s55 offen offset:1024 sc1
v_mfma_f32_16x16x32_bf16 v[68:71], a[196:199], a[76:79], v[68:71]
ds_read_b128 a[168:171], %[vXr3] offset:24576
v_mul_f32_e64 v24, v24, v160
v_mfma_f32_16x16x32_bf16 v[72:75], a[200:203], a[72:75], v[72:75]
ds_read_b128 a[172:175], %[vXr3] offset:25600
v_mfma_f32_16x16x32_bf16 v[76:79], a[200:203], a[76:79], v[76:79]
v_mul_f32_e64 v25, v25, v161
v_mfma_f32_16x16x32_bf16 v[80:83], a[204:207], a[72:75], v[80:83]
ds_read_b128 a[176:179], %[vXr3] offset:26624
v_mfma_f32_16x16x32_bf16 v[84:87], a[204:207], a[76:79], v[84:87]
buffer_load_dwordx4 %[vL16], %[sWS], s48 offen offset:2048 sc1 lds
v_mfma_f32_16x16x32_bf16 v[88:91], a[208:211], a[72:75], v[88:91]
v_exp_f32_e64 v24, v24
v_mfma_f32_16x16x32_bf16 v[92:95], a[208:211], a[76:79], v[92:95]
buffer_load_dwordx4 %[vL16], %[sWS], s48 offen offset:3072 sc1 lds
v_mfma_f32_16x16x32_bf16 v[96:99], a[212:215], a[72:75], v[96:99]
v_exp_f32 v25, v25
v_add_f32 v24, 1.0, v24
v_mfma_f32_16x16x32_bf16 v[100:103], a[212:215], a[76:79], v[100:103]
s_waitcnt vmcnt(16) lgkmcnt(0)
v_add_f32 v25, 1.0, v25
v_rcp_f32_e64 v24, v24
v_mfma_f32_16x16x32_bf16 v[32:35], a[144:147], a[80:83], v[32:35]
s_add_u32 s55, s26, 491520
ds_read_b128 a[180:183], %[vXr3] offset:27648
v_mfma_f32_16x16x32_bf16 v[36:39], a[144:147], a[84:87], v[36:39]
ds_read_b128 a[184:187], %[vXr3] offset:28672
v_mfma_f32_16x16x32_bf16 v[40:43], a[148:151], a[80:83], v[40:43]
s_and_b32 s55, s55, 524287
s_or_b32 s55, s55, s74
buffer_load_dwordx4 a[112:115], %[vY4], %[sWS], s55 offen offset:0 sc1
v_mfma_f32_16x16x32_bf16 v[44:47], a[148:151], a[84:87], v[44:47]
ds_read_b128 a[188:191], %[vXr3] offset:29696
v_mfma_f32_16x16x32_bf16 v[48:51], a[152:155], a[80:83], v[48:51]
ds_read_b128 a[192:195], %[vXr3] offset:30720
v_rcp_f32 v25, v25
v_mfma_f32_16x16x32_bf16 v[52:55], a[152:155], a[84:87], v[52:55]
v_mul_f32_e64 v160, v160, v24
v_mfma_f32_16x16x32_bf16 v[56:59], a[156:159], a[80:83], v[56:59]
ds_read_b128 a[196:199], %[vXr3] offset:31744
v_mul_f32_e64 v161, v161, v25
v_mfma_f32_16x16x32_bf16 v[60:63], a[156:159], a[84:87], v[60:63]
ds_read_b128 a[200:203], %[vXr3] offset:32768
v_mfma_f32_16x16x32_bf16 v[64:67], a[160:163], a[80:83], v[64:67]
buffer_load_dwordx4 a[116:119], %[vY4], %[sWS], s55 offen offset:1024 sc1
v_mfma_f32_16x16x32_bf16 v[68:71], a[160:163], a[84:87], v[68:71]
ds_read_b128 a[204:207], %[vXr3] offset:33792
v_fma_f32 v26, v162, v162, s50
v_mfma_f32_16x16x32_bf16 v[72:75], a[164:167], a[80:83], v[72:75]
ds_read_b128 a[208:211], %[vXr3] offset:34816
v_mfma_f32_16x16x32_bf16 v[76:79], a[164:167], a[84:87], v[76:79]
s_add_u32 m0, %[sLx], 40960
v_fma_f32 v27, v163, v163, s50
v_mul_f32_e64 v26, v26, v162
v_mfma_f32_16x16x32_bf16 v[80:83], a[168:171], a[80:83], v[80:83]
s_add_u32 s48, s48, 4096
ds_read_b128 a[212:215], %[vXr3] offset:35840
v_mfma_f32_16x16x32_bf16 v[84:87], a[168:171], a[84:87], v[84:87]
buffer_load_dwordx4 %[vL16], %[sWS], s48 offen offset:0 sc1 lds
v_mfma_f32_16x16x32_bf16 v[88:91], a[172:175], a[80:83], v[88:91]
v_mul_f32_e64 v27, v27, v163
v_mfma_f32_16x16x32_bf16 v[92:95], a[172:175], a[84:87], v[92:95]
buffer_load_dwordx4 %[vL16], %[sWS], s48 offen offset:1024 sc1 lds
v_mfma_f32_16x16x32_bf16 v[96:99], a[176:179], a[80:83], v[96:99]
v_exp_f32_e64 v26, v26
v_mfma_f32_16x16x32_bf16 v[100:103], a[176:179], a[84:87], v[100:103]
s_waitcnt vmcnt(12) lgkmcnt(0)
s_barrier
v_exp_f32 v27, v27
v_add_f32 v26, 1.0, v26
v_mfma_f32_16x16x32_bf16 v[32:35], a[180:183], a[88:91], v[32:35]
s_add_u32 s55, s26, 507904
ds_read_b128 a[144:147], %[vXr0] offset:0
v_mfma_f32_16x16x32_bf16 v[36:39], a[180:183], a[92:95], v[36:39]
ds_read_b128 a[148:151], %[vXr0] offset:1024
v_add_f32 v27, 1.0, v27
v_mfma_f32_16x16x32_bf16 v[40:43], a[184:187], a[88:91], v[40:43]
s_and_b32 s55, s55, 524287
s_or_b32 s55, s55, s74
buffer_load_dwordx4 a[120:123], %[vY4], %[sWS], s55 offen offset:0 sc1
v_mfma_f32_16x16x32_bf16 v[44:47], a[184:187], a[92:95], v[44:47]
ds_read_b128 a[152:155], %[vXr0] offset:2048
v_mfma_f32_16x16x32_bf16 v[48:51], a[188:191], a[88:91], v[48:51]
ds_read_b128 a[156:159], %[vXr0] offset:3072
v_mfma_f32_16x16x32_bf16 v[52:55], a[188:191], a[92:95], v[52:55]
v_rcp_f32_e64 v26, v26
v_mfma_f32_16x16x32_bf16 v[56:59], a[192:195], a[88:91], v[56:59]
ds_read_b128 a[160:163], %[vXr0] offset:4096
v_mfma_f32_16x16x32_bf16 v[60:63], a[192:195], a[92:95], v[60:63]
ds_read_b128 a[164:167], %[vXr0] offset:5120
v_mfma_f32_16x16x32_bf16 v[64:67], a[196:199], a[88:91], v[64:67]
buffer_load_dwordx4 a[124:127], %[vY4], %[sWS], s55 offen offset:1024 sc1
v_mfma_f32_16x16x32_bf16 v[68:71], a[196:199], a[92:95], v[68:71]
ds_read_b128 a[168:171], %[vXr0] offset:6144
v_rcp_f32_e64 v27, v27
v_mfma_f32_16x16x32_bf16 v[72:75], a[200:203], a[88:91], v[72:75]
ds_read_b128 a[172:175], %[vXr0] offset:7168
v_mfma_f32_16x16x32_bf16 v[76:79], a[200:203], a[92:95], v[76:79]
v_mul_f32 v162, v162, v26
v_mul_f32 v163, v163, v27
v_mfma_f32_16x16x32_bf16 v[80:83], a[204:207], a[88:91], v[80:83]
ds_read_b128 a[176:179], %[vXr0] offset:8192
v_mfma_f32_16x16x32_bf16 v[84:87], a[204:207], a[92:95], v[84:87]
buffer_load_dwordx4 %[vL16], %[sWS], s48 offen offset:2048 sc1 lds
v_mfma_f32_16x16x32_bf16 v[88:91], a[208:211], a[88:91], v[88:91]
v_fma_f32 v24, v164, v164, s50
v_fma_f32 v25, v165, v165, s50
v_mfma_f32_16x16x32_bf16 v[92:95], a[208:211], a[92:95], v[92:95]
LPb_7:
s_cbranch_execz LPb_11
buffer_load_dwordx4 %[vL16], %[sWS], s48 offen offset:3072 sc1 lds
v_mfma_f32_16x16x32_bf16 v[96:99], a[212:215], a[88:91], v[96:99]
v_mul_f32 v24, v24, v164
v_mul_f32 v25, v25, v165
v_mfma_f32_16x16x32_bf16 v[100:103], a[212:215], a[92:95], v[100:103]
s_waitcnt vmcnt(14) lgkmcnt(0)
v_exp_f32 v24, v24
v_mfma_f32_16x16x32_bf16 v[32:35], a[144:147], a[96:99], v[32:35]
s_add_u32 m0, %[sLx], 45056
ds_read_b128 a[180:183], %[vXr0] offset:9216
v_mfma_f32_16x16x32_bf16 v[36:39], a[144:147], a[100:103], v[36:39]
s_lshl1_add_u32 s48, s54, s73
ds_read_b128 a[184:187], %[vXr0] offset:10240
v_mfma_f32_16x16x32_bf16 v[40:43], a[148:151], a[96:99], v[40:43]
buffer_load_dwordx4 %[vL16], %[sWS], s48 offen offset:0 sc1 lds
v_mfma_f32_16x16x32_bf16 v[44:47], a[148:151], a[100:103], v[44:47]
s_add_u32 s55, s26, 524288
ds_read_b128 a[188:191], %[vXr0] offset:11264
v_mfma_f32_16x16x32_bf16 v[48:51], a[152:155], a[96:99], v[48:51]
ds_read_b128 a[192:195], %[vXr0] offset:12288
v_exp_f32 v25, v25
v_mfma_f32_16x16x32_bf16 v[52:55], a[152:155], a[100:103], v[52:55]
s_and_b32 s55, s55, 524287
s_or_b32 s55, s55, s75
buffer_load_dwordx4 a[0:3], %[vY4], %[sWS], s55 offen offset:0 sc1
v_mfma_f32_16x16x32_bf16 v[56:59], a[156:159], a[96:99], v[56:59]
ds_read_b128 a[196:199], %[vXr0] offset:13312
v_add_f32_e64 v24, 1.0, v24
v_mfma_f32_16x16x32_bf16 v[60:63], a[156:159], a[100:103], v[60:63]
ds_read_b128 a[200:203], %[vXr0] offset:14336
v_mfma_f32_16x16x32_bf16 v[64:67], a[160:163], a[96:99], v[64:67]
v_add_f32_e64 v25, 1.0, v25
v_mfma_f32_16x16x32_bf16 v[68:71], a[160:163], a[100:103], v[68:71]
ds_read_b128 a[204:207], %[vXr0] offset:15360
v_mfma_f32_16x16x32_bf16 v[72:75], a[164:167], a[96:99], v[72:75]
ds_read_b128 a[208:211], %[vXr0] offset:16384
v_rcp_f32_e64 v24, v24
v_mfma_f32_16x16x32_bf16 v[76:79], a[164:167], a[100:103], v[76:79]
buffer_load_dwordx4 a[4:7], %[vY4], %[sWS], s55 offen offset:1024 sc1
v_mfma_f32_16x16x32_bf16 v[80:83], a[168:171], a[96:99], v[80:83]
s_add_u32 m0, %[sLx], 73728
ds_read_b128 a[212:215], %[vXr0] offset:17408
v_mfma_f32_16x16x32_bf16 v[84:87], a[168:171], a[100:103], v[84:87]
v_rcp_f32_e64 v25, v25
v_mfma_f32_16x16x32_bf16 v[88:91], a[172:175], a[96:99], v[88:91]
s_add_u32 s54, s26, 65536
s_and_b32 s54, s54, 458752
s_add_u32 s48, s54, s72
buffer_load_dwordx4 %[vL16], %[sWS], s48 offen offset:0 sc1 lds
v_mul_f32 v164, v164, v24
v_mfma_f32_16x16x32_bf16 v[92:95], a[172:175], a[100:103], v[92:95]
v_mul_f32_e64 v165, v165, v25
v_fma_f32 v26, v166, v166, s50
v_mfma_f32_16x16x32_bf16 v[96:99], a[176:179], a[96:99], v[96:99]
buffer_load_dwordx4 %[vL16], %[sWS], s48 offen offset:1024 sc1 lds
v_mfma_f32_16x16x32_bf16 v[100:103], a[176:179], a[100:103], v[100:103]
s_waitcnt vmcnt(15) lgkmcnt(0)
v_fma_f32 v27, v167, v167, s50
v_mul_f32 v26, v26, v166
v_mfma_f32_16x16x32_bf16 v[32:35], a[180:183], a[104:107], v[32:35]
s_add_u32 s55, s26, 540672
ds_read_b128 a[144:147], %[vXr0] offset:18432
v_mfma_f32_16x16x32_bf16 v[36:39], a[180:183], a[108:111], v[36:39]
ds_read_b128 a[148:151], %[vXr0] offset:19456
v_mul_f32 v27, v27, v167
v_mfma_f32_16x16x32_bf16 v[40:43], a[184:187], a[104:107], v[40:43]
s_and_b32 s55, s55, 524287
s_or_b32 s55, s55, s75
buffer_load_dwordx4 a[8:11], %[vY4], %[sWS], s55 offen offset:0 sc1
v_mfma_f32_16x16x32_bf16 v[44:47], a[184:187], a[108:111], v[44:47]
ds_read_b128 a[152:155], %[vXr0] offset:20480
v_mfma_f32_16x16x32_bf16 v[48:51], a[188:191], a[104:107], v[48:51]
ds_read_b128 a[156:159], %[vXr0] offset:21504
v_mfma_f32_16x16x32_bf16 v[52:55], a[188:191], a[108:111], v[52:55]
v_exp_f32_e64 v26, v26
)"
R"(v_mfma_f32_16x16x32_bf16 v[56:59], a[192:195], a[104:107], v[56:59]
ds_read_b128 a[160:163], %[vXr0] offset:22528
v_mfma_f32_16x16x32_bf16 v[60:63], a[192:195], a[108:111], v[60:63]
ds_read_b128 a[164:167], %[vXr0] offset:23552
v_mfma_f32_16x16x32_bf16 v[64:67], a[196:199], a[104:107], v[64:67]
buffer_load_dwordx4 a[12:15], %[vY4], %[sWS], s55 offen offset:1024 sc1
v_mfma_f32_16x16x32_bf16 v[68:71], a[196:199], a[108:111], v[68:71]
ds_read_b128 a[168:171], %[vXr0] offset:24576
v_exp_f32_e64 v27, v27
v_mfma_f32_16x16x32_bf16 v[72:75], a[200:203], a[104:107], v[72:75]
ds_read_b128 a[172:175], %[vXr0] offset:25600
v_mfma_f32_16x16x32_bf16 v[76:79], a[200:203], a[108:111], v[76:79]
v_add_f32 v26, 1.0, v26
v_add_f32 v27, 1.0, v27
v_mfma_f32_16x16x32_bf16 v[80:83], a[204:207], a[104:107], v[80:83]
ds_read_b128 a[176:179], %[vXr0] offset:26624
v_mfma_f32_16x16x32_bf16 v[84:87], a[204:207], a[108:111], v[84:87]
buffer_load_dwordx4 %[vL16], %[sWS], s48 offen offset:2048 sc1 lds
v_mfma_f32_16x16x32_bf16 v[88:91], a[208:211], a[104:107], v[88:91]
v_rcp_f32_e64 v26, v26
v_mfma_f32_16x16x32_bf16 v[92:95], a[208:211], a[108:111], v[92:95]
buffer_load_dwordx4 %[vL16], %[sWS], s48 offen offset:3072 sc1 lds
v_mfma_f32_16x16x32_bf16 v[96:99], a[212:215], a[104:107], v[96:99]
v_rcp_f32_e64 v27, v27
v_mfma_f32_16x16x32_bf16 v[100:103], a[212:215], a[108:111], v[100:103]
s_waitcnt vmcnt(15) lgkmcnt(0)
v_mul_f32 v166, v166, v26
v_mul_f32 v167, v167, v27
v_mfma_f32_16x16x32_bf16 v[32:35], a[144:147], a[112:115], v[32:35]
s_add_u32 s55, s26, 557056
ds_read_b128 a[180:183], %[vXr0] offset:27648
v_mfma_f32_16x16x32_bf16 v[36:39], a[144:147], a[116:119], v[36:39]
ds_read_b128 a[184:187], %[vXr0] offset:28672
v_accvgpr_read_b32 v192, a248
v_mfma_f32_16x16x32_bf16 v[40:43], a[148:151], a[112:115], v[40:43]
s_and_b32 s55, s55, 524287
s_or_b32 s55, s55, s75
buffer_load_dwordx4 a[16:19], %[vY4], %[sWS], s55 offen offset:0 sc1
v_mfma_f32_16x16x32_bf16 v[44:47], a[148:151], a[116:119], v[44:47]
ds_read_b128 a[188:191], %[vXr0] offset:29696
v_mfma_f32_16x16x32_bf16 v[48:51], a[152:155], a[112:115], v[48:51]
ds_read_b128 a[192:195], %[vXr0] offset:30720
v_accvgpr_read_b32 v193, a249
v_mfma_f32_16x16x32_bf16 v[52:55], a[152:155], a[116:119], v[52:55]
v_accvgpr_read_b32 v194, a250
v_mfma_f32_16x16x32_bf16 v[56:59], a[156:159], a[112:115], v[56:59]
ds_read_b128 a[196:199], %[vXr0] offset:31744
v_accvgpr_read_b32 v195, a251
v_mfma_f32_16x16x32_bf16 v[60:63], a[156:159], a[116:119], v[60:63]
ds_read_b128 a[200:203], %[vXr0] offset:32768
v_mfma_f32_16x16x32_bf16 v[64:67], a[160:163], a[112:115], v[64:67]
buffer_load_dwordx4 a[20:23], %[vY4], %[sWS], s55 offen offset:1024 sc1
v_mfma_f32_16x16x32_bf16 v[68:71], a[160:163], a[116:119], v[68:71]
ds_read_b128 a[204:207], %[vXr0] offset:33792
v_accvgpr_read_b32 v196, a252
v_mfma_f32_16x16x32_bf16 v[72:75], a[164:167], a[112:115], v[72:75]
ds_read_b128 a[208:211], %[vXr0] offset:34816
v_mfma_f32_16x16x32_bf16 v[76:79], a[164:167], a[116:119], v[76:79]
s_add_u32 m0, %[sLx], 77824
v_accvgpr_read_b32 v197, a253
v_accvgpr_read_b32 v198, a254
v_mfma_f32_16x16x32_bf16 v[80:83], a[168:171], a[112:115], v[80:83]
s_add_u32 s48, s48, 4096
ds_read_b128 a[212:215], %[vXr0] offset:35840
v_mfma_f32_16x16x32_bf16 v[84:87], a[168:171], a[116:119], v[84:87]
buffer_load_dwordx4 %[vL16], %[sWS], s48 offen offset:0 sc1 lds
v_mfma_f32_16x16x32_bf16 v[88:91], a[172:175], a[112:115], v[88:91]
v_accvgpr_read_b32 v199, a255
v_fma_f32 v230, v227, v192, v193
v_mfma_f32_16x16x32_bf16 v[92:95], a[172:175], a[116:119], v[92:95]
buffer_load_dwordx4 %[vL16], %[sWS], s48 offen offset:1024 sc1 lds
v_mfma_f32_16x16x32_bf16 v[96:99], a[176:179], a[112:115], v[96:99]
v_fma_f32 v192, v229, v192, v193
v_fma_f32 v168, v168, v226, v230
v_mfma_f32_16x16x32_bf16 v[100:103], a[176:179], a[116:119], v[100:103]
s_waitcnt vmcnt(12) lgkmcnt(0)
s_barrier
v_fma_f32 v172, v172, v228, v192
v_fma_f32 v231, v227, v194, v195
v_mfma_f32_16x16x32_bf16 v[32:35], a[180:183], a[120:123], v[32:35]
s_add_u32 s55, s26, 573440
ds_read_b128 a[144:147], %[vXr1] offset:0
v_mfma_f32_16x16x32_bf16 v[36:39], a[180:183], a[124:127], v[36:39]
ds_read_b128 a[148:151], %[vXr1] offset:1024
v_fma_f32 v194, v229, v194, v195
v_mfma_f32_16x16x32_bf16 v[40:43], a[184:187], a[120:123], v[40:43]
s_and_b32 s55, s55, 524287
s_or_b32 s55, s55, s75
buffer_load_dwordx4 a[24:27], %[vY4], %[sWS], s55 offen offset:0 sc1
v_mfma_f32_16x16x32_bf16 v[44:47], a[184:187], a[124:127], v[44:47]
ds_read_b128 a[152:155], %[vXr1] offset:2048
v_mfma_f32_16x16x32_bf16 v[48:51], a[188:191], a[120:123], v[48:51]
ds_read_b128 a[156:159], %[vXr1] offset:3072
v_fma_f32 v169, v169, v226, v231
v_mfma_f32_16x16x32_bf16 v[52:55], a[188:191], a[124:127], v[52:55]
v_fma_f32 v173, v173, v228, v194
v_mfma_f32_16x16x32_bf16 v[56:59], a[192:195], a[120:123], v[56:59]
ds_read_b128 a[160:163], %[vXr1] offset:4096
v_fma_f32 v230, v227, v196, v197
v_mfma_f32_16x16x32_bf16 v[60:63], a[192:195], a[124:127], v[60:63]
ds_read_b128 a[164:167], %[vXr1] offset:5120
v_mfma_f32_16x16x32_bf16 v[64:67], a[196:199], a[120:123], v[64:67]
buffer_load_dwordx4 a[28:31], %[vY4], %[sWS], s55 offen offset:1024 sc1
v_mfma_f32_16x16x32_bf16 v[68:71], a[196:199], a[124:127], v[68:71]
ds_read_b128 a[168:171], %[vXr1] offset:6144
v_fma_f32 v196, v229, v196, v197
v_mfma_f32_16x16x32_bf16 v[72:75], a[200:203], a[120:123], v[72:75]
ds_read_b128 a[172:175], %[vXr1] offset:7168
v_mfma_f32_16x16x32_bf16 v[76:79], a[200:203], a[124:127], v[76:79]
v_fma_f32 v170, v170, v226, v230
v_fma_f32 v174, v174, v228, v196
v_mfma_f32_16x16x32_bf16 v[80:83], a[204:207], a[120:123], v[80:83]
ds_read_b128 a[176:179], %[vXr1] offset:8192
v_mfma_f32_16x16x32_bf16 v[84:87], a[204:207], a[124:127], v[84:87]
buffer_load_dwordx4 %[vL16], %[sWS], s48 offen offset:2048 sc1 lds
v_mfma_f32_16x16x32_bf16 v[88:91], a[208:211], a[120:123], v[88:91]
v_fma_f32 v231, v227, v198, v199
v_fma_f32 v198, v229, v198, v199
v_mfma_f32_16x16x32_bf16 v[92:95], a[208:211], a[124:127], v[92:95]
buffer_load_dwordx4 %[vL16], %[sWS], s48 offen offset:3072 sc1 lds
v_mfma_f32_16x16x32_bf16 v[96:99], a[212:215], a[120:123], v[96:99]
v_fma_f32 v171, v171, v226, v231
v_fma_f32 v175, v175, v228, v198
v_mfma_f32_16x16x32_bf16 v[100:103], a[212:215], a[124:127], v[100:103]
s_waitcnt vmcnt(14) lgkmcnt(0)
v_fma_f32 v24, v168, v168, s50
v_fma_f32 v25, v169, v169, s50
v_mfma_f32_16x16x32_bf16 v[32:35], a[144:147], a[0:3], v[32:35]
s_add_u32 m0, %[sLx], 81920
ds_read_b128 a[180:183], %[vXr1] offset:9216
v_mfma_f32_16x16x32_bf16 v[36:39], a[144:147], a[4:7], v[36:39]
s_lshl1_add_u32 s48, s54, s73
ds_read_b128 a[184:187], %[vXr1] offset:10240
v_mul_f32 v24, v24, v168
v_mfma_f32_16x16x32_bf16 v[40:43], a[148:151], a[0:3], v[40:43]
buffer_load_dwordx4 %[vL16], %[sWS], s48 offen offset:0 sc1 lds
v_mfma_f32_16x16x32_bf16 v[44:47], a[148:151], a[4:7], v[44:47]
s_add_u32 s55, s26, 589824
ds_read_b128 a[188:191], %[vXr1] offset:11264
v_mfma_f32_16x16x32_bf16 v[48:51], a[152:155], a[0:3], v[48:51]
ds_read_b128 a[192:195], %[vXr1] offset:12288
v_mul_f32 v25, v25, v169
v_mfma_f32_16x16x32_bf16 v[52:55], a[152:155], a[4:7], v[52:55]
s_and_b32 s55, s55, 524287
s_or_b32 s55, s55, s75
buffer_load_dwordx4 a[32:35], %[vY4], %[sWS], s55 offen offset:0 sc1
v_mfma_f32_16x16x32_bf16 v[56:59], a[156:159], a[0:3], v[56:59]
ds_read_b128 a[196:199], %[vXr1] offset:13312
v_mfma_f32_16x16x32_bf16 v[60:63], a[156:159], a[4:7], v[60:63]
ds_read_b128 a[200:203], %[vXr1] offset:14336
v_mfma_f32_16x16x32_bf16 v[64:67], a[160:163], a[0:3], v[64:67]
v_exp_f32_e64 v24, v24
v_mfma_f32_16x16x32_bf16 v[68:71], a[160:163], a[4:7], v[68:71]
ds_read_b128 a[204:207], %[vXr1] offset:15360
v_mfma_f32_16x16x32_bf16 v[72:75], a[164:167], a[0:3], v[72:75]
ds_read_b128 a[208:211], %[vXr1] offset:16384
v_exp_f32_e64 v25, v25
v_mfma_f32_16x16x32_bf16 v[76:79], a[164:167], a[4:7], v[76:79]
buffer_load_dwordx4 a[36:39], %[vY4], %[sWS], s55 offen offset:1024 sc1
v_mfma_f32_16x16x32_bf16 v[80:83], a[168:171], a[0:3], v[80:83]
s_add_u32 m0, %[sLx], 110592
ds_read_b128 a[212:215], %[vXr1] offset:17408
v_mfma_f32_16x16x32_bf16 v[84:87], a[168:171], a[4:7], v[84:87]
v_add_f32 v24, 1.0, v24
v_add_f32 v25, 1.0, v25
v_mfma_f32_16x16x32_bf16 v[88:91], a[172:175], a[0:3], v[88:91]
s_add_u32 s54, s26, 131072
s_and_b32 s54, s54, 458752
s_add_u32 s48, s54, s72
buffer_load_dwordx4 %[vL16], %[sWS], s48 offen offset:0 sc1 lds
v_mfma_f32_16x16x32_bf16 v[92:95], a[172:175], a[4:7], v[92:95]
v_rcp_f32 v24, v24
v_mfma_f32_16x16x32_bf16 v[96:99], a[176:179], a[0:3], v[96:99]
buffer_load_dwordx4 %[vL16], %[sWS], s48 offen offset:1024 sc1 lds
v_mfma_f32_16x16x32_bf16 v[100:103], a[176:179], a[4:7], v[100:103]
s_waitcnt vmcnt(15) lgkmcnt(0)
v_rcp_f32 v25, v25
v_mul_f32_e64 v168, v168, v24
v_mfma_f32_16x16x32_bf16 v[32:35], a[180:183], a[8:11], v[32:35]
s_add_u32 s55, s26, 606208
ds_read_b128 a[144:147], %[vXr1] offset:18432
v_mfma_f32_16x16x32_bf16 v[36:39], a[180:183], a[12:15], v[36:39]
ds_read_b128 a[148:151], %[vXr1] offset:19456
v_mul_f32 v169, v169, v25
v_mfma_f32_16x16x32_bf16 v[40:43], a[184:187], a[8:11], v[40:43]
s_and_b32 s55, s55, 524287
s_or_b32 s55, s55, s75
buffer_load_dwordx4 a[40:43], %[vY4], %[sWS], s55 offen offset:0 sc1
v_mfma_f32_16x16x32_bf16 v[44:47], a[184:187], a[12:15], v[44:47]
ds_read_b128 a[152:155], %[vXr1] offset:20480
v_mfma_f32_16x16x32_bf16 v[48:51], a[188:191], a[8:11], v[48:51]
ds_read_b128 a[156:159], %[vXr1] offset:21504
v_fma_f32 v26, v170, v170, s50
v_mfma_f32_16x16x32_bf16 v[52:55], a[188:191], a[12:15], v[52:55]
v_fma_f32 v27, v171, v171, s50
v_mfma_f32_16x16x32_bf16 v[56:59], a[192:195], a[8:11], v[56:59]
ds_read_b128 a[160:163], %[vXr1] offset:22528
v_mul_f32_e64 v26, v26, v170
v_mfma_f32_16x16x32_bf16 v[60:63], a[192:195], a[12:15], v[60:63]
ds_read_b128 a[164:167], %[vXr1] offset:23552
)"
R"(v_mfma_f32_16x16x32_bf16 v[64:67], a[196:199], a[8:11], v[64:67]
buffer_load_dwordx4 a[44:47], %[vY4], %[sWS], s55 offen offset:1024 sc1
v_mfma_f32_16x16x32_bf16 v[68:71], a[196:199], a[12:15], v[68:71]
ds_read_b128 a[168:171], %[vXr1] offset:24576
v_mul_f32_e64 v27, v27, v171
v_mfma_f32_16x16x32_bf16 v[72:75], a[200:203], a[8:11], v[72:75]
ds_read_b128 a[172:175], %[vXr1] offset:25600
v_mfma_f32_16x16x32_bf16 v[76:79], a[200:203], a[12:15], v[76:79]
v_exp_f32_e64 v26, v26
v_mfma_f32_16x16x32_bf16 v[80:83], a[204:207], a[8:11], v[80:83]
ds_read_b128 a[176:179], %[vXr1] offset:26624
v_mfma_f32_16x16x32_bf16 v[84:87], a[204:207], a[12:15], v[84:87]
buffer_load_dwordx4 %[vL16], %[sWS], s48 offen offset:2048 sc1 lds
v_mfma_f32_16x16x32_bf16 v[88:91], a[208:211], a[8:11], v[88:91]
v_exp_f32_e64 v27, v27
v_mfma_f32_16x16x32_bf16 v[92:95], a[208:211], a[12:15], v[92:95]
buffer_load_dwordx4 %[vL16], %[sWS], s48 offen offset:3072 sc1 lds
v_mfma_f32_16x16x32_bf16 v[96:99], a[212:215], a[8:11], v[96:99]
v_add_f32 v26, 1.0, v26
v_add_f32 v27, 1.0, v27
v_mfma_f32_16x16x32_bf16 v[100:103], a[212:215], a[12:15], v[100:103]
s_waitcnt vmcnt(15) lgkmcnt(0)
v_rcp_f32 v26, v26
v_mfma_f32_16x16x32_bf16 v[32:35], a[144:147], a[16:19], v[32:35]
s_add_u32 s55, s26, 622592
ds_read_b128 a[180:183], %[vXr1] offset:27648
v_mfma_f32_16x16x32_bf16 v[36:39], a[144:147], a[20:23], v[36:39]
ds_read_b128 a[184:187], %[vXr1] offset:28672
v_mfma_f32_16x16x32_bf16 v[40:43], a[148:151], a[16:19], v[40:43]
s_and_b32 s55, s55, 524287
s_or_b32 s55, s55, s75
buffer_load_dwordx4 a[48:51], %[vY4], %[sWS], s55 offen offset:0 sc1
v_mfma_f32_16x16x32_bf16 v[44:47], a[148:151], a[20:23], v[44:47]
ds_read_b128 a[188:191], %[vXr1] offset:29696
v_mfma_f32_16x16x32_bf16 v[48:51], a[152:155], a[16:19], v[48:51]
ds_read_b128 a[192:195], %[vXr1] offset:30720
v_mfma_f32_16x16x32_bf16 v[52:55], a[152:155], a[20:23], v[52:55]
v_rcp_f32 v27, v27
v_mul_f32_e64 v170, v170, v26
s_add_u32 s58, %[sNt], 0
s_and_b32 s58, s58, 3
v_mfma_f32_16x16x32_bf16 v[56:59], a[156:159], a[16:19], v[56:59]
ds_read_b128 a[196:199], %[vXr1] offset:31744
v_mfma_f32_16x16x32_bf16 v[60:63], a[156:159], a[20:23], v[60:63]
ds_read_b128 a[200:203], %[vXr1] offset:32768
v_mul_f32 v171, v171, v27
v_mfma_f32_16x16x32_bf16 v[64:67], a[160:163], a[16:19], v[64:67]
buffer_load_dwordx4 a[52:55], %[vY4], %[sWS], s55 offen offset:1024 sc1
s_lshl_b32 s58, s58, 14
v_mfma_f32_16x16x32_bf16 v[68:71], a[160:163], a[20:23], v[68:71]
ds_read_b128 a[204:207], %[vXr1] offset:33792
v_mfma_f32_16x16x32_bf16 v[72:75], a[164:167], a[16:19], v[72:75]
ds_read_b128 a[208:211], %[vXr1] offset:34816
v_mfma_f32_16x16x32_bf16 v[76:79], a[164:167], a[20:23], v[76:79]
s_add_u32 m0, %[sLx], 114688
v_fma_f32 v24, v172, v172, s50
v_fma_f32 v25, v173, v173, s50
s_add_u32 s58, s58, s36
s_add_u32 s59, %[sNt], 1
v_mfma_f32_16x16x32_bf16 v[80:83], a[168:171], a[16:19], v[80:83]
s_add_u32 s48, s48, 4096
ds_read_b128 a[212:215], %[vXr1] offset:35840
v_mfma_f32_16x16x32_bf16 v[84:87], a[168:171], a[20:23], v[84:87]
buffer_load_dwordx4 %[vL16], %[sWS], s48 offen offset:0 sc1 lds
v_mul_f32_e64 v24, v24, v172
v_mfma_f32_16x16x32_bf16 v[88:91], a[172:175], a[16:19], v[88:91]
v_mul_f32_e64 v25, v25, v173
s_and_b32 s59, s59, 3
s_lshl_b32 s59, s59, 14
v_mfma_f32_16x16x32_bf16 v[92:95], a[172:175], a[20:23], v[92:95]
buffer_load_dwordx4 %[vL16], %[sWS], s48 offen offset:1024 sc1 lds
v_mfma_f32_16x16x32_bf16 v[96:99], a[176:179], a[16:19], v[96:99]
v_exp_f32 v24, v24
s_add_u32 s59, s59, s36
v_mfma_f32_16x16x32_bf16 v[100:103], a[176:179], a[20:23], v[100:103]
s_waitcnt vmcnt(12) lgkmcnt(0)
s_barrier
v_exp_f32_e64 v25, v25
s_add_u32 s60, %[sNt], 2
s_and_b32 s60, s60, 3
v_mfma_f32_16x16x32_bf16 v[32:35], a[180:183], a[24:27], v[32:35]
s_add_u32 s55, s26, 638976
ds_read_b128 a[144:147], %[vXr2] offset:0
v_mfma_f32_16x16x32_bf16 v[36:39], a[180:183], a[28:31], v[36:39]
ds_read_b128 a[148:151], %[vXr2] offset:1024
v_mfma_f32_16x16x32_bf16 v[40:43], a[184:187], a[24:27], v[40:43]
s_and_b32 s55, s55, 524287
s_or_b32 s55, s55, s75
buffer_load_dwordx4 a[56:59], %[vY4], %[sWS], s55 offen offset:0 sc1
v_add_f32 v24, 1.0, v24
v_mfma_f32_16x16x32_bf16 v[44:47], a[184:187], a[28:31], v[44:47]
ds_read_b128 a[152:155], %[vXr2] offset:2048
v_mfma_f32_16x16x32_bf16 v[48:51], a[188:191], a[24:27], v[48:51]
ds_read_b128 a[156:159], %[vXr2] offset:3072
v_mfma_f32_16x16x32_bf16 v[52:55], a[188:191], a[28:31], v[52:55]
v_add_f32_e64 v25, 1.0, v25
s_lshl_b32 s60, s60, 14
s_add_u32 s60, s60, s36
v_mfma_f32_16x16x32_bf16 v[56:59], a[192:195], a[24:27], v[56:59]
ds_read_b128 a[160:163], %[vXr2] offset:4096
v_mfma_f32_16x16x32_bf16 v[60:63], a[192:195], a[28:31], v[60:63]
ds_read_b128 a[164:167], %[vXr2] offset:5120
v_rcp_f32_e64 v24, v24
v_mfma_f32_16x16x32_bf16 v[64:67], a[196:199], a[24:27], v[64:67]
buffer_load_dwordx4 a[60:63], %[vY4], %[sWS], s55 offen offset:1024 sc1
v_mfma_f32_16x16x32_bf16 v[68:71], a[196:199], a[28:31], v[68:71]
ds_read_b128 a[168:171], %[vXr2] offset:6144
v_mfma_f32_16x16x32_bf16 v[72:75], a[200:203], a[24:27], v[72:75]
ds_read_b128 a[172:175], %[vXr2] offset:7168
v_mfma_f32_16x16x32_bf16 v[76:79], a[200:203], a[28:31], v[76:79]
v_rcp_f32_e64 v25, v25
s_add_u32 s61, %[sNt], 3
s_and_b32 s61, s61, 3
v_mfma_f32_16x16x32_bf16 v[80:83], a[204:207], a[24:27], v[80:83]
ds_read_b128 a[176:179], %[vXr2] offset:8192
v_mfma_f32_16x16x32_bf16 v[84:87], a[204:207], a[28:31], v[84:87]
buffer_load_dwordx4 %[vL16], %[sWS], s48 offen offset:2048 sc1 lds
v_mul_f32_e64 v172, v172, v24
v_mfma_f32_16x16x32_bf16 v[88:91], a[208:211], a[24:27], v[88:91]
v_mul_f32 v173, v173, v25
s_lshl_b32 s61, s61, 14
v_mfma_f32_16x16x32_bf16 v[92:95], a[208:211], a[28:31], v[92:95]
buffer_load_dwordx4 %[vL16], %[sWS], s48 offen offset:3072 sc1 lds
v_fma_f32 v26, v174, v174, s50
v_mfma_f32_16x16x32_bf16 v[96:99], a[212:215], a[24:27], v[96:99]
v_fma_f32 v27, v175, v175, s50
s_add_u32 s61, s61, s36
s_mov_b32 s62, s37
v_mfma_f32_16x16x32_bf16 v[100:103], a[212:215], a[28:31], v[100:103]
s_waitcnt vmcnt(14) lgkmcnt(0)
v_mul_f32 v26, v26, v174
v_mul_f32_e64 v27, v27, v175
v_add_u32 v249, 4096, %[vL16]
v_lshrrev_b32 v250, 1, %[vL16]
v_mfma_f32_16x16x32_bf16 v[32:35], a[144:147], a[32:35], v[32:35]
s_add_u32 m0, %[sLx], 118784
ds_read_b128 a[180:183], %[vXr2] offset:9216
v_mfma_f32_16x16x32_bf16 v[36:39], a[144:147], a[36:39], v[36:39]
s_lshl1_add_u32 s48, s54, s73
ds_read_b128 a[184:187], %[vXr2] offset:10240
v_mfma_f32_16x16x32_bf16 v[40:43], a[148:151], a[32:35], v[40:43]
buffer_load_dwordx4 %[vL16], %[sWS], s48 offen offset:0 sc1 lds
v_mfma_f32_16x16x32_bf16 v[44:47], a[148:151], a[36:39], v[44:47]
s_add_u32 s55, s26, 655360
ds_read_b128 a[188:191], %[vXr2] offset:11264
v_mfma_f32_16x16x32_bf16 v[48:51], a[152:155], a[32:35], v[48:51]
ds_read_b128 a[192:195], %[vXr2] offset:12288
v_mfma_f32_16x16x32_bf16 v[52:55], a[152:155], a[36:39], v[52:55]
s_and_b32 s55, s55, 524287
s_or_b32 s55, s55, s75
buffer_load_dwordx4 a[64:67], %[vY4], %[sWS], s55 offen offset:0 sc1
v_exp_f32 v26, v26
v_mfma_f32_16x16x32_bf16 v[56:59], a[156:159], a[32:35], v[56:59]
ds_read_b128 a[196:199], %[vXr2] offset:13312
v_mfma_f32_16x16x32_bf16 v[60:63], a[156:159], a[36:39], v[60:63]
ds_read_b128 a[200:203], %[vXr2] offset:14336
v_mfma_f32_16x16x32_bf16 v[64:67], a[160:163], a[32:35], v[64:67]
v_exp_f32_e64 v27, v27
v_mfma_f32_16x16x32_bf16 v[68:71], a[160:163], a[36:39], v[68:71]
ds_read_b128 a[204:207], %[vXr2] offset:15360
v_add_f32_e64 v26, 1.0, v26
v_mfma_f32_16x16x32_bf16 v[72:75], a[164:167], a[32:35], v[72:75]
ds_read_b128 a[208:211], %[vXr2] offset:16384
v_mfma_f32_16x16x32_bf16 v[76:79], a[164:167], a[36:39], v[76:79]
buffer_load_dwordx4 a[68:71], %[vY4], %[sWS], s55 offen offset:1024 sc1
v_mfma_f32_16x16x32_bf16 v[80:83], a[168:171], a[32:35], v[80:83]
s_add_u32 m0, %[sLx], 0
ds_read_b128 a[212:215], %[vXr2] offset:17408
v_mfma_f32_16x16x32_bf16 v[84:87], a[168:171], a[36:39], v[84:87]
v_add_f32 v27, 1.0, v27
v_mfma_f32_16x16x32_bf16 v[88:91], a[172:175], a[32:35], v[88:91]
s_add_u32 s54, s26, 196608
s_and_b32 s54, s54, 458752
s_add_u32 s48, s54, s72
buffer_load_dwordx4 %[vL16], %[sWS], s48 offen offset:0 sc1 lds
v_rcp_f32 v26, v26
v_mfma_f32_16x16x32_bf16 v[92:95], a[172:175], a[36:39], v[92:95]
v_rcp_f32_e64 v27, v27
v_mfma_f32_16x16x32_bf16 v[96:99], a[176:179], a[32:35], v[96:99]
buffer_load_dwordx4 %[vL16], %[sWS], s48 offen offset:1024 sc1 lds
v_mfma_f32_16x16x32_bf16 v[100:103], a[176:179], a[36:39], v[100:103]
s_waitcnt vmcnt(15) lgkmcnt(0)
v_mul_f32 v174, v174, v26
v_mul_f32 v175, v175, v27
v_mfma_f32_16x16x32_bf16 v[32:35], a[180:183], a[40:43], v[32:35]
s_add_u32 s55, s26, 671744
ds_read_b128 a[144:147], %[vXr2] offset:18432
v_mfma_f32_16x16x32_bf16 v[36:39], a[180:183], a[44:47], v[36:39]
ds_read_b128 a[148:151], %[vXr2] offset:19456
v_mfma_f32_16x16x32_bf16 v[40:43], a[184:187], a[40:43], v[40:43]
s_and_b32 s55, s55, 524287
s_or_b32 s55, s55, s75
buffer_load_dwordx4 a[72:75], %[vY4], %[sWS], s55 offen offset:0 sc1
v_mfma_f32_16x16x32_bf16 v[44:47], a[184:187], a[44:47], v[44:47]
ds_read_b128 a[152:155], %[vXr2] offset:20480
v_mfma_f32_16x16x32_bf16 v[48:51], a[188:191], a[40:43], v[48:51]
ds_read_b128 a[156:159], %[vXr2] offset:21504
v_mfma_f32_16x16x32_bf16 v[52:55], a[188:191], a[44:47], v[52:55]
v_mfma_f32_16x16x32_bf16 v[56:59], a[192:195], a[40:43], v[56:59]
ds_read_b128 a[160:163], %[vXr2] offset:22528
)"
R"(v_mfma_f32_16x16x32_bf16 v[60:63], a[192:195], a[44:47], v[60:63]
ds_read_b128 a[164:167], %[vXr2] offset:23552
v_mfma_f32_16x16x32_bf16 v[64:67], a[196:199], a[40:43], v[64:67]
buffer_load_dwordx4 a[76:79], %[vY4], %[sWS], s55 offen offset:1024 sc1
v_mfma_f32_16x16x32_bf16 v[68:71], a[196:199], a[44:47], v[68:71]
ds_read_b128 a[168:171], %[vXr2] offset:24576
v_mfma_f32_16x16x32_bf16 v[72:75], a[200:203], a[40:43], v[72:75]
ds_read_b128 a[172:175], %[vXr2] offset:25600
v_mfma_f32_16x16x32_bf16 v[76:79], a[200:203], a[44:47], v[76:79]
v_mfma_f32_16x16x32_bf16 v[80:83], a[204:207], a[40:43], v[80:83]
ds_read_b128 a[176:179], %[vXr2] offset:26624
v_mfma_f32_16x16x32_bf16 v[84:87], a[204:207], a[44:47], v[84:87]
buffer_load_dwordx4 %[vL16], %[sWS], s48 offen offset:2048 sc1 lds
v_mfma_f32_16x16x32_bf16 v[88:91], a[208:211], a[40:43], v[88:91]
v_mfma_f32_16x16x32_bf16 v[92:95], a[208:211], a[44:47], v[92:95]
buffer_load_dwordx4 %[vL16], %[sWS], s48 offen offset:3072 sc1 lds
v_mfma_f32_16x16x32_bf16 v[96:99], a[212:215], a[40:43], v[96:99]
v_mfma_f32_16x16x32_bf16 v[100:103], a[212:215], a[44:47], v[100:103]
s_waitcnt vmcnt(15) lgkmcnt(0)
v_mfma_f32_16x16x32_bf16 v[32:35], a[144:147], a[48:51], v[32:35]
s_add_u32 s55, s26, 688128
ds_read_b128 a[180:183], %[vXr2] offset:27648
v_mfma_f32_16x16x32_bf16 v[36:39], a[144:147], a[52:55], v[36:39]
ds_read_b128 a[184:187], %[vXr2] offset:28672
v_mfma_f32_16x16x32_bf16 v[40:43], a[148:151], a[48:51], v[40:43]
s_and_b32 s55, s55, 524287
s_or_b32 s55, s55, s75
buffer_load_dwordx4 a[80:83], %[vY4], %[sWS], s55 offen offset:0 sc1
v_mfma_f32_16x16x32_bf16 v[44:47], a[148:151], a[52:55], v[44:47]
ds_read_b128 a[188:191], %[vXr2] offset:29696
v_mfma_f32_16x16x32_bf16 v[48:51], a[152:155], a[48:51], v[48:51]
ds_read_b128 a[192:195], %[vXr2] offset:30720
v_mfma_f32_16x16x32_bf16 v[52:55], a[152:155], a[52:55], v[52:55]
v_mfma_f32_16x16x32_bf16 v[56:59], a[156:159], a[48:51], v[56:59]
ds_read_b128 a[196:199], %[vXr2] offset:31744
v_mfma_f32_16x16x32_bf16 v[60:63], a[156:159], a[52:55], v[60:63]
ds_read_b128 a[200:203], %[vXr2] offset:32768
v_mfma_f32_16x16x32_bf16 v[64:67], a[160:163], a[48:51], v[64:67]
buffer_load_dwordx4 a[84:87], %[vY4], %[sWS], s55 offen offset:1024 sc1
v_mfma_f32_16x16x32_bf16 v[68:71], a[160:163], a[52:55], v[68:71]
ds_read_b128 a[204:207], %[vXr2] offset:33792
v_mfma_f32_16x16x32_bf16 v[72:75], a[164:167], a[48:51], v[72:75]
ds_read_b128 a[208:211], %[vXr2] offset:34816
v_mfma_f32_16x16x32_bf16 v[76:79], a[164:167], a[52:55], v[76:79]
s_add_u32 m0, %[sLx], 4096
v_mfma_f32_16x16x32_bf16 v[80:83], a[168:171], a[48:51], v[80:83]
s_add_u32 s48, s48, 4096
ds_read_b128 a[212:215], %[vXr2] offset:35840
v_mfma_f32_16x16x32_bf16 v[84:87], a[168:171], a[52:55], v[84:87]
buffer_load_dwordx4 %[vL16], %[sWS], s48 offen offset:0 sc1 lds
v_mfma_f32_16x16x32_bf16 v[88:91], a[172:175], a[48:51], v[88:91]
v_mfma_f32_16x16x32_bf16 v[92:95], a[172:175], a[52:55], v[92:95]
buffer_load_dwordx4 %[vL16], %[sWS], s48 offen offset:1024 sc1 lds
v_mfma_f32_16x16x32_bf16 v[96:99], a[176:179], a[48:51], v[96:99]
v_mfma_f32_16x16x32_bf16 v[100:103], a[176:179], a[52:55], v[100:103]
s_waitcnt vmcnt(12) lgkmcnt(0)
s_barrier
v_mfma_f32_16x16x32_bf16 v[32:35], a[180:183], a[56:59], v[32:35]
s_add_u32 s55, s26, 704512
ds_read_b128 a[144:147], %[vXr3] offset:0
v_mfma_f32_16x16x32_bf16 v[36:39], a[180:183], a[60:63], v[36:39]
ds_read_b128 a[148:151], %[vXr3] offset:1024
v_mfma_f32_16x16x32_bf16 v[40:43], a[184:187], a[56:59], v[40:43]
s_and_b32 s55, s55, 524287
s_or_b32 s55, s55, s75
buffer_load_dwordx4 a[88:91], %[vY4], %[sWS], s55 offen offset:0 sc1
v_mfma_f32_16x16x32_bf16 v[44:47], a[184:187], a[60:63], v[44:47]
ds_read_b128 a[152:155], %[vXr3] offset:2048
v_mfma_f32_16x16x32_bf16 v[48:51], a[188:191], a[56:59], v[48:51]
ds_read_b128 a[156:159], %[vXr3] offset:3072
v_mfma_f32_16x16x32_bf16 v[52:55], a[188:191], a[60:63], v[52:55]
v_mfma_f32_16x16x32_bf16 v[56:59], a[192:195], a[56:59], v[56:59]
ds_read_b128 a[160:163], %[vXr3] offset:4096
v_mfma_f32_16x16x32_bf16 v[60:63], a[192:195], a[60:63], v[60:63]
ds_read_b128 a[164:167], %[vXr3] offset:5120
v_mfma_f32_16x16x32_bf16 v[64:67], a[196:199], a[56:59], v[64:67]
buffer_load_dwordx4 a[92:95], %[vY4], %[sWS], s55 offen offset:1024 sc1
v_mfma_f32_16x16x32_bf16 v[68:71], a[196:199], a[60:63], v[68:71]
LPb_8:
s_cbranch_execz LPb_12
ds_read_b128 a[168:171], %[vXr3] offset:6144
v_mfma_f32_16x16x32_bf16 v[72:75], a[200:203], a[56:59], v[72:75]
ds_read_b128 a[172:175], %[vXr3] offset:7168
v_mfma_f32_16x16x32_bf16 v[76:79], a[200:203], a[60:63], v[76:79]
v_mfma_f32_16x16x32_bf16 v[80:83], a[204:207], a[56:59], v[80:83]
ds_read_b128 a[176:179], %[vXr3] offset:8192
v_mfma_f32_16x16x32_bf16 v[84:87], a[204:207], a[60:63], v[84:87]
buffer_load_dwordx4 %[vL16], %[sWS], s48 offen offset:2048 sc1 lds
v_mfma_f32_16x16x32_bf16 v[88:91], a[208:211], a[56:59], v[88:91]
v_mfma_f32_16x16x32_bf16 v[92:95], a[208:211], a[60:63], v[92:95]
buffer_load_dwordx4 %[vL16], %[sWS], s48 offen offset:3072 sc1 lds
v_mfma_f32_16x16x32_bf16 v[96:99], a[212:215], a[56:59], v[96:99]
v_mfma_f32_16x16x32_bf16 v[100:103], a[212:215], a[60:63], v[100:103]
s_waitcnt vmcnt(14) lgkmcnt(0)
v_mfma_f32_16x16x32_bf16 v[32:35], a[144:147], a[64:67], v[32:35]
s_add_u32 m0, %[sLx], 8192
ds_read_b128 a[180:183], %[vXr3] offset:9216
v_mfma_f32_16x16x32_bf16 v[36:39], a[144:147], a[68:71], v[36:39]
s_lshl1_add_u32 s48, s54, s73
ds_read_b128 a[184:187], %[vXr3] offset:10240
v_mfma_f32_16x16x32_bf16 v[40:43], a[148:151], a[64:67], v[40:43]
buffer_load_dwordx4 %[vL16], %[sWS], s48 offen offset:0 sc1 lds
v_mfma_f32_16x16x32_bf16 v[44:47], a[148:151], a[68:71], v[44:47]
s_add_u32 s55, s26, 720896
ds_read_b128 a[188:191], %[vXr3] offset:11264
v_mfma_f32_16x16x32_bf16 v[48:51], a[152:155], a[64:67], v[48:51]
ds_read_b128 a[192:195], %[vXr3] offset:12288
v_mfma_f32_16x16x32_bf16 v[52:55], a[152:155], a[68:71], v[52:55]
s_and_b32 s55, s55, 524287
s_or_b32 s55, s55, s75
buffer_load_dwordx4 a[96:99], %[vY4], %[sWS], s55 offen offset:0 sc1
v_mfma_f32_16x16x32_bf16 v[56:59], a[156:159], a[64:67], v[56:59]
ds_read_b128 a[196:199], %[vXr3] offset:13312
v_mfma_f32_16x16x32_bf16 v[60:63], a[156:159], a[68:71], v[60:63]
ds_read_b128 a[200:203], %[vXr3] offset:14336
v_mfma_f32_16x16x32_bf16 v[64:67], a[160:163], a[64:67], v[64:67]
v_mfma_f32_16x16x32_bf16 v[68:71], a[160:163], a[68:71], v[68:71]
ds_read_b128 a[204:207], %[vXr3] offset:15360
v_mfma_f32_16x16x32_bf16 v[72:75], a[164:167], a[64:67], v[72:75]
ds_read_b128 a[208:211], %[vXr3] offset:16384
v_mfma_f32_16x16x32_bf16 v[76:79], a[164:167], a[68:71], v[76:79]
buffer_load_dwordx4 a[100:103], %[vY4], %[sWS], s55 offen offset:1024 sc1
v_mfma_f32_16x16x32_bf16 v[80:83], a[168:171], a[64:67], v[80:83]
s_add_u32 m0, %[sLx], 36864
ds_read_b128 a[212:215], %[vXr3] offset:17408
v_mfma_f32_16x16x32_bf16 v[84:87], a[168:171], a[68:71], v[84:87]
v_mfma_f32_16x16x32_bf16 v[88:91], a[172:175], a[64:67], v[88:91]
s_add_u32 s54, s26, 262144
s_and_b32 s54, s54, 458752
s_add_u32 s48, s54, s72
buffer_load_dwordx4 %[vL16], %[sWS], s48 offen offset:0 sc1 lds
v_mfma_f32_16x16x32_bf16 v[92:95], a[172:175], a[68:71], v[92:95]
v_mfma_f32_16x16x32_bf16 v[96:99], a[176:179], a[64:67], v[96:99]
buffer_load_dwordx4 %[vL16], %[sWS], s48 offen offset:1024 sc1 lds
v_mfma_f32_16x16x32_bf16 v[100:103], a[176:179], a[68:71], v[100:103]
s_waitcnt vmcnt(15) lgkmcnt(0)
v_mfma_f32_16x16x32_bf16 v[32:35], a[180:183], a[72:75], v[32:35]
s_add_u32 s55, s26, 737280
ds_read_b128 a[144:147], %[vXr3] offset:18432
v_mfma_f32_16x16x32_bf16 v[36:39], a[180:183], a[76:79], v[36:39]
ds_read_b128 a[148:151], %[vXr3] offset:19456
v_mfma_f32_16x16x32_bf16 v[40:43], a[184:187], a[72:75], v[40:43]
s_and_b32 s55, s55, 524287
s_or_b32 s55, s55, s75
buffer_load_dwordx4 a[104:107], %[vY4], %[sWS], s55 offen offset:0 sc1
v_mfma_f32_16x16x32_bf16 v[44:47], a[184:187], a[76:79], v[44:47]
ds_read_b128 a[152:155], %[vXr3] offset:20480
v_mfma_f32_16x16x32_bf16 v[48:51], a[188:191], a[72:75], v[48:51]
ds_read_b128 a[156:159], %[vXr3] offset:21504
v_mfma_f32_16x16x32_bf16 v[52:55], a[188:191], a[76:79], v[52:55]
v_mfma_f32_16x16x32_bf16 v[56:59], a[192:195], a[72:75], v[56:59]
ds_read_b128 a[160:163], %[vXr3] offset:22528
v_mfma_f32_16x16x32_bf16 v[60:63], a[192:195], a[76:79], v[60:63]
ds_read_b128 a[164:167], %[vXr3] offset:23552
v_mfma_f32_16x16x32_bf16 v[64:67], a[196:199], a[72:75], v[64:67]
buffer_load_dwordx4 a[108:111], %[vY4], %[sWS], s55 offen offset:1024 sc1
v_mfma_f32_16x16x32_bf16 v[68:71], a[196:199], a[76:79], v[68:71]
ds_read_b128 a[168:171], %[vXr3] offset:24576
v_mfma_f32_16x16x32_bf16 v[72:75], a[200:203], a[72:75], v[72:75]
ds_read_b128 a[172:175], %[vXr3] offset:25600
v_mfma_f32_16x16x32_bf16 v[76:79], a[200:203], a[76:79], v[76:79]
v_mfma_f32_16x16x32_bf16 v[80:83], a[204:207], a[72:75], v[80:83]
ds_read_b128 a[176:179], %[vXr3] offset:26624
v_mfma_f32_16x16x32_bf16 v[84:87], a[204:207], a[76:79], v[84:87]
buffer_load_dwordx4 %[vL16], %[sWS], s48 offen offset:2048 sc1 lds
v_mfma_f32_16x16x32_bf16 v[88:91], a[208:211], a[72:75], v[88:91]
v_mfma_f32_16x16x32_bf16 v[92:95], a[208:211], a[76:79], v[92:95]
buffer_load_dwordx4 %[vL16], %[sWS], s48 offen offset:3072 sc1 lds
v_mfma_f32_16x16x32_bf16 v[96:99], a[212:215], a[72:75], v[96:99]
v_mfma_f32_16x16x32_bf16 v[100:103], a[212:215], a[76:79], v[100:103]
s_waitcnt vmcnt(15) lgkmcnt(0)
v_mfma_f32_16x16x32_bf16 v[32:35], a[144:147], a[80:83], v[32:35]
s_add_u32 s55, s26, 753664
ds_read_b128 a[180:183], %[vXr3] offset:27648
v_mfma_f32_16x16x32_bf16 v[36:39], a[144:147], a[84:87], v[36:39]
ds_read_b128 a[184:187], %[vXr3] offset:28672
v_mfma_f32_16x16x32_bf16 v[40:43], a[148:151], a[80:83], v[40:43]
s_and_b32 s55, s55, 524287
s_or_b32 s55, s55, s75
buffer_load_dwordx4 a[112:115], %[vY4], %[sWS], s55 offen offset:0 sc1
v_mfma_f32_16x16x32_bf16 v[44:47], a[148:151], a[84:87], v[44:47]
ds_read_b128 a[188:191], %[vXr3] offset:29696
v_mfma_f32_16x16x32_bf16 v[48:51], a[152:155], a[80:83], v[48:51]
ds_read_b128 a[192:195], %[vXr3] offset:30720
v_mfma_f32_16x16x32_bf16 v[52:55], a[152:155], a[84:87], v[52:55]
v_mfma_f32_16x16x32_bf16 v[56:59], a[156:159], a[80:83], v[56:59]
ds_read_b128 a[196:199], %[vXr3] offset:31744
v_mfma_f32_16x16x32_bf16 v[60:63], a[156:159], a[84:87], v[60:63]
ds_read_b128 a[200:203], %[vXr3] offset:32768
v_mfma_f32_16x16x32_bf16 v[64:67], a[160:163], a[80:83], v[64:67]
buffer_load_dwordx4 a[116:119], %[vY4], %[sWS], s55 offen offset:1024 sc1
v_mfma_f32_16x16x32_bf16 v[68:71], a[160:163], a[84:87], v[68:71]
ds_read_b128 a[204:207], %[vXr3] offset:33792
v_mfma_f32_16x16x32_bf16 v[72:75], a[164:167], a[80:83], v[72:75]
ds_read_b128 a[208:211], %[vXr3] offset:34816
v_mfma_f32_16x16x32_bf16 v[76:79], a[164:167], a[84:87], v[76:79]
s_add_u32 m0, %[sLx], 40960
v_mfma_f32_16x16x32_bf16 v[80:83], a[168:171], a[80:83], v[80:83]
s_add_u32 s48, s48, 4096
ds_read_b128 a[212:215], %[vXr3] offset:35840
v_mfma_f32_16x16x32_bf16 v[84:87], a[168:171], a[84:87], v[84:87]
)"
R"(buffer_load_dwordx4 %[vL16], %[sWS], s48 offen offset:0 sc1 lds
v_mfma_f32_16x16x32_bf16 v[88:91], a[172:175], a[80:83], v[88:91]
v_mfma_f32_16x16x32_bf16 v[92:95], a[172:175], a[84:87], v[92:95]
buffer_load_dwordx4 %[vL16], %[sWS], s48 offen offset:1024 sc1 lds
v_mfma_f32_16x16x32_bf16 v[96:99], a[176:179], a[80:83], v[96:99]
v_mfma_f32_16x16x32_bf16 v[100:103], a[176:179], a[84:87], v[100:103]
s_waitcnt vmcnt(12) lgkmcnt(0)
s_barrier
v_mfma_f32_16x16x32_bf16 v[32:35], a[180:183], a[88:91], v[32:35]
s_add_u32 s55, s26, 770048
ds_read_b128 a[144:147], %[vXr0] offset:0
v_mfma_f32_16x16x32_bf16 v[36:39], a[180:183], a[92:95], v[36:39]
ds_read_b128 a[148:151], %[vXr0] offset:1024
v_mfma_f32_16x16x32_bf16 v[40:43], a[184:187], a[88:91], v[40:43]
s_and_b32 s55, s55, 524287
s_or_b32 s55, s55, s75
buffer_load_dwordx4 a[120:123], %[vY4], %[sWS], s55 offen offset:0 sc1
v_mfma_f32_16x16x32_bf16 v[44:47], a[184:187], a[92:95], v[44:47]
ds_read_b128 a[152:155], %[vXr0] offset:2048
v_mfma_f32_16x16x32_bf16 v[48:51], a[188:191], a[88:91], v[48:51]
ds_read_b128 a[156:159], %[vXr0] offset:3072
v_mfma_f32_16x16x32_bf16 v[52:55], a[188:191], a[92:95], v[52:55]
v_mfma_f32_16x16x32_bf16 v[56:59], a[192:195], a[88:91], v[56:59]
ds_read_b128 a[160:163], %[vXr0] offset:4096
v_mfma_f32_16x16x32_bf16 v[60:63], a[192:195], a[92:95], v[60:63]
ds_read_b128 a[164:167], %[vXr0] offset:5120
v_mfma_f32_16x16x32_bf16 v[64:67], a[196:199], a[88:91], v[64:67]
buffer_load_dwordx4 a[124:127], %[vY4], %[sWS], s55 offen offset:1024 sc1
v_mfma_f32_16x16x32_bf16 v[68:71], a[196:199], a[92:95], v[68:71]
ds_read_b128 a[168:171], %[vXr0] offset:6144
v_mfma_f32_16x16x32_bf16 v[72:75], a[200:203], a[88:91], v[72:75]
ds_read_b128 a[172:175], %[vXr0] offset:7168
v_mfma_f32_16x16x32_bf16 v[76:79], a[200:203], a[92:95], v[76:79]
v_mfma_f32_16x16x32_bf16 v[80:83], a[204:207], a[88:91], v[80:83]
ds_read_b128 a[176:179], %[vXr0] offset:8192
v_mfma_f32_16x16x32_bf16 v[84:87], a[204:207], a[92:95], v[84:87]
buffer_load_dwordx4 %[vL16], %[sWS], s48 offen offset:2048 sc1 lds
v_mfma_f32_16x16x32_bf16 v[88:91], a[208:211], a[88:91], v[88:91]
v_mfma_f32_16x16x32_bf16 v[92:95], a[208:211], a[92:95], v[92:95]
buffer_load_dwordx4 %[vL16], %[sWS], s48 offen offset:3072 sc1 lds
v_mfma_f32_16x16x32_bf16 v[96:99], a[212:215], a[88:91], v[96:99]
v_mfma_f32_16x16x32_bf16 v[100:103], a[212:215], a[92:95], v[100:103]
s_waitcnt vmcnt(14) lgkmcnt(0)
v_mfma_f32_16x16x32_bf16 v[32:35], a[144:147], a[96:99], v[32:35]
ds_read_b128 a[180:183], %[vXr0] offset:9216
v_mfma_f32_16x16x32_bf16 v[36:39], a[144:147], a[100:103], v[36:39]
ds_read_b128 a[184:187], %[vXr0] offset:10240
v_mfma_f32_16x16x32_bf16 v[40:43], a[148:151], a[96:99], v[40:43]
global_load_dword v255, v254, s[98:99]
v_mfma_f32_16x16x32_bf16 v[44:47], a[148:151], a[100:103], v[44:47]
s_add_u32 m0, %[sLx], 45056
ds_read_b128 a[188:191], %[vXr0] offset:11264
v_mfma_f32_16x16x32_bf16 v[48:51], a[152:155], a[96:99], v[48:51]
s_lshl1_add_u32 s48, s54, s73
ds_read_b128 a[192:195], %[vXr0] offset:12288
v_mfma_f32_16x16x32_bf16 v[52:55], a[152:155], a[100:103], v[52:55]
buffer_load_dwordx4 %[vL16], %[sWS], s48 offen offset:0 sc1 lds
v_mfma_f32_16x16x32_bf16 v[56:59], a[156:159], a[96:99], v[56:59]
ds_read_b128 a[196:199], %[vXr0] offset:13312
v_mfma_f32_16x16x32_bf16 v[60:63], a[156:159], a[100:103], v[60:63]
ds_read_b128 a[200:203], %[vXr0] offset:14336
v_mfma_f32_16x16x32_bf16 v[64:67], a[160:163], a[96:99], v[64:67]
s_add_u32 s55, s26, 786432
v_mfma_f32_16x16x32_bf16 v[68:71], a[160:163], a[100:103], v[68:71]
s_and_b32 s55, s55, 524287
ds_read_b128 a[204:207], %[vXr0] offset:15360
v_mfma_f32_16x16x32_bf16 v[72:75], a[164:167], a[96:99], v[72:75]
s_or_b32 s55, s55, s75
ds_read_b128 a[208:211], %[vXr0] offset:16384
v_mfma_f32_16x16x32_bf16 v[76:79], a[164:167], a[100:103], v[76:79]
buffer_load_dwordx4 a[0:3], %[vY4], %[sWS], s55 offen offset:0 sc1
v_mfma_f32_16x16x32_bf16 v[80:83], a[168:171], a[96:99], v[80:83]
ds_read_b128 a[212:215], %[vXr0] offset:17408
v_mfma_f32_16x16x32_bf16 v[84:87], a[168:171], a[100:103], v[84:87]
buffer_load_dwordx4 a[4:7], %[vY4], %[sWS], s55 offen offset:1024 sc1
v_mfma_f32_16x16x32_bf16 v[88:91], a[172:175], a[96:99], v[88:91]
s_add_u32 m0, %[sLx], 73728
v_mfma_f32_16x16x32_bf16 v[92:95], a[172:175], a[100:103], v[92:95]
s_add_u32 s54, s26, 327680
s_and_b32 s54, s54, 458752
s_add_u32 s48, s54, s72
buffer_load_dwordx4 %[vL16], %[sWS], s48 offen offset:0 sc1 lds
v_mfma_f32_16x16x32_bf16 v[96:99], a[176:179], a[96:99], v[96:99]
buffer_load_dwordx4 %[vL16], %[sWS], s48 offen offset:1024 sc1 lds
v_mfma_f32_16x16x32_bf16 v[100:103], a[176:179], a[100:103], v[100:103]
s_waitcnt vmcnt(16) lgkmcnt(0)
v_mfma_f32_16x16x32_bf16 v[32:35], a[180:183], a[104:107], v[32:35]
s_add_u32 s55, s26, 802816
ds_read_b128 a[144:147], %[vXr0] offset:18432
v_mfma_f32_16x16x32_bf16 v[36:39], a[180:183], a[108:111], v[36:39]
ds_read_b128 a[148:151], %[vXr0] offset:19456
v_mfma_f32_16x16x32_bf16 v[40:43], a[184:187], a[104:107], v[40:43]
s_and_b32 s55, s55, 524287
s_or_b32 s55, s55, s75
buffer_load_dwordx4 a[8:11], %[vY4], %[sWS], s55 offen offset:0 sc1
v_mfma_f32_16x16x32_bf16 v[44:47], a[184:187], a[108:111], v[44:47]
ds_read_b128 a[152:155], %[vXr0] offset:20480
v_mfma_f32_16x16x32_bf16 v[48:51], a[188:191], a[104:107], v[48:51]
ds_read_b128 a[156:159], %[vXr0] offset:21504
v_mfma_f32_16x16x32_bf16 v[52:55], a[188:191], a[108:111], v[52:55]
v_mfma_f32_16x16x32_bf16 v[56:59], a[192:195], a[104:107], v[56:59]
ds_read_b128 a[160:163], %[vXr0] offset:22528
v_mfma_f32_16x16x32_bf16 v[60:63], a[192:195], a[108:111], v[60:63]
ds_read_b128 a[164:167], %[vXr0] offset:23552
v_mfma_f32_16x16x32_bf16 v[64:67], a[196:199], a[104:107], v[64:67]
buffer_load_dwordx4 a[12:15], %[vY4], %[sWS], s55 offen offset:1024 sc1
v_mfma_f32_16x16x32_bf16 v[68:71], a[196:199], a[108:111], v[68:71]
ds_read_b128 a[168:171], %[vXr0] offset:24576
v_mfma_f32_16x16x32_bf16 v[72:75], a[200:203], a[104:107], v[72:75]
ds_read_b128 a[172:175], %[vXr0] offset:25600
v_mfma_f32_16x16x32_bf16 v[76:79], a[200:203], a[108:111], v[76:79]
v_mfma_f32_16x16x32_bf16 v[80:83], a[204:207], a[104:107], v[80:83]
ds_read_b128 a[176:179], %[vXr0] offset:26624
v_mfma_f32_16x16x32_bf16 v[84:87], a[204:207], a[108:111], v[84:87]
buffer_load_dwordx4 %[vL16], %[sWS], s48 offen offset:2048 sc1 lds
v_mfma_f32_16x16x32_bf16 v[88:91], a[208:211], a[104:107], v[88:91]
v_mfma_f32_16x16x32_bf16 v[92:95], a[208:211], a[108:111], v[92:95]
buffer_load_dwordx4 %[vL16], %[sWS], s48 offen offset:3072 sc1 lds
v_mfma_f32_16x16x32_bf16 v[96:99], a[212:215], a[104:107], v[96:99]
v_mfma_f32_16x16x32_bf16 v[100:103], a[212:215], a[108:111], v[100:103]
s_waitcnt vmcnt(16) lgkmcnt(0)
v_mfma_f32_16x16x32_bf16 v[32:35], a[144:147], a[112:115], v[32:35]
s_add_u32 s55, s26, 819200
ds_read_b128 a[180:183], %[vXr0] offset:27648
v_mfma_f32_16x16x32_bf16 v[36:39], a[144:147], a[116:119], v[36:39]
ds_read_b128 a[184:187], %[vXr0] offset:28672
v_mfma_f32_16x16x32_bf16 v[40:43], a[148:151], a[112:115], v[40:43]
s_and_b32 s55, s55, 524287
s_or_b32 s55, s55, s75
buffer_load_dwordx4 a[16:19], %[vY4], %[sWS], s55 offen offset:0 sc1
v_mfma_f32_16x16x32_bf16 v[44:47], a[148:151], a[116:119], v[44:47]
ds_read_b128 a[188:191], %[vXr0] offset:29696
v_mfma_f32_16x16x32_bf16 v[48:51], a[152:155], a[112:115], v[48:51]
ds_read_b128 a[192:195], %[vXr0] offset:30720
v_mfma_f32_16x16x32_bf16 v[52:55], a[152:155], a[116:119], v[52:55]
v_mfma_f32_16x16x32_bf16 v[56:59], a[156:159], a[112:115], v[56:59]
ds_read_b128 a[196:199], %[vXr0] offset:31744
v_mfma_f32_16x16x32_bf16 v[60:63], a[156:159], a[116:119], v[60:63]
ds_read_b128 a[200:203], %[vXr0] offset:32768
v_mfma_f32_16x16x32_bf16 v[64:67], a[160:163], a[112:115], v[64:67]
buffer_load_dwordx4 a[20:23], %[vY4], %[sWS], s55 offen offset:1024 sc1
v_mfma_f32_16x16x32_bf16 v[68:71], a[160:163], a[116:119], v[68:71]
ds_read_b128 a[204:207], %[vXr0] offset:33792
v_mfma_f32_16x16x32_bf16 v[72:75], a[164:167], a[112:115], v[72:75]
ds_read_b128 a[208:211], %[vXr0] offset:34816
v_mfma_f32_16x16x32_bf16 v[76:79], a[164:167], a[116:119], v[76:79]
s_add_u32 m0, %[sLx], 77824
v_mfma_f32_16x16x32_bf16 v[80:83], a[168:171], a[112:115], v[80:83]
s_add_u32 s48, s48, 4096
ds_read_b128 a[212:215], %[vXr0] offset:35840
v_mfma_f32_16x16x32_bf16 v[84:87], a[168:171], a[116:119], v[84:87]
buffer_load_dwordx4 %[vL16], %[sWS], s48 offen offset:0 sc1 lds
v_mfma_f32_16x16x32_bf16 v[88:91], a[172:175], a[112:115], v[88:91]
v_mfma_f32_16x16x32_bf16 v[92:95], a[172:175], a[116:119], v[92:95]
buffer_load_dwordx4 %[vL16], %[sWS], s48 offen offset:1024 sc1 lds
v_mfma_f32_16x16x32_bf16 v[96:99], a[176:179], a[112:115], v[96:99]
v_mfma_f32_16x16x32_bf16 v[100:103], a[176:179], a[116:119], v[100:103]
s_waitcnt vmcnt(12) lgkmcnt(0)
s_barrier
v_mfma_f32_16x16x32_bf16 v[32:35], a[180:183], a[120:123], v[32:35]
s_add_u32 s55, s26, 835584
ds_read_b128 a[144:147], %[vXr1] offset:0
v_mfma_f32_16x16x32_bf16 v[36:39], a[180:183], a[124:127], v[36:39]
ds_read_b128 a[148:151], %[vXr1] offset:1024
v_mfma_f32_16x16x32_bf16 v[40:43], a[184:187], a[120:123], v[40:43]
s_and_b32 s55, s55, 524287
s_or_b32 s55, s55, s75
buffer_load_dwordx4 a[24:27], %[vY4], %[sWS], s55 offen offset:0 sc1
v_mfma_f32_16x16x32_bf16 v[44:47], a[184:187], a[124:127], v[44:47]
ds_read_b128 a[152:155], %[vXr1] offset:2048
v_mfma_f32_16x16x32_bf16 v[48:51], a[188:191], a[120:123], v[48:51]
ds_read_b128 a[156:159], %[vXr1] offset:3072
v_mfma_f32_16x16x32_bf16 v[52:55], a[188:191], a[124:127], v[52:55]
v_mfma_f32_16x16x32_bf16 v[56:59], a[192:195], a[120:123], v[56:59]
ds_read_b128 a[160:163], %[vXr1] offset:4096
v_mfma_f32_16x16x32_bf16 v[60:63], a[192:195], a[124:127], v[60:63]
ds_read_b128 a[164:167], %[vXr1] offset:5120
v_mfma_f32_16x16x32_bf16 v[64:67], a[196:199], a[120:123], v[64:67]
buffer_load_dwordx4 a[28:31], %[vY4], %[sWS], s55 offen offset:1024 sc1
v_mfma_f32_16x16x32_bf16 v[68:71], a[196:199], a[124:127], v[68:71]
ds_read_b128 a[168:171], %[vXr1] offset:6144
v_mfma_f32_16x16x32_bf16 v[72:75], a[200:203], a[120:123], v[72:75]
ds_read_b128 a[172:175], %[vXr1] offset:7168
v_mfma_f32_16x16x32_bf16 v[76:79], a[200:203], a[124:127], v[76:79]
v_mfma_f32_16x16x32_bf16 v[80:83], a[204:207], a[120:123], v[80:83]
ds_read_b128 a[176:179], %[vXr1] offset:8192
v_mfma_f32_16x16x32_bf16 v[84:87], a[204:207], a[124:127], v[84:87]
buffer_load_dwordx4 %[vL16], %[sWS], s48 offen offset:2048 sc1 lds
v_mfma_f32_16x16x32_bf16 v[88:91], a[208:211], a[120:123], v[88:91]
v_mfma_f32_16x16x32_bf16 v[92:95], a[208:211], a[124:127], v[92:95]
buffer_load_dwordx4 %[vL16], %[sWS], s48 offen offset:3072 sc1 lds
v_mfma_f32_16x16x32_bf16 v[96:99], a[212:215], a[120:123], v[96:99]
v_mfma_f32_16x16x32_bf16 v[100:103], a[212:215], a[124:127], v[100:103]
s_waitcnt vmcnt(14) lgkmcnt(0)
v_mfma_f32_16x16x32_bf16 v[32:35], a[144:147], a[0:3], v[32:35]
s_add_u32 m0, %[sLx], 81920
ds_read_b128 a[180:183], %[vXr1] offset:9216
v_mfma_f32_16x16x32_bf16 v[36:39], a[144:147], a[4:7], v[36:39]
s_lshl1_add_u32 s48, s54, s73
ds_read_b128 a[184:187], %[vXr1] offset:10240
)"
R"(v_mfma_f32_16x16x32_bf16 v[40:43], a[148:151], a[0:3], v[40:43]
buffer_load_dwordx4 %[vL16], %[sWS], s48 offen offset:0 sc1 lds
v_mfma_f32_16x16x32_bf16 v[44:47], a[148:151], a[4:7], v[44:47]
s_add_u32 s55, s26, 851968
ds_read_b128 a[188:191], %[vXr1] offset:11264
v_mfma_f32_16x16x32_bf16 v[48:51], a[152:155], a[0:3], v[48:51]
ds_read_b128 a[192:195], %[vXr1] offset:12288
v_mfma_f32_16x16x32_bf16 v[52:55], a[152:155], a[4:7], v[52:55]
s_and_b32 s55, s55, 524287
s_or_b32 s55, s55, s75
buffer_load_dwordx4 a[32:35], %[vY4], %[sWS], s55 offen offset:0 sc1
v_mfma_f32_16x16x32_bf16 v[56:59], a[156:159], a[0:3], v[56:59]
ds_read_b128 a[196:199], %[vXr1] offset:13312
v_mfma_f32_16x16x32_bf16 v[60:63], a[156:159], a[4:7], v[60:63]
ds_read_b128 a[200:203], %[vXr1] offset:14336
v_mfma_f32_16x16x32_bf16 v[64:67], a[160:163], a[0:3], v[64:67]
v_mfma_f32_16x16x32_bf16 v[68:71], a[160:163], a[4:7], v[68:71]
ds_read_b128 a[204:207], %[vXr1] offset:15360
v_mfma_f32_16x16x32_bf16 v[72:75], a[164:167], a[0:3], v[72:75]
ds_read_b128 a[208:211], %[vXr1] offset:16384
v_mfma_f32_16x16x32_bf16 v[76:79], a[164:167], a[4:7], v[76:79]
buffer_load_dwordx4 a[36:39], %[vY4], %[sWS], s55 offen offset:1024 sc1
v_mfma_f32_16x16x32_bf16 v[80:83], a[168:171], a[0:3], v[80:83]
s_add_u32 m0, %[sLx], 110592
ds_read_b128 a[212:215], %[vXr1] offset:17408
v_mfma_f32_16x16x32_bf16 v[84:87], a[168:171], a[4:7], v[84:87]
v_mfma_f32_16x16x32_bf16 v[88:91], a[172:175], a[0:3], v[88:91]
s_add_u32 s54, s26, 393216
s_and_b32 s54, s54, 458752
s_add_u32 s48, s54, s72
buffer_load_dwordx4 %[vL16], %[sWS], s48 offen offset:0 sc1 lds
v_mfma_f32_16x16x32_bf16 v[92:95], a[172:175], a[4:7], v[92:95]
v_mfma_f32_16x16x32_bf16 v[96:99], a[176:179], a[0:3], v[96:99]
buffer_load_dwordx4 %[vL16], %[sWS], s48 offen offset:1024 sc1 lds
v_mfma_f32_16x16x32_bf16 v[100:103], a[176:179], a[4:7], v[100:103]
s_waitcnt vmcnt(15) lgkmcnt(0)
v_mfma_f32_16x16x32_bf16 v[32:35], a[180:183], a[8:11], v[32:35]
s_add_u32 s55, s26, 868352
ds_read_b128 a[144:147], %[vXr1] offset:18432
v_mfma_f32_16x16x32_bf16 v[36:39], a[180:183], a[12:15], v[36:39]
ds_read_b128 a[148:151], %[vXr1] offset:19456
v_mfma_f32_16x16x32_bf16 v[40:43], a[184:187], a[8:11], v[40:43]
s_and_b32 s55, s55, 524287
s_or_b32 s55, s55, s75
buffer_load_dwordx4 a[40:43], %[vY4], %[sWS], s55 offen offset:0 sc1
v_mfma_f32_16x16x32_bf16 v[44:47], a[184:187], a[12:15], v[44:47]
ds_read_b128 a[152:155], %[vXr1] offset:20480
v_mfma_f32_16x16x32_bf16 v[48:51], a[188:191], a[8:11], v[48:51]
ds_read_b128 a[156:159], %[vXr1] offset:21504
v_mfma_f32_16x16x32_bf16 v[52:55], a[188:191], a[12:15], v[52:55]
v_mfma_f32_16x16x32_bf16 v[56:59], a[192:195], a[8:11], v[56:59]
ds_read_b128 a[160:163], %[vXr1] offset:22528
v_mfma_f32_16x16x32_bf16 v[60:63], a[192:195], a[12:15], v[60:63]
ds_read_b128 a[164:167], %[vXr1] offset:23552
v_mfma_f32_16x16x32_bf16 v[64:67], a[196:199], a[8:11], v[64:67]
buffer_load_dwordx4 a[44:47], %[vY4], %[sWS], s55 offen offset:1024 sc1
v_mfma_f32_16x16x32_bf16 v[68:71], a[196:199], a[12:15], v[68:71]
ds_read_b128 a[168:171], %[vXr1] offset:24576
v_mfma_f32_16x16x32_bf16 v[72:75], a[200:203], a[8:11], v[72:75]
ds_read_b128 a[172:175], %[vXr1] offset:25600
v_mfma_f32_16x16x32_bf16 v[76:79], a[200:203], a[12:15], v[76:79]
v_mfma_f32_16x16x32_bf16 v[80:83], a[204:207], a[8:11], v[80:83]
ds_read_b128 a[176:179], %[vXr1] offset:26624
v_mfma_f32_16x16x32_bf16 v[84:87], a[204:207], a[12:15], v[84:87]
buffer_load_dwordx4 %[vL16], %[sWS], s48 offen offset:2048 sc1 lds
v_mfma_f32_16x16x32_bf16 v[88:91], a[208:211], a[8:11], v[88:91]
v_mfma_f32_16x16x32_bf16 v[92:95], a[208:211], a[12:15], v[92:95]
buffer_load_dwordx4 %[vL16], %[sWS], s48 offen offset:3072 sc1 lds
v_mfma_f32_16x16x32_bf16 v[96:99], a[212:215], a[8:11], v[96:99]
v_mfma_f32_16x16x32_bf16 v[100:103], a[212:215], a[12:15], v[100:103]
s_waitcnt vmcnt(15) lgkmcnt(0)
v_mfma_f32_16x16x32_bf16 v[32:35], a[144:147], a[16:19], v[32:35]
s_add_u32 s55, s26, 884736
ds_read_b128 a[180:183], %[vXr1] offset:27648
v_mfma_f32_16x16x32_bf16 v[36:39], a[144:147], a[20:23], v[36:39]
ds_read_b128 a[184:187], %[vXr1] offset:28672
v_mfma_f32_16x16x32_bf16 v[40:43], a[148:151], a[16:19], v[40:43]
s_and_b32 s55, s55, 524287
s_or_b32 s55, s55, s75
buffer_load_dwordx4 a[48:51], %[vY4], %[sWS], s55 offen offset:0 sc1
v_mfma_f32_16x16x32_bf16 v[44:47], a[148:151], a[20:23], v[44:47]
ds_read_b128 a[188:191], %[vXr1] offset:29696
v_mfma_f32_16x16x32_bf16 v[48:51], a[152:155], a[16:19], v[48:51]
ds_read_b128 a[192:195], %[vXr1] offset:30720
v_mfma_f32_16x16x32_bf16 v[52:55], a[152:155], a[20:23], v[52:55]
v_mfma_f32_16x16x32_bf16 v[56:59], a[156:159], a[16:19], v[56:59]
ds_read_b128 a[196:199], %[vXr1] offset:31744
v_mfma_f32_16x16x32_bf16 v[60:63], a[156:159], a[20:23], v[60:63]
ds_read_b128 a[200:203], %[vXr1] offset:32768
v_mfma_f32_16x16x32_bf16 v[64:67], a[160:163], a[16:19], v[64:67]
buffer_load_dwordx4 a[52:55], %[vY4], %[sWS], s55 offen offset:1024 sc1
v_mfma_f32_16x16x32_bf16 v[68:71], a[160:163], a[20:23], v[68:71]
ds_read_b128 a[204:207], %[vXr1] offset:33792
v_mfma_f32_16x16x32_bf16 v[72:75], a[164:167], a[16:19], v[72:75]
ds_read_b128 a[208:211], %[vXr1] offset:34816
v_mfma_f32_16x16x32_bf16 v[76:79], a[164:167], a[20:23], v[76:79]
s_add_u32 m0, %[sLx], 114688
v_mfma_f32_16x16x32_bf16 v[80:83], a[168:171], a[16:19], v[80:83]
s_add_u32 s48, s48, 4096
ds_read_b128 a[212:215], %[vXr1] offset:35840
v_mfma_f32_16x16x32_bf16 v[84:87], a[168:171], a[20:23], v[84:87]
buffer_load_dwordx4 %[vL16], %[sWS], s48 offen offset:0 sc1 lds
v_mfma_f32_16x16x32_bf16 v[88:91], a[172:175], a[16:19], v[88:91]
v_mfma_f32_16x16x32_bf16 v[92:95], a[172:175], a[20:23], v[92:95]
buffer_load_dwordx4 %[vL16], %[sWS], s48 offen offset:1024 sc1 lds
v_mfma_f32_16x16x32_bf16 v[96:99], a[176:179], a[16:19], v[96:99]
v_mfma_f32_16x16x32_bf16 v[100:103], a[176:179], a[20:23], v[100:103]
s_waitcnt vmcnt(12) lgkmcnt(0)
s_barrier
v_mfma_f32_16x16x32_bf16 v[32:35], a[180:183], a[24:27], v[32:35]
s_add_u32 s55, s26, 901120
ds_read_b128 a[144:147], %[vXr2] offset:0
v_mfma_f32_16x16x32_bf16 v[36:39], a[180:183], a[28:31], v[36:39]
ds_read_b128 a[148:151], %[vXr2] offset:1024
v_mfma_f32_16x16x32_bf16 v[40:43], a[184:187], a[24:27], v[40:43]
s_and_b32 s55, s55, 524287
s_or_b32 s55, s55, s75
buffer_load_dwordx4 a[56:59], %[vY4], %[sWS], s55 offen offset:0 sc1
v_mfma_f32_16x16x32_bf16 v[44:47], a[184:187], a[28:31], v[44:47]
ds_read_b128 a[152:155], %[vXr2] offset:2048
v_mfma_f32_16x16x32_bf16 v[48:51], a[188:191], a[24:27], v[48:51]
ds_read_b128 a[156:159], %[vXr2] offset:3072
v_mfma_f32_16x16x32_bf16 v[52:55], a[188:191], a[28:31], v[52:55]
v_mfma_f32_16x16x32_bf16 v[56:59], a[192:195], a[24:27], v[56:59]
ds_read_b128 a[160:163], %[vXr2] offset:4096
v_mfma_f32_16x16x32_bf16 v[60:63], a[192:195], a[28:31], v[60:63]
ds_read_b128 a[164:167], %[vXr2] offset:5120
v_mfma_f32_16x16x32_bf16 v[64:67], a[196:199], a[24:27], v[64:67]
buffer_load_dwordx4 a[60:63], %[vY4], %[sWS], s55 offen offset:1024 sc1
v_mfma_f32_16x16x32_bf16 v[68:71], a[196:199], a[28:31], v[68:71]
ds_read_b128 a[168:171], %[vXr2] offset:6144
v_mfma_f32_16x16x32_bf16 v[72:75], a[200:203], a[24:27], v[72:75]
ds_read_b128 a[172:175], %[vXr2] offset:7168
v_mfma_f32_16x16x32_bf16 v[76:79], a[200:203], a[28:31], v[76:79]
v_mfma_f32_16x16x32_bf16 v[80:83], a[204:207], a[24:27], v[80:83]
ds_read_b128 a[176:179], %[vXr2] offset:8192
v_mfma_f32_16x16x32_bf16 v[84:87], a[204:207], a[28:31], v[84:87]
buffer_load_dwordx4 %[vL16], %[sWS], s48 offen offset:2048 sc1 lds
v_mfma_f32_16x16x32_bf16 v[88:91], a[208:211], a[24:27], v[88:91]
v_mfma_f32_16x16x32_bf16 v[92:95], a[208:211], a[28:31], v[92:95]
buffer_load_dwordx4 %[vL16], %[sWS], s48 offen offset:3072 sc1 lds
v_mfma_f32_16x16x32_bf16 v[96:99], a[212:215], a[24:27], v[96:99]
v_mfma_f32_16x16x32_bf16 v[100:103], a[212:215], a[28:31], v[100:103]
s_waitcnt vmcnt(14) lgkmcnt(0)
v_mfma_f32_16x16x32_bf16 v[32:35], a[144:147], a[32:35], v[32:35]
s_add_u32 m0, %[sLx], 118784
ds_read_b128 a[180:183], %[vXr2] offset:9216
v_mfma_f32_16x16x32_bf16 v[36:39], a[144:147], a[36:39], v[36:39]
s_lshl1_add_u32 s48, s54, s73
ds_read_b128 a[184:187], %[vXr2] offset:10240
v_mfma_f32_16x16x32_bf16 v[40:43], a[148:151], a[32:35], v[40:43]
buffer_load_dwordx4 %[vL16], %[sWS], s48 offen offset:0 sc1 lds
v_mfma_f32_16x16x32_bf16 v[44:47], a[148:151], a[36:39], v[44:47]
s_add_u32 s55, s26, 917504
ds_read_b128 a[188:191], %[vXr2] offset:11264
v_mfma_f32_16x16x32_bf16 v[48:51], a[152:155], a[32:35], v[48:51]
ds_read_b128 a[192:195], %[vXr2] offset:12288
v_mfma_f32_16x16x32_bf16 v[52:55], a[152:155], a[36:39], v[52:55]
s_and_b32 s55, s55, 524287
s_or_b32 s55, s55, s75
buffer_load_dwordx4 a[64:67], %[vY4], %[sWS], s55 offen offset:0 sc1
v_mfma_f32_16x16x32_bf16 v[56:59], a[156:159], a[32:35], v[56:59]
ds_read_b128 a[196:199], %[vXr2] offset:13312
v_mfma_f32_16x16x32_bf16 v[60:63], a[156:159], a[36:39], v[60:63]
ds_read_b128 a[200:203], %[vXr2] offset:14336
v_mfma_f32_16x16x32_bf16 v[64:67], a[160:163], a[32:35], v[64:67]
v_mfma_f32_16x16x32_bf16 v[68:71], a[160:163], a[36:39], v[68:71]
ds_read_b128 a[204:207], %[vXr2] offset:15360
v_mfma_f32_16x16x32_bf16 v[72:75], a[164:167], a[32:35], v[72:75]
ds_read_b128 a[208:211], %[vXr2] offset:16384
v_mfma_f32_16x16x32_bf16 v[76:79], a[164:167], a[36:39], v[76:79]
buffer_load_dwordx4 a[68:71], %[vY4], %[sWS], s55 offen offset:1024 sc1
v_mfma_f32_16x16x32_bf16 v[80:83], a[168:171], a[32:35], v[80:83]
s_add_u32 m0, %[sLx], 0
ds_read_b128 a[212:215], %[vXr2] offset:17408
v_mfma_f32_16x16x32_bf16 v[84:87], a[168:171], a[36:39], v[84:87]
v_mfma_f32_16x16x32_bf16 v[88:91], a[172:175], a[32:35], v[88:91]
s_add_u32 s54, s26, 458752
s_and_b32 s54, s54, 458752
s_add_u32 s48, s54, s72
buffer_load_dwordx4 %[vL16], %[sWS], s48 offen offset:0 sc1 lds
v_mfma_f32_16x16x32_bf16 v[92:95], a[172:175], a[36:39], v[92:95]
v_mfma_f32_16x16x32_bf16 v[96:99], a[176:179], a[32:35], v[96:99]
buffer_load_dwordx4 %[vL16], %[sWS], s48 offen offset:1024 sc1 lds
v_mfma_f32_16x16x32_bf16 v[100:103], a[176:179], a[36:39], v[100:103]
s_waitcnt vmcnt(15) lgkmcnt(0)
v_mfma_f32_16x16x32_bf16 v[32:35], a[180:183], a[40:43], v[32:35]
s_add_u32 s55, s26, 933888
ds_read_b128 a[144:147], %[vXr2] offset:18432
v_mfma_f32_16x16x32_bf16 v[36:39], a[180:183], a[44:47], v[36:39]
ds_read_b128 a[148:151], %[vXr2] offset:19456
v_mfma_f32_16x16x32_bf16 v[40:43], a[184:187], a[40:43], v[40:43]
s_and_b32 s55, s55, 524287
s_or_b32 s55, s55, s75
buffer_load_dwordx4 a[72:75], %[vY4], %[sWS], s55 offen offset:0 sc1
v_mfma_f32_16x16x32_bf16 v[44:47], a[184:187], a[44:47], v[44:47]
ds_read_b128 a[152:155], %[vXr2] offset:20480
v_mfma_f32_16x16x32_bf16 v[48:51], a[188:191], a[40:43], v[48:51]
ds_read_b128 a[156:159], %[vXr2] offset:21504
v_mfma_f32_16x16x32_bf16 v[52:55], a[188:191], a[44:47], v[52:55]
)"
R"(v_mfma_f32_16x16x32_bf16 v[56:59], a[192:195], a[40:43], v[56:59]
ds_read_b128 a[160:163], %[vXr2] offset:22528
v_mfma_f32_16x16x32_bf16 v[60:63], a[192:195], a[44:47], v[60:63]
ds_read_b128 a[164:167], %[vXr2] offset:23552
v_mfma_f32_16x16x32_bf16 v[64:67], a[196:199], a[40:43], v[64:67]
buffer_load_dwordx4 a[76:79], %[vY4], %[sWS], s55 offen offset:1024 sc1
v_mfma_f32_16x16x32_bf16 v[68:71], a[196:199], a[44:47], v[68:71]
ds_read_b128 a[168:171], %[vXr2] offset:24576
v_mfma_f32_16x16x32_bf16 v[72:75], a[200:203], a[40:43], v[72:75]
ds_read_b128 a[172:175], %[vXr2] offset:25600
v_mfma_f32_16x16x32_bf16 v[76:79], a[200:203], a[44:47], v[76:79]
LPb_9:
s_cbranch_execz LPb_13
v_mfma_f32_16x16x32_bf16 v[80:83], a[204:207], a[40:43], v[80:83]
ds_read_b128 a[176:179], %[vXr2] offset:26624
v_mfma_f32_16x16x32_bf16 v[84:87], a[204:207], a[44:47], v[84:87]
buffer_load_dwordx4 %[vL16], %[sWS], s48 offen offset:2048 sc1 lds
v_mfma_f32_16x16x32_bf16 v[88:91], a[208:211], a[40:43], v[88:91]
v_mfma_f32_16x16x32_bf16 v[92:95], a[208:211], a[44:47], v[92:95]
buffer_load_dwordx4 %[vL16], %[sWS], s48 offen offset:3072 sc1 lds
v_mfma_f32_16x16x32_bf16 v[96:99], a[212:215], a[40:43], v[96:99]
v_mfma_f32_16x16x32_bf16 v[100:103], a[212:215], a[44:47], v[100:103]
s_waitcnt vmcnt(15) lgkmcnt(0)
v_mfma_f32_16x16x32_bf16 v[32:35], a[144:147], a[48:51], v[32:35]
s_add_u32 s55, s26, 950272
ds_read_b128 a[180:183], %[vXr2] offset:27648
v_mfma_f32_16x16x32_bf16 v[36:39], a[144:147], a[52:55], v[36:39]
ds_read_b128 a[184:187], %[vXr2] offset:28672
v_mfma_f32_16x16x32_bf16 v[40:43], a[148:151], a[48:51], v[40:43]
s_and_b32 s55, s55, 524287
s_or_b32 s55, s55, s75
buffer_load_dwordx4 a[80:83], %[vY4], %[sWS], s55 offen offset:0 sc1
v_mfma_f32_16x16x32_bf16 v[44:47], a[148:151], a[52:55], v[44:47]
ds_read_b128 a[188:191], %[vXr2] offset:29696
v_mfma_f32_16x16x32_bf16 v[48:51], a[152:155], a[48:51], v[48:51]
ds_read_b128 a[192:195], %[vXr2] offset:30720
v_mfma_f32_16x16x32_bf16 v[52:55], a[152:155], a[52:55], v[52:55]
v_mfma_f32_16x16x32_bf16 v[56:59], a[156:159], a[48:51], v[56:59]
ds_read_b128 a[196:199], %[vXr2] offset:31744
v_mfma_f32_16x16x32_bf16 v[60:63], a[156:159], a[52:55], v[60:63]
ds_read_b128 a[200:203], %[vXr2] offset:32768
v_mfma_f32_16x16x32_bf16 v[64:67], a[160:163], a[48:51], v[64:67]
buffer_load_dwordx4 a[84:87], %[vY4], %[sWS], s55 offen offset:1024 sc1
v_mfma_f32_16x16x32_bf16 v[68:71], a[160:163], a[52:55], v[68:71]
ds_read_b128 a[204:207], %[vXr2] offset:33792
v_mfma_f32_16x16x32_bf16 v[72:75], a[164:167], a[48:51], v[72:75]
ds_read_b128 a[208:211], %[vXr2] offset:34816
v_mfma_f32_16x16x32_bf16 v[76:79], a[164:167], a[52:55], v[76:79]
s_add_u32 m0, %[sLx], 4096
v_mfma_f32_16x16x32_bf16 v[80:83], a[168:171], a[48:51], v[80:83]
s_add_u32 s48, s48, 4096
ds_read_b128 a[212:215], %[vXr2] offset:35840
v_mfma_f32_16x16x32_bf16 v[84:87], a[168:171], a[52:55], v[84:87]
buffer_load_dwordx4 %[vL16], %[sWS], s48 offen offset:0 sc1 lds
v_mfma_f32_16x16x32_bf16 v[88:91], a[172:175], a[48:51], v[88:91]
v_mfma_f32_16x16x32_bf16 v[92:95], a[172:175], a[52:55], v[92:95]
buffer_load_dwordx4 %[vL16], %[sWS], s48 offen offset:1024 sc1 lds
v_mfma_f32_16x16x32_bf16 v[96:99], a[176:179], a[48:51], v[96:99]
v_mfma_f32_16x16x32_bf16 v[100:103], a[176:179], a[52:55], v[100:103]
s_waitcnt vmcnt(12) lgkmcnt(0)
s_barrier
v_mfma_f32_16x16x32_bf16 v[32:35], a[180:183], a[56:59], v[32:35]
s_add_u32 s55, s26, 966656
ds_read_b128 a[144:147], %[vXr3] offset:0
v_mfma_f32_16x16x32_bf16 v[36:39], a[180:183], a[60:63], v[36:39]
ds_read_b128 a[148:151], %[vXr3] offset:1024
v_mfma_f32_16x16x32_bf16 v[40:43], a[184:187], a[56:59], v[40:43]
s_and_b32 s55, s55, 524287
s_or_b32 s55, s55, s75
buffer_load_dwordx4 a[88:91], %[vY4], %[sWS], s55 offen offset:0 sc1
v_mfma_f32_16x16x32_bf16 v[44:47], a[184:187], a[60:63], v[44:47]
ds_read_b128 a[152:155], %[vXr3] offset:2048
v_mfma_f32_16x16x32_bf16 v[48:51], a[188:191], a[56:59], v[48:51]
ds_read_b128 a[156:159], %[vXr3] offset:3072
v_mfma_f32_16x16x32_bf16 v[52:55], a[188:191], a[60:63], v[52:55]
v_mfma_f32_16x16x32_bf16 v[56:59], a[192:195], a[56:59], v[56:59]
ds_read_b128 a[160:163], %[vXr3] offset:4096
v_mfma_f32_16x16x32_bf16 v[60:63], a[192:195], a[60:63], v[60:63]
ds_read_b128 a[164:167], %[vXr3] offset:5120
v_mfma_f32_16x16x32_bf16 v[64:67], a[196:199], a[56:59], v[64:67]
buffer_load_dwordx4 a[92:95], %[vY4], %[sWS], s55 offen offset:1024 sc1
v_mfma_f32_16x16x32_bf16 v[68:71], a[196:199], a[60:63], v[68:71]
ds_read_b128 a[168:171], %[vXr3] offset:6144
v_mfma_f32_16x16x32_bf16 v[72:75], a[200:203], a[56:59], v[72:75]
ds_read_b128 a[172:175], %[vXr3] offset:7168
v_mfma_f32_16x16x32_bf16 v[76:79], a[200:203], a[60:63], v[76:79]
v_mfma_f32_16x16x32_bf16 v[80:83], a[204:207], a[56:59], v[80:83]
ds_read_b128 a[176:179], %[vXr3] offset:8192
v_mfma_f32_16x16x32_bf16 v[84:87], a[204:207], a[60:63], v[84:87]
buffer_load_dwordx4 %[vL16], %[sWS], s48 offen offset:2048 sc1 lds
v_mfma_f32_16x16x32_bf16 v[88:91], a[208:211], a[56:59], v[88:91]
v_mfma_f32_16x16x32_bf16 v[92:95], a[208:211], a[60:63], v[92:95]
buffer_load_dwordx4 %[vL16], %[sWS], s48 offen offset:3072 sc1 lds
v_mfma_f32_16x16x32_bf16 v[96:99], a[212:215], a[56:59], v[96:99]
v_mfma_f32_16x16x32_bf16 v[100:103], a[212:215], a[60:63], v[100:103]
s_waitcnt vmcnt(14) lgkmcnt(0)
v_mfma_f32_16x16x32_bf16 v[32:35], a[144:147], a[64:67], v[32:35]
s_add_u32 m0, %[sLx], 8192
ds_read_b128 a[180:183], %[vXr3] offset:9216
v_mfma_f32_16x16x32_bf16 v[36:39], a[144:147], a[68:71], v[36:39]
s_lshl1_add_u32 s48, s54, s73
ds_read_b128 a[184:187], %[vXr3] offset:10240
v_mfma_f32_16x16x32_bf16 v[40:43], a[148:151], a[64:67], v[40:43]
buffer_load_dwordx4 %[vL16], %[sWS], s48 offen offset:0 sc1 lds
v_mfma_f32_16x16x32_bf16 v[44:47], a[148:151], a[68:71], v[44:47]
s_add_u32 s55, s26, 983040
ds_read_b128 a[188:191], %[vXr3] offset:11264
v_mfma_f32_16x16x32_bf16 v[48:51], a[152:155], a[64:67], v[48:51]
ds_read_b128 a[192:195], %[vXr3] offset:12288
v_mfma_f32_16x16x32_bf16 v[52:55], a[152:155], a[68:71], v[52:55]
s_and_b32 s55, s55, 524287
s_or_b32 s55, s55, s75
buffer_load_dwordx4 a[96:99], %[vY4], %[sWS], s55 offen offset:0 sc1
v_mfma_f32_16x16x32_bf16 v[56:59], a[156:159], a[64:67], v[56:59]
ds_read_b128 a[196:199], %[vXr3] offset:13312
v_mfma_f32_16x16x32_bf16 v[60:63], a[156:159], a[68:71], v[60:63]
ds_read_b128 a[200:203], %[vXr3] offset:14336
v_mfma_f32_16x16x32_bf16 v[64:67], a[160:163], a[64:67], v[64:67]
v_mfma_f32_16x16x32_bf16 v[68:71], a[160:163], a[68:71], v[68:71]
ds_read_b128 a[204:207], %[vXr3] offset:15360
v_mfma_f32_16x16x32_bf16 v[72:75], a[164:167], a[64:67], v[72:75]
ds_read_b128 a[208:211], %[vXr3] offset:16384
v_mfma_f32_16x16x32_bf16 v[76:79], a[164:167], a[68:71], v[76:79]
buffer_load_dwordx4 a[100:103], %[vY4], %[sWS], s55 offen offset:1024 sc1
v_mfma_f32_16x16x32_bf16 v[80:83], a[168:171], a[64:67], v[80:83]
s_add_u32 m0, %[sLx], 36864
ds_read_b128 a[212:215], %[vXr3] offset:17408
v_mfma_f32_16x16x32_bf16 v[84:87], a[168:171], a[68:71], v[84:87]
s_add_u32 s48, s21, 0
v_mfma_f32_16x16x32_bf16 v[88:91], a[172:175], a[64:67], v[88:91]
buffer_load_dwordx4 %[vL16], %[sWS], s48 offen offset:0 sc1 lds
v_mfma_f32_16x16x32_bf16 v[92:95], a[172:175], a[68:71], v[92:95]
v_mfma_f32_16x16x32_bf16 v[96:99], a[176:179], a[64:67], v[96:99]
buffer_load_dwordx4 %[vL16], %[sWS], s48 offen offset:1024 sc1 lds
v_mfma_f32_16x16x32_bf16 v[100:103], a[176:179], a[68:71], v[100:103]
s_waitcnt vmcnt(15) lgkmcnt(0)
v_mfma_f32_16x16x32_bf16 v[32:35], a[180:183], a[72:75], v[32:35]
s_add_u32 s55, s26, 999424
ds_read_b128 a[144:147], %[vXr3] offset:18432
v_mfma_f32_16x16x32_bf16 v[36:39], a[180:183], a[76:79], v[36:39]
ds_read_b128 a[148:151], %[vXr3] offset:19456
v_mfma_f32_16x16x32_bf16 v[40:43], a[184:187], a[72:75], v[40:43]
s_and_b32 s55, s55, 524287
s_or_b32 s55, s55, s75
buffer_load_dwordx4 a[104:107], %[vY4], %[sWS], s55 offen offset:0 sc1
v_mfma_f32_16x16x32_bf16 v[44:47], a[184:187], a[76:79], v[44:47]
ds_read_b128 a[152:155], %[vXr3] offset:20480
v_mfma_f32_16x16x32_bf16 v[48:51], a[188:191], a[72:75], v[48:51]
ds_read_b128 a[156:159], %[vXr3] offset:21504
v_mfma_f32_16x16x32_bf16 v[52:55], a[188:191], a[76:79], v[52:55]
v_mfma_f32_16x16x32_bf16 v[56:59], a[192:195], a[72:75], v[56:59]
ds_read_b128 a[160:163], %[vXr3] offset:22528
v_mfma_f32_16x16x32_bf16 v[60:63], a[192:195], a[76:79], v[60:63]
ds_read_b128 a[164:167], %[vXr3] offset:23552
v_mfma_f32_16x16x32_bf16 v[64:67], a[196:199], a[72:75], v[64:67]
buffer_load_dwordx4 a[108:111], %[vY4], %[sWS], s55 offen offset:1024 sc1
v_mfma_f32_16x16x32_bf16 v[68:71], a[196:199], a[76:79], v[68:71]
ds_read_b128 a[168:171], %[vXr3] offset:24576
v_mfma_f32_16x16x32_bf16 v[72:75], a[200:203], a[72:75], v[72:75]
ds_read_b128 a[172:175], %[vXr3] offset:25600
v_mfma_f32_16x16x32_bf16 v[76:79], a[200:203], a[76:79], v[76:79]
v_mfma_f32_16x16x32_bf16 v[80:83], a[204:207], a[72:75], v[80:83]
ds_read_b128 a[176:179], %[vXr3] offset:26624
v_mfma_f32_16x16x32_bf16 v[84:87], a[204:207], a[76:79], v[84:87]
buffer_load_dwordx4 %[vL16], %[sWS], s48 offen offset:2048 sc1 lds
v_mfma_f32_16x16x32_bf16 v[88:91], a[208:211], a[72:75], v[88:91]
v_mfma_f32_16x16x32_bf16 v[92:95], a[208:211], a[76:79], v[92:95]
buffer_load_dwordx4 %[vL16], %[sWS], s48 offen offset:3072 sc1 lds
v_mfma_f32_16x16x32_bf16 v[96:99], a[212:215], a[72:75], v[96:99]
v_mfma_f32_16x16x32_bf16 v[100:103], a[212:215], a[76:79], v[100:103]
s_waitcnt vmcnt(15) lgkmcnt(0)
v_mfma_f32_16x16x32_bf16 v[32:35], a[144:147], a[80:83], v[32:35]
s_add_u32 s55, s26, 1015808
ds_read_b128 a[180:183], %[vXr3] offset:27648
v_mfma_f32_16x16x32_bf16 v[36:39], a[144:147], a[84:87], v[36:39]
ds_read_b128 a[184:187], %[vXr3] offset:28672
v_mfma_f32_16x16x32_bf16 v[40:43], a[148:151], a[80:83], v[40:43]
s_and_b32 s55, s55, 524287
s_or_b32 s55, s55, s75
buffer_load_dwordx4 a[112:115], %[vY4], %[sWS], s55 offen offset:0 sc1
v_mfma_f32_16x16x32_bf16 v[44:47], a[148:151], a[84:87], v[44:47]
ds_read_b128 a[188:191], %[vXr3] offset:29696
v_mfma_f32_16x16x32_bf16 v[48:51], a[152:155], a[80:83], v[48:51]
ds_read_b128 a[192:195], %[vXr3] offset:30720
v_mfma_f32_16x16x32_bf16 v[52:55], a[152:155], a[84:87], v[52:55]
v_mfma_f32_16x16x32_bf16 v[56:59], a[156:159], a[80:83], v[56:59]
ds_read_b128 a[196:199], %[vXr3] offset:31744
v_mfma_f32_16x16x32_bf16 v[60:63], a[156:159], a[84:87], v[60:63]
ds_read_b128 a[200:203], %[vXr3] offset:32768
v_mfma_f32_16x16x32_bf16 v[64:67], a[160:163], a[80:83], v[64:67]
buffer_load_dwordx4 a[116:119], %[vY4], %[sWS], s55 offen offset:1024 sc1
v_mfma_f32_16x16x32_bf16 v[68:71], a[160:163], a[84:87], v[68:71]
ds_read_b128 a[204:207], %[vXr3] offset:33792
v_mfma_f32_16x16x32_bf16 v[72:75], a[164:167], a[80:83], v[72:75]
ds_read_b128 a[208:211], %[vXr3] offset:34816
v_mfma_f32_16x16x32_bf16 v[76:79], a[164:167], a[84:87], v[76:79]
s_add_u32 m0, %[sLx], 40960
v_mfma_f32_16x16x32_bf16 v[80:83], a[168:171], a[80:83], v[80:83]
s_add_u32 s48, s21, 4096
ds_read_b128 a[212:215], %[vXr3] offset:35840
v_mfma_f32_16x16x32_bf16 v[84:87], a[168:171], a[84:87], v[84:87]
)"
R"(buffer_load_dwordx4 %[vL16], %[sWS], s48 offen offset:0 sc1 lds
v_mfma_f32_16x16x32_bf16 v[88:91], a[172:175], a[80:83], v[88:91]
v_mfma_f32_16x16x32_bf16 v[92:95], a[172:175], a[84:87], v[92:95]
buffer_load_dwordx4 %[vL16], %[sWS], s48 offen offset:1024 sc1 lds
v_mfma_f32_16x16x32_bf16 v[96:99], a[176:179], a[80:83], v[96:99]
v_mfma_f32_16x16x32_bf16 v[100:103], a[176:179], a[84:87], v[100:103]
s_waitcnt vmcnt(12) lgkmcnt(0)
s_barrier
v_mfma_f32_16x16x32_bf16 v[32:35], a[180:183], a[88:91], v[32:35]
s_add_u32 s55, s26, 1032192
ds_read_b128 a[144:147], %[vXr0] offset:0
v_mfma_f32_16x16x32_bf16 v[36:39], a[180:183], a[92:95], v[36:39]
ds_read_b128 a[148:151], %[vXr0] offset:1024
v_mfma_f32_16x16x32_bf16 v[40:43], a[184:187], a[88:91], v[40:43]
s_and_b32 s55, s55, 524287
s_or_b32 s55, s55, s75
buffer_load_dwordx4 a[120:123], %[vY4], %[sWS], s55 offen offset:0 sc1
v_mfma_f32_16x16x32_bf16 v[44:47], a[184:187], a[92:95], v[44:47]
ds_read_b128 a[152:155], %[vXr0] offset:2048
v_mfma_f32_16x16x32_bf16 v[48:51], a[188:191], a[88:91], v[48:51]
ds_read_b128 a[156:159], %[vXr0] offset:3072
v_mfma_f32_16x16x32_bf16 v[52:55], a[188:191], a[92:95], v[52:55]
v_mfma_f32_16x16x32_bf16 v[56:59], a[192:195], a[88:91], v[56:59]
ds_read_b128 a[160:163], %[vXr0] offset:4096
v_mfma_f32_16x16x32_bf16 v[60:63], a[192:195], a[92:95], v[60:63]
ds_read_b128 a[164:167], %[vXr0] offset:5120
v_mfma_f32_16x16x32_bf16 v[64:67], a[196:199], a[88:91], v[64:67]
buffer_load_dwordx4 a[124:127], %[vY4], %[sWS], s55 offen offset:1024 sc1
v_mfma_f32_16x16x32_bf16 v[68:71], a[196:199], a[92:95], v[68:71]
ds_read_b128 a[168:171], %[vXr0] offset:6144
v_mfma_f32_16x16x32_bf16 v[72:75], a[200:203], a[88:91], v[72:75]
ds_read_b128 a[172:175], %[vXr0] offset:7168
v_mfma_f32_16x16x32_bf16 v[76:79], a[200:203], a[92:95], v[76:79]
v_mfma_f32_16x16x32_bf16 v[80:83], a[204:207], a[88:91], v[80:83]
ds_read_b128 a[176:179], %[vXr0] offset:8192
v_mfma_f32_16x16x32_bf16 v[84:87], a[204:207], a[92:95], v[84:87]
buffer_load_dwordx4 %[vL16], %[sWS], s48 offen offset:2048 sc1 lds
v_mfma_f32_16x16x32_bf16 v[88:91], a[208:211], a[88:91], v[88:91]
v_mfma_f32_16x16x32_bf16 v[92:95], a[208:211], a[92:95], v[92:95]
buffer_load_dwordx4 %[vL16], %[sWS], s48 offen offset:3072 sc1 lds
v_mfma_f32_16x16x32_bf16 v[96:99], a[212:215], a[88:91], v[96:99]
v_mfma_f32_16x16x32_bf16 v[100:103], a[212:215], a[92:95], v[100:103]
s_waitcnt vmcnt(14) lgkmcnt(0)
v_mfma_f32_16x16x32_bf16 v[32:35], a[144:147], a[96:99], v[32:35]
s_add_u32 m0, %[sLx], 45056
ds_read_b128 a[180:183], %[vXr0] offset:9216
v_mfma_f32_16x16x32_bf16 v[36:39], a[144:147], a[100:103], v[36:39]
s_add_u32 s48, s21, 8192
ds_read_b128 a[184:187], %[vXr0] offset:10240
v_mfma_f32_16x16x32_bf16 v[40:43], a[148:151], a[96:99], v[40:43]
buffer_load_dwordx4 %[vL16], %[sWS], s48 offen offset:0 sc1 lds
v_mfma_f32_16x16x32_bf16 v[44:47], a[148:151], a[100:103], v[44:47]
ds_read_b128 a[188:191], %[vXr0] offset:11264
v_mfma_f32_16x16x32_bf16 v[48:51], a[152:155], a[96:99], v[48:51]
ds_read_b128 a[192:195], %[vXr0] offset:12288
v_mfma_f32_16x16x32_bf16 v[52:55], a[152:155], a[100:103], v[52:55]
v_mfma_f32_16x16x32_bf16 v[56:59], a[156:159], a[96:99], v[56:59]
ds_read_b128 a[196:199], %[vXr0] offset:13312
v_mfma_f32_16x16x32_bf16 v[60:63], a[156:159], a[100:103], v[60:63]
ds_read_b128 a[200:203], %[vXr0] offset:14336
v_mfma_f32_16x16x32_bf16 v[64:67], a[160:163], a[96:99], v[64:67]
v_mfma_f32_16x16x32_bf16 v[68:71], a[160:163], a[100:103], v[68:71]
s_add_u32 m0, %[sLx], 73728
ds_read_b128 a[204:207], %[vXr0] offset:15360
v_mfma_f32_16x16x32_bf16 v[72:75], a[164:167], a[96:99], v[72:75]
s_add_u32 s48, s22, 0
ds_read_b128 a[208:211], %[vXr0] offset:16384
v_mfma_f32_16x16x32_bf16 v[76:79], a[164:167], a[100:103], v[76:79]
buffer_load_dwordx4 %[vL16], %[sWS], s48 offen offset:0 sc1 lds
v_mfma_f32_16x16x32_bf16 v[80:83], a[168:171], a[96:99], v[80:83]
ds_read_b128 a[212:215], %[vXr0] offset:17408
v_mfma_f32_16x16x32_bf16 v[84:87], a[168:171], a[100:103], v[84:87]
v_mfma_f32_16x16x32_bf16 v[88:91], a[172:175], a[96:99], v[88:91]
v_mfma_f32_16x16x32_bf16 v[92:95], a[172:175], a[100:103], v[92:95]
buffer_load_dwordx4 %[vL16], %[sWS], s48 offen offset:1024 sc1 lds
v_mfma_f32_16x16x32_bf16 v[96:99], a[176:179], a[96:99], v[96:99]
v_mfma_f32_16x16x32_bf16 v[100:103], a[176:179], a[100:103], v[100:103]
s_waitcnt vmcnt(13) lgkmcnt(0)
v_mfma_f32_16x16x32_bf16 v[32:35], a[180:183], a[104:107], v[32:35]
ds_read_b128 a[144:147], %[vXr0] offset:18432
v_mfma_f32_16x16x32_bf16 v[36:39], a[180:183], a[108:111], v[36:39]
ds_read_b128 a[148:151], %[vXr0] offset:19456
v_mfma_f32_16x16x32_bf16 v[40:43], a[184:187], a[104:107], v[40:43]
buffer_load_dwordx4 %[vL16], %[sWS], s48 offen offset:2048 sc1 lds
v_mfma_f32_16x16x32_bf16 v[44:47], a[184:187], a[108:111], v[44:47]
ds_read_b128 a[152:155], %[vXr0] offset:20480
v_mfma_f32_16x16x32_bf16 v[48:51], a[188:191], a[104:107], v[48:51]
ds_read_b128 a[156:159], %[vXr0] offset:21504
v_mfma_f32_16x16x32_bf16 v[52:55], a[188:191], a[108:111], v[52:55]
v_mfma_f32_16x16x32_bf16 v[56:59], a[192:195], a[104:107], v[56:59]
ds_read_b128 a[160:163], %[vXr0] offset:22528
v_mfma_f32_16x16x32_bf16 v[60:63], a[192:195], a[108:111], v[60:63]
ds_read_b128 a[164:167], %[vXr0] offset:23552
v_mfma_f32_16x16x32_bf16 v[64:67], a[196:199], a[104:107], v[64:67]
v_mfma_f32_16x16x32_bf16 v[68:71], a[196:199], a[108:111], v[68:71]
ds_read_b128 a[168:171], %[vXr0] offset:24576
v_mfma_f32_16x16x32_bf16 v[72:75], a[200:203], a[104:107], v[72:75]
ds_read_b128 a[172:175], %[vXr0] offset:25600
v_mfma_f32_16x16x32_bf16 v[76:79], a[200:203], a[108:111], v[76:79]
v_mfma_f32_16x16x32_bf16 v[80:83], a[204:207], a[104:107], v[80:83]
ds_read_b128 a[176:179], %[vXr0] offset:26624
v_mfma_f32_16x16x32_bf16 v[84:87], a[204:207], a[108:111], v[84:87]
buffer_load_dwordx4 %[vL16], %[sWS], s48 offen offset:3072 sc1 lds
v_mfma_f32_16x16x32_bf16 v[88:91], a[208:211], a[104:107], v[88:91]
v_mfma_f32_16x16x32_bf16 v[92:95], a[208:211], a[108:111], v[92:95]
v_mfma_f32_16x16x32_bf16 v[96:99], a[212:215], a[104:107], v[96:99]
v_mfma_f32_16x16x32_bf16 v[100:103], a[212:215], a[108:111], v[100:103]
s_waitcnt vmcnt(11) lgkmcnt(0)
v_mfma_f32_16x16x32_bf16 v[32:35], a[144:147], a[112:115], v[32:35]
s_add_u32 m0, %[sLx], 77824
ds_read_b128 a[180:183], %[vXr0] offset:27648
v_mfma_f32_16x16x32_bf16 v[36:39], a[144:147], a[116:119], v[36:39]
s_add_u32 s48, s22, 4096
ds_read_b128 a[184:187], %[vXr0] offset:28672
v_mfma_f32_16x16x32_bf16 v[40:43], a[148:151], a[112:115], v[40:43]
buffer_load_dwordx4 %[vL16], %[sWS], s48 offen offset:0 sc1 lds
v_mfma_f32_16x16x32_bf16 v[44:47], a[148:151], a[116:119], v[44:47]
ds_read_b128 a[188:191], %[vXr0] offset:29696
v_mfma_f32_16x16x32_bf16 v[48:51], a[152:155], a[112:115], v[48:51]
ds_read_b128 a[192:195], %[vXr0] offset:30720
v_mfma_f32_16x16x32_bf16 v[52:55], a[152:155], a[116:119], v[52:55]
v_mfma_f32_16x16x32_bf16 v[56:59], a[156:159], a[112:115], v[56:59]
ds_read_b128 a[196:199], %[vXr0] offset:31744
v_mfma_f32_16x16x32_bf16 v[60:63], a[156:159], a[116:119], v[60:63]
ds_read_b128 a[200:203], %[vXr0] offset:32768
v_mfma_f32_16x16x32_bf16 v[64:67], a[160:163], a[112:115], v[64:67]
v_mfma_f32_16x16x32_bf16 v[68:71], a[160:163], a[116:119], v[68:71]
ds_read_b128 a[204:207], %[vXr0] offset:33792
v_mfma_f32_16x16x32_bf16 v[72:75], a[164:167], a[112:115], v[72:75]
ds_read_b128 a[208:211], %[vXr0] offset:34816
v_mfma_f32_16x16x32_bf16 v[76:79], a[164:167], a[116:119], v[76:79]
v_mfma_f32_16x16x32_bf16 v[80:83], a[168:171], a[112:115], v[80:83]
ds_read_b128 a[212:215], %[vXr0] offset:35840
v_mfma_f32_16x16x32_bf16 v[84:87], a[168:171], a[116:119], v[84:87]
buffer_load_dwordx4 %[vL16], %[sWS], s48 offen offset:1024 sc1 lds
v_mfma_f32_16x16x32_bf16 v[88:91], a[172:175], a[112:115], v[88:91]
v_mfma_f32_16x16x32_bf16 v[92:95], a[172:175], a[116:119], v[92:95]
v_mfma_f32_16x16x32_bf16 v[96:99], a[176:179], a[112:115], v[96:99]
v_mfma_f32_16x16x32_bf16 v[100:103], a[176:179], a[116:119], v[100:103]
s_waitcnt vmcnt(6) lgkmcnt(0)
s_barrier
v_mfma_f32_16x16x32_bf16 v[32:35], a[180:183], a[120:123], v[32:35]
s_nop 0
ds_read_b128 a[144:147], %[vXr1] offset:0
v_mfma_f32_16x16x32_bf16 v[36:39], a[180:183], a[124:127], v[36:39]
s_nop 0
ds_read_b128 a[148:151], %[vXr1] offset:1024
v_mfma_f32_16x16x32_bf16 v[40:43], a[184:187], a[120:123], v[40:43]
s_nop 0
v_mfma_f32_16x16x32_bf16 v[44:47], a[184:187], a[124:127], v[44:47]
s_nop 0
ds_read_b128 a[152:155], %[vXr1] offset:2048
v_mfma_f32_16x16x32_bf16 v[48:51], a[188:191], a[120:123], v[48:51]
s_nop 0
ds_read_b128 a[156:159], %[vXr1] offset:3072
v_mfma_f32_16x16x32_bf16 v[52:55], a[188:191], a[124:127], v[52:55]
v_mul_f32 v32, v32, v104
v_mul_f32 v33, v33, v105
v_mfma_f32_16x16x32_bf16 v[56:59], a[192:195], a[120:123], v[56:59]
ds_read_b128 a[160:163], %[vXr1] offset:4096
v_mul_f32_e64 v34, v34, v106
v_mfma_f32_16x16x32_bf16 v[60:63], a[192:195], a[124:127], v[60:63]
ds_read_b128 a[164:167], %[vXr1] offset:5120
v_mul_f32_e64 v35, v35, v107
v_mfma_f32_16x16x32_bf16 v[64:67], a[196:199], a[120:123], v[64:67]
v_cvt_pk_bf16_f32 v104, v32, v33
v_cvt_pk_bf16_f32 v105, v34, v35
v_mfma_f32_16x16x32_bf16 v[68:71], a[196:199], a[124:127], v[68:71]
ds_read_b128 a[168:171], %[vXr1] offset:6144
v_max_u32_e64 v104, 1, v104
v_mfma_f32_16x16x32_bf16 v[72:75], a[200:203], a[120:123], v[72:75]
ds_read_b128 a[172:175], %[vXr1] offset:7168
v_mul_f32_e64 v36, v36, v108
v_mfma_f32_16x16x32_bf16 v[76:79], a[200:203], a[124:127], v[76:79]
v_mul_f32 v37, v37, v109
v_mul_f32 v38, v38, v110
v_mfma_f32_16x16x32_bf16 v[80:83], a[204:207], a[120:123], v[80:83]
ds_read_b128 a[176:179], %[vXr1] offset:8192
v_mul_f32_e64 v39, v39, v111
v_mfma_f32_16x16x32_bf16 v[84:87], a[204:207], a[124:127], v[84:87]
v_cvt_pk_bf16_f32 v108, v36, v37
v_cvt_pk_bf16_f32 v109, v38, v39
v_mfma_f32_16x16x32_bf16 v[88:91], a[208:211], a[120:123], v[88:91]
v_max_u32 v108, 1, v108
v_mul_f32 v40, v40, v112
v_mfma_f32_16x16x32_bf16 v[92:95], a[208:211], a[124:127], v[92:95]
v_mul_f32 v41, v41, v113
v_mul_f32 v42, v42, v114
v_mfma_f32_16x16x32_bf16 v[96:99], a[212:215], a[120:123], v[96:99]
v_mul_f32_e64 v43, v43, v115
v_cvt_pk_bf16_f32 v106, v40, v41
v_mfma_f32_16x16x32_bf16 v[100:103], a[212:215], a[124:127], v[100:103]
v_cvt_pk_bf16_f32 v107, v42, v43
buffer_store_dwordx4 v[104:107], %[vL16], %[sWS], s58 offen offset:0 sc1
v_mul_f32 v44, v44, v116
v_mul_f32 v45, v45, v117
v_mul_f32 v46, v46, v118
v_mul_f32 v47, v47, v119
v_cvt_pk_bf16_f32 v110, v44, v45
v_cvt_pk_bf16_f32 v111, v46, v47
)"
R"(buffer_store_dwordx4 v[108:111], %[vL16], %[sWS], s58 offen offset:1024 sc1
v_pk_mul_f32 v[48:49], v[48:49], v[120:121]
v_pk_mul_f32 v[50:51], v[50:51], v[122:123]
v_cvt_pk_bf16_f32 v120, v48, v49
v_cvt_pk_bf16_f32 v121, v50, v51
v_max_u32_e64 v120, 1, v120
v_pk_mul_f32 v[52:53], v[52:53], v[124:125]
v_pk_mul_f32 v[54:55], v[54:55], v[126:127]
v_cvt_pk_bf16_f32 v124, v52, v53
v_cvt_pk_bf16_f32 v125, v54, v55
v_max_u32_e64 v124, 1, v124
v_pk_mul_f32 v[56:57], v[56:57], v[128:129]
v_pk_mul_f32 v[58:59], v[58:59], v[130:131]
v_cvt_pk_bf16_f32 v122, v56, v57
v_cvt_pk_bf16_f32 v123, v58, v59
buffer_store_dwordx4 v[120:123], %[vL16], %[sWS], s58 offen offset:2048 sc1
v_pk_mul_f32 v[60:61], v[60:61], v[132:133]
v_pk_mul_f32 v[62:63], v[62:63], v[134:135]
v_cvt_pk_bf16_f32 v126, v60, v61
v_cvt_pk_bf16_f32 v127, v62, v63
buffer_store_dwordx4 v[124:127], %[vL16], %[sWS], s58 offen offset:3072 sc1
v_pk_mul_f32 v[64:65], v[64:65], v[136:137]
v_pk_mul_f32 v[66:67], v[66:67], v[138:139]
v_cvt_pk_bf16_f32 v136, v64, v65
v_cvt_pk_bf16_f32 v137, v66, v67
v_max_u32_e64 v136, 1, v136
v_pk_mul_f32 v[68:69], v[68:69], v[140:141]
v_pk_mul_f32 v[70:71], v[70:71], v[142:143]
v_cvt_pk_bf16_f32 v140, v68, v69
v_cvt_pk_bf16_f32 v141, v70, v71
v_max_u32_e64 v140, 1, v140
v_pk_mul_f32 v[72:73], v[72:73], v[144:145]
v_pk_mul_f32 v[74:75], v[74:75], v[146:147]
v_cvt_pk_bf16_f32 v138, v72, v73
v_cvt_pk_bf16_f32 v139, v74, v75
buffer_store_dwordx4 v[136:139], v249, %[sWS], s58 offen offset:0 sc1
v_pk_mul_f32 v[76:77], v[76:77], v[148:149]
v_pk_mul_f32 v[78:79], v[78:79], v[150:151]
v_cvt_pk_bf16_f32 v142, v76, v77
v_cvt_pk_bf16_f32 v143, v78, v79
buffer_store_dwordx4 v[140:143], v249, %[sWS], s58 offen offset:1024 sc1
v_pk_mul_f32 v[80:81], v[80:81], v[152:153]
v_pk_mul_f32 v[82:83], v[82:83], v[154:155]
v_cvt_pk_bf16_f32 v152, v80, v81
v_cvt_pk_bf16_f32 v153, v82, v83
v_max_u32_e64 v152, 1, v152
v_pk_mul_f32 v[84:85], v[84:85], v[156:157]
v_pk_mul_f32 v[86:87], v[86:87], v[158:159]
v_cvt_pk_bf16_f32 v156, v84, v85
v_cvt_pk_bf16_f32 v157, v86, v87
v_max_u32_e64 v156, 1, v156
v_pk_mul_f32 v[88:89], v[88:89], v[160:161]
v_pk_mul_f32 v[90:91], v[90:91], v[162:163]
v_cvt_pk_bf16_f32 v154, v88, v89
v_cvt_pk_bf16_f32 v155, v90, v91
buffer_store_dwordx4 v[152:155], v249, %[sWS], s58 offen offset:2048 sc1
v_pk_mul_f32 v[92:93], v[92:93], v[164:165]
v_pk_mul_f32 v[94:95], v[94:95], v[166:167]
v_cvt_pk_bf16_f32 v158, v92, v93
v_cvt_pk_bf16_f32 v159, v94, v95
buffer_store_dwordx4 v[156:159], v249, %[sWS], s58 offen offset:3072 sc1
v_pk_mul_f32 v[96:97], v[96:97], v[168:169]
v_pk_mul_f32 v[98:99], v[98:99], v[170:171]
v_cvt_pk_bf16_f32 v168, v96, v97
v_cvt_pk_bf16_f32 v169, v98, v99
v_max_u32_e64 v168, 1, v168
buffer_store_dwordx2 v[168:169], %[vU8], %[sWS], 0 offen offset:0 sc1
v_pk_mul_f32 v[100:101], v[100:101], v[172:173]
v_pk_mul_f32 v[102:103], v[102:103], v[174:175]
v_cvt_pk_bf16_f32 v172, v100, v101
v_cvt_pk_bf16_f32 v173, v102, v103
v_max_u32 v172, 1, v172
buffer_store_dwordx2 v[172:173], %[vU8], %[sWS], 0 offen offset:1024 sc1
s_waitcnt lgkmcnt(0)
L_g5:
v_mfma_f32_16x16x32_bf16 v[176:179], a[144:147], v[104:107], 0
ds_read_b128 a[180:183], %[vXr1] offset:9216
v_mfma_f32_16x16x32_bf16 v[212:215], a[144:147], v[108:111], 0
ds_read_b128 a[184:187], %[vXr1] offset:10240
v_mfma_f32_16x16x32_bf16 v[180:183], a[148:151], v[104:107], 0
buffer_load_dwordx4 %[vL16], %[sWS], s48 offen offset:2048 sc1 lds
v_mfma_f32_16x16x32_bf16 v[216:219], a[148:151], v[108:111], 0
ds_read_b128 a[188:191], %[vXr1] offset:11264
v_mfma_f32_16x16x32_bf16 v[184:187], a[152:155], v[104:107], 0
ds_read_b128 a[192:195], %[vXr1] offset:12288
v_mfma_f32_16x16x32_bf16 v[220:223], a[152:155], v[108:111], 0
buffer_load_dwordx4 %[vL16], %[sWS], s48 offen offset:3072 sc1 lds
v_mfma_f32_16x16x32_bf16 v[188:191], a[156:159], v[104:107], 0
ds_read_b128 a[196:199], %[vXr1] offset:13312
v_mfma_f32_16x16x32_bf16 v[224:227], a[156:159], v[108:111], 0
ds_read_b128 a[200:203], %[vXr1] offset:14336
v_mfma_f32_16x16x32_bf16 v[192:195], a[160:163], v[104:107], 0
v_mfma_f32_16x16x32_bf16 v[228:231], a[160:163], v[108:111], 0
s_add_u32 m0, %[sLx], 81920
ds_read_b128 a[204:207], %[vXr1] offset:15360
v_mfma_f32_16x16x32_bf16 v[196:199], a[164:167], v[104:107], 0
s_add_u32 s48, s22, 8192
ds_read_b128 a[208:211], %[vXr1] offset:16384
v_mfma_f32_16x16x32_bf16 v[232:235], a[164:167], v[108:111], 0
buffer_load_dwordx4 %[vL16], %[sWS], s48 offen offset:0 sc1 lds
v_mfma_f32_16x16x32_bf16 v[200:203], a[168:171], v[104:107], 0
s_add_u32 m0, %[sLx], 110592
ds_read_b128 a[212:215], %[vXr1] offset:17408
v_mfma_f32_16x16x32_bf16 v[236:239], a[168:171], v[108:111], 0
s_add_u32 s48, s23, 0
v_mfma_f32_16x16x32_bf16 v[204:207], a[172:175], v[104:107], 0
buffer_load_dwordx4 %[vL16], %[sWS], s48 offen offset:0 sc1 lds
v_mfma_f32_16x16x32_bf16 v[240:243], a[172:175], v[108:111], 0
v_mfma_f32_16x16x32_bf16 v[208:211], a[176:179], v[104:107], 0
buffer_load_dwordx4 %[vL16], %[sWS], s48 offen offset:1024 sc1 lds
v_mfma_f32_16x16x32_bf16 v[244:247], a[176:179], v[108:111], 0
s_waitcnt lgkmcnt(0)
v_mfma_f32_16x16x32_bf16 v[176:179], a[180:183], v[120:123], v[176:179]
ds_read_b128 a[144:147], %[vXr1] offset:18432
v_mfma_f32_16x16x32_bf16 v[212:215], a[180:183], v[124:127], v[212:215]
ds_read_b128 a[148:151], %[vXr1] offset:19456
v_mfma_f32_16x16x32_bf16 v[180:183], a[184:187], v[120:123], v[180:183]
buffer_load_dwordx4 %[vL16], %[sWS], s48 offen offset:2048 sc1 lds
v_mfma_f32_16x16x32_bf16 v[216:219], a[184:187], v[124:127], v[216:219]
ds_read_b128 a[152:155], %[vXr1] offset:20480
v_mfma_f32_16x16x32_bf16 v[184:187], a[188:191], v[120:123], v[184:187]
ds_read_b128 a[156:159], %[vXr1] offset:21504
v_mfma_f32_16x16x32_bf16 v[220:223], a[188:191], v[124:127], v[220:223]
buffer_load_dwordx4 v[32:35], %[vL16], %[sWS], s59 offen offset:0 sc1
v_mfma_f32_16x16x32_bf16 v[188:191], a[192:195], v[120:123], v[188:191]
ds_read_b128 a[160:163], %[vXr1] offset:22528
v_mfma_f32_16x16x32_bf16 v[224:227], a[192:195], v[124:127], v[224:227]
ds_read_b128 a[164:167], %[vXr1] offset:23552
v_mfma_f32_16x16x32_bf16 v[192:195], a[196:199], v[120:123], v[192:195]
v_mfma_f32_16x16x32_bf16 v[228:231], a[196:199], v[124:127], v[228:231]
ds_read_b128 a[168:171], %[vXr1] offset:24576
v_mfma_f32_16x16x32_bf16 v[196:199], a[200:203], v[120:123], v[196:199]
ds_read_b128 a[172:175], %[vXr1] offset:25600
v_mfma_f32_16x16x32_bf16 v[232:235], a[200:203], v[124:127], v[232:235]
buffer_load_dwordx4 v[36:39], %[vL16], %[sWS], s59 offen offset:1024 sc1
v_mfma_f32_16x16x32_bf16 v[200:203], a[204:207], v[120:123], v[200:203]
ds_read_b128 a[176:179], %[vXr1] offset:26624
v_mfma_f32_16x16x32_bf16 v[236:239], a[204:207], v[124:127], v[236:239]
buffer_load_dwordx4 v[40:43], %[vL16], %[sWS], s59 offen offset:2048 sc1
v_mfma_f32_16x16x32_bf16 v[204:207], a[208:211], v[120:123], v[204:207]
v_mfma_f32_16x16x32_bf16 v[240:243], a[208:211], v[124:127], v[240:243]
buffer_load_dwordx4 v[44:47], %[vL16], %[sWS], s59 offen offset:3072 sc1
v_mfma_f32_16x16x32_bf16 v[208:211], a[212:215], v[120:123], v[208:211]
buffer_load_dwordx4 %[vL16], %[sWS], s48 offen offset:3072 sc1 lds
v_mfma_f32_16x16x32_bf16 v[244:247], a[212:215], v[124:127], v[244:247]
LPb_10:
s_cbranch_execz LRb
s_waitcnt lgkmcnt(0)
v_mfma_f32_16x16x32_bf16 v[176:179], a[144:147], v[136:139], v[176:179]
ds_read_b128 a[180:183], %[vXr1] offset:27648
v_mfma_f32_16x16x32_bf16 v[212:215], a[144:147], v[140:143], v[212:215]
ds_read_b128 a[184:187], %[vXr1] offset:28672
v_mfma_f32_16x16x32_bf16 v[180:183], a[148:151], v[136:139], v[180:183]
buffer_load_dwordx4 v[48:51], %[vL16], %[sWS], s60 offen offset:0 sc1
v_mfma_f32_16x16x32_bf16 v[216:219], a[148:151], v[140:143], v[216:219]
ds_read_b128 a[188:191], %[vXr1] offset:29696
v_mfma_f32_16x16x32_bf16 v[184:187], a[152:155], v[136:139], v[184:187]
ds_read_b128 a[192:195], %[vXr1] offset:30720
v_mfma_f32_16x16x32_bf16 v[220:223], a[152:155], v[140:143], v[220:223]
v_mfma_f32_16x16x32_bf16 v[188:191], a[156:159], v[136:139], v[188:191]
ds_read_b128 a[196:199], %[vXr1] offset:31744
v_mfma_f32_16x16x32_bf16 v[224:227], a[156:159], v[140:143], v[224:227]
ds_read_b128 a[200:203], %[vXr1] offset:32768
v_mfma_f32_16x16x32_bf16 v[192:195], a[160:163], v[136:139], v[192:195]
buffer_load_dwordx4 v[52:55], %[vL16], %[sWS], s60 offen offset:1024 sc1
v_mfma_f32_16x16x32_bf16 v[228:231], a[160:163], v[140:143], v[228:231]
ds_read_b128 a[204:207], %[vXr1] offset:33792
v_mfma_f32_16x16x32_bf16 v[196:199], a[164:167], v[136:139], v[196:199]
ds_read_b128 a[208:211], %[vXr1] offset:34816
v_mfma_f32_16x16x32_bf16 v[232:235], a[164:167], v[140:143], v[232:235]
s_add_u32 m0, %[sLx], 114688
v_mfma_f32_16x16x32_bf16 v[200:203], a[168:171], v[136:139], v[200:203]
s_add_u32 s48, s23, 4096
ds_read_b128 a[212:215], %[vXr1] offset:35840
v_mfma_f32_16x16x32_bf16 v[236:239], a[168:171], v[140:143], v[236:239]
buffer_load_dwordx4 %[vL16], %[sWS], s48 offen offset:0 sc1 lds
v_mfma_f32_16x16x32_bf16 v[204:207], a[172:175], v[136:139], v[204:207]
v_mfma_f32_16x16x32_bf16 v[240:243], a[172:175], v[140:143], v[240:243]
buffer_load_dwordx4 %[vL16], %[sWS], s48 offen offset:1024 sc1 lds
v_mfma_f32_16x16x32_bf16 v[208:211], a[176:179], v[136:139], v[208:211]
v_mfma_f32_16x16x32_bf16 v[244:247], a[176:179], v[140:143], v[244:247]
s_waitcnt vmcnt(12) lgkmcnt(0)
s_barrier
v_mfma_f32_16x16x32_bf16 v[176:179], a[180:183], v[152:155], v[176:179]
ds_read_b128 a[144:147], %[vXr2] offset:0
v_mfma_f32_16x16x32_bf16 v[212:215], a[180:183], v[156:159], v[212:215]
ds_read_b128 a[148:151], %[vXr2] offset:1024
v_mfma_f32_16x16x32_bf16 v[180:183], a[184:187], v[152:155], v[180:183]
buffer_load_dwordx4 v[56:59], %[vL16], %[sWS], s60 offen offset:2048 sc1
v_mfma_f32_16x16x32_bf16 v[216:219], a[184:187], v[156:159], v[216:219]
ds_read_b128 a[152:155], %[vXr2] offset:2048
v_mfma_f32_16x16x32_bf16 v[184:187], a[188:191], v[152:155], v[184:187]
ds_read_b128 a[156:159], %[vXr2] offset:3072
v_mfma_f32_16x16x32_bf16 v[220:223], a[188:191], v[156:159], v[220:223]
v_mfma_f32_16x16x32_bf16 v[188:191], a[192:195], v[152:155], v[188:191]
ds_read_b128 a[160:163], %[vXr2] offset:4096
v_mfma_f32_16x16x32_bf16 v[224:227], a[192:195], v[156:159], v[224:227]
ds_read_b128 a[164:167], %[vXr2] offset:5120
v_mfma_f32_16x16x32_bf16 v[192:195], a[196:199], v[152:155], v[192:195]
buffer_load_dwordx4 v[60:63], %[vL16], %[sWS], s60 offen offset:3072 sc1
v_mfma_f32_16x16x32_bf16 v[228:231], a[196:199], v[156:159], v[228:231]
)"
R"(ds_read_b128 a[168:171], %[vXr2] offset:6144
v_mfma_f32_16x16x32_bf16 v[196:199], a[200:203], v[152:155], v[196:199]
ds_read_b128 a[172:175], %[vXr2] offset:7168
v_mfma_f32_16x16x32_bf16 v[232:235], a[200:203], v[156:159], v[232:235]
v_mfma_f32_16x16x32_bf16 v[200:203], a[204:207], v[152:155], v[200:203]
ds_read_b128 a[176:179], %[vXr2] offset:8192
v_mfma_f32_16x16x32_bf16 v[236:239], a[204:207], v[156:159], v[236:239]
buffer_load_dwordx4 %[vL16], %[sWS], s48 offen offset:2048 sc1 lds
v_mfma_f32_16x16x32_bf16 v[204:207], a[208:211], v[152:155], v[204:207]
s_waitcnt vmcnt(10)
v_mfma_f32_16x16x32_bf16 v[240:243], a[208:211], v[156:159], v[240:243]
buffer_load_dwordx4 %[vL16], %[sWS], s48 offen offset:3072 sc1 lds
v_min_u32 v248, v32, v36
v_mfma_f32_16x16x32_bf16 v[208:211], a[212:215], v[152:155], v[208:211]
v_cmp_eq_u32_e64 vcc, 0, v248
s_cbranch_vccnz L_sl17_%=
L_ok17_%=:
v_mfma_f32_16x16x32_bf16 v[244:247], a[212:215], v[156:159], v[244:247]
s_waitcnt lgkmcnt(0)
v_mfma_f32_16x16x32_bf16 v[176:179], a[144:147], v[32:35], v[176:179]
s_add_u32 m0, %[sLx], 118784
ds_read_b128 a[180:183], %[vXr2] offset:9216
v_mfma_f32_16x16x32_bf16 v[212:215], a[144:147], v[36:39], v[212:215]
s_add_u32 s48, s23, 8192
ds_read_b128 a[184:187], %[vXr2] offset:10240
v_mfma_f32_16x16x32_bf16 v[180:183], a[148:151], v[32:35], v[180:183]
buffer_load_dwordx4 %[vL16], %[sWS], s48 offen offset:0 sc1 lds
v_mfma_f32_16x16x32_bf16 v[216:219], a[148:151], v[36:39], v[216:219]
ds_read_b128 a[188:191], %[vXr2] offset:11264
v_mfma_f32_16x16x32_bf16 v[184:187], a[152:155], v[32:35], v[184:187]
ds_read_b128 a[192:195], %[vXr2] offset:12288
v_mfma_f32_16x16x32_bf16 v[220:223], a[152:155], v[36:39], v[220:223]
buffer_load_dwordx4 v[64:67], %[vL16], %[sWS], s61 offen offset:0 sc1
v_mfma_f32_16x16x32_bf16 v[188:191], a[156:159], v[32:35], v[188:191]
ds_read_b128 a[196:199], %[vXr2] offset:13312
v_mfma_f32_16x16x32_bf16 v[224:227], a[156:159], v[36:39], v[224:227]
ds_read_b128 a[200:203], %[vXr2] offset:14336
v_mfma_f32_16x16x32_bf16 v[192:195], a[160:163], v[32:35], v[192:195]
v_mfma_f32_16x16x32_bf16 v[228:231], a[160:163], v[36:39], v[228:231]
ds_read_b128 a[204:207], %[vXr2] offset:15360
v_mfma_f32_16x16x32_bf16 v[196:199], a[164:167], v[32:35], v[196:199]
ds_read_b128 a[208:211], %[vXr2] offset:16384
v_mfma_f32_16x16x32_bf16 v[232:235], a[164:167], v[36:39], v[232:235]
buffer_load_dwordx4 v[68:71], %[vL16], %[sWS], s61 offen offset:1024 sc1
v_mfma_f32_16x16x32_bf16 v[200:203], a[168:171], v[32:35], v[200:203]
s_add_u32 m0, %[sLx], 0
ds_read_b128 a[212:215], %[vXr2] offset:17408
v_mfma_f32_16x16x32_bf16 v[236:239], a[168:171], v[36:39], v[236:239]
s_add_u32 s48, s24, 0
v_mfma_f32_16x16x32_bf16 v[204:207], a[172:175], v[32:35], v[204:207]
buffer_load_dwordx4 %[vL16], %[sWS], s48 offen offset:0 sc1 lds
s_waitcnt vmcnt(13)
v_mfma_f32_16x16x32_bf16 v[240:243], a[172:175], v[36:39], v[240:243]
v_min_u32 v248, v40, v44
v_mfma_f32_16x16x32_bf16 v[208:211], a[176:179], v[32:35], v[208:211]
buffer_load_dwordx4 %[vL16], %[sWS], s48 offen offset:1024 sc1 lds
v_cmp_eq_u32_e64 vcc, 0, v248
s_cbranch_vccnz L_sl18_%=
L_ok18_%=:
v_mfma_f32_16x16x32_bf16 v[244:247], a[176:179], v[36:39], v[244:247]
s_waitcnt lgkmcnt(0)
v_mfma_f32_16x16x32_bf16 v[176:179], a[180:183], v[40:43], v[176:179]
ds_read_b128 a[144:147], %[vXr2] offset:18432
v_mfma_f32_16x16x32_bf16 v[212:215], a[180:183], v[44:47], v[212:215]
ds_read_b128 a[148:151], %[vXr2] offset:19456
v_mfma_f32_16x16x32_bf16 v[180:183], a[184:187], v[40:43], v[180:183]
buffer_load_dwordx4 v[72:75], %[vL16], %[sWS], s61 offen offset:2048 sc1
v_mfma_f32_16x16x32_bf16 v[216:219], a[184:187], v[44:47], v[216:219]
ds_read_b128 a[152:155], %[vXr2] offset:20480
v_mfma_f32_16x16x32_bf16 v[184:187], a[188:191], v[40:43], v[184:187]
ds_read_b128 a[156:159], %[vXr2] offset:21504
v_mfma_f32_16x16x32_bf16 v[220:223], a[188:191], v[44:47], v[220:223]
v_mfma_f32_16x16x32_bf16 v[188:191], a[192:195], v[40:43], v[188:191]
ds_read_b128 a[160:163], %[vXr2] offset:22528
v_mfma_f32_16x16x32_bf16 v[224:227], a[192:195], v[44:47], v[224:227]
ds_read_b128 a[164:167], %[vXr2] offset:23552
v_mfma_f32_16x16x32_bf16 v[192:195], a[196:199], v[40:43], v[192:195]
buffer_load_dwordx4 v[76:79], %[vL16], %[sWS], s61 offen offset:3072 sc1
v_mfma_f32_16x16x32_bf16 v[228:231], a[196:199], v[44:47], v[228:231]
ds_read_b128 a[168:171], %[vXr2] offset:24576
v_mfma_f32_16x16x32_bf16 v[196:199], a[200:203], v[40:43], v[196:199]
ds_read_b128 a[172:175], %[vXr2] offset:25600
v_mfma_f32_16x16x32_bf16 v[232:235], a[200:203], v[44:47], v[232:235]
v_mfma_f32_16x16x32_bf16 v[200:203], a[204:207], v[40:43], v[200:203]
ds_read_b128 a[176:179], %[vXr2] offset:26624
v_mfma_f32_16x16x32_bf16 v[236:239], a[204:207], v[44:47], v[236:239]
buffer_load_dwordx4 %[vL16], %[sWS], s48 offen offset:2048 sc1 lds
v_mfma_f32_16x16x32_bf16 v[204:207], a[208:211], v[40:43], v[204:207]
s_waitcnt vmcnt(14)
v_mfma_f32_16x16x32_bf16 v[240:243], a[208:211], v[44:47], v[240:243]
buffer_load_dwordx4 %[vL16], %[sWS], s48 offen offset:3072 sc1 lds
v_min_u32 v248, v48, v52
v_mfma_f32_16x16x32_bf16 v[208:211], a[212:215], v[40:43], v[208:211]
v_cmp_eq_u32_e64 vcc, 0, v248
s_cbranch_vccnz L_sl19_%=
L_ok19_%=:
v_mfma_f32_16x16x32_bf16 v[244:247], a[212:215], v[44:47], v[244:247]
s_waitcnt lgkmcnt(0)
v_mfma_f32_16x16x32_bf16 v[176:179], a[144:147], v[48:51], v[176:179]
ds_read_b128 a[180:183], %[vXr2] offset:27648
v_mfma_f32_16x16x32_bf16 v[212:215], a[144:147], v[52:55], v[212:215]
ds_read_b128 a[184:187], %[vXr2] offset:28672
v_mfma_f32_16x16x32_bf16 v[180:183], a[148:151], v[48:51], v[180:183]
buffer_load_dwordx4 v[80:83], v249, %[sWS], s59 offen offset:0 sc1
v_mfma_f32_16x16x32_bf16 v[216:219], a[148:151], v[52:55], v[216:219]
ds_read_b128 a[188:191], %[vXr2] offset:29696
v_mfma_f32_16x16x32_bf16 v[184:187], a[152:155], v[48:51], v[184:187]
ds_read_b128 a[192:195], %[vXr2] offset:30720
v_mfma_f32_16x16x32_bf16 v[220:223], a[152:155], v[52:55], v[220:223]
v_mfma_f32_16x16x32_bf16 v[188:191], a[156:159], v[48:51], v[188:191]
ds_read_b128 a[196:199], %[vXr2] offset:31744
v_mfma_f32_16x16x32_bf16 v[224:227], a[156:159], v[52:55], v[224:227]
ds_read_b128 a[200:203], %[vXr2] offset:32768
v_mfma_f32_16x16x32_bf16 v[192:195], a[160:163], v[48:51], v[192:195]
buffer_load_dwordx4 v[84:87], v249, %[sWS], s59 offen offset:1024 sc1
v_mfma_f32_16x16x32_bf16 v[228:231], a[160:163], v[52:55], v[228:231]
ds_read_b128 a[204:207], %[vXr2] offset:33792
v_mfma_f32_16x16x32_bf16 v[196:199], a[164:167], v[48:51], v[196:199]
ds_read_b128 a[208:211], %[vXr2] offset:34816
v_mfma_f32_16x16x32_bf16 v[232:235], a[164:167], v[52:55], v[232:235]
s_add_u32 m0, %[sLx], 4096
v_mfma_f32_16x16x32_bf16 v[200:203], a[168:171], v[48:51], v[200:203]
s_add_u32 s48, s24, 4096
ds_read_b128 a[212:215], %[vXr2] offset:35840
v_mfma_f32_16x16x32_bf16 v[236:239], a[168:171], v[52:55], v[236:239]
buffer_load_dwordx4 %[vL16], %[sWS], s48 offen offset:0 sc1 lds
v_mfma_f32_16x16x32_bf16 v[204:207], a[172:175], v[48:51], v[204:207]
s_waitcnt vmcnt(14)
v_mfma_f32_16x16x32_bf16 v[240:243], a[172:175], v[52:55], v[240:243]
buffer_load_dwordx4 %[vL16], %[sWS], s48 offen offset:1024 sc1 lds
v_min_u32 v248, v56, v60
v_mfma_f32_16x16x32_bf16 v[208:211], a[176:179], v[48:51], v[208:211]
v_cmp_eq_u32 vcc, 0, v248
s_cbranch_vccnz L_sl20_%=
L_ok20_%=:
v_mfma_f32_16x16x32_bf16 v[244:247], a[176:179], v[52:55], v[244:247]
s_waitcnt vmcnt(12) lgkmcnt(0)
s_barrier
v_mfma_f32_16x16x32_bf16 v[176:179], a[180:183], v[56:59], v[176:179]
ds_read_b128 a[144:147], %[vXr3] offset:0
v_mfma_f32_16x16x32_bf16 v[212:215], a[180:183], v[60:63], v[212:215]
ds_read_b128 a[148:151], %[vXr3] offset:1024
v_mfma_f32_16x16x32_bf16 v[180:183], a[184:187], v[56:59], v[180:183]
buffer_load_dwordx4 v[88:91], v249, %[sWS], s59 offen offset:2048 sc1
v_mfma_f32_16x16x32_bf16 v[216:219], a[184:187], v[60:63], v[216:219]
ds_read_b128 a[152:155], %[vXr3] offset:2048
v_mfma_f32_16x16x32_bf16 v[184:187], a[188:191], v[56:59], v[184:187]
ds_read_b128 a[156:159], %[vXr3] offset:3072
v_mfma_f32_16x16x32_bf16 v[220:223], a[188:191], v[60:63], v[220:223]
v_mfma_f32_16x16x32_bf16 v[188:191], a[192:195], v[56:59], v[188:191]
ds_read_b128 a[160:163], %[vXr3] offset:4096
v_mfma_f32_16x16x32_bf16 v[224:227], a[192:195], v[60:63], v[224:227]
ds_read_b128 a[164:167], %[vXr3] offset:5120
v_mfma_f32_16x16x32_bf16 v[192:195], a[196:199], v[56:59], v[192:195]
buffer_load_dwordx4 v[92:95], v249, %[sWS], s59 offen offset:3072 sc1
v_mfma_f32_16x16x32_bf16 v[228:231], a[196:199], v[60:63], v[228:231]
ds_read_b128 a[168:171], %[vXr3] offset:6144
v_mfma_f32_16x16x32_bf16 v[196:199], a[200:203], v[56:59], v[196:199]
ds_read_b128 a[172:175], %[vXr3] offset:7168
v_mfma_f32_16x16x32_bf16 v[232:235], a[200:203], v[60:63], v[232:235]
v_mfma_f32_16x16x32_bf16 v[200:203], a[204:207], v[56:59], v[200:203]
ds_read_b128 a[176:179], %[vXr3] offset:8192
v_mfma_f32_16x16x32_bf16 v[236:239], a[204:207], v[60:63], v[236:239]
buffer_load_dwordx4 %[vL16], %[sWS], s48 offen offset:2048 sc1 lds
v_mfma_f32_16x16x32_bf16 v[204:207], a[208:211], v[56:59], v[204:207]
s_waitcnt vmcnt(13)
v_mfma_f32_16x16x32_bf16 v[240:243], a[208:211], v[60:63], v[240:243]
buffer_load_dwordx4 %[vL16], %[sWS], s48 offen offset:3072 sc1 lds
v_min_u32 v248, v64, v68
v_mfma_f32_16x16x32_bf16 v[208:211], a[212:215], v[56:59], v[208:211]
v_cmp_eq_u32_e64 vcc, 0, v248
s_cbranch_vccnz L_sl21_%=
L_ok21_%=:
v_mfma_f32_16x16x32_bf16 v[244:247], a[212:215], v[60:63], v[244:247]
s_waitcnt lgkmcnt(0)
v_mfma_f32_16x16x32_bf16 v[176:179], a[144:147], v[64:67], v[176:179]
s_add_u32 m0, %[sLx], 8192
ds_read_b128 a[180:183], %[vXr3] offset:9216
v_mfma_f32_16x16x32_bf16 v[212:215], a[144:147], v[68:71], v[212:215]
s_add_u32 s48, s24, 8192
ds_read_b128 a[184:187], %[vXr3] offset:10240
v_mfma_f32_16x16x32_bf16 v[180:183], a[148:151], v[64:67], v[180:183]
buffer_load_dwordx4 %[vL16], %[sWS], s48 offen offset:0 sc1 lds
v_mfma_f32_16x16x32_bf16 v[216:219], a[148:151], v[68:71], v[216:219]
ds_read_b128 a[188:191], %[vXr3] offset:11264
v_mfma_f32_16x16x32_bf16 v[184:187], a[152:155], v[64:67], v[184:187]
ds_read_b128 a[192:195], %[vXr3] offset:12288
v_mfma_f32_16x16x32_bf16 v[220:223], a[152:155], v[68:71], v[220:223]
v_mfma_f32_16x16x32_bf16 v[188:191], a[156:159], v[64:67], v[188:191]
ds_read_b128 a[196:199], %[vXr3] offset:13312
v_mfma_f32_16x16x32_bf16 v[224:227], a[156:159], v[68:71], v[224:227]
ds_read_b128 a[200:203], %[vXr3] offset:14336
v_mfma_f32_16x16x32_bf16 v[192:195], a[160:163], v[64:67], v[192:195]
v_mfma_f32_16x16x32_bf16 v[228:231], a[160:163], v[68:71], v[228:231]
ds_read_b128 a[204:207], %[vXr3] offset:15360
v_mfma_f32_16x16x32_bf16 v[196:199], a[164:167], v[64:67], v[196:199]
ds_read_b128 a[208:211], %[vXr3] offset:16384
v_mfma_f32_16x16x32_bf16 v[232:235], a[164:167], v[68:71], v[232:235]
buffer_load_dwordx4 v[96:99], v249, %[sWS], s60 offen offset:0 sc1
v_mfma_f32_16x16x32_bf16 v[200:203], a[168:171], v[64:67], v[200:203]
)"
R"(ds_read_b128 a[212:215], %[vXr3] offset:17408
v_mfma_f32_16x16x32_bf16 v[236:239], a[168:171], v[68:71], v[236:239]
v_mfma_f32_16x16x32_bf16 v[204:207], a[172:175], v[64:67], v[204:207]
s_waitcnt vmcnt(12)
v_mfma_f32_16x16x32_bf16 v[240:243], a[172:175], v[68:71], v[240:243]
buffer_load_dwordx4 v[100:103], v249, %[sWS], s60 offen offset:1024 sc1
v_min_u32 v248, v72, v76
v_mfma_f32_16x16x32_bf16 v[208:211], a[176:179], v[64:67], v[208:211]
v_cmp_eq_u32_e64 vcc, 0, v248
s_cbranch_vccnz L_sl22_%=
L_ok22_%=:
v_mfma_f32_16x16x32_bf16 v[244:247], a[176:179], v[68:71], v[244:247]
s_waitcnt lgkmcnt(0)
v_mfma_f32_16x16x32_bf16 v[176:179], a[180:183], v[72:75], v[176:179]
ds_read_b128 a[144:147], %[vXr3] offset:18432
v_mfma_f32_16x16x32_bf16 v[212:215], a[180:183], v[76:79], v[212:215]
ds_read_b128 a[148:151], %[vXr3] offset:19456
v_mfma_f32_16x16x32_bf16 v[180:183], a[184:187], v[72:75], v[180:183]
buffer_load_dwordx4 v[32:35], v249, %[sWS], s60 offen offset:2048 sc1
v_mfma_f32_16x16x32_bf16 v[216:219], a[184:187], v[76:79], v[216:219]
ds_read_b128 a[152:155], %[vXr3] offset:20480
v_mfma_f32_16x16x32_bf16 v[184:187], a[188:191], v[72:75], v[184:187]
ds_read_b128 a[156:159], %[vXr3] offset:21504
v_mfma_f32_16x16x32_bf16 v[220:223], a[188:191], v[76:79], v[220:223]
v_mfma_f32_16x16x32_bf16 v[188:191], a[192:195], v[72:75], v[188:191]
ds_read_b128 a[160:163], %[vXr3] offset:22528
v_mfma_f32_16x16x32_bf16 v[224:227], a[192:195], v[76:79], v[224:227]
ds_read_b128 a[164:167], %[vXr3] offset:23552
v_mfma_f32_16x16x32_bf16 v[192:195], a[196:199], v[72:75], v[192:195]
v_mfma_f32_16x16x32_bf16 v[228:231], a[196:199], v[76:79], v[228:231]
ds_read_b128 a[168:171], %[vXr3] offset:24576
v_mfma_f32_16x16x32_bf16 v[196:199], a[200:203], v[72:75], v[196:199]
ds_read_b128 a[172:175], %[vXr3] offset:25600
v_mfma_f32_16x16x32_bf16 v[232:235], a[200:203], v[76:79], v[232:235]
v_mfma_f32_16x16x32_bf16 v[200:203], a[204:207], v[72:75], v[200:203]
ds_read_b128 a[176:179], %[vXr3] offset:26624
v_mfma_f32_16x16x32_bf16 v[236:239], a[204:207], v[76:79], v[236:239]
buffer_load_dwordx4 v[36:39], v249, %[sWS], s60 offen offset:3072 sc1
v_mfma_f32_16x16x32_bf16 v[204:207], a[208:211], v[72:75], v[204:207]
s_waitcnt vmcnt(11)
v_mfma_f32_16x16x32_bf16 v[240:243], a[208:211], v[76:79], v[240:243]
v_min_u32 v248, v80, v84
v_mfma_f32_16x16x32_bf16 v[208:211], a[212:215], v[72:75], v[208:211]
v_cmp_eq_u32_e64 vcc, 0, v248
s_cbranch_vccnz L_sl23_%=
L_ok23_%=:
v_mfma_f32_16x16x32_bf16 v[244:247], a[212:215], v[76:79], v[244:247]
s_waitcnt lgkmcnt(0)
v_mfma_f32_16x16x32_bf16 v[176:179], a[144:147], v[80:83], v[176:179]
ds_read_b128 a[180:183], %[vXr3] offset:27648
v_mfma_f32_16x16x32_bf16 v[212:215], a[144:147], v[84:87], v[212:215]
ds_read_b128 a[184:187], %[vXr3] offset:28672
v_mfma_f32_16x16x32_bf16 v[180:183], a[148:151], v[80:83], v[180:183]
buffer_load_dwordx4 v[40:43], v249, %[sWS], s61 offen offset:0 sc1
v_mfma_f32_16x16x32_bf16 v[216:219], a[148:151], v[84:87], v[216:219]
ds_read_b128 a[188:191], %[vXr3] offset:29696
v_mfma_f32_16x16x32_bf16 v[184:187], a[152:155], v[80:83], v[184:187]
ds_read_b128 a[192:195], %[vXr3] offset:30720
v_mfma_f32_16x16x32_bf16 v[220:223], a[152:155], v[84:87], v[220:223]
v_mfma_f32_16x16x32_bf16 v[188:191], a[156:159], v[80:83], v[188:191]
ds_read_b128 a[196:199], %[vXr3] offset:31744
v_mfma_f32_16x16x32_bf16 v[224:227], a[156:159], v[84:87], v[224:227]
ds_read_b128 a[200:203], %[vXr3] offset:32768
v_mfma_f32_16x16x32_bf16 v[192:195], a[160:163], v[80:83], v[192:195]
buffer_load_dwordx4 v[44:47], v249, %[sWS], s61 offen offset:1024 sc1
v_mfma_f32_16x16x32_bf16 v[228:231], a[160:163], v[84:87], v[228:231]
ds_read_b128 a[204:207], %[vXr3] offset:33792
v_mfma_f32_16x16x32_bf16 v[196:199], a[164:167], v[80:83], v[196:199]
ds_read_b128 a[208:211], %[vXr3] offset:34816
v_mfma_f32_16x16x32_bf16 v[232:235], a[164:167], v[84:87], v[232:235]
s_add_u32 m0, s27, 36864
v_mfma_f32_16x16x32_bf16 v[200:203], a[168:171], v[80:83], v[200:203]
s_add_u32 s48, s25, 0
ds_read_b128 a[212:215], %[vXr3] offset:35840
v_mfma_f32_16x16x32_bf16 v[236:239], a[168:171], v[84:87], v[236:239]
buffer_load_dwordx4 %[vL16], %[sWS], s48 offen offset:0 sc1 lds
v_mfma_f32_16x16x32_bf16 v[204:207], a[172:175], v[80:83], v[204:207]
s_waitcnt vmcnt(10)
v_mfma_f32_16x16x32_bf16 v[240:243], a[172:175], v[84:87], v[240:243]
buffer_load_dwordx4 %[vL16], %[sWS], s48 offen offset:1024 sc1 lds
v_min_u32_e64 v248, v88, v92
v_mfma_f32_16x16x32_bf16 v[208:211], a[176:179], v[80:83], v[208:211]
v_cmp_eq_u32 vcc, 0, v248
s_cbranch_vccnz L_sl24_%=
L_ok24_%=:
v_mfma_f32_16x16x32_bf16 v[244:247], a[176:179], v[84:87], v[244:247]
s_waitcnt vmcnt(8) lgkmcnt(0)
s_barrier
v_mfma_f32_16x16x32_bf16 v[176:179], a[180:183], v[88:91], v[176:179]
ds_read_b128 a[144:147], %[vXr0] offset:0
v_mfma_f32_16x16x32_bf16 v[212:215], a[180:183], v[92:95], v[212:215]
ds_read_b128 a[148:151], %[vXr0] offset:1024
v_mfma_f32_16x16x32_bf16 v[180:183], a[184:187], v[88:91], v[180:183]
buffer_load_dwordx4 v[48:51], v249, %[sWS], s61 offen offset:2048 sc1
v_mfma_f32_16x16x32_bf16 v[216:219], a[184:187], v[92:95], v[216:219]
ds_read_b128 a[152:155], %[vXr0] offset:2048
v_mfma_f32_16x16x32_bf16 v[184:187], a[188:191], v[88:91], v[184:187]
ds_read_b128 a[156:159], %[vXr0] offset:3072
v_mfma_f32_16x16x32_bf16 v[220:223], a[188:191], v[92:95], v[220:223]
v_mfma_f32_16x16x32_bf16 v[188:191], a[192:195], v[88:91], v[188:191]
ds_read_b128 a[160:163], %[vXr0] offset:4096
v_mfma_f32_16x16x32_bf16 v[224:227], a[192:195], v[92:95], v[224:227]
ds_read_b128 a[164:167], %[vXr0] offset:5120
v_mfma_f32_16x16x32_bf16 v[192:195], a[196:199], v[88:91], v[192:195]
buffer_load_dwordx4 v[52:55], v249, %[sWS], s61 offen offset:3072 sc1
v_mfma_f32_16x16x32_bf16 v[228:231], a[196:199], v[92:95], v[228:231]
ds_read_b128 a[168:171], %[vXr0] offset:6144
v_mfma_f32_16x16x32_bf16 v[196:199], a[200:203], v[88:91], v[196:199]
ds_read_b128 a[172:175], %[vXr0] offset:7168
v_mfma_f32_16x16x32_bf16 v[232:235], a[200:203], v[92:95], v[232:235]
v_mfma_f32_16x16x32_bf16 v[200:203], a[204:207], v[88:91], v[200:203]
ds_read_b128 a[176:179], %[vXr0] offset:8192
v_mfma_f32_16x16x32_bf16 v[236:239], a[204:207], v[92:95], v[236:239]
buffer_load_dwordx4 %[vL16], %[sWS], s48 offen offset:2048 sc1 lds
v_mfma_f32_16x16x32_bf16 v[204:207], a[208:211], v[88:91], v[204:207]
s_waitcnt vmcnt(9)
v_mfma_f32_16x16x32_bf16 v[240:243], a[208:211], v[92:95], v[240:243]
buffer_load_dwordx4 %[vL16], %[sWS], s48 offen offset:3072 sc1 lds
v_min_u32 v248, v96, v100
v_mfma_f32_16x16x32_bf16 v[208:211], a[212:215], v[88:91], v[208:211]
v_cmp_eq_u32_e64 vcc, 0, v248
s_cbranch_vccnz L_sl25_%=
L_ok25_%=:
v_mfma_f32_16x16x32_bf16 v[244:247], a[212:215], v[92:95], v[244:247]
s_waitcnt lgkmcnt(0)
v_mfma_f32_16x16x32_bf16 v[176:179], a[144:147], v[96:99], v[176:179]
s_add_u32 m0, s27, 40960
ds_read_b128 a[180:183], %[vXr0] offset:9216
v_mfma_f32_16x16x32_bf16 v[212:215], a[144:147], v[100:103], v[212:215]
s_add_u32 s48, s25, 4096
ds_read_b128 a[184:187], %[vXr0] offset:10240
v_mfma_f32_16x16x32_bf16 v[180:183], a[148:151], v[96:99], v[180:183]
buffer_load_dwordx4 %[vL16], %[sWS], s48 offen offset:0 sc1 lds
v_mfma_f32_16x16x32_bf16 v[216:219], a[148:151], v[100:103], v[216:219]
ds_read_b128 a[188:191], %[vXr0] offset:11264
v_mfma_f32_16x16x32_bf16 v[184:187], a[152:155], v[96:99], v[184:187]
ds_read_b128 a[192:195], %[vXr0] offset:12288
v_mfma_f32_16x16x32_bf16 v[220:223], a[152:155], v[100:103], v[220:223]
v_mfma_f32_16x16x32_bf16 v[188:191], a[156:159], v[96:99], v[188:191]
ds_read_b128 a[196:199], %[vXr0] offset:13312
v_mfma_f32_16x16x32_bf16 v[224:227], a[156:159], v[100:103], v[224:227]
ds_read_b128 a[200:203], %[vXr0] offset:14336
v_mfma_f32_16x16x32_bf16 v[192:195], a[160:163], v[96:99], v[192:195]
v_mfma_f32_16x16x32_bf16 v[228:231], a[160:163], v[100:103], v[228:231]
ds_read_b128 a[204:207], %[vXr0] offset:15360
v_mfma_f32_16x16x32_bf16 v[196:199], a[164:167], v[96:99], v[196:199]
ds_read_b128 a[208:211], %[vXr0] offset:16384
v_mfma_f32_16x16x32_bf16 v[232:235], a[164:167], v[100:103], v[232:235]
buffer_load_dwordx4 v[56:59], %[vL16], %[sWS], s62 offen offset:0 sc1
v_mfma_f32_16x16x32_bf16 v[200:203], a[168:171], v[96:99], v[200:203]
ds_read_b128 a[212:215], %[vXr0] offset:17408
v_mfma_f32_16x16x32_bf16 v[236:239], a[168:171], v[100:103], v[236:239]
v_mfma_f32_16x16x32_bf16 v[204:207], a[172:175], v[96:99], v[204:207]
s_waitcnt vmcnt(10)
v_mfma_f32_16x16x32_bf16 v[240:243], a[172:175], v[100:103], v[240:243]
buffer_load_dwordx4 v[60:63], %[vL16], %[sWS], s62 offen offset:1024 sc1
v_min_u32 v248, v32, v36
v_mfma_f32_16x16x32_bf16 v[208:211], a[176:179], v[96:99], v[208:211]
v_cmp_eq_u32_e64 vcc, 0, v248
s_cbranch_vccnz L_sl26_%=
L_ok26_%=:
v_mfma_f32_16x16x32_bf16 v[244:247], a[176:179], v[100:103], v[244:247]
s_waitcnt lgkmcnt(0)
v_mfma_f32_16x16x32_bf16 v[176:179], a[180:183], v[32:35], v[176:179]
ds_read_b128 a[144:147], %[vXr0] offset:18432
v_mfma_f32_16x16x32_bf16 v[212:215], a[180:183], v[36:39], v[212:215]
ds_read_b128 a[148:151], %[vXr0] offset:19456
v_mfma_f32_16x16x32_bf16 v[180:183], a[184:187], v[32:35], v[180:183]
buffer_load_dwordx4 v[64:67], %[vL16], %[sWS], s62 offen offset:2048 sc1
v_mfma_f32_16x16x32_bf16 v[216:219], a[184:187], v[36:39], v[216:219]
ds_read_b128 a[152:155], %[vXr0] offset:20480
v_mfma_f32_16x16x32_bf16 v[184:187], a[188:191], v[32:35], v[184:187]
ds_read_b128 a[156:159], %[vXr0] offset:21504
v_mfma_f32_16x16x32_bf16 v[220:223], a[188:191], v[36:39], v[220:223]
v_mfma_f32_16x16x32_bf16 v[188:191], a[192:195], v[32:35], v[188:191]
ds_read_b128 a[160:163], %[vXr0] offset:22528
v_mfma_f32_16x16x32_bf16 v[224:227], a[192:195], v[36:39], v[224:227]
ds_read_b128 a[164:167], %[vXr0] offset:23552
v_mfma_f32_16x16x32_bf16 v[192:195], a[196:199], v[32:35], v[192:195]
v_mfma_f32_16x16x32_bf16 v[228:231], a[196:199], v[36:39], v[228:231]
ds_read_b128 a[168:171], %[vXr0] offset:24576
v_mfma_f32_16x16x32_bf16 v[196:199], a[200:203], v[32:35], v[196:199]
ds_read_b128 a[172:175], %[vXr0] offset:25600
v_mfma_f32_16x16x32_bf16 v[232:235], a[200:203], v[36:39], v[232:235]
v_mfma_f32_16x16x32_bf16 v[200:203], a[204:207], v[32:35], v[200:203]
ds_read_b128 a[176:179], %[vXr0] offset:26624
v_mfma_f32_16x16x32_bf16 v[236:239], a[204:207], v[36:39], v[236:239]
buffer_load_dwordx4 v[68:71], %[vL16], %[sWS], s62 offen offset:3072 sc1
v_mfma_f32_16x16x32_bf16 v[204:207], a[208:211], v[32:35], v[204:207]
s_waitcnt vmcnt(11)
v_mfma_f32_16x16x32_bf16 v[240:243], a[208:211], v[36:39], v[240:243]
v_min_u32 v248, v40, v44
v_mfma_f32_16x16x32_bf16 v[208:211], a[212:215], v[32:35], v[208:211]
v_cmp_eq_u32_e64 vcc, 0, v248
s_cbranch_vccnz L_sl27_%=
L_ok27_%=:
v_mfma_f32_16x16x32_bf16 v[244:247], a[212:215], v[36:39], v[244:247]
s_waitcnt lgkmcnt(0)
v_mfma_f32_16x16x32_bf16 v[176:179], a[144:147], v[40:43], v[176:179]
ds_read_b128 a[180:183], %[vXr0] offset:27648
)"
R"(v_mfma_f32_16x16x32_bf16 v[212:215], a[144:147], v[44:47], v[212:215]
ds_read_b128 a[184:187], %[vXr0] offset:28672
v_mfma_f32_16x16x32_bf16 v[180:183], a[148:151], v[40:43], v[180:183]
v_mfma_f32_16x16x32_bf16 v[216:219], a[148:151], v[44:47], v[216:219]
ds_read_b128 a[188:191], %[vXr0] offset:29696
v_mfma_f32_16x16x32_bf16 v[184:187], a[152:155], v[40:43], v[184:187]
ds_read_b128 a[192:195], %[vXr0] offset:30720
v_mfma_f32_16x16x32_bf16 v[220:223], a[152:155], v[44:47], v[220:223]
v_mfma_f32_16x16x32_bf16 v[188:191], a[156:159], v[40:43], v[188:191]
ds_read_b128 a[196:199], %[vXr0] offset:31744
v_mfma_f32_16x16x32_bf16 v[224:227], a[156:159], v[44:47], v[224:227]
ds_read_b128 a[200:203], %[vXr0] offset:32768
v_mfma_f32_16x16x32_bf16 v[192:195], a[160:163], v[40:43], v[192:195]
v_mfma_f32_16x16x32_bf16 v[228:231], a[160:163], v[44:47], v[228:231]
ds_read_b128 a[204:207], %[vXr0] offset:33792
v_mfma_f32_16x16x32_bf16 v[196:199], a[164:167], v[40:43], v[196:199]
ds_read_b128 a[208:211], %[vXr0] offset:34816
v_mfma_f32_16x16x32_bf16 v[232:235], a[164:167], v[44:47], v[232:235]
v_mfma_f32_16x16x32_bf16 v[200:203], a[168:171], v[40:43], v[200:203]
ds_read_b128 a[212:215], %[vXr0] offset:35840
v_mfma_f32_16x16x32_bf16 v[236:239], a[168:171], v[44:47], v[236:239]
v_mfma_f32_16x16x32_bf16 v[204:207], a[172:175], v[40:43], v[204:207]
s_waitcnt vmcnt(7)
v_mfma_f32_16x16x32_bf16 v[240:243], a[172:175], v[44:47], v[240:243]
v_min_u32 v248, v48, v52
v_mfma_f32_16x16x32_bf16 v[208:211], a[176:179], v[40:43], v[208:211]
v_cmp_eq_u32 vcc, 0, v248
s_cbranch_vccnz L_sl28_%=
L_ok28_%=:
v_mfma_f32_16x16x32_bf16 v[244:247], a[176:179], v[44:47], v[244:247]
s_waitcnt vmcnt(4) lgkmcnt(0)
s_barrier
v_mfma_f32_16x16x32_bf16 v[176:179], a[180:183], v[48:51], v[176:179]
ds_read_b128 a[144:147], %[vXr1] offset:0
v_mfma_f32_16x16x32_bf16 v[212:215], a[180:183], v[52:55], v[212:215]
ds_read_b128 a[148:151], %[vXr1] offset:1024
v_mfma_f32_16x16x32_bf16 v[180:183], a[184:187], v[48:51], v[180:183]
v_mfma_f32_16x16x32_bf16 v[216:219], a[184:187], v[52:55], v[216:219]
ds_read_b128 a[152:155], %[vXr1] offset:2048
v_mfma_f32_16x16x32_bf16 v[184:187], a[188:191], v[48:51], v[184:187]
ds_read_b128 a[156:159], %[vXr1] offset:3072
v_mfma_f32_16x16x32_bf16 v[220:223], a[188:191], v[52:55], v[220:223]
v_mfma_f32_16x16x32_bf16 v[188:191], a[192:195], v[48:51], v[188:191]
ds_read_b128 a[160:163], %[vXr1] offset:4096
v_mfma_f32_16x16x32_bf16 v[224:227], a[192:195], v[52:55], v[224:227]
ds_read_b128 a[164:167], %[vXr1] offset:5120
v_mfma_f32_16x16x32_bf16 v[192:195], a[196:199], v[48:51], v[192:195]
v_mfma_f32_16x16x32_bf16 v[228:231], a[196:199], v[52:55], v[228:231]
ds_read_b128 a[168:171], %[vXr1] offset:6144
v_mfma_f32_16x16x32_bf16 v[196:199], a[200:203], v[48:51], v[196:199]
ds_read_b128 a[172:175], %[vXr1] offset:7168
v_mfma_f32_16x16x32_bf16 v[232:235], a[200:203], v[52:55], v[232:235]
v_mfma_f32_16x16x32_bf16 v[200:203], a[204:207], v[48:51], v[200:203]
ds_read_b128 a[176:179], %[vXr1] offset:8192
v_mfma_f32_16x16x32_bf16 v[236:239], a[204:207], v[52:55], v[236:239]
v_mfma_f32_16x16x32_bf16 v[204:207], a[208:211], v[48:51], v[204:207]
s_waitcnt vmcnt(2)
v_mfma_f32_16x16x32_bf16 v[240:243], a[208:211], v[52:55], v[240:243]
v_min3_u32 v248, v56, v58, v60
v_min_u32 v248, v248, v62
v_mfma_f32_16x16x32_bf16 v[208:211], a[212:215], v[48:51], v[208:211]
v_cmp_eq_u32_e64 vcc, 0, v248
s_cbranch_vccnz L_sl29_%=
L_ok29_%=:
v_mfma_f32_16x16x32_bf16 v[244:247], a[212:215], v[52:55], v[244:247]
s_waitcnt lgkmcnt(0)
v_mfma_f32_16x16x32_bf16 v[176:179], a[144:147], v[56:59], v[176:179]
ds_read_b128 a[180:183], %[vXr1] offset:9216
v_mfma_f32_16x16x32_bf16 v[212:215], a[144:147], v[60:63], v[212:215]
ds_read_b128 a[184:187], %[vXr1] offset:10240
v_mfma_f32_16x16x32_bf16 v[180:183], a[148:151], v[56:59], v[180:183]
v_mfma_f32_16x16x32_bf16 v[216:219], a[148:151], v[60:63], v[216:219]
ds_read_b128 a[188:191], %[vXr1] offset:11264
v_mfma_f32_16x16x32_bf16 v[184:187], a[152:155], v[56:59], v[184:187]
ds_read_b128 a[192:195], %[vXr1] offset:12288
v_mfma_f32_16x16x32_bf16 v[220:223], a[152:155], v[60:63], v[220:223]
v_mfma_f32_16x16x32_bf16 v[188:191], a[156:159], v[56:59], v[188:191]
ds_read_b128 a[196:199], %[vXr1] offset:13312
v_mfma_f32_16x16x32_bf16 v[224:227], a[156:159], v[60:63], v[224:227]
ds_read_b128 a[200:203], %[vXr1] offset:14336
v_mfma_f32_16x16x32_bf16 v[192:195], a[160:163], v[56:59], v[192:195]
v_mfma_f32_16x16x32_bf16 v[228:231], a[160:163], v[60:63], v[228:231]
ds_read_b128 a[204:207], %[vXr1] offset:15360
v_mfma_f32_16x16x32_bf16 v[196:199], a[164:167], v[56:59], v[196:199]
ds_read_b128 a[208:211], %[vXr1] offset:16384
v_mfma_f32_16x16x32_bf16 v[232:235], a[164:167], v[60:63], v[232:235]
v_mfma_f32_16x16x32_bf16 v[200:203], a[168:171], v[56:59], v[200:203]
ds_read_b128 a[212:215], %[vXr1] offset:17408
v_mfma_f32_16x16x32_bf16 v[236:239], a[168:171], v[60:63], v[236:239]
v_mfma_f32_16x16x32_bf16 v[204:207], a[172:175], v[56:59], v[204:207]
s_waitcnt vmcnt(0)
v_mfma_f32_16x16x32_bf16 v[240:243], a[172:175], v[60:63], v[240:243]
v_min3_u32 v248, v64, v66, v68
v_min_u32 v248, v248, v70
v_mfma_f32_16x16x32_bf16 v[208:211], a[176:179], v[56:59], v[208:211]
v_cmp_eq_u32 vcc, 0, v248
s_cbranch_vccnz L_sl30_%=
L_ok30_%=:
v_mfma_f32_16x16x32_bf16 v[244:247], a[176:179], v[60:63], v[244:247]
s_waitcnt lgkmcnt(0)
s_cmp_eq_u32 %[sOwn], 0
s_cbranch_scc1 L_own5_%=
v_mfma_f32_16x16x32_bf16 v[176:179], a[180:183], v[64:67], v[176:179]
s_add_u32 s49, s34, -8388608
v_mfma_f32_16x16x32_bf16 v[212:215], a[180:183], v[68:71], v[212:215]
s_add_u32 s53, s49, 4096
v_mfma_f32_16x16x32_bf16 v[180:183], a[184:187], v[64:67], v[180:183]
LPb_11:
s_cbranch_execz LRb
s_add_u32 s55, s35, -1048576
v_mfma_f32_16x16x32_bf16 v[216:219], a[184:187], v[68:71], v[216:219]
s_nop 0
v_mfma_f32_16x16x32_bf16 v[184:187], a[188:191], v[64:67], v[184:187]
s_nop 0
v_mfma_f32_16x16x32_bf16 v[220:223], a[188:191], v[68:71], v[220:223]
v_cvt_pk_bf16_f32 v176, v176, v177
v_cvt_pk_bf16_f32 v177, v178, v179
v_max_u32 v176, 1, v176
v_max_u32 v177, 1, v177
v_mfma_f32_16x16x32_bf16 v[188:191], a[192:195], v[64:67], v[188:191]
v_cvt_pk_bf16_f32 v212, v212, v213
v_cvt_pk_bf16_f32 v213, v214, v215
v_max_u32 v212, 1, v212
v_max_u32 v213, 1, v213
v_mfma_f32_16x16x32_bf16 v[224:227], a[192:195], v[68:71], v[224:227]
v_cvt_pk_bf16_f32 v178, v180, v181
v_cvt_pk_bf16_f32 v179, v182, v183
v_max_u32 v178, 1, v178
v_max_u32 v179, 1, v179
v_mfma_f32_16x16x32_bf16 v[192:195], a[196:199], v[64:67], v[192:195]
buffer_store_dwordx4 v[176:179], %[vL16], %[sWS], s49 offen offset:0 sc1
v_cvt_pk_bf16_f32 v214, v216, v217
v_cvt_pk_bf16_f32 v215, v218, v219
v_max_u32 v214, 1, v214
v_max_u32 v215, 1, v215
v_mfma_f32_16x16x32_bf16 v[228:231], a[196:199], v[68:71], v[228:231]
buffer_store_dwordx4 v[212:215], %[vL16], %[sWS], s53 offen offset:0 sc1
v_cvt_pk_bf16_f32 v180, v184, v185
v_cvt_pk_bf16_f32 v181, v186, v187
v_max_u32 v180, 1, v180
v_max_u32 v181, 1, v181
v_mfma_f32_16x16x32_bf16 v[196:199], a[200:203], v[64:67], v[196:199]
v_cvt_pk_bf16_f32 v216, v220, v221
v_cvt_pk_bf16_f32 v217, v222, v223
v_max_u32 v216, 1, v216
v_max_u32 v217, 1, v217
v_mfma_f32_16x16x32_bf16 v[232:235], a[200:203], v[68:71], v[232:235]
v_cvt_pk_bf16_f32 v182, v188, v189
v_cvt_pk_bf16_f32 v183, v190, v191
v_max_u32 v182, 1, v182
v_max_u32 v183, 1, v183
v_mfma_f32_16x16x32_bf16 v[200:203], a[204:207], v[64:67], v[200:203]
buffer_store_dwordx4 v[180:183], %[vL16], %[sWS], s49 offen offset:1024 sc1
v_cvt_pk_bf16_f32 v218, v224, v225
v_cvt_pk_bf16_f32 v219, v226, v227
v_max_u32 v218, 1, v218
v_max_u32 v219, 1, v219
v_mfma_f32_16x16x32_bf16 v[236:239], a[204:207], v[68:71], v[236:239]
buffer_store_dwordx4 v[216:219], %[vL16], %[sWS], s53 offen offset:1024 sc1
v_cvt_pk_bf16_f32 v184, v192, v193
v_cvt_pk_bf16_f32 v185, v194, v195
v_max_u32 v184, 1, v184
v_max_u32 v185, 1, v185
v_mfma_f32_16x16x32_bf16 v[204:207], a[208:211], v[64:67], v[204:207]
v_cvt_pk_bf16_f32 v220, v228, v229
v_cvt_pk_bf16_f32 v221, v230, v231
v_max_u32 v220, 1, v220
v_max_u32 v221, 1, v221
v_mfma_f32_16x16x32_bf16 v[240:243], a[208:211], v[68:71], v[240:243]
v_cvt_pk_bf16_f32 v186, v196, v197
v_cvt_pk_bf16_f32 v187, v198, v199
v_max_u32 v186, 1, v186
v_max_u32 v187, 1, v187
v_mfma_f32_16x16x32_bf16 v[208:211], a[212:215], v[64:67], v[208:211]
buffer_store_dwordx4 v[184:187], %[vL16], %[sWS], s49 offen offset:2048 sc1
v_cvt_pk_bf16_f32 v222, v232, v233
v_cvt_pk_bf16_f32 v223, v234, v235
v_max_u32 v222, 1, v222
v_max_u32 v223, 1, v223
v_mfma_f32_16x16x32_bf16 v[244:247], a[212:215], v[68:71], v[244:247]
buffer_store_dwordx4 v[220:223], %[vL16], %[sWS], s53 offen offset:2048 sc1
v_cvt_pk_bf16_f32 v188, v200, v201
v_cvt_pk_bf16_f32 v189, v202, v203
v_max_u32 v188, 1, v188
v_max_u32 v189, 1, v189
v_cvt_pk_bf16_f32 v224, v236, v237
v_cvt_pk_bf16_f32 v225, v238, v239
v_max_u32 v224, 1, v224
v_max_u32 v225, 1, v225
v_cvt_pk_bf16_f32 v190, v204, v205
v_cvt_pk_bf16_f32 v191, v206, v207
v_max_u32 v190, 1, v190
v_max_u32 v191, 1, v191
buffer_store_dwordx4 v[188:191], %[vL16], %[sWS], s49 offen offset:3072 sc1
v_cvt_pk_bf16_f32 v226, v240, v241
v_cvt_pk_bf16_f32 v227, v242, v243
v_max_u32 v226, 1, v226
v_max_u32 v227, 1, v227
buffer_store_dwordx4 v[224:227], %[vL16], %[sWS], s53 offen offset:3072 sc1
v_cvt_pk_bf16_f32 v192, v208, v209
v_cvt_pk_bf16_f32 v193, v210, v211
)"
R"(v_max_u32 v192, 1, v192
v_max_u32 v193, 1, v193
buffer_store_dwordx2 v[192:193], v250, %[sWS], s55 offen offset:0 sc1
v_cvt_pk_bf16_f32 v228, v244, v245
v_cvt_pk_bf16_f32 v229, v246, v247
v_max_u32 v228, 1, v228
v_max_u32 v229, 1, v229
buffer_store_dwordx2 v[228:229], v250, %[sWS], s55 offen offset:512 sc1
L_join5_%=:
s_barrier
s_branch L_end2_%=
L_sl2_%=:
s_cmp_eq_u32 s69, 0
s_cbranch_scc1 L_ok2_%=
s_sub_u32 s69, s69, 1
s_add_u32 s53, s32, 0
buffer_load_dwordx4 v[80:83], %[yRc], %[sWS], s53 offen offset:0 sc1
buffer_load_dwordx4 v[84:87], %[yRc], %[sWS], s53 offen offset:1024 sc1
buffer_load_dwordx2 v[88:89], %[yL8], %[sWS], s33 offen offset:0 sc1
s_add_u32 s53, s32, 2097152
s_xor_b32 s53, s53, 8192
buffer_load_dwordx4 v[90:93], %[yRc], %[sWS], s53 offen offset:0 sc1
buffer_load_dwordx4 v[94:97], %[yRc], %[sWS], s53 offen offset:1024 sc1
buffer_load_dwordx2 v[98:99], %[yL8], %[sWS], s33 offen offset:1024 sc1
s_add_u32 s53, s32, 4194304
buffer_load_dwordx4 v[100:103], %[yRc], %[sWS], s53 offen offset:0 sc1
buffer_load_dwordx4 v[104:107], %[yRc], %[sWS], s53 offen offset:1024 sc1
buffer_load_dwordx2 v[108:109], %[yL8], %[sWS], s33 offen offset:2048 sc1
s_waitcnt vmcnt(0)
v_min3_u32 v234, v80, v81, v82
v_min3_u32 v234, v234, v83, v84
v_min3_u32 v234, v234, v85, v86
v_min3_u32 v234, v234, v87, v88
v_min_u32 v234, v234, v89
v_min3_u32 v235, v90, v91, v92
v_min3_u32 v235, v235, v93, v94
v_min3_u32 v235, v235, v95, v96
v_min3_u32 v235, v235, v97, v98
v_min_u32_e64 v235, v235, v99
v_min3_u32 v236, v100, v101, v102
v_min3_u32 v236, v236, v103, v104
v_min3_u32 v236, v236, v105, v106
v_min3_u32 v236, v236, v107, v108
v_min_u32_e64 v236, v236, v109
v_min3_u32 v234, v234, v235, v236
v_cmp_eq_u32 vcc, 0, v234
s_cbranch_vccnz L_sl2_%=
s_branch L_ok2_%=
L_sl3_%=:
s_cmp_eq_u32 s69, 0
s_cbranch_scc1 L_ok3_%=
s_sub_u32 s69, s69, 1
buffer_load_dwordx4 v[232:235], %[vS], %[sWS], 0 offen offset:0 sc1
buffer_load_dwordx4 v[236:239], %[vS], %[sWS], 0 offen offset:16 sc1
buffer_load_dwordx4 v[240:243], %[vS], %[sWS], 0 offen offset:32 sc1
buffer_load_dwordx4 v[244:247], %[vS], %[sWS], 0 offen offset:48 sc1
s_waitcnt vmcnt(0)
v_and_b32 v248, v233, v235
v_and_b32 v249, v237, v239
v_and_b32 v250, v241, v243
v_and_b32 v251, v245, v247
v_and_b32 v248, v248, v249
v_and_b32 v250, v250, v251
v_and_b32 v248, v248, v250
v_cmp_lt_i32 vcc, -1, v248
s_cbranch_vccnz L_sl3_%=
s_branch L_ok3_%=
L_sl4_%=:
s_cmp_eq_u32 s69, 0
s_cbranch_scc1 L_ok4_%=
s_sub_u32 s69, s69, 1
buffer_load_dwordx2 v[192:193], %[vA2], %[sWS], 0 offen offset:0 sc1
buffer_load_dwordx2 v[194:195], %[vA2], %[sWS], 0 offen offset:128 sc1
buffer_load_dwordx2 v[196:197], %[vA2], %[sWS], 0 offen offset:256 sc1
buffer_load_dwordx2 v[198:199], %[vA2], %[sWS], 0 offen offset:384 sc1
buffer_load_dwordx2 v[200:201], %[vA2], %[sWS], 0 offen offset:512 sc1
buffer_load_dwordx2 v[202:203], %[vA2], %[sWS], 0 offen offset:640 sc1
buffer_load_dwordx2 v[204:205], %[vA2], %[sWS], 0 offen offset:768 sc1
buffer_load_dwordx2 v[206:207], %[vA2], %[sWS], 0 offen offset:896 sc1
buffer_load_dwordx2 v[208:209], %[vA2], %[sWS], 0 offen offset:1024 sc1
s_waitcnt vmcnt(0)
v_min3_u32 v232, v192, v194, v196
v_min3_u32 v232, v232, v198, v200
v_min3_u32 v232, v232, v202, v204
v_min3_u32 v232, v232, v206, v208
v_cmp_eq_u32 vcc, 0, v232
s_cbranch_vccnz L_sl4_%=
s_branch L_ok4_%=
L_sl5_%=:
s_cmp_eq_u32 s69, 0
s_cbranch_scc1 L_ok5_%=
s_sub_u32 s69, s69, 1
s_sleep 1
buffer_load_dwordx2 v[232:233], %[vEr], %[sWS], 0 offen offset:0 sc1
buffer_load_dwordx2 v[234:235], %[vEr], %[sWS], 0 offen offset:1024 sc1
buffer_load_dwordx2 v[236:237], %[vEr], %[sWS], 0 offen offset:2048 sc1
buffer_load_dwordx2 v[238:239], %[vEr], %[sWS], 0 offen offset:3072 sc1
buffer_load_dwordx2 v[240:241], %[vEr], %[sWS], s53 offen offset:0 sc1
buffer_load_dwordx2 v[242:243], %[vEr], %[sWS], s53 offen offset:1024 sc1
buffer_load_dwordx2 v[244:245], %[vEr], %[sWS], s53 offen offset:2048 sc1
buffer_load_dwordx2 v[246:247], %[vEr], %[sWS], s53 offen offset:3072 sc1
s_waitcnt vmcnt(0)
v_and_b32 v248, v233, v235
v_and_b32 v249, v237, v239
v_and_b32 v250, v241, v243
v_and_b32 v251, v245, v247
v_and_b32 v248, v248, v249
v_and_b32 v250, v250, v251
v_and_b32 v248, v248, v250
v_cmp_lt_i32 vcc, -1, v248
s_cbranch_vccnz L_sl5_%=
s_branch L_ok5_%=
L_sl6_%=:
s_cmp_eq_u32 s69, 0
s_cbranch_scc1 L_ok6_%=
s_sub_u32 s69, s69, 1
buffer_load_dwordx4 v[192:195], %[vA1], %[sWS], 0 offen offset:0 sc1
buffer_load_dwordx4 v[196:199], %[vA1], %[sWS], 0 offen offset:16 sc1
buffer_load_dwordx4 v[200:203], %[vA1], %[sWS], 0 offen offset:128 sc1
buffer_load_dwordx4 v[204:207], %[vA1], %[sWS], 0 offen offset:144 sc1
buffer_load_dwordx4 v[208:211], %[vA1], %[sWS], 0 offen offset:256 sc1
buffer_load_dwordx4 v[212:215], %[vA1], %[sWS], 0 offen offset:272 sc1
buffer_load_dwordx4 v[216:219], %[vA1], %[sWS], 0 offen offset:384 sc1
buffer_load_dwordx4 v[220:223], %[vA1], %[sWS], 0 offen offset:400 sc1
s_waitcnt vmcnt(0)
v_min3_u32 v230, v192, v196, v200
v_min3_u32 v230, v230, v204, v208
v_min3_u32 v230, v230, v212, v216
v_min_u32 v230, v230, v220
v_cmp_eq_u32_e64 vcc, 0, v230
s_cbranch_vccnz L_sl6_%=
s_branch L_ok6_%=
L_sl7_%=:
s_cmp_eq_u32 s69, 0
s_cbranch_scc1 L_ok7_%=
s_sub_u32 s69, s69, 1
buffer_load_dwordx4 a[216:219], %[vA1], %[sWS], 0 offen offset:512 sc1
buffer_load_dwordx4 a[220:223], %[vA1], %[sWS], 0 offen offset:528 sc1
buffer_load_dwordx4 a[224:227], %[vA1], %[sWS], 0 offen offset:640 sc1
buffer_load_dwordx4 a[228:231], %[vA1], %[sWS], 0 offen offset:656 sc1
buffer_load_dwordx4 a[232:235], %[vA1], %[sWS], 0 offen offset:768 sc1
buffer_load_dwordx4 a[236:239], %[vA1], %[sWS], 0 offen offset:784 sc1
buffer_load_dwordx4 a[240:243], %[vA1], %[sWS], 0 offen offset:896 sc1
buffer_load_dwordx4 a[244:247], %[vA1], %[sWS], 0 offen offset:912 sc1
buffer_load_dwordx4 a[248:251], %[vA1], %[sWS], 0 offen offset:1024 sc1
buffer_load_dwordx4 a[252:255], %[vA1], %[sWS], 0 offen offset:1040 sc1
s_waitcnt vmcnt(0)
v_accvgpr_read_b32 v192, a216
v_accvgpr_read_b32 v193, a220
v_accvgpr_read_b32 v194, a224
v_accvgpr_read_b32 v195, a228
v_accvgpr_read_b32 v196, a232
v_accvgpr_read_b32 v197, a236
v_accvgpr_read_b32 v198, a240
v_accvgpr_read_b32 v199, a244
v_accvgpr_read_b32 v200, a248
v_accvgpr_read_b32 v201, a252
v_min3_u32 v230, v192, v193, v194
v_min3_u32 v230, v230, v195, v196
v_min3_u32 v230, v230, v197, v198
v_min3_u32 v230, v230, v199, v200
v_min_u32 v230, v230, v201
v_cmp_eq_u32 vcc, 0, v230
s_cbranch_vccnz L_sl7_%=
s_branch L_ok7_%=
L_sl8_%=:
s_cmp_eq_u32 s69, 0
s_cbranch_scc1 L_ok8_%=
s_sub_u32 s69, s69, 1
buffer_load_dword v104, %[yCan], %[sWS], s31 offen offset:0 sc1
s_waitcnt vmcnt(0)
v_cmp_eq_u32 vcc, 0, v104
s_cbranch_vccnz L_sl8_%=
s_branch L_ok8_%=
L_sl9_%=:
s_cmp_eq_u32 s69, 0
s_cbranch_scc1 L_ok9_%=
s_sub_u32 s69, s69, 1
s_add_u32 s57, s31, 0
buffer_load_dwordx4 a[0:3], %[vL16], %[sWS], s57 offen offset:0 sc1
s_add_u32 s57, s31, 0
buffer_load_dwordx4 a[4:7], %[vL16], %[sWS], s57 offen offset:1024 sc1
s_add_u32 s57, s31, 2048
buffer_load_dwordx4 a[8:11], %[vL16], %[sWS], s57 offen offset:0 sc1
s_add_u32 s57, s31, 2048
buffer_load_dwordx4 a[12:15], %[vL16], %[sWS], s57 offen offset:1024 sc1
s_add_u32 s57, s31, 4096
buffer_load_dwordx4 a[16:19], %[vL16], %[sWS], s57 offen offset:0 sc1
s_add_u32 s57, s31, 4096
buffer_load_dwordx4 a[20:23], %[vL16], %[sWS], s57 offen offset:1024 sc1
s_add_u32 s57, s31, 6144
buffer_load_dwordx4 a[24:27], %[vL16], %[sWS], s57 offen offset:0 sc1
s_add_u32 s57, s31, 6144
buffer_load_dwordx4 a[28:31], %[vL16], %[sWS], s57 offen offset:1024 sc1
s_add_u32 s57, s31, 524288
buffer_load_dwordx4 a[32:35], %[vL16], %[sWS], s57 offen offset:0 sc1
s_add_u32 s57, s31, 524288
buffer_load_dwordx4 a[36:39], %[vL16], %[sWS], s57 offen offset:1024 sc1
s_add_u32 s57, s31, 526336
buffer_load_dwordx4 a[40:43], %[vL16], %[sWS], s57 offen offset:0 sc1
)"
R"(s_add_u32 s57, s31, 526336
buffer_load_dwordx4 a[44:47], %[vL16], %[sWS], s57 offen offset:1024 sc1
s_add_u32 s57, s31, 528384
buffer_load_dwordx4 a[48:51], %[vL16], %[sWS], s57 offen offset:0 sc1
s_add_u32 s57, s31, 528384
buffer_load_dwordx4 a[52:55], %[vL16], %[sWS], s57 offen offset:1024 sc1
s_add_u32 s57, s31, 530432
buffer_load_dwordx4 a[56:59], %[vL16], %[sWS], s57 offen offset:0 sc1
s_add_u32 s57, s31, 530432
buffer_load_dwordx4 a[60:63], %[vL16], %[sWS], s57 offen offset:1024 sc1
s_add_u32 s57, s31, 1048576
buffer_load_dwordx4 a[64:67], %[vL16], %[sWS], s57 offen offset:0 sc1
s_add_u32 s57, s31, 1048576
buffer_load_dwordx4 a[68:71], %[vL16], %[sWS], s57 offen offset:1024 sc1
s_add_u32 s57, s31, 1050624
buffer_load_dwordx4 a[72:75], %[vL16], %[sWS], s57 offen offset:0 sc1
s_add_u32 s57, s31, 1050624
buffer_load_dwordx4 a[76:79], %[vL16], %[sWS], s57 offen offset:1024 sc1
s_add_u32 s57, s31, 1052672
buffer_load_dwordx4 a[80:83], %[vL16], %[sWS], s57 offen offset:0 sc1
s_add_u32 s57, s31, 1052672
buffer_load_dwordx4 a[84:87], %[vL16], %[sWS], s57 offen offset:1024 sc1
s_add_u32 s57, s31, 1054720
buffer_load_dwordx4 a[88:91], %[vL16], %[sWS], s57 offen offset:0 sc1
s_add_u32 s57, s31, 1054720
buffer_load_dwordx4 a[92:95], %[vL16], %[sWS], s57 offen offset:1024 sc1
buffer_load_dwordx4 v[232:235], %[vS], %[sWS], 0 offen offset:0 sc1
buffer_load_dwordx4 v[236:239], %[vS], %[sWS], 0 offen offset:16 sc1
buffer_load_dwordx4 v[240:243], %[vS], %[sWS], 0 offen offset:32 sc1
buffer_load_dwordx4 v[244:247], %[vS], %[sWS], 0 offen offset:48 sc1
s_waitcnt vmcnt(0)
v_accvgpr_read_b32 v104, a0
v_accvgpr_read_b32 v105, a4
v_accvgpr_read_b32 v106, a8
v_accvgpr_read_b32 v107, a12
v_accvgpr_read_b32 v108, a16
v_accvgpr_read_b32 v109, a20
v_min3_u32 v104, v104, v105, v106
v_min3_u32 v104, v104, v107, v108
v_min_u32 v104, v104, v109
v_cmp_eq_u32 vcc, 0, v104
s_cbranch_vccnz L_sl9_%=
s_branch L_ok9_%=
L_sl10_%=:
s_cmp_eq_u32 s69, 0
s_cbranch_scc1 L_ok10_%=
s_sub_u32 s69, s69, 1
s_add_u32 s57, s31, 6144
buffer_load_dwordx4 a[24:27], %[vL16], %[sWS], s57 offen offset:0 sc1
s_add_u32 s57, s31, 6144
buffer_load_dwordx4 a[28:31], %[vL16], %[sWS], s57 offen offset:1024 sc1
s_add_u32 s57, s31, 524288
buffer_load_dwordx4 a[32:35], %[vL16], %[sWS], s57 offen offset:0 sc1
s_add_u32 s57, s31, 524288
buffer_load_dwordx4 a[36:39], %[vL16], %[sWS], s57 offen offset:1024 sc1
s_add_u32 s57, s31, 526336
buffer_load_dwordx4 a[40:43], %[vL16], %[sWS], s57 offen offset:0 sc1
s_add_u32 s57, s31, 526336
buffer_load_dwordx4 a[44:47], %[vL16], %[sWS], s57 offen offset:1024 sc1
s_add_u32 s57, s31, 528384
buffer_load_dwordx4 a[48:51], %[vL16], %[sWS], s57 offen offset:0 sc1
s_add_u32 s57, s31, 528384
buffer_load_dwordx4 a[52:55], %[vL16], %[sWS], s57 offen offset:1024 sc1
s_add_u32 s57, s31, 530432
buffer_load_dwordx4 a[56:59], %[vL16], %[sWS], s57 offen offset:0 sc1
s_add_u32 s57, s31, 530432
buffer_load_dwordx4 a[60:63], %[vL16], %[sWS], s57 offen offset:1024 sc1
s_add_u32 s57, s31, 1048576
buffer_load_dwordx4 a[64:67], %[vL16], %[sWS], s57 offen offset:0 sc1
s_add_u32 s57, s31, 1048576
buffer_load_dwordx4 a[68:71], %[vL16], %[sWS], s57 offen offset:1024 sc1
s_add_u32 s57, s31, 1050624
buffer_load_dwordx4 a[72:75], %[vL16], %[sWS], s57 offen offset:0 sc1
s_add_u32 s57, s31, 1050624
buffer_load_dwordx4 a[76:79], %[vL16], %[sWS], s57 offen offset:1024 sc1
s_add_u32 s57, s31, 1052672
buffer_load_dwordx4 a[80:83], %[vL16], %[sWS], s57 offen offset:0 sc1
s_add_u32 s57, s31, 1052672
buffer_load_dwordx4 a[84:87], %[vL16], %[sWS], s57 offen offset:1024 sc1
s_add_u32 s57, s31, 1054720
buffer_load_dwordx4 a[88:91], %[vL16], %[sWS], s57 offen offset:0 sc1
s_add_u32 s57, s31, 1054720
buffer_load_dwordx4 a[92:95], %[vL16], %[sWS], s57 offen offset:1024 sc1
s_add_u32 s57, s31, 1572864
buffer_load_dwordx4 a[96:99], %[vL16], %[sWS], s57 offen offset:0 sc1
s_add_u32 s57, s31, 1572864
buffer_load_dwordx4 a[100:103], %[vL16], %[sWS], s57 offen offset:1024 sc1
s_add_u32 s57, s31, 1574912
buffer_load_dwordx4 a[104:107], %[vL16], %[sWS], s57 offen offset:0 sc1
s_add_u32 s57, s31, 1574912
buffer_load_dwordx4 a[108:111], %[vL16], %[sWS], s57 offen offset:1024 sc1
s_add_u32 s57, s31, 1576960
buffer_load_dwordx4 a[112:115], %[vL16], %[sWS], s57 offen offset:0 sc1
s_add_u32 s57, s31, 1576960
buffer_load_dwordx4 a[116:119], %[vL16], %[sWS], s57 offen offset:1024 sc1
buffer_load_dwordx4 v[232:235], %[vS], %[sWS], 0 offen offset:0 sc1
buffer_load_dwordx4 v[236:239], %[vS], %[sWS], 0 offen offset:16 sc1
buffer_load_dwordx4 v[240:243], %[vS], %[sWS], 0 offen offset:32 sc1
buffer_load_dwordx4 v[244:247], %[vS], %[sWS], 0 offen offset:48 sc1
s_waitcnt vmcnt(0)
v_accvgpr_read_b32 v104, a24
v_accvgpr_read_b32 v105, a28
v_accvgpr_read_b32 v106, a32
v_accvgpr_read_b32 v107, a34
v_accvgpr_read_b32 v108, a36
v_accvgpr_read_b32 v109, a38
v_accvgpr_read_b32 v110, a40
v_accvgpr_read_b32 v111, a44
v_min3_u32 v104, v104, v105, v106
v_min3_u32 v104, v104, v107, v108
v_min3_u32 v104, v104, v109, v110
v_min_u32 v104, v104, v111
v_cmp_eq_u32 vcc, 0, v104
s_cbranch_vccnz L_sl10_%=
s_branch L_ok10_%=
L_sl11_%=:
s_cmp_eq_u32 s69, 0
s_cbranch_scc1 L_ok11_%=
s_sub_u32 s69, s69, 1
s_add_u32 s57, s31, 528384
buffer_load_dwordx4 a[48:51], %[vL16], %[sWS], s57 offen offset:0 sc1
s_add_u32 s57, s31, 528384
buffer_load_dwordx4 a[52:55], %[vL16], %[sWS], s57 offen offset:1024 sc1
s_add_u32 s57, s31, 530432
buffer_load_dwordx4 a[56:59], %[vL16], %[sWS], s57 offen offset:0 sc1
s_add_u32 s57, s31, 530432
buffer_load_dwordx4 a[60:63], %[vL16], %[sWS], s57 offen offset:1024 sc1
s_add_u32 s57, s31, 1048576
buffer_load_dwordx4 a[64:67], %[vL16], %[sWS], s57 offen offset:0 sc1
s_add_u32 s57, s31, 1048576
buffer_load_dwordx4 a[68:71], %[vL16], %[sWS], s57 offen offset:1024 sc1
s_add_u32 s57, s31, 1050624
buffer_load_dwordx4 a[72:75], %[vL16], %[sWS], s57 offen offset:0 sc1
s_add_u32 s57, s31, 1050624
buffer_load_dwordx4 a[76:79], %[vL16], %[sWS], s57 offen offset:1024 sc1
s_add_u32 s57, s31, 1052672
buffer_load_dwordx4 a[80:83], %[vL16], %[sWS], s57 offen offset:0 sc1
s_add_u32 s57, s31, 1052672
buffer_load_dwordx4 a[84:87], %[vL16], %[sWS], s57 offen offset:1024 sc1
s_add_u32 s57, s31, 1054720
buffer_load_dwordx4 a[88:91], %[vL16], %[sWS], s57 offen offset:0 sc1
s_add_u32 s57, s31, 1054720
buffer_load_dwordx4 a[92:95], %[vL16], %[sWS], s57 offen offset:1024 sc1
s_add_u32 s57, s31, 1572864
buffer_load_dwordx4 a[96:99], %[vL16], %[sWS], s57 offen offset:0 sc1
s_add_u32 s57, s31, 1572864
buffer_load_dwordx4 a[100:103], %[vL16], %[sWS], s57 offen offset:1024 sc1
s_add_u32 s57, s31, 1574912
buffer_load_dwordx4 a[104:107], %[vL16], %[sWS], s57 offen offset:0 sc1
s_add_u32 s57, s31, 1574912
buffer_load_dwordx4 a[108:111], %[vL16], %[sWS], s57 offen offset:1024 sc1
s_add_u32 s57, s31, 1576960
buffer_load_dwordx4 a[112:115], %[vL16], %[sWS], s57 offen offset:0 sc1
s_add_u32 s57, s31, 1576960
buffer_load_dwordx4 a[116:119], %[vL16], %[sWS], s57 offen offset:1024 sc1
s_add_u32 s57, s31, 1579008
buffer_load_dwordx4 a[120:123], %[vL16], %[sWS], s57 offen offset:0 sc1
s_add_u32 s57, s31, 1579008
buffer_load_dwordx4 a[124:127], %[vL16], %[sWS], s57 offen offset:1024 sc1
s_add_u32 s57, s31, 25165824
buffer_load_dwordx4 a[128:131], %[vL16], %[sWS], s57 offen offset:0 sc1
s_add_u32 s57, s31, 25165824
buffer_load_dwordx4 a[132:135], %[vL16], %[sWS], s57 offen offset:1024 sc1
s_add_u32 s57, s31, 25167872
buffer_load_dwordx4 a[136:139], %[vL16], %[sWS], s57 offen offset:0 sc1
s_add_u32 s57, s31, 25167872
buffer_load_dwordx4 a[140:143], %[vL16], %[sWS], s57 offen offset:1024 sc1
buffer_load_dwordx4 v[232:235], %[vS], %[sWS], 0 offen offset:0 sc1
buffer_load_dwordx4 v[236:239], %[vS], %[sWS], 0 offen offset:16 sc1
buffer_load_dwordx4 v[240:243], %[vS], %[sWS], 0 offen offset:32 sc1
buffer_load_dwordx4 v[244:247], %[vS], %[sWS], 0 offen offset:48 sc1
s_waitcnt vmcnt(0)
v_accvgpr_read_b32 v104, a48
v_accvgpr_read_b32 v105, a52
v_accvgpr_read_b32 v106, a56
v_accvgpr_read_b32 v107, a60
v_accvgpr_read_b32 v108, a64
v_accvgpr_read_b32 v109, a66
v_accvgpr_read_b32 v110, a68
v_accvgpr_read_b32 v111, a70
v_min3_u32 v104, v104, v105, v106
v_min3_u32 v104, v104, v107, v108
v_min3_u32 v104, v104, v109, v110
v_min_u32 v104, v104, v111
v_cmp_eq_u32 vcc, 0, v104
s_cbranch_vccnz L_sl11_%=
s_branch L_ok11_%=
L_sl12_%=:
s_cmp_eq_u32 s69, 0
s_cbranch_scc1 L_ok12_%=
s_sub_u32 s69, s69, 1
s_add_u32 s57, s31, 1050624
buffer_load_dwordx4 a[72:75], %[vL16], %[sWS], s57 offen offset:0 sc1
s_add_u32 s57, s31, 1050624
buffer_load_dwordx4 a[76:79], %[vL16], %[sWS], s57 offen offset:1024 sc1
s_add_u32 s57, s31, 1052672
buffer_load_dwordx4 a[80:83], %[vL16], %[sWS], s57 offen offset:0 sc1
s_add_u32 s57, s31, 1052672
buffer_load_dwordx4 a[84:87], %[vL16], %[sWS], s57 offen offset:1024 sc1
s_add_u32 s57, s31, 1054720
)"
R"(buffer_load_dwordx4 a[88:91], %[vL16], %[sWS], s57 offen offset:0 sc1
s_add_u32 s57, s31, 1054720
buffer_load_dwordx4 a[92:95], %[vL16], %[sWS], s57 offen offset:1024 sc1
s_add_u32 s57, s31, 1572864
buffer_load_dwordx4 a[96:99], %[vL16], %[sWS], s57 offen offset:0 sc1
s_add_u32 s57, s31, 1572864
buffer_load_dwordx4 a[100:103], %[vL16], %[sWS], s57 offen offset:1024 sc1
s_add_u32 s57, s31, 1574912
buffer_load_dwordx4 a[104:107], %[vL16], %[sWS], s57 offen offset:0 sc1
s_add_u32 s57, s31, 1574912
buffer_load_dwordx4 a[108:111], %[vL16], %[sWS], s57 offen offset:1024 sc1
s_add_u32 s57, s31, 1576960
buffer_load_dwordx4 a[112:115], %[vL16], %[sWS], s57 offen offset:0 sc1
s_add_u32 s57, s31, 1576960
buffer_load_dwordx4 a[116:119], %[vL16], %[sWS], s57 offen offset:1024 sc1
s_add_u32 s57, s31, 1579008
buffer_load_dwordx4 a[120:123], %[vL16], %[sWS], s57 offen offset:0 sc1
s_add_u32 s57, s31, 1579008
buffer_load_dwordx4 a[124:127], %[vL16], %[sWS], s57 offen offset:1024 sc1
s_add_u32 s57, s31, 25165824
buffer_load_dwordx4 a[128:131], %[vL16], %[sWS], s57 offen offset:0 sc1
s_add_u32 s57, s31, 25165824
buffer_load_dwordx4 a[132:135], %[vL16], %[sWS], s57 offen offset:1024 sc1
s_add_u32 s57, s31, 25167872
buffer_load_dwordx4 a[136:139], %[vL16], %[sWS], s57 offen offset:0 sc1
s_add_u32 s57, s31, 25167872
buffer_load_dwordx4 a[140:143], %[vL16], %[sWS], s57 offen offset:1024 sc1
buffer_load_dwordx4 v[232:235], %[vS], %[sWS], 0 offen offset:0 sc1
buffer_load_dwordx4 v[236:239], %[vS], %[sWS], 0 offen offset:16 sc1
buffer_load_dwordx4 v[240:243], %[vS], %[sWS], 0 offen offset:32 sc1
buffer_load_dwordx4 v[244:247], %[vS], %[sWS], 0 offen offset:48 sc1
s_waitcnt vmcnt(0)
v_accvgpr_read_b32 v104, a72
v_accvgpr_read_b32 v105, a76
v_accvgpr_read_b32 v106, a80
v_accvgpr_read_b32 v107, a84
v_accvgpr_read_b32 v108, a88
v_accvgpr_read_b32 v109, a92
v_min3_u32 v104, v104, v105, v106
v_min3_u32 v104, v104, v107, v108
v_min_u32 v104, v104, v109
v_cmp_eq_u32 vcc, 0, v104
s_cbranch_vccnz L_sl12_%=
s_branch L_ok12_%=
L_sl13_%=:
s_cmp_eq_u32 s69, 0
s_cbranch_scc1 L_ok13_%=
s_sub_u32 s69, s69, 1
s_add_u32 s57, s31, 1572864
buffer_load_dwordx4 a[96:99], %[vL16], %[sWS], s57 offen offset:0 sc1
s_add_u32 s57, s31, 1572864
buffer_load_dwordx4 a[100:103], %[vL16], %[sWS], s57 offen offset:1024 sc1
s_add_u32 s57, s31, 1574912
buffer_load_dwordx4 a[104:107], %[vL16], %[sWS], s57 offen offset:0 sc1
s_add_u32 s57, s31, 1574912
buffer_load_dwordx4 a[108:111], %[vL16], %[sWS], s57 offen offset:1024 sc1
s_add_u32 s57, s31, 1576960
buffer_load_dwordx4 a[112:115], %[vL16], %[sWS], s57 offen offset:0 sc1
s_add_u32 s57, s31, 1576960
buffer_load_dwordx4 a[116:119], %[vL16], %[sWS], s57 offen offset:1024 sc1
s_add_u32 s57, s31, 1579008
buffer_load_dwordx4 a[120:123], %[vL16], %[sWS], s57 offen offset:0 sc1
s_add_u32 s57, s31, 1579008
buffer_load_dwordx4 a[124:127], %[vL16], %[sWS], s57 offen offset:1024 sc1
s_add_u32 s57, s31, 25165824
buffer_load_dwordx4 a[128:131], %[vL16], %[sWS], s57 offen offset:0 sc1
s_add_u32 s57, s31, 25165824
buffer_load_dwordx4 a[132:135], %[vL16], %[sWS], s57 offen offset:1024 sc1
s_add_u32 s57, s31, 25167872
buffer_load_dwordx4 a[136:139], %[vL16], %[sWS], s57 offen offset:0 sc1
s_add_u32 s57, s31, 25167872
buffer_load_dwordx4 a[140:143], %[vL16], %[sWS], s57 offen offset:1024 sc1
s_waitcnt vmcnt(0)
v_accvgpr_read_b32 v104, a96
v_accvgpr_read_b32 v105, a100
v_accvgpr_read_b32 v106, a104
v_accvgpr_read_b32 v107, a106
v_accvgpr_read_b32 v108, a108
v_accvgpr_read_b32 v109, a110
v_accvgpr_read_b32 v110, a112
v_accvgpr_read_b32 v111, a116
v_min3_u32 v104, v104, v105, v106
v_min3_u32 v104, v104, v107, v108
v_min3_u32 v104, v104, v109, v110
v_min_u32 v104, v104, v111
v_cmp_eq_u32 vcc, 0, v104
s_cbranch_vccnz L_sl13_%=
s_branch L_ok13_%=
L_sl14_%=:
s_cmp_eq_u32 s69, 0
s_cbranch_scc1 L_ok14_%=
s_sub_u32 s69, s69, 1
s_add_u32 s57, s31, 1579008
buffer_load_dwordx4 a[120:123], %[vL16], %[sWS], s57 offen offset:0 sc1
s_add_u32 s57, s31, 1579008
buffer_load_dwordx4 a[124:127], %[vL16], %[sWS], s57 offen offset:1024 sc1
s_add_u32 s57, s31, 25165824
buffer_load_dwordx4 a[128:131], %[vL16], %[sWS], s57 offen offset:0 sc1
s_add_u32 s57, s31, 25165824
buffer_load_dwordx4 a[132:135], %[vL16], %[sWS], s57 offen offset:1024 sc1
s_add_u32 s57, s31, 25167872
buffer_load_dwordx4 a[136:139], %[vL16], %[sWS], s57 offen offset:0 sc1
s_add_u32 s57, s31, 25167872
buffer_load_dwordx4 a[140:143], %[vL16], %[sWS], s57 offen offset:1024 sc1
s_waitcnt vmcnt(0)
v_accvgpr_read_b32 v104, a120
v_accvgpr_read_b32 v105, a124
v_accvgpr_read_b32 v106, a128
v_accvgpr_read_b32 v107, a132
v_accvgpr_read_b32 v108, a136
v_accvgpr_read_b32 v109, a138
v_accvgpr_read_b32 v110, a140
v_accvgpr_read_b32 v111, a142
v_min3_u32 v104, v104, v105, v106
v_min3_u32 v104, v104, v107, v108
v_min3_u32 v104, v104, v109, v110
v_min_u32 v104, v104, v111
v_cmp_eq_u32 vcc, 0, v104
s_cbranch_vccnz L_sl14_%=
s_branch L_ok14_%=
L_sl15_%=:
s_cmp_eq_u32 s69, 0
s_cbranch_scc1 L_ok15_%=
s_sub_u32 s69, s69, 1
buffer_load_dword v252, %[vFl], %[sWS], 0 offen offset:0 sc1
s_waitcnt vmcnt(0)
v_cmp_eq_u32 vcc, 0, v252
s_cbranch_vccnz L_sl15_%=
s_branch L_ok15_%=
L_sl16_%=:
s_cmp_eq_u32 s69, 0
s_cbranch_scc1 L_ok16_%=
s_sub_u32 s69, s69, 1
buffer_load_dword v252, v251, %[sWS], 0 offen offset:0 sc1
s_waitcnt vmcnt(0)
v_cmp_eq_u32 vcc, 0, v252
s_cbranch_vccnz L_sl16_%=
s_branch L_ok16_%=
L_sl17_%=:
s_cmp_eq_u32 s69, 0
s_cbranch_scc1 L_ok17_%=
s_sub_u32 s69, s69, 1
buffer_load_dwordx4 v[32:35], %[vL16], %[sWS], s59 offen offset:0 sc1
buffer_load_dwordx4 v[36:39], %[vL16], %[sWS], s59 offen offset:1024 sc1
buffer_load_dwordx4 v[40:43], %[vL16], %[sWS], s59 offen offset:2048 sc1
buffer_load_dwordx4 v[44:47], %[vL16], %[sWS], s59 offen offset:3072 sc1
buffer_load_dwordx4 v[48:51], %[vL16], %[sWS], s60 offen offset:0 sc1
buffer_load_dwordx4 v[52:55], %[vL16], %[sWS], s60 offen offset:1024 sc1
buffer_load_dwordx4 v[56:59], %[vL16], %[sWS], s60 offen offset:2048 sc1
buffer_load_dwordx4 v[60:63], %[vL16], %[sWS], s60 offen offset:3072 sc1
s_waitcnt vmcnt(0)
v_min_u32 v248, v32, v36
v_cmp_eq_u32 vcc, 0, v248
s_cbranch_vccnz L_sl17_%=
s_branch L_ok17_%=
L_sl18_%=:
s_cmp_eq_u32 s69, 0
s_cbranch_scc1 L_ok18_%=
s_sub_u32 s69, s69, 1
buffer_load_dwordx4 v[40:43], %[vL16], %[sWS], s59 offen offset:2048 sc1
buffer_load_dwordx4 v[44:47], %[vL16], %[sWS], s59 offen offset:3072 sc1
buffer_load_dwordx4 v[48:51], %[vL16], %[sWS], s60 offen offset:0 sc1
buffer_load_dwordx4 v[52:55], %[vL16], %[sWS], s60 offen offset:1024 sc1
buffer_load_dwordx4 v[56:59], %[vL16], %[sWS], s60 offen offset:2048 sc1
buffer_load_dwordx4 v[60:63], %[vL16], %[sWS], s60 offen offset:3072 sc1
buffer_load_dwordx4 v[64:67], %[vL16], %[sWS], s61 offen offset:0 sc1
buffer_load_dwordx4 v[68:71], %[vL16], %[sWS], s61 offen offset:1024 sc1
s_waitcnt vmcnt(0)
v_min_u32 v248, v40, v44
v_cmp_eq_u32 vcc, 0, v248
s_cbranch_vccnz L_sl18_%=
s_branch L_ok18_%=
L_sl19_%=:
s_cmp_eq_u32 s69, 0
s_cbranch_scc1 L_ok19_%=
s_sub_u32 s69, s69, 1
buffer_load_dwordx4 v[48:51], %[vL16], %[sWS], s60 offen offset:0 sc1
buffer_load_dwordx4 v[52:55], %[vL16], %[sWS], s60 offen offset:1024 sc1
buffer_load_dwordx4 v[56:59], %[vL16], %[sWS], s60 offen offset:2048 sc1
buffer_load_dwordx4 v[60:63], %[vL16], %[sWS], s60 offen offset:3072 sc1
buffer_load_dwordx4 v[64:67], %[vL16], %[sWS], s61 offen offset:0 sc1
buffer_load_dwordx4 v[68:71], %[vL16], %[sWS], s61 offen offset:1024 sc1
buffer_load_dwordx4 v[72:75], %[vL16], %[sWS], s61 offen offset:2048 sc1
buffer_load_dwordx4 v[76:79], %[vL16], %[sWS], s61 offen offset:3072 sc1
s_waitcnt vmcnt(0)
v_min_u32 v248, v48, v52
v_cmp_eq_u32 vcc, 0, v248
s_cbranch_vccnz L_sl19_%=
s_branch L_ok19_%=
L_sl20_%=:
s_cmp_eq_u32 s69, 0
s_cbranch_scc1 L_ok20_%=
s_sub_u32 s69, s69, 1
buffer_load_dwordx4 v[56:59], %[vL16], %[sWS], s60 offen offset:2048 sc1
buffer_load_dwordx4 v[60:63], %[vL16], %[sWS], s60 offen offset:3072 sc1
buffer_load_dwordx4 v[64:67], %[vL16], %[sWS], s61 offen offset:0 sc1
buffer_load_dwordx4 v[68:71], %[vL16], %[sWS], s61 offen offset:1024 sc1
buffer_load_dwordx4 v[72:75], %[vL16], %[sWS], s61 offen offset:2048 sc1
buffer_load_dwordx4 v[76:79], %[vL16], %[sWS], s61 offen offset:3072 sc1
buffer_load_dwordx4 v[80:83], v249, %[sWS], s59 offen offset:0 sc1
)"
R"(buffer_load_dwordx4 v[84:87], v249, %[sWS], s59 offen offset:1024 sc1
s_waitcnt vmcnt(0)
v_min_u32 v248, v56, v60
v_cmp_eq_u32 vcc, 0, v248
s_cbranch_vccnz L_sl20_%=
s_branch L_ok20_%=
L_sl21_%=:
s_cmp_eq_u32 s69, 0
s_cbranch_scc1 L_ok21_%=
s_sub_u32 s69, s69, 1
buffer_load_dwordx4 v[64:67], %[vL16], %[sWS], s61 offen offset:0 sc1
buffer_load_dwordx4 v[68:71], %[vL16], %[sWS], s61 offen offset:1024 sc1
buffer_load_dwordx4 v[72:75], %[vL16], %[sWS], s61 offen offset:2048 sc1
buffer_load_dwordx4 v[76:79], %[vL16], %[sWS], s61 offen offset:3072 sc1
buffer_load_dwordx4 v[80:83], v249, %[sWS], s59 offen offset:0 sc1
buffer_load_dwordx4 v[84:87], v249, %[sWS], s59 offen offset:1024 sc1
buffer_load_dwordx4 v[88:91], v249, %[sWS], s59 offen offset:2048 sc1
buffer_load_dwordx4 v[92:95], v249, %[sWS], s59 offen offset:3072 sc1
s_waitcnt vmcnt(0)
v_min_u32 v248, v64, v68
v_cmp_eq_u32 vcc, 0, v248
s_cbranch_vccnz L_sl21_%=
s_branch L_ok21_%=
L_sl22_%=:
s_cmp_eq_u32 s69, 0
s_cbranch_scc1 L_ok22_%=
s_sub_u32 s69, s69, 1
buffer_load_dwordx4 v[72:75], %[vL16], %[sWS], s61 offen offset:2048 sc1
buffer_load_dwordx4 v[76:79], %[vL16], %[sWS], s61 offen offset:3072 sc1
buffer_load_dwordx4 v[80:83], v249, %[sWS], s59 offen offset:0 sc1
buffer_load_dwordx4 v[84:87], v249, %[sWS], s59 offen offset:1024 sc1
buffer_load_dwordx4 v[88:91], v249, %[sWS], s59 offen offset:2048 sc1
buffer_load_dwordx4 v[92:95], v249, %[sWS], s59 offen offset:3072 sc1
buffer_load_dwordx4 v[96:99], v249, %[sWS], s60 offen offset:0 sc1
buffer_load_dwordx4 v[100:103], v249, %[sWS], s60 offen offset:1024 sc1
s_waitcnt vmcnt(0)
v_min_u32 v248, v72, v76
v_cmp_eq_u32 vcc, 0, v248
s_cbranch_vccnz L_sl22_%=
s_branch L_ok22_%=
L_sl23_%=:
s_cmp_eq_u32 s69, 0
s_cbranch_scc1 L_ok23_%=
s_sub_u32 s69, s69, 1
buffer_load_dwordx4 v[80:83], v249, %[sWS], s59 offen offset:0 sc1
buffer_load_dwordx4 v[84:87], v249, %[sWS], s59 offen offset:1024 sc1
buffer_load_dwordx4 v[88:91], v249, %[sWS], s59 offen offset:2048 sc1
buffer_load_dwordx4 v[92:95], v249, %[sWS], s59 offen offset:3072 sc1
buffer_load_dwordx4 v[96:99], v249, %[sWS], s60 offen offset:0 sc1
buffer_load_dwordx4 v[100:103], v249, %[sWS], s60 offen offset:1024 sc1
buffer_load_dwordx4 v[32:35], v249, %[sWS], s60 offen offset:2048 sc1
buffer_load_dwordx4 v[36:39], v249, %[sWS], s60 offen offset:3072 sc1
s_waitcnt vmcnt(0)
v_min_u32 v248, v80, v84
v_cmp_eq_u32 vcc, 0, v248
s_cbranch_vccnz L_sl23_%=
s_branch L_ok23_%=
L_sl24_%=:
s_cmp_eq_u32 s69, 0
s_cbranch_scc1 L_ok24_%=
s_sub_u32 s69, s69, 1
buffer_load_dwordx4 v[88:91], v249, %[sWS], s59 offen offset:2048 sc1
buffer_load_dwordx4 v[92:95], v249, %[sWS], s59 offen offset:3072 sc1
buffer_load_dwordx4 v[96:99], v249, %[sWS], s60 offen offset:0 sc1
buffer_load_dwordx4 v[100:103], v249, %[sWS], s60 offen offset:1024 sc1
buffer_load_dwordx4 v[32:35], v249, %[sWS], s60 offen offset:2048 sc1
buffer_load_dwordx4 v[36:39], v249, %[sWS], s60 offen offset:3072 sc1
buffer_load_dwordx4 v[40:43], v249, %[sWS], s61 offen offset:0 sc1
buffer_load_dwordx4 v[44:47], v249, %[sWS], s61 offen offset:1024 sc1
s_waitcnt vmcnt(0)
v_min_u32 v248, v88, v92
v_cmp_eq_u32 vcc, 0, v248
s_cbranch_vccnz L_sl24_%=
s_branch L_ok24_%=
L_sl25_%=:
s_cmp_eq_u32 s69, 0
s_cbranch_scc1 L_ok25_%=
s_sub_u32 s69, s69, 1
buffer_load_dwordx4 v[96:99], v249, %[sWS], s60 offen offset:0 sc1
buffer_load_dwordx4 v[100:103], v249, %[sWS], s60 offen offset:1024 sc1
buffer_load_dwordx4 v[32:35], v249, %[sWS], s60 offen offset:2048 sc1
buffer_load_dwordx4 v[36:39], v249, %[sWS], s60 offen offset:3072 sc1
buffer_load_dwordx4 v[40:43], v249, %[sWS], s61 offen offset:0 sc1
buffer_load_dwordx4 v[44:47], v249, %[sWS], s61 offen offset:1024 sc1
buffer_load_dwordx4 v[48:51], v249, %[sWS], s61 offen offset:2048 sc1
buffer_load_dwordx4 v[52:55], v249, %[sWS], s61 offen offset:3072 sc1
s_waitcnt vmcnt(0)
v_min_u32 v248, v96, v100
v_cmp_eq_u32 vcc, 0, v248
s_cbranch_vccnz L_sl25_%=
s_branch L_ok25_%=
L_sl26_%=:
s_cmp_eq_u32 s69, 0
s_cbranch_scc1 L_ok26_%=
s_sub_u32 s69, s69, 1
buffer_load_dwordx4 v[32:35], v249, %[sWS], s60 offen offset:2048 sc1
buffer_load_dwordx4 v[36:39], v249, %[sWS], s60 offen offset:3072 sc1
buffer_load_dwordx4 v[40:43], v249, %[sWS], s61 offen offset:0 sc1
buffer_load_dwordx4 v[44:47], v249, %[sWS], s61 offen offset:1024 sc1
buffer_load_dwordx4 v[48:51], v249, %[sWS], s61 offen offset:2048 sc1
buffer_load_dwordx4 v[52:55], v249, %[sWS], s61 offen offset:3072 sc1
buffer_load_dwordx4 v[56:59], %[vL16], %[sWS], s62 offen offset:0 sc1
buffer_load_dwordx4 v[60:63], %[vL16], %[sWS], s62 offen offset:1024 sc1
s_waitcnt vmcnt(0)
v_min_u32 v248, v32, v36
v_cmp_eq_u32 vcc, 0, v248
s_cbranch_vccnz L_sl26_%=
s_branch L_ok26_%=
L_sl27_%=:
s_cmp_eq_u32 s69, 0
s_cbranch_scc1 L_ok27_%=
s_sub_u32 s69, s69, 1
buffer_load_dwordx4 v[40:43], v249, %[sWS], s61 offen offset:0 sc1
buffer_load_dwordx4 v[44:47], v249, %[sWS], s61 offen offset:1024 sc1
buffer_load_dwordx4 v[48:51], v249, %[sWS], s61 offen offset:2048 sc1
buffer_load_dwordx4 v[52:55], v249, %[sWS], s61 offen offset:3072 sc1
buffer_load_dwordx4 v[56:59], %[vL16], %[sWS], s62 offen offset:0 sc1
buffer_load_dwordx4 v[60:63], %[vL16], %[sWS], s62 offen offset:1024 sc1
buffer_load_dwordx4 v[64:67], %[vL16], %[sWS], s62 offen offset:2048 sc1
buffer_load_dwordx4 v[68:71], %[vL16], %[sWS], s62 offen offset:3072 sc1
s_waitcnt vmcnt(0)
v_min_u32 v248, v40, v44
v_cmp_eq_u32 vcc, 0, v248
s_cbranch_vccnz L_sl27_%=
s_branch L_ok27_%=
L_sl28_%=:
s_cmp_eq_u32 s69, 0
s_cbranch_scc1 L_ok28_%=
s_sub_u32 s69, s69, 1
buffer_load_dwordx4 v[48:51], v249, %[sWS], s61 offen offset:2048 sc1
buffer_load_dwordx4 v[52:55], v249, %[sWS], s61 offen offset:3072 sc1
buffer_load_dwordx4 v[56:59], %[vL16], %[sWS], s62 offen offset:0 sc1
buffer_load_dwordx4 v[60:63], %[vL16], %[sWS], s62 offen offset:1024 sc1
buffer_load_dwordx4 v[64:67], %[vL16], %[sWS], s62 offen offset:2048 sc1
buffer_load_dwordx4 v[68:71], %[vL16], %[sWS], s62 offen offset:3072 sc1
s_waitcnt vmcnt(0)
v_min_u32 v248, v48, v52
v_cmp_eq_u32 vcc, 0, v248
s_cbranch_vccnz L_sl28_%=
s_branch L_ok28_%=
L_sl29_%=:
s_cmp_eq_u32 s69, 0
s_cbranch_scc1 L_ok29_%=
s_sub_u32 s69, s69, 1
buffer_load_dwordx4 v[56:59], %[vL16], %[sWS], s62 offen offset:0 sc1
buffer_load_dwordx4 v[60:63], %[vL16], %[sWS], s62 offen offset:1024 sc1
buffer_load_dwordx4 v[64:67], %[vL16], %[sWS], s62 offen offset:2048 sc1
buffer_load_dwordx4 v[68:71], %[vL16], %[sWS], s62 offen offset:3072 sc1
s_waitcnt vmcnt(0)
v_min3_u32 v248, v56, v58, v60
v_min_u32 v248, v248, v62
v_cmp_eq_u32 vcc, 0, v248
s_cbranch_vccnz L_sl29_%=
s_branch L_ok29_%=
L_sl30_%=:
s_cmp_eq_u32 s69, 0
s_cbranch_scc1 L_ok30_%=
s_sub_u32 s69, s69, 1
buffer_load_dwordx4 v[64:67], %[vL16], %[sWS], s62 offen offset:2048 sc1
buffer_load_dwordx4 v[68:71], %[vL16], %[sWS], s62 offen offset:3072 sc1
s_waitcnt vmcnt(0)
v_min3_u32 v248, v64, v66, v68
v_min_u32 v248, v248, v70
v_cmp_eq_u32_e64 vcc, 0, v248
s_cbranch_vccnz L_sl30_%=
s_branch L_ok30_%=
L_own5_%=:
LPb_12:
s_cbranch_execz LRb
v_mfma_f32_16x16x32_bf16 v[176:179], a[180:183], v[64:67], v[176:179]
s_nop 0
v_mfma_f32_16x16x32_bf16 v[212:215], a[180:183], v[68:71], v[212:215]
s_nop 0
v_mfma_f32_16x16x32_bf16 v[180:183], a[184:187], v[64:67], v[180:183]
s_nop 0
v_mfma_f32_16x16x32_bf16 v[216:219], a[184:187], v[68:71], v[216:219]
s_nop 0
v_mfma_f32_16x16x32_bf16 v[184:187], a[188:191], v[64:67], v[184:187]
s_nop 0
v_mfma_f32_16x16x32_bf16 v[220:223], a[188:191], v[68:71], v[220:223]
ds_write_b128 %[vXr2], v[176:179] offset:0
v_mfma_f32_16x16x32_bf16 v[188:191], a[192:195], v[64:67], v[188:191]
ds_write_b128 %[vXr2], v[212:215] offset:9216
v_mfma_f32_16x16x32_bf16 v[224:227], a[192:195], v[68:71], v[224:227]
ds_write_b128 %[vXr2], v[180:183] offset:1024
v_mfma_f32_16x16x32_bf16 v[192:195], a[196:199], v[64:67], v[192:195]
ds_write_b128 %[vXr2], v[216:219] offset:10240
v_mfma_f32_16x16x32_bf16 v[228:231], a[196:199], v[68:71], v[228:231]
ds_write_b128 %[vXr2], v[184:187] offset:2048
v_mfma_f32_16x16x32_bf16 v[196:199], a[200:203], v[64:67], v[196:199]
ds_write_b128 %[vXr2], v[220:223] offset:11264
v_mfma_f32_16x16x32_bf16 v[232:235], a[200:203], v[68:71], v[232:235]
ds_write_b128 %[vXr2], v[188:191] offset:3072
v_mfma_f32_16x16x32_bf16 v[200:203], a[204:207], v[64:67], v[200:203]
ds_write_b128 %[vXr2], v[224:227] offset:12288
v_mfma_f32_16x16x32_bf16 v[236:239], a[204:207], v[68:71], v[236:239]
ds_write_b128 %[vXr2], v[192:195] offset:4096
v_mfma_f32_16x16x32_bf16 v[204:207], a[208:211], v[64:67], v[204:207]
ds_write_b128 %[vXr2], v[228:231] offset:13312
v_mfma_f32_16x16x32_bf16 v[240:243], a[208:211], v[68:71], v[240:243]
ds_write_b128 %[vXr2], v[196:199] offset:5120
)"
R"(v_mfma_f32_16x16x32_bf16 v[208:211], a[212:215], v[64:67], v[208:211]
ds_write_b128 %[vXr2], v[232:235] offset:14336
v_mfma_f32_16x16x32_bf16 v[244:247], a[212:215], v[68:71], v[244:247]
ds_write_b128 %[vXr2], v[200:203] offset:6144
s_nop 2
ds_write_b128 %[vXr2], v[236:239] offset:15360
s_nop 2
ds_write_b128 %[vXr2], v[204:207] offset:7168
s_nop 2
ds_write_b128 %[vXr2], v[240:243] offset:16384
s_nop 2
ds_write_b128 %[vXr2], v[208:211] offset:8192
s_nop 2
ds_write_b128 %[vXr2], v[244:247] offset:17408
s_nop 2
s_nop 2
s_nop 2
s_nop 2
s_waitcnt lgkmcnt(0)
s_branch L_join5_%=
L_end2_%=:
LPb_13:
s_cbranch_execz LRb
L_pe3:
)"
    : "+{s16}"(r_s16), "+{s17}"(r_s17), "+{s18}"(r_s18), "+{s19}"(r_s19), "+{s20}"(r_s20), "+{s21}"(r_s21), "+{s22}"(r_s22), "+{s23}"(r_s23), "+{s24}"(r_s24), "+{s25}"(r_s25), "+{s26}"(r_s26), "+{s27}"(r_s27), "+{s28}"(r_s28), "+{s29}"(r_s29), "+{s30}"(r_s30), "+{s31}"(r_s31), "+{s32}"(r_s32), "+{s33}"(r_s33), "+{s34}"(r_s34), "+{s35}"(r_s35), "+{s36}"(r_s36), "+{s37}"(r_s37), "+{s38}"(r_s38), "+{s39}"(r_s39), "+{s40}"(r_s40), "+{s41}"(r_s41), "+{s42}"(r_s42), "+{s43}"(r_s43), "+{s44}"(r_s44), "+{s45}"(r_s45), "+{s46}"(r_s46), "+{s47}"(r_s47), "+{s69}"(r_s69), "+{s71}"(r_s71), "+{s76}"(r_s76), "+{v[96:127]}"(r_v96), "+{v[128:159]}"(r_v128), "+{v[160:191]}"(r_v160), "+{v[192:223]}"(r_v192), "+{v[224:255]}"(r_v224), "+v"(tid), "+s"(bid), [yAS] "+{v24}"(yAS), [yAS2] "+{v25}"(yAS2), [yCan] "+{v26}"(yCan), [yF] "+{v27}"(yF), [yF2] "+{v28}"(yF2), [yF8] "+{v29}"(yF8), [yL8] "+{v30}"(yL8), [yRc] "+{v32}"(yRc), [ySt] "+{v33}"(ySt), [yU1] "+{v34}"(yU1), [yV1] "+{v35}"(yV1), [yX2] "+{v36}"(yX2)
    : [sB] "s"(sB), [sInv] "s"(sInv), [sLx] "s"(sLx), [sMsk] "s"(sMsk), [sNt] "s"(sNt), [sOwn] "s"(sOwn), [sWS] "s"(sWS), [vA1] "v"(vA1), [vA2] "v"(vA2), [vB] "v"(vB), [vCp] "v"(vCp), [vEr] "v"(vEr), [vEw4] "v"(vEw4), [vFl] "v"(vFl), [vFs] "v"(vFs), [vL16] "v"(vL16), [vOwn] "v"(vOwn), [vS] "v"(vS), [vT1] "v"(vT1), [vTr] "v"(vTr), [vTw] "v"(vTw), [vU8] "v"(vU8), [vXr0] "v"(vXr0), [vXr1] "v"(vXr1), [vXr2] "v"(vXr2), [vXr3] "v"(vXr3), [vY4] "v"(vY4)
    : "memory", "vcc", "scc", "m0","v31","v37","v38","v39","v40","v41","v42","v43","v44","v45","v46","v47","v48","v49","v50","v51","v52","v53","v54","v55","v56","v57","v58","v59","v60","v61","v62","v63","v64","v65","v66","v67","v68","v69","v70","v71","v72","v73","v74","v75","v76","v77","v78","v79","v80","v81","v82","v83","v84","v85","v86","v87","v88","v89","v90","v91","v92","v93","v94","v95","a0","a1","a2","a3","a4","a5","a6","a7","a8","a9","a10","a11","a12","a13","a14","a15","a16","a17","a18","a19","a20","a21","a22","a23","a24","a25","a26","a27","a28","a29","a30","a31","a32","a33","a34","a35","a36","a37","a38","a39","a40","a41","a42","a43","a44","a45","a46","a47","a48","a49","a50","a51","a52","a53","a54","a55","a56","a57","a58","a59","a60","a61","a62","a63","a64","a65","a66","a67","a68","a69","a70","a71","a72","a73","a74","a75","a76","a77","a78","a79","a80","a81","a82","a83","a84","a85","a86","a87","a88","a89","a90","a91","a92","a93","a94","a95","a96","a97","a98","a99","a100","a101","a102","a103","a104","a105","a106","a107","a108","a109","a110","a111","a112","a113","a114","a115","a116","a117","a118","a119","a120","a121","a122","a123","a124","a125","a126","a127","a128","a129","a130","a131","a132","a133","a134","a135","a136","a137","a138","a139","a140","a141","a142","a143","a144","a145","a146","a147","a148","a149","a150","a151","a152","a153","a154","a155","a156","a157","a158","a159","a160","a161","a162","a163","a164","a165","a166","a167","a168","a169","a170","a171","a172","a173","a174","a175","a176","a177","a178","a179","a180","a181","a182","a183","a184","a185","a186","a187","a188","a189","a190","a191","a192","a193","a194","a195","a196","a197","a198","a199","a200","a201","a202","a203","a204","a205","a206","a207","a208","a209","a210","a211","a212","a213","a214","a215","a216","a217","a218","a219","a220","a221","a222","a223","a224","a225","a226","a227","a228","a229","a230","a231","a232","a233","a234","a235","a236","a237","a238","a239","a240","a241","a242","a243","a244","a245","a246","a247","a248","a249","a250","a251","a252","a253","a254","a255","s48","s49","s50","s51","s53","s54","s55","s57","s58","s59","s60","s61","s62","s72","s73","s74","s75","s98","s99");
  }
  bid = RFL(bid);
  {
  const unsigned hp = RFL(r_s71) >> 13, half = RFL(r_s76);
  const unsigned lane = tid & 63, w = RFL(tid >> 6), b = w & 1, hf = w >> 1;
  const unsigned xcd = bid & 7, jj = bid >> 3, kq = (xcd ^ KQX) & 3, rb = half * 4 + (jj >> 3), mt = 2 * rb + (jj & 1), nt = (jj >> 1) & 3, sHl = (jj & 7);
  const unsigned ldsb = 0, OOB = 0x7f000000u, vCp = CODE_OFF;
  const srd_t sWS = make_srd(ws, WS_END);
  const unsigned sLx = (ldsb + w * 9216), s2Xo = (WS_PK + (nt * 128 + kq * 32 + w) * 9216), sFBr = nr(WS_FB, hp) + (((2 * mt + hf) & 15) * 4 + kq) * 128;
  const unsigned r64 = ((sFBr) >> 9) & 15, gw4 = ((bid * 4 + w) * 4) & 4095;
  const unsigned vY2 = nr(WS_O + kq * 4 * MB + r64 * 262144, hp) + b * 2048 + lane * 16, sHl15 = sHl << 15;
  const unsigned ct = kq * 4 + nt, j = lane & 15, g = lane >> 4, sg = mt * 4 + w;
  const unsigned vL16 = (lane * 16), vL8 = lane * 8;
  const unsigned vXr0 = ldsb + vL16, vXr1 = vXr0 + 36864, vXr2 = vXr0 + 2 * 36864, vXr3 = vXr0 + 3 * 36864;
  const unsigned bLo = RFL(r_s16), bHi = RFL(r_s17), oLo = RFL(r_s18), oHi = RFL(r_s19), sPo3 = WS_PAR + 131072 + gw4 * 32, sPo4 = WS_PAR + 262144 + gw4 * 8;
  const srd_t sXin = make_srd(x, S_ * D_ * 2), sB = make_srd((void*)(bLo | (size_t)bHi << 32), S_ * 2), sOut = make_srd((void*)(oLo | (size_t)oHi << 32), S_ * D_ * 2);
  const unsigned vU1 = vXr2 + (b * 9 + hf * 4) * 1024, vV1 = vXr2 + (b * 9 + 8) * 1024, vX2 = ldsb + 4 * 36864 + 1024 + tid * 16;
  const unsigned s = half * 1024 + (r64 >> 1) * 128 + kq * 32 + rowperm(b, j), d0 = nt * 144 + 64 * hf + 8 * g, d8 = nt * 144 + 128 + 4 * g, m8 = (hf - 1) & OOB;
  const unsigned vR = (s * D_ + d0) * 2, vR8 = (s * D_ + d8) * 2 | m8;
  const unsigned vG = WS_MOD + (2 * D_ + d0) * 4, vG8 = WS_MOD + (2 * D_ + d8) * 4, vG58 = vG8 + 3 * D_ * 4, vRc = vL16 + hf * 2048;
  const unsigned vFC = WS_FB + (tid >> 1) * 128 + (tid & 1) * 16;
  const unsigned sOwn = w ^ kq, tl = ((mt & 7) * 4 + nt) * 4, sx = (kq - w - 1) & 3, sSnd = nr(WS_E2 + sx * 2 * MB + (tl + w) * 16384, hp), sRcv = nr(WS_E2 + (tl + kq) * 16384, hp) + b * 4096;
  const unsigned er = nr(WS_E2R, hp) + tl * 8192, sSnd2 = er + hf * 16384 + b * 4096 + sx * 1024, sRcv2 = RFL(r_s33), sRcv5 = sRcv2 - MB;
  const unsigned fc = bid, strip = fc / 9, fn = fc - strip * 9, fc2 = 256 + (fc & 31), strip2 = fc2 / 9, fn2 = fc2 - strip2 * 9, m2 = fc < 32 ? 0 : OOB;
  const unsigned sJob = RFL(win_stride(strip, fn) * 1024), sJob2 = RFL(fc < 32 ? win_stride(strip2, fn2) * 1024 : 0), wb = WS_PK + (unsigned)PK_WIN + win_base(strip, fn) * 1024, wb2 = WS_PK + (unsigned)PK_WIN + win_base(strip2, fn2) * 1024;
  const unsigned vW = wb + vL16, vW2 = wb2 + vL16, vT = WS_MOD + 13824 + (lane & 3) * 1152 + g * 16;
  const unsigned pk = ((sOwn - 1) & 3) * 8 + (lane >> 3), mp = pk < 18 ? 0 : OOB, vWp = (wb + pk * sJob + (lane & 7) * 128) | mp, vWp2 = (wb2 + pk * sJob2 + (lane & 7) * 128) | mp | m2;
  const unsigned vPk = pk, vPl = (lane & 7) * 128 | mp, sWb = wb, sWb2 = wb2 | m2;
  const unsigned mj = (j | sOwn) == 0 ? 0 : OOB;
  const unsigned yb = nr(WS_Y, hp) + ((mt & 7) * 4 + kq) * 16384, ybb = yb + b * 1024, k0 = (nt * 144 + 64 * hf) >> 5, dg = ((nt & 1) * 2 + g), yl = (j * 16 + (dg & 3) * 256), hAS = (g * 32 | mj), hSt = (rowperm(0, j) * 64 | (g == 0 ? 0 : OOB)), hF8 = (j * 16 + (g >> 1) * 256 + (g & 1) * 8);
  const unsigned aS1 = RFL(r_s28), aS2 = RFL(r_s29), ySb = RFL(r_s44);
  const unsigned Y0 = RFL(r_s40), Y1 = RFL(r_s41), Y2 = RFL(r_s42), Y8 = RFL(r_s43);
  unsigned yAS = aS1 | hAS, yAS2 = aS2 | hAS, ySt = ySb + hSt;
  unsigned yF = yl + (dg < 4 ? Y0 : Y1), yF2 = yl + (dg < 4 ? Y1 : Y2), yF8 = Y8 + hF8, yU1 = vU1, yV1 = vV1, yX2 = vX2, yRc = vRc, yL8 = vL8;
  const unsigned ck8 = 18 * (j >> 2) + 4 * (j & 2) + 4; unsigned yCan = yoff(ck8 >> 2) + (j & 1) * 1024 + (ck8 & 3) * 256;
  const unsigned long long sMsk = hf * 0x3f800000u; const unsigned sInv = RFL(__float_as_uint(hf ? 1.f / 80 : 1.f / 64));
  const unsigned tbl = ldsb + 4 * 36864 + w * 256, vOwn = vXr1 + w * 9216;
  const unsigned s3Yo = RFL(r_s31), vS = s3Yo + 24 * MB + 4096 + (lane & 31) * 64, ctb = RFL(r_s30), vA2 = ctb + 16 * 1152 + j * 8, vA1 = ctb + g * 32;
  const unsigned vTw = tbl + (lane & 31) * 4, vTr = tbl + g * 32, vT1 = tbl + rowperm(0, j) * 4;
  const unsigned ex = RFL(r_s45), hEw = ((ct >> 3) * 16384 + (ct & 7) * 1024 + g * 64 + (j >> 2) * 16), vEw4 = ex + hEw, hEr = ((lane >> 5) * 16384 + (lane & 31) * 8), vEr = ex + hEr;
  const unsigned sMt16 = RFL(r_s26), fz = RFL(r_s46), vFl = fz + (j & 7) * 4, vFs = lane == 0 ? fz + (sMt16 >> 14) : OOB;
  const unsigned vB = half * 2048 + (sMt16 >> 8) + w * 64 + 2 * rowperm(0, j), y4b = RFL(r_s47), vY4 = y4b + vL16;
  const unsigned s3Xo = WS_PK + (unsigned)PK_WIN + ct * 324 * 1024 + w * 9216, sXo4 = nr(WS_Z2 + (ct * 32 + w) * 16384, hp), sXr4 = nr(WS_Z2 + 8 * MB, hp) + (ct >> 1) * 16384 + (ct & 1) * 4096 + w * 1024;
  const unsigned sUo = nr(WS_U + ((sg & 31) * 4 + kq) * 65536, hp), sUr = RFL(r_s37), sNt = nt, vU8 = sUr + (nt >> 1) * 2048 + (nt & 1) * 512 + hF8;
  const unsigned vG5 = vG + 3 * D_ * 4;
  const unsigned hw = w < 3 ? 5120 * w : 13312, sLh = ldsb + hw, x5 = WS_PK + (unsigned)PK_WOUT + (nt * 72 + kq * 18) * 9216, s5X4 = x5 + 16 * 9216 + hw;
  const unsigned s5X0 = x5 + (4 * nt + w) * 9216, s5X1 = x5 + (4 * ((nt + 1 + hf) & 3) + b) * 9216, s5X2 = x5 + (4 * ((nt + 3 - 2 * hf) & 3) + w) * 9216, s5X3 = x5 + (4 * ((nt + 2 + hf) & 3) + 2 + b) * 9216;
  asm volatile(
R"(L_pb4:
s_nop 4
.p2align 3
buffer_load_dwordx4 v[194:197], %[vG5], %[sWS], 0 offen offset:0 sc1
buffer_load_dwordx4 v[198:201], %[vG5], %[sWS], 0 offen offset:16 sc1
buffer_load_dwordx4 v[202:205], %[vG5], %[sWS], 0 offen offset:128 sc1
buffer_load_dwordx4 v[206:209], %[vG5], %[sWS], 0 offen offset:144 sc1
buffer_load_dwordx4 v[210:213], %[vG58], %[sWS], 0 offen offset:0 sc1
ds_read_b128 v[40:43], %[vU1] offset:0
ds_read_b128 v[44:47], %[vU1] offset:1024
ds_read_b128 v[48:51], %[vU1] offset:2048
ds_read_b128 v[52:55], %[vU1] offset:3072
ds_read_b128 v[56:59], %[vV1] offset:0
ds_read_b128 v[184:187], %[vX2] offset:0
ds_read_b128 v[188:191], %[vX2] offset:4096
ds_read_b64 v[192:193], %[vX2] offset:8192
s_add_u32 s53, s32, -8388608
buffer_load_dwordx4 v[80:83], %[vRc], %[sWS], s53 offen offset:0 sc1
buffer_load_dwordx4 v[84:87], %[vRc], %[sWS], s53 offen offset:1024 sc1
buffer_load_dwordx2 v[88:89], %[vL8], %[sWS], %[sRcv5] offen offset:0 sc1
s_add_u32 s53, s32, -6291456
s_xor_b32 s53, s53, 8192
buffer_load_dwordx4 v[90:93], %[vRc], %[sWS], s53 offen offset:0 sc1
buffer_load_dwordx4 v[94:97], %[vRc], %[sWS], s53 offen offset:1024 sc1
buffer_load_dwordx2 v[98:99], %[vL8], %[sWS], %[sRcv5] offen offset:1024 sc1
s_add_u32 s53, s32, -4194304
buffer_load_dwordx4 v[100:103], %[vRc], %[sWS], s53 offen offset:0 sc1
buffer_load_dwordx4 v[104:107], %[vRc], %[sWS], s53 offen offset:1024 sc1
buffer_load_dwordx2 v[108:109], %[vL8], %[sWS], %[sRcv5] offen offset:2048 sc1
s_waitcnt vmcnt(9)
s_waitcnt lgkmcnt(0)
v_lshlrev_b32_e64 v60, 16, v184
v_and_b32 v61, 4294901760, v184
v_lshlrev_b32_e64 v62, 16, v185
v_and_b32 v63, 4294901760, v185
v_lshlrev_b32_e64 v64, 16, v186
v_and_b32 v65, 4294901760, v186
v_lshlrev_b32_e64 v66, 16, v187
v_and_b32 v67, 4294901760, v187
v_lshlrev_b32_e64 v68, 16, v188
v_and_b32 v69, 4294901760, v188
v_lshlrev_b32_e64 v70, 16, v189
v_and_b32 v71, 4294901760, v189
v_lshlrev_b32_e64 v72, 16, v190
v_and_b32 v73, 4294901760, v190
v_lshlrev_b32_e64 v74, 16, v191
v_and_b32 v75, 4294901760, v191
v_lshlrev_b32_e64 v76, 16, v192
v_and_b32 v77, 4294901760, v192
v_lshlrev_b32_e64 v78, 16, v193
v_and_b32 v79, 4294901760, v193
v_pk_fma_f32 v[40:41], v[40:41], v[194:195], v[60:61]
v_pk_fma_f32 v[42:43], v[42:43], v[196:197], v[62:63]
v_pk_fma_f32 v[44:45], v[44:45], v[198:199], v[64:65]
v_pk_fma_f32 v[46:47], v[46:47], v[200:201], v[66:67]
v_pk_fma_f32 v[48:49], v[48:49], v[202:203], v[68:69]
v_pk_fma_f32 v[50:51], v[50:51], v[204:205], v[70:71]
v_pk_fma_f32 v[52:53], v[52:53], v[206:207], v[72:73]
v_pk_fma_f32 v[54:55], v[54:55], v[208:209], v[74:75]
v_pk_fma_f32 v[56:57], v[56:57], v[210:211], v[76:77]
v_pk_fma_f32 v[58:59], v[58:59], v[212:213], v[78:79]
s_waitcnt vmcnt(0)
v_min3_u32 v234, v80, v81, v82
v_min3_u32 v234, v234, v83, v84
v_min3_u32 v234, v234, v85, v86
v_min3_u32 v234, v234, v87, v88
v_min_u32 v234, v234, v89
v_min3_u32 v235, v90, v91, v92
v_min3_u32 v235, v235, v93, v94
v_min3_u32 v235, v235, v95, v96
v_min3_u32 v235, v235, v97, v98
v_min_u32_e64 v235, v235, v99
v_min3_u32 v236, v100, v101, v102
v_min3_u32 v236, v236, v103, v104
v_min3_u32 v236, v236, v105, v106
v_min3_u32 v236, v236, v107, v108
v_min_u32_e64 v236, v236, v109
v_min3_u32 v234, v234, v235, v236
v_cmp_eq_u32 vcc, 0, v234
s_cbranch_vccnz L_sl31_%=
L_ok31_%=:
v_lshlrev_b32_e64 v60, 16, v80
v_and_b32 v61, 4294901760, v80
v_lshlrev_b32_e64 v62, 16, v81
v_and_b32 v63, 4294901760, v81
v_lshlrev_b32_e64 v64, 16, v82
v_and_b32 v65, 4294901760, v82
v_lshlrev_b32_e64 v66, 16, v83
v_and_b32 v67, 4294901760, v83
v_lshlrev_b32_e64 v68, 16, v84
v_and_b32 v69, 4294901760, v84
v_lshlrev_b32_e64 v70, 16, v85
v_and_b32 v71, 4294901760, v85
v_lshlrev_b32_e64 v72, 16, v86
v_and_b32 v73, 4294901760, v86
v_lshlrev_b32_e64 v74, 16, v87
v_and_b32 v75, 4294901760, v87
v_lshlrev_b32_e64 v76, 16, v88
v_and_b32 v77, 4294901760, v88
v_lshlrev_b32_e64 v78, 16, v89
v_and_b32 v79, 4294901760, v89
v_lshlrev_b32_e64 v112, 16, v90
v_and_b32 v113, 4294901760, v90
v_lshlrev_b32_e64 v114, 16, v91
v_and_b32 v115, 4294901760, v91
v_lshlrev_b32_e64 v116, 16, v92
v_and_b32 v117, 4294901760, v92
v_lshlrev_b32_e64 v118, 16, v93
v_and_b32 v119, 4294901760, v93
v_lshlrev_b32_e64 v120, 16, v94
v_and_b32 v121, 4294901760, v94
v_lshlrev_b32_e64 v122, 16, v95
v_and_b32 v123, 4294901760, v95
v_lshlrev_b32_e64 v124, 16, v96
v_and_b32 v125, 4294901760, v96
v_lshlrev_b32_e64 v126, 16, v97
v_and_b32 v127, 4294901760, v97
v_lshlrev_b32_e64 v128, 16, v98
v_and_b32 v129, 4294901760, v98
v_lshlrev_b32_e64 v130, 16, v99
v_and_b32 v131, 4294901760, v99
v_pk_add_f32 v[60:61], v[60:61], v[112:113]
v_pk_add_f32 v[62:63], v[62:63], v[114:115]
v_pk_add_f32 v[64:65], v[64:65], v[116:117]
v_pk_add_f32 v[66:67], v[66:67], v[118:119]
v_pk_add_f32 v[68:69], v[68:69], v[120:121]
v_pk_add_f32 v[70:71], v[70:71], v[122:123]
v_pk_add_f32 v[72:73], v[72:73], v[124:125]
v_pk_add_f32 v[74:75], v[74:75], v[126:127]
v_pk_add_f32 v[76:77], v[76:77], v[128:129]
v_pk_add_f32 v[78:79], v[78:79], v[130:131]
v_lshlrev_b32_e64 v112, 16, v100
v_and_b32 v113, 4294901760, v100
v_lshlrev_b32_e64 v114, 16, v101
v_and_b32 v115, 4294901760, v101
v_lshlrev_b32_e64 v116, 16, v102
v_and_b32 v117, 4294901760, v102
v_lshlrev_b32_e64 v118, 16, v103
v_and_b32 v119, 4294901760, v103
v_lshlrev_b32_e64 v120, 16, v104
v_and_b32 v121, 4294901760, v104
v_lshlrev_b32_e64 v122, 16, v105
v_and_b32 v123, 4294901760, v105
v_lshlrev_b32_e64 v124, 16, v106
v_and_b32 v125, 4294901760, v106
v_lshlrev_b32_e64 v126, 16, v107
v_and_b32 v127, 4294901760, v107
v_lshlrev_b32_e64 v128, 16, v108
v_and_b32 v129, 4294901760, v108
v_lshlrev_b32_e64 v130, 16, v109
v_and_b32 v131, 4294901760, v109
v_pk_add_f32 v[60:61], v[60:61], v[112:113]
v_pk_add_f32 v[62:63], v[62:63], v[114:115]
v_pk_add_f32 v[64:65], v[64:65], v[116:117]
v_pk_add_f32 v[66:67], v[66:67], v[118:119]
v_pk_add_f32 v[68:69], v[68:69], v[120:121]
v_pk_add_f32 v[70:71], v[70:71], v[122:123]
v_pk_add_f32 v[72:73], v[72:73], v[124:125]
v_pk_add_f32 v[74:75], v[74:75], v[126:127]
v_pk_add_f32 v[76:77], v[76:77], v[128:129]
v_pk_add_f32 v[78:79], v[78:79], v[130:131]
v_pk_fma_f32 v[40:41], v[60:61], v[194:195], v[40:41]
v_pk_fma_f32 v[42:43], v[62:63], v[196:197], v[42:43]
v_pk_fma_f32 v[44:45], v[64:65], v[198:199], v[44:45]
v_pk_fma_f32 v[46:47], v[66:67], v[200:201], v[46:47]
v_pk_fma_f32 v[48:49], v[68:69], v[202:203], v[48:49]
v_pk_fma_f32 v[50:51], v[70:71], v[204:205], v[50:51]
v_pk_fma_f32 v[52:53], v[72:73], v[206:207], v[52:53]
v_pk_fma_f32 v[54:55], v[74:75], v[208:209], v[54:55]
v_pk_fma_f32 v[56:57], v[76:77], v[210:211], v[56:57]
v_pk_fma_f32 v[58:59], v[78:79], v[212:213], v[58:59]
v_cvt_pk_bf16_f32 v80, v40, v41
v_cvt_pk_bf16_f32 v81, v42, v43
v_cvt_pk_bf16_f32 v82, v44, v45
v_cvt_pk_bf16_f32 v83, v46, v47
v_cvt_pk_bf16_f32 v84, v48, v49
v_cvt_pk_bf16_f32 v85, v50, v51
v_cvt_pk_bf16_f32 v86, v52, v53
v_cvt_pk_bf16_f32 v87, v54, v55
v_cvt_pk_bf16_f32 v88, v56, v57
v_cvt_pk_bf16_f32 v89, v58, v59
buffer_store_dwordx4 v[80:83], %[vR], %[sOut], 0 offen offset:0 sc1
buffer_store_dwordx4 v[84:87], %[vR], %[sOut], 0 offen offset:64 sc1
buffer_store_dwordx2 v[88:89], %[vR8], %[sOut], 0 offen offset:0 sc1
s_branch L_end3_%=
L_sl31_%=:
s_cmp_eq_u32 s69, 0
s_cbranch_scc1 L_ok31_%=
s_sub_u32 s69, s69, 1
s_add_u32 s53, s32, -8388608
buffer_load_dwordx4 v[80:83], %[vRc], %[sWS], s53 offen offset:0 sc1
buffer_load_dwordx4 v[84:87], %[vRc], %[sWS], s53 offen offset:1024 sc1
buffer_load_dwordx2 v[88:89], %[vL8], %[sWS], %[sRcv5] offen offset:0 sc1
s_add_u32 s53, s32, -6291456
s_xor_b32 s53, s53, 8192
buffer_load_dwordx4 v[90:93], %[vRc], %[sWS], s53 offen offset:0 sc1
buffer_load_dwordx4 v[94:97], %[vRc], %[sWS], s53 offen offset:1024 sc1
buffer_load_dwordx2 v[98:99], %[vL8], %[sWS], %[sRcv5] offen offset:1024 sc1
s_add_u32 s53, s32, -4194304
buffer_load_dwordx4 v[100:103], %[vRc], %[sWS], s53 offen offset:0 sc1
)"
R"(buffer_load_dwordx4 v[104:107], %[vRc], %[sWS], s53 offen offset:1024 sc1
buffer_load_dwordx2 v[108:109], %[vL8], %[sWS], %[sRcv5] offen offset:2048 sc1
s_waitcnt vmcnt(0)
v_min3_u32 v234, v80, v81, v82
v_min3_u32 v234, v234, v83, v84
v_min3_u32 v234, v234, v85, v86
v_min3_u32 v234, v234, v87, v88
v_min_u32 v234, v234, v89
v_min3_u32 v235, v90, v91, v92
v_min3_u32 v235, v235, v93, v94
v_min3_u32 v235, v235, v95, v96
v_min3_u32 v235, v235, v97, v98
v_min_u32_e64 v235, v235, v99
v_min3_u32 v236, v100, v101, v102
v_min3_u32 v236, v236, v103, v104
v_min3_u32 v236, v236, v105, v106
v_min3_u32 v236, v236, v107, v108
v_min_u32_e64 v236, v236, v109
v_min3_u32 v234, v234, v235, v236
v_cmp_eq_u32 vcc, 0, v234
s_cbranch_vccnz L_sl31_%=
s_branch L_ok31_%=
L_end3_%=:
code_end_lbl:
L_pe4:
)"
    : "+{s16}"(r_s16), "+{s17}"(r_s17), "+{s18}"(r_s18), "+{s19}"(r_s19), "+{s20}"(r_s20), "+{s21}"(r_s21), "+{s22}"(r_s22), "+{s23}"(r_s23), "+{s24}"(r_s24), "+{s25}"(r_s25), "+{s26}"(r_s26), "+{s27}"(r_s27), "+{s28}"(r_s28), "+{s29}"(r_s29), "+{s30}"(r_s30), "+{s31}"(r_s31), "+{s32}"(r_s32), "+{s33}"(r_s33), "+{s34}"(r_s34), "+{s35}"(r_s35), "+{s36}"(r_s36), "+{s37}"(r_s37), "+{s38}"(r_s38), "+{s39}"(r_s39), "+{s40}"(r_s40), "+{s41}"(r_s41), "+{s42}"(r_s42), "+{s43}"(r_s43), "+{s44}"(r_s44), "+{s45}"(r_s45), "+{s46}"(r_s46), "+{s47}"(r_s47), "+{s69}"(r_s69), "+{s71}"(r_s71), "+{s76}"(r_s76)
    : [sOut] "s"(sOut), [sRcv5] "s"(sRcv5), [sWS] "s"(sWS), [vG5] "v"(vG5), [vG58] "v"(vG58), [vL8] "v"(vL8), [vR] "v"(vR), [vR8] "v"(vR8), [vRc] "v"(vRc), [vU1] "v"(vU1), [vV1] "v"(vV1), [vX2] "v"(vX2)
    : "memory", "vcc", "scc", "m0","v40","v41","v42","v43","v44","v45","v46","v47","v48","v49","v50","v51","v52","v53","v54","v55","v56","v57","v58","v59","v60","v61","v62","v63","v64","v65","v66","v67","v68","v69","v70","v71","v72","v73","v74","v75","v76","v77","v78","v79","v80","v81","v82","v83","v84","v85","v86","v87","v88","v89","v90","v91","v92","v93","v94","v95","v96","v97","v98","v99","v100","v101","v102","v103","v104","v105","v106","v107","v108","v109","v110","v111","v112","v113","v114","v115","v116","v117","v118","v119","v120","v121","v122","v123","v124","v125","v126","v127","v128","v129","v130","v131","v132","v133","v134","v135","v136","v137","v138","v139","v140","v141","v142","v143","v144","v145","v146","v147","v148","v149","v150","v151","v152","v153","v154","v155","v156","v157","v158","v159","v160","v161","v162","v163","v164","v165","v166","v167","v168","v169","v170","v171","v172","v173","v174","v175","v176","v177","v178","v179","v180","v181","v182","v183","v184","v185","v186","v187","v188","v189","v190","v191","v192","v193","v194","v195","v196","v197","v198","v199","v200","v201","v202","v203","v204","v205","v206","v207","v208","v209","v210","v211","v212","v213","v214","v215","v216","v217","v218","v219","v220","v221","v222","v223","v224","v225","v226","v227","v228","v229","v230","v231","v232","v233","v234","v235","v236","v237","v238","v239","v240","v241","v242","v243","v244","v245","v246","v247","v248","v249","v250","v251","v252","v253","v254","v255","a0","a1","a2","a3","a4","a5","a6","a7","a8","a9","a10","a11","a12","a13","a14","a15","a16","a17","a18","a19","a20","a21","a22","a23","a24","a25","a26","a27","a28","a29","a30","a31","a32","a33","a34","a35","a36","a37","a38","a39","a40","a41","a42","a43","a44","a45","a46","a47","a48","a49","a50","a51","a52","a53","a54","a55","a56","a57","a58","a59","a60","a61","a62","a63","a64","a65","a66","a67","a68","a69","a70","a71","a72","a73","a74","a75","a76","a77","a78","a79","a80","a81","a82","a83","a84","a85","a86","a87","a88","a89","a90","a91","a92","a93","a94","a95","a96","a97","a98","a99","a100","a101","a102","a103","a104","a105","a106","a107","a108","a109","a110","a111","a112","a113","a114","a115","a116","a117","a118","a119","a120","a121","a122","a123","a124","a125","a126","a127","a128","a129","a130","a131","a132","a133","a134","a135","a136","a137","a138","a139","a140","a141","a142","a143","a144","a145","a146","a147","a148","a149","a150","a151","a152","a153","a154","a155","a156","a157","a158","a159","a160","a161","a162","a163","a164","a165","a166","a167","a168","a169","a170","a171","a172","a173","a174","a175","a176","a177","a178","a179","a180","a181","a182","a183","a184","a185","a186","a187","a188","a189","a190","a191","a192","a193","a194","a195","a196","a197","a198","a199","a200","a201","a202","a203","a204","a205","a206","a207","a208","a209","a210","a211","a212","a213","a214","a215","a216","a217","a218","a219","a220","a221","a222","a223","a224","a225","a226","a227","a228","a229","a230","a231","a232","a233","a234","a235","a236","a237","a238","a239","a240","a241","a242","a243","a244","a245","a246","a247","a248","a249","a250","a251","a252","a253","a254","a255","s48","s49","s50","s51","s52","s53","s54","s55","s56","s57","s58","s59","s60","s61","s62","s63","s64","s65","s66","s67","s68","s70","s72","s73","s74","s75","s77","s78","s79","s80","s81","s82","s83","s84","s85","s86","s87","s88","s89","s90","s91","s92","s93","s94","s95","s96","s97","s98","s99");
  }
}

extern "C" void kernel_launch(void* const* d_in, const int* in_sizes, int n_in, void* d_out, int out_size, void* d_ws, size_t ws_size, hipStream_t stream) {
  const bf16_t* x = (const bf16_t*)d_in[0]; const bf16_t* kc = (const bf16_t*)d_in[1]; const bf16_t* vc = (const bf16_t*)d_in[2];
  const bf16_t* wq = (const bf16_t*)d_in[3]; const bf16_t* wo = (const bf16_t*)d_in[4];
  const bf16_t* w_in = (const bf16_t*)d_in[6]; const bf16_t* w_sp = (const bf16_t*)d_in[7]; const bf16_t* b_sp = (const bf16_t*)d_in[8]; const bf16_t* w_out = (const bf16_t*)d_in[9];
  const bf16_t* c = (const bf16_t*)d_in[10]; const bf16_t* w_mod = (const bf16_t*)d_in[11];
  char* ws = (char*)d_ws;
  (void)hipFuncSetAttribute((const void*)k_main, hipFuncAttributeMaxDynamicSharedMemorySize, MAIN_LDS);
  k_pre<<<616, 256, 0, stream>>>((const char*)c, (const char*)w_mod, (const char*)x, ws, (const char*)b_sp, (const char*)wo, (const char*)w_in, (const char*)w_out, (const char*)kc, (const char*)vc, (const char*)w_sp, (const char*)d_out);
  k_main<<<256, 256, MAIN_LDS, stream>>>(ws, wq, kc, vc, w_sp, x, b_sp, (bf16_t*)d_out);
}
```
